```python
import torch

def _softmax(x, axis=-1):
    return torch.softmax(x, dim=axis)
def _gelu(x):
    return torch.nn.functional.gelu(x, approximate="tanh")
def _silu(x):
    return torch.nn.functional.silu(x)
def _sigmoid(x):
    return torch.sigmoid(x)
def _layernorm(x, axis=-1, eps=1e-5):
    m = x.mean(dim=axis, keepdim=True)
    v = x.var(dim=axis, keepdim=True, unbiased=False)
    return (x - m) / torch.sqrt(v + eps)
def _scatter_add(tgt, idx, src, axis=0):
    return tgt.clone().index_add_(axis, idx, src)
def _segment_sum(src, seg, n):
    out = torch.zeros((n,) + src.shape[1:], dtype=src.dtype)
    return out.index_add_(0, seg, src)
def _pad(x, pw):
    flat = tuple(v for pair in reversed(pw) for v in pair)
    return torch.nn.functional.pad(x, flat)

kernel_name = 'gated_mix__ssm_conv_s4d__linear_attn_block__prompt'

def setup_inputs():

    _w = (lambda a: (lambda t: t.to(torch.bfloat16) if t.is_floating_point() else t)(torch.as_tensor(a)))
    def _A_setup():
        Bsz, L, D, N = 1, 2048, 640, 192
        return {
            "u": _w((lambda *s: torch.randn(*s, dtype=torch.bfloat16) * 0.05)(Bsz, L, D)),
            "log_A_real": _w((lambda *s: torch.randn(*s, dtype=torch.bfloat16) * 6.4)(D, N) * 0.5),
            "A_imag": _w((lambda *s: torch.randn(*s, dtype=torch.bfloat16) * 0.05)(D, N) * 3.14),
            "C_real": _w((lambda *s: torch.randn(*s, dtype=torch.bfloat16) * 0.2)(D, N) / N ** 0.5),
            "C_imag": _w((lambda *s: torch.randn(*s, dtype=torch.bfloat16) * 0.2)(D, N) / N ** 0.5),
            "log_dt": _w((lambda *s: torch.randn(*s, dtype=torch.bfloat16) * 0.05)(D) * 0.5 - 4.0),
            "D_skip": _w((lambda *s: torch.randn(*s, dtype=torch.bfloat16) * 0.05)(D)),
        }
    _A_in = _A_setup()

    def _B_setup():
        B, S, D = 1, 2048, 640
        return {
            "x": _w((lambda *s: torch.randn(*s, dtype=torch.bfloat16) * 0.05)(B, S, D)),
            "wqkv": _w((lambda *s: torch.randn(*s, dtype=torch.bfloat16) * 0.8)(D, 3 * D)),
            "wo": _w((lambda *s: torch.randn(*s, dtype=torch.bfloat16) * 0.05)(D, D)),
        }
    _B_in = _B_setup()

    return {
        "A_u": _A_in["u"],
        "A_log_A_real": _A_in["log_A_real"],
        "A_A_imag": _A_in["A_imag"],
        "A_C_real": _A_in["C_real"],
        "A_C_imag": _A_in["C_imag"],
        "A_log_dt": _A_in["log_dt"],
        "A_D_skip": _A_in["D_skip"],
        "B_x": _B_in["x"],
        "B_wqkv": _B_in["wqkv"],
        "B_wo": _B_in["wo"],
        "gate_w": _w((lambda *s: torch.randn(*s, dtype=torch.bfloat16) * 0.05)(640) * 16.0),
    }

def reference(A_u, A_log_A_real, A_A_imag, A_C_real, A_C_imag, A_log_dt, A_D_skip, B_x, B_wqkv, B_wo, gate_w):
    def _A_ref(u, log_A_real, A_imag, C_real, C_imag, log_dt, D_skip):
        L = u.shape[1]
        dt = torch.exp(log_dt)
        A = -torch.exp(log_A_real) + 1j * A_imag
        C = (C_real + 1j * C_imag) * dt[:, None]
        pos = (lambda x, dt: x.to(dt))(torch.arange(L), log_dt.dtype)
        P = torch.exp((A * dt[:, None])[:, :, None] * pos[None, None, :])
        K = 2.0 * torch.einsum("dn,dnl->dl", C, P).real
        L2 = 2 * L
        u_t = (lambda x, *d: x.permute(*d))(u, 0, 2, 1)
        Kf = (lambda x, *a, **k: torch.fft.rfft(x.float(), *a, **k))(K, L2)
        Uf = (lambda x, *a, **k: torch.fft.rfft(x.float(), *a, **k))(u_t, L2)
        y = (lambda x, *a, **k: torch.fft.irfft(x, *a, **k).to(torch.bfloat16))(Uf * Kf[None, :, :], L2)[:, :, :L]
        y = (lambda x, *d: x.permute(*d))(y, 0, 2, 1)
        return y + u * D_skip[None, None, :]

    def _B_ref(x, wqkv, wo):
        B, S, D = x.shape
        H = 32; Dh = D // H
        qkv = (lambda x, *d: x.permute(*d))((x @ wqkv).reshape(B, S, 3, H, Dh), 2, 0, 3, 1, 4)
        q, k, v = qkv[0], qkv[1], qkv[2]
        phi = lambda t: torch.where(t > 0, t + 1, torch.exp(t)) + 1e-6
        pq, pk = phi(q), phi(k)
        kv = torch.einsum("bhsd,bhse->bhde", pk, v)
        z = torch.einsum("bhsd->bhd", pk)
        num = torch.einsum("bhqd,bhde->bhqe", pq, kv)
        den = torch.einsum("bhqd,bhd->bhq", pq, z)[..., None] + 1e-6
        o = num / den
        return (lambda x, *d: x.permute(*d))(o, 0, 2, 1, 3).reshape(B, S, D) @ wo

    x = A_u
    g = _sigmoid((x * gate_w).sum(-1, keepdims=True))
    a = _A_ref(x, A_log_A_real, A_A_imag, A_C_real, A_C_imag, A_log_dt, A_D_skip)
    b = _B_ref(x, B_wqkv, B_wo)
    return g * a + (1 - g) * b
```

```cpp
#include <hip/hip_runtime.h>
#include <hip/hip_bf16.h>
typedef __hip_bfloat16 bf16;
constexpr int L=2048, D=640, N=192, H=32, DH=20;
__device__ __forceinline__ float b2f(bf16 v){ return __bfloat162float(v);}
__device__ __forceinline__ float rb(float v){ return __bfloat162float(__float2bfloat16(v)); }

__global__ void k_kgen(const bf16* logA,const bf16* Aim,const bf16* Cr,const bf16* Ci,const bf16* logdt,float* K){
  int d=blockIdx.y; int l=blockIdx.x*256+threadIdx.x;
  float dt=rb(expf(b2f(logdt[d])));
  float pos=rb((float)l);
  float acc=0;
  for(int n=0;n<N;n++){
    float ar=-rb(expf(b2f(logA[d*N+n])))*dt; float ai=b2f(Aim[d*N+n])*dt;
    float cr=b2f(Cr[d*N+n])*dt, ci=b2f(Ci[d*N+n])*dt;
    float e=expf(ar*pos); float s,c; sincosf(ai*pos,&s,&c);
    acc+=cr*e*c-ci*e*s;
  }
  K[d*L+l]=2.f*acc;
}
__global__ void k_conv(const bf16* u,const float* K,const bf16* Dskip,float* a){
  int d=blockIdx.y; int l=blockIdx.x*256+threadIdx.x;
  float acc=0;
  for(int j=0;j<=l;j++) acc+=K[d*L+j]*b2f(u[(l-j)*D+d]);
  float y=rb(acc);
  a[l*D+d]=rb(y+rb(b2f(u[l*D+d])*b2f(Dskip[d])));
}
__global__ void k_gemm(const bf16* x,const bf16* w,float* out,int Kd,int Nn){
  int n=blockIdx.x*256+threadIdx.x; int m=blockIdx.y; if(n>=Nn) return;
  float acc=0; for(int k=0;k<Kd;k++) acc+=b2f(x[m*Kd+k])*b2f(w[k*Nn+n]);
  out[m*Nn+n]=rb(acc);
}
__device__ __forceinline__ float phi(float t){ float r = t>0? rb(t+1.f): rb(expf(t)); return rb(r+1e-6f);}
__global__ void k_kv(const float* qkv,float* kv,float* z){
  int h=blockIdx.x; int t=threadIdx.x;
  if(t<400){ int dd=t/20,e=t%20; float acc=0; for(int s=0;s<L;s++) acc+=phi(qkv[s*1920+640+h*20+dd])*qkv[s*1920+1280+h*20+e]; kv[h*400+t]=rb(acc);}
  else if(t<420){ int dd=t-400; float acc=0; for(int s=0;s<L;s++) acc+=phi(qkv[s*1920+640+h*20+dd]); z[h*20+dd]=rb(acc);}
}
__global__ void k_o(const float* qkv,const float* kv,const float* z,bf16* o){
  int s=blockIdx.x; int c=threadIdx.x; int h=c/20,e=c%20;
  float num=0,den=0; for(int dd=0;dd<20;dd++){ float p=phi(qkv[s*1920+h*20+dd]); num+=p*kv[h*400+dd*20+e]; den+=p*z[h*20+dd]; }
  num=rb(num); den=rb(rb(den)+1e-6f);
  o[s*D+c]=__float2bfloat16(num/den);
}
__global__ void k_final(const bf16* x,const bf16* gw,const float* a,const float* b,bf16* out){
  int s=blockIdx.x; int c=threadIdx.x;
  __shared__ float red[640];
  red[c]=rb(b2f(x[s*D+c])*b2f(gw[c])); __syncthreads();
  __shared__ float gs;
  if(c==0){ float acc=0; for(int i=0;i<640;i++) acc+=red[i]; acc=rb(acc); gs=rb(1.f/(1.f+expf(-acc))); }
  __syncthreads();
  float g=gs;
  out[s*D+c]=__float2bfloat16(rb(g*a[s*D+c])+rb(rb(1.f-g)*b[s*D+c]));
}
extern "C" void kernel_launch(void* const* d_in, const int* in_sizes, int n_in, void* d_out, int out_size, void* d_ws, size_t ws_size, hipStream_t stream){
  const bf16* u=(const bf16*)d_in[0]; const bf16* logA=(const bf16*)d_in[1]; const bf16* Aim=(const bf16*)d_in[2];
  const bf16* Cr=(const bf16*)d_in[3]; const bf16* Ci=(const bf16*)d_in[4]; const bf16* logdt=(const bf16*)d_in[5];
  const bf16* Dsk=(const bf16*)d_in[6]; const bf16* wqkv=(const bf16*)d_in[8]; const bf16* wo=(const bf16*)d_in[9]; const bf16* gw=(const bf16*)d_in[10];
  float* ws=(float*)d_ws;
  float* K=ws; float* a=K+D*L; float* qkv=a+L*D; float* kv=qkv+L*1920; float* z=kv+H*400; float* b=z+H*20; bf16* o=(bf16*)(b+L*D);
  k_kgen<<<dim3(L/256,D),256,0,stream>>>(logA,Aim,Cr,Ci,logdt,K);
  k_conv<<<dim3(L/256,D),256,0,stream>>>(u,K,Dsk,a);
  k_gemm<<<dim3(8,L),256,0,stream>>>(u,wqkv,qkv,640,1920);
  k_kv<<<H,448,0,stream>>>(qkv,kv,z);
  k_o<<<L,640,0,stream>>>(qkv,kv,z,o);
  k_gemm<<<dim3(3,L),256,0,stream>>>(o,wo,b,640,640);
  k_final<<<L,640,0,stream>>>(u,gw,a,b,(bf16*)d_out);
}
```

```cpp
#include <hip/hip_runtime.h>
#include <hip/hip_bf16.h>
#include <stdint.h>

typedef unsigned short u16;
typedef __attribute__((ext_vector_type(8))) short s16x8;
typedef __attribute__((ext_vector_type(4))) short s16x4;
typedef __attribute__((ext_vector_type(4))) float f32x4;
typedef __attribute__((ext_vector_type(16))) float f32x16;
typedef __attribute__((ext_vector_type(4))) unsigned u32x4;
typedef __attribute__((ext_vector_type(2))) unsigned u32x2;
struct __attribute__((packed, aligned(2))) U16B { u32x4 v; };
typedef __attribute__((address_space(3))) s16x4* lds_s16x4_p;

constexpr int L = 2048, D = 640, NS = 192, NH = 32;
constexpr int UT_PAD = 32, UT_STRIDE = L + UT_PAD;
constexpr size_t WS_UT   = 0;
constexpr size_t WS_KIMG = WS_UT + (size_t)D * UT_STRIDE * 2;
constexpr size_t WS_PQ   = WS_KIMG + (size_t)D * 8192;
constexpr size_t WS_PART = WS_PQ + (size_t)L * D * 2;
constexpr size_t WS_GATE = WS_PART + (size_t)16 * 32 * 420 * 4;
constexpr size_t WS_WOF  = WS_GATE + (size_t)L * 4;
constexpr size_t WS_KVT  = WS_WOF + (size_t)D * D * 2;
constexpr size_t WS_AT   = WS_KVT + (size_t)32 * 2 * 64 * 16;
constexpr size_t WS_END  = WS_AT + (size_t)D * L * 2;

__device__ __forceinline__ float b2f(u16 v) { return __uint_as_float(((unsigned)v) << 16); }
__device__ __forceinline__ u16 f2b(float f) { __hip_bfloat16 h = __float2bfloat16(f); return *reinterpret_cast<u16*>(&h); }
__device__ __forceinline__ float rb(float f) { return b2f(f2b(f)); }
__device__ __forceinline__ float phi_f(float t) {
  float r = t > 0.f ? rb(t + 1.f) : rb(__expf(t));
  return rb(r + 1e-6f);
}
__device__ __forceinline__ unsigned pack2(float lo, float hi) { return (unsigned)f2b(lo) | ((unsigned)f2b(hi) << 16); }
__device__ __forceinline__ unsigned packhi(float lo, float hi) { return __builtin_amdgcn_perm(__float_as_uint(hi), __float_as_uint(lo), 0x07060302u); }

struct cf { float r, i; };
__device__ __forceinline__ cf cmul(cf a, cf b) { cf o; o.r = a.r * b.r - a.i * b.i; o.i = a.r * b.i + a.i * b.r; return o; }
__device__ __forceinline__ cf zpow(float ar, float ai, float p) {
  float e = expf(ar * p); float s, c; sincosf(ai * p, &s, &c); cf o; o.r = e * c; o.i = e * s; return o;
}

constexpr int K1_LDS = 50688;
constexpr int NB_GEMM = 192, NB_TR = 64, NB_WO = 50, NB_KG = 640;

template <bool ISQ>
__device__ __forceinline__ void gemm_role(char* smem, const u16* __restrict__ x, const u16* __restrict__ W,
                                          u16* __restrict__ pq, float* __restrict__ part, int rowblk, int cb, int rot) {
  const int tid = threadIdx.x, wave = tid >> 6, lane = tid & 63;
  const int row0 = rowblk * 128;
  const int seg0 = ISQ ? 160 * (cb - 8) : 640 + 80 * cb;
  const int seg1 = ISQ ? seg0 + 80 : 1280 + 80 * cb;
  char* sA = smem; char* sB = smem + 20480;
  const u16* ga[4]; int la[4];
#pragma unroll
  for (int i = 0; i < 4; i++) { int id = tid + 256 * i; int r = id >> 3, ch = id & 7; ga[i] = x + (size_t)(row0 + r) * D + ch * 8; la[i] = r * 160 + ch * 16; }
  const u16* gb[5]; int lb[5];
#pragma unroll
  for (int i = 0; i < 5; i++) { int id = tid + 256 * i; int kr = id / 20, ch = id % 20; int col = (ch < 10 ? seg0 + ch * 8 : seg1 + (ch - 10) * 8);
    gb[i] = W + (size_t)kr * 1920 + col; lb[i] = kr * 320 + (kr >> 3) * 32 + ch * 16; }
  u32x4 ra[4], rbv[5];
  f32x4 acc[2][10];
#pragma unroll
  for (int m = 0; m < 2; m++)
#pragma unroll
    for (int c = 0; c < 10; c++) acc[m][c] = (f32x4){0.f, 0.f, 0.f, 0.f};
  int kt = rot;
  {
    int k0 = kt * 64;
#pragma unroll
    for (int i = 0; i < 4; i++) ra[i] = *(const u32x4*)(ga[i] + k0);
#pragma unroll
    for (int i = 0; i < 5; i++) rbv[i] = *(const u32x4*)(gb[i] + (size_t)k0 * 1920);
  }
  const int g = lane >> 4, fr = lane & 15;
  const int aoff = (wave * 32 + fr) * 160 + g * 16;
  const int q4 = (lane & 15) >> 2, p4 = lane & 3;
  const int boff = (8 * g + q4) * 320 + g * 32 + p4 * 8;
  for (int t = 0; t < 10; t++) {
    __syncthreads();
#pragma unroll
    for (int i = 0; i < 4; i++) *(u32x4*)(sA + la[i]) = ra[i];
#pragma unroll
    for (int i = 0; i < 5; i++) *(u32x4*)(sB + lb[i]) = rbv[i];
    if (t < 9) {
      kt = kt + 1; if (kt >= 10) kt = 0;
      int k0 = kt * 64;
#pragma unroll
      for (int i = 0; i < 4; i++) ra[i] = *(const u32x4*)(ga[i] + k0);
#pragma unroll
      for (int i = 0; i < 5; i++) rbv[i] = *(const u32x4*)(gb[i] + (size_t)k0 * 1920);
    }
    __syncthreads();
#pragma unroll
    for (int ks = 0; ks < 2; ks++) {
      s16x8 af[2];
#pragma unroll
      for (int m = 0; m < 2; m++) af[m] = *(const s16x8*)(sA + aoff + m * 16 * 160 + ks * 64);
#pragma unroll
      for (int c = 0; c < 10; c++) {
        const char* bp = sB + boff + ks * (32 * 320 + 4 * 32) + c * 32;
        s16x4 b0 = __builtin_amdgcn_ds_read_tr16_b64_v4i16((lds_s16x4_p)(bp));
        s16x4 b1 = __builtin_amdgcn_ds_read_tr16_b64_v4i16((lds_s16x4_p)(bp + 4 * 320));
        s16x8 bf = __builtin_shufflevector(b0, b1, 0, 1, 2, 3, 4, 5, 6, 7);
#pragma unroll
        for (int m = 0; m < 2; m++) {
          if (ISQ) acc[m][c] = __builtin_amdgcn_mfma_f32_16x16x32_bf16(bf, af[m], acc[m][c], 0, 0, 0);
          else     acc[m][c] = __builtin_amdgcn_mfma_f32_16x16x32_bf16(af[m], bf, acc[m][c], 0, 0, 0);
        }
      }
    }
  }
  if (ISQ) {
#pragma unroll
    for (int m = 0; m < 2; m++)
#pragma unroll
      for (int c = 0; c < 10; c++) {
        u32x2 o;
        o[0] = pack2(phi_f(rb(acc[m][c][0])), phi_f(rb(acc[m][c][1])));
        o[1] = pack2(phi_f(rb(acc[m][c][2])), phi_f(rb(acc[m][c][3])));
        *(u32x2*)(pq + (size_t)(row0 + wave * 32 + 16 * m + fr) * D + seg0 + 16 * c + 4 * g) = o;
      }
  } else {
    s16x8 pk[5], vv[5];
#pragma unroll
    for (int c = 0; c < 5; c++) {
      u32x4 a, b;
      a[0] = pack2(phi_f(rb(acc[0][c][0])), phi_f(rb(acc[0][c][1])));
      a[1] = pack2(phi_f(rb(acc[0][c][2])), phi_f(rb(acc[0][c][3])));
      a[2] = pack2(phi_f(rb(acc[1][c][0])), phi_f(rb(acc[1][c][1])));
      a[3] = pack2(phi_f(rb(acc[1][c][2])), phi_f(rb(acc[1][c][3])));
      b[0] = pack2(acc[0][5 + c][0], acc[0][5 + c][1]);
      b[1] = pack2(acc[0][5 + c][2], acc[0][5 + c][3]);
      b[2] = pack2(acc[1][5 + c][0], acc[1][5 + c][1]);
      b[3] = pack2(acc[1][5 + c][2], acc[1][5 + c][3]);
      pk[c] = __builtin_bit_cast(s16x8, a); vv[c] = __builtin_bit_cast(s16x8, b);
    }
    const s16x8 ones = {0x3F80, 0x3F80, 0x3F80, 0x3F80, 0x3F80, 0x3F80, 0x3F80, 0x3F80};
    __syncthreads();
    float* red = (float*)smem + wave * 1680;
#pragma unroll
    for (int i = 0; i < 5; i++) {
#pragma unroll
      for (int j = 0; j < 5; j++) {
        if (j - i > 1 || i - j > 1) continue;
        f32x4 r = __builtin_amdgcn_mfma_f32_16x16x32_bf16(pk[i], vv[j], (f32x4){0.f, 0.f, 0.f, 0.f}, 0, 0, 0);
        int ecol = 16 * j + fr; int eh = ecol / 20;
#pragma unroll
        for (int e = 0; e < 4; e++) {
          int dcol = 16 * i + 4 * g + e; int dh = dcol / 20;
          if (dh == eh) red[dh * 420 + (dcol - 20 * dh) * 20 + (ecol - 20 * eh)] = r[e];
        }
      }
      f32x4 r = __builtin_amdgcn_mfma_f32_16x16x32_bf16(pk[i], ones, (f32x4){0.f, 0.f, 0.f, 0.f}, 0, 0, 0);
      if (fr == 0) {
#pragma unroll
        for (int e = 0; e < 4; e++) { int dcol = 16 * i + 4 * g + e; int dh = dcol / 20; red[dh * 420 + 400 + dcol - 20 * dh] = r[e]; }
      }
    }
    __syncthreads();
    const float* rr = (const float*)smem;
    for (int i = tid; i < 1680; i += 256) {
      float s = (rr[i] + rr[1680 + i]) + (rr[3360 + i] + rr[5040 + i]);
      part[((size_t)rowblk * 32 + 4 * cb) * 420 + i] = s;
    }
  }
}

__device__ __forceinline__ void tr_role(char* smem, const u16* __restrict__ x, const u16* __restrict__ gw,
                                        u16* __restrict__ uT, float* __restrict__ gate, int blk) {
  const int tid = threadIdx.x, wave = tid >> 6, lane = tid & 63;
  const int l0 = blk * 32;
  constexpr int RS = 1296;
#pragma unroll
  for (int i = 0; i < 10; i++) { int id = tid + 256 * i; int r = id / 80, ch = id % 80;
    *(u32x4*)(smem + r * RS + ch * 16) = *(const u32x4*)(x + (size_t)(l0 + r) * D + ch * 8); }
  float gwr[10];
#pragma unroll
  for (int i = 0; i < 10; i++) gwr[i] = b2f(gw[lane + 64 * i]);
  __syncthreads();
  for (int rr = 0; rr < 8; rr++) {
    int r = wave * 8 + rr; float s = 0.f;
#pragma unroll
    for (int i = 0; i < 10; i++) s += rb(b2f(*(const u16*)(smem + r * RS + (lane + 64 * i) * 2)) * gwr[i]);
#pragma unroll
    for (int o = 32; o > 0; o >>= 1) s += __shfl_xor(s, o);
    if (lane == 0) { float a = rb(s); gate[l0 + r] = rb(1.f / (1.f + __expf(-a))); }
  }
#pragma unroll
  for (int i = 0; i < 10; i++) {
    int id = tid + 256 * i; int d = id >> 2, oc = id & 3;
    u32x4 v;
#pragma unroll
    for (int e = 0; e < 4; e++) {
      unsigned lo = *(const u16*)(smem + (8 * oc + 2 * e) * RS + d * 2);
      unsigned hi = *(const u16*)(smem + (8 * oc + 2 * e + 1) * RS + d * 2);
      v[e] = lo | (hi << 16);
    }
    *(u32x4*)(uT + (size_t)d * UT_STRIDE + UT_PAD + l0 + 8 * oc) = v;
    if (blk == 0) *(u32x4*)(uT + (size_t)d * UT_STRIDE + 8 * oc) = (u32x4){0u, 0u, 0u, 0u};
  }
}

__device__ __forceinline__ void wo_role(const u16* __restrict__ wo, u16* __restrict__ woF, int blk) {
#pragma unroll
  for (int i = 0; i < 4; i++) {
    int id = blk * 1024 + i * 256 + threadIdx.x;
    int lane = id & 63; int f = id >> 6;
    int ct = f % 5; int ks = (f / 5) % 5; int w = (f / 25) & 3; int cb = f / 100;
    int k = 160 * w + 32 * ks + 8 * (lane >> 4); int n = 80 * cb + 16 * ct + (lane & 15);
    u32x4 v;
#pragma unroll
    for (int e = 0; e < 4; e++) {
      unsigned lo = wo[(size_t)(k + 2 * e) * D + n]; unsigned hi = wo[(size_t)(k + 2 * e + 1) * D + n];
      v[e] = lo | (hi << 16);
    }
    *(u32x4*)(woF + (size_t)id * 8) = v;
  }
}

__device__ __forceinline__ void put_A(char* p, cf w) {
  float r = w.r, i = -w.i;
  float rh = __uint_as_float(__float_as_uint(r) & 0xffff0000u), ih = __uint_as_float(__float_as_uint(i) & 0xffff0000u);
  float rl = r - rh, il = i - ih;
  u32x4 v; v[0] = packhi(r, r); v[1] = packhi(rl, i); v[2] = packhi(i, il); v[3] = packhi(rl, il);
  *(u32x4*)p = v;
}
__device__ __forceinline__ void put_B(char* p, cf w) {
  float r = w.r, i = w.i;
  float rh = __uint_as_float(__float_as_uint(r) & 0xffff0000u), ih = __uint_as_float(__float_as_uint(i) & 0xffff0000u);
  float rl = r - rh, il = i - ih;
  u32x4 v; v[0] = packhi(r, rl); v[1] = packhi(r, i); v[2] = packhi(il, i); v[3] = packhi(rl, il);
  *(u32x4*)p = v;
}
__device__ __forceinline__ void kgen_role(char* smem, const u16* __restrict__ logA, const u16* __restrict__ Aim,
                                          const u16* __restrict__ Cr, const u16* __restrict__ Ci, const u16* __restrict__ logdt,
                                          u16* __restrict__ kimg, int d) {
  const int tid = threadIdx.x, wave = tid >> 6, lane = tid & 63;
  constexpr int RS = 1056;
  f32x4 acc[3];
  if (wave < 3) {
    const int n = wave * 64 + lane;
    const float dt = rb(expf(b2f(logdt[d])));
    const float ar = -rb(expf(b2f(logA[d * NS + n]))) * dt;
    const float ai = b2f(Aim[d * NS + n]) * dt;
    cf C; C.r = b2f(Cr[d * NS + n]) * dt; C.i = b2f(Ci[d * NS + n]) * dt;
    char* tb = smem + wave * (16 * RS);
    char* wp = tb + lane * 16;
    const char* rp = tb + (lane & 15) * RS + (lane >> 4) * 16;
    {
      cf z8 = zpow(ar, ai, 8.f); cf e; e.r = 1.f; e.i = 0.f;
      put_B(wp, e); e = z8; put_B(wp + RS, e);
#pragma unroll
      for (int c = 2; c < 16; c++) { e = cmul(e, z8); put_B(wp + c * RS, e); }
    }
    __builtin_amdgcn_wave_barrier();
    s16x8 bfr[16];
#pragma unroll
    for (int k = 0; k < 16; k++) bfr[k] = *(const s16x8*)(rp + k * 64);
    __builtin_amdgcn_wave_barrier();
    cf S[8];
    S[1] = zpow(ar, ai, 1.f);
#pragma unroll
    for (int s = 2; s < 8; s++) S[s] = cmul(S[s - 1], S[1]);
    const cf z128 = zpow(ar, ai, 128.f);
    cf G = C;
    put_A(wp, G);
#pragma unroll
    for (int s = 1; s < 8; s++) put_A(wp + s * RS, cmul(G, S[s]));
    G = cmul(G, z128);
    put_A(wp + 8 * RS, G);
#pragma unroll
    for (int s = 1; s < 8; s++) put_A(wp + (8 + s) * RS, cmul(G, S[s]));
    __builtin_amdgcn_wave_barrier();
    {
      f32x4 a = {0.f, 0.f, 0.f, 0.f};
#pragma unroll
      for (int k = 0; k < 16; k++) a = __builtin_amdgcn_mfma_f32_16x16x32_bf16(*(const s16x8*)(rp + k * 64), bfr[k], a, 0, 0, 0);
      acc[0] = a;
    }
    __builtin_amdgcn_wave_barrier();
#pragma unroll
    for (int h = 0; h < 2; h++) {
      G = cmul(G, z128);
      put_A(wp + (4 * h) * RS, G);
#pragma unroll
      for (int s = 1; s < 4; s++) put_A(wp + (4 * h + s) * RS, cmul(G, S[2 * s]));
    }
#pragma unroll
    for (int h = 0; h < 4; h++) {
      G = cmul(G, z128);
      put_A(wp + (8 + 2 * h) * RS, G);
      put_A(wp + (9 + 2 * h) * RS, cmul(G, S[4]));
    }
    __builtin_amdgcn_wave_barrier();
    {
      f32x4 a = {0.f, 0.f, 0.f, 0.f};
#pragma unroll
      for (int k = 0; k < 16; k++) a = __builtin_amdgcn_mfma_f32_16x16x32_bf16(*(const s16x8*)(rp + k * 64), bfr[k], a, 0, 0, 0);
      acc[1] = a;
    }
    __builtin_amdgcn_wave_barrier();
#pragma unroll
    for (int h = 0; h < 9; h++) { G = cmul(G, z128); put_A(wp + h * RS, G); }
    __builtin_amdgcn_wave_barrier();
    {
      f32x4 a = {0.f, 0.f, 0.f, 0.f};
#pragma unroll
      for (int k = 0; k < 16; k++) a = __builtin_amdgcn_mfma_f32_16x16x32_bf16(*(const s16x8*)(rp + k * 64), bfr[k], a, 0, 0, 0);
      acc[2] = a;
    }
  }
  __syncthreads();
  float* red = (float*)smem;
  if (wave < 3) {
#pragma unroll
    for (int t = 0; t < 3; t++)
#pragma unroll
      for (int e = 0; e < 4; e++) red[wave * 768 + (16 * t + 4 * (lane >> 4) + e) * 16 + (lane & 15)] = acc[t][e];
  }
  __syncthreads();
  {
    const int j0 = tid * 8;
    u32x4 hi, lo;
    float kv[8];
#pragma unroll
    for (int e = 0; e < 8; e++) {
      int j = j0 + 7 - e;
      int p = (int)rb((float)j);
      int h = p >> 7, c = (p >> 3) & 15, s = p & 7;
      int row = h < 2 ? 8 * h + s : (h < 4 ? 16 + 4 * (h - 2) + (s >> 1) : (h < 8 ? 24 + 2 * (h - 4) + (s >> 2) : 24 + h));
      int idx = row * 16 + c;
      kv[e] = 2.f * ((red[idx] + red[768 + idx]) + red[1536 + idx]);
    }
#pragma unroll
    for (int e = 0; e < 4; e++) {
      float a = kv[2 * e], b = kv[2 * e + 1];
      u16 ah = f2b(a), bh = f2b(b);
      hi[e] = (unsigned)ah | ((unsigned)bh << 16);
      lo[e] = pack2(a - b2f(ah), b - b2f(bh));
    }
    u16* dst = kimg + (size_t)d * 4096 + (j0 >> 5) * 64 + (j0 & 31);
    *(u32x4*)dst = hi;
    *(u32x4*)(dst + 32) = lo;
  }
}

__global__ __launch_bounds__(256) void k1(const u16* __restrict__ x, const u16* __restrict__ logA, const u16* __restrict__ Aim,
                                          const u16* __restrict__ Cr, const u16* __restrict__ Ci, const u16* __restrict__ logdt,
                                          const u16* __restrict__ wqkv, const u16* __restrict__ wo, const u16* __restrict__ gw,
                                          char* __restrict__ ws) {
  __shared__ __attribute__((aligned(16))) char smem[K1_LDS];
  int b = blockIdx.x;
  if (b < NB_GEMM) {
    int xcd = b & 7, idx = b >> 3;
    int rowblk = 2 * xcd + idx / 12, cb = idx % 12;
    int rot = (idx * 3 + xcd) % 10;
    if (cb < 8) gemm_role<false>(smem, x, wqkv, (u16*)(ws + WS_PQ), (float*)(ws + WS_PART), rowblk, cb, rot);
    else        gemm_role<true>(smem, x, wqkv, (u16*)(ws + WS_PQ), (float*)(ws + WS_PART), rowblk, cb, rot);
    return;
  }
  b -= NB_GEMM;
  if (b < NB_TR) { tr_role(smem, x, gw, (u16*)(ws + WS_UT), (float*)(ws + WS_GATE), b); return; }
  b -= NB_TR;
  if (b < NB_WO) { wo_role(wo, (u16*)(ws + WS_WOF), b); return; }
  b -= NB_WO;
  kgen_role(smem, logA, Aim, Cr, Ci, logdt, (u16*)(ws + WS_KIMG), b);
}

constexpr int K2_LDS = 96 * 144 + 32768;
__global__ __launch_bounds__(256) void k2(const u16* __restrict__ Dskip, char* __restrict__ ws) {
  __shared__ __attribute__((aligned(16))) char smem[K2_LDS];
  const int tid = threadIdx.x, wave = tid >> 6, lane = tid & 63;
  if (blockIdx.x >= D) {
    const int hd = blockIdx.x - D;
    const float* part = (const float*)(ws + WS_PART);
    float* s = (float*)smem;
    for (int i = tid; i < 420; i += 256) {
      float a = 0.f;
#pragma unroll
      for (int r = 0; r < 16; r++) a += part[((size_t)r * 32 + hd) * 420 + i];
      s[i] = rb(a);
    }
    __syncthreads();
    if (tid < 128) {
      int ks = tid >> 6, r = lane & 31, h = lane >> 5;
      u32x4 v;
#pragma unroll
      for (int e = 0; e < 4; e++) {
        float f[2];
#pragma unroll
        for (int t = 0; t < 2; t++) { int k = 16 * ks + 8 * h + 2 * e + t; f[t] = k < 20 ? (r < 20 ? s[k * 20 + r] : s[400 + k]) : 0.f; }
        v[e] = pack2(f[0], f[1]);
      }
      *(u32x4*)(ws + WS_KVT + ((size_t)(hd * 2 + ks) * 64 + lane) * 16) = v;
    }
    return;
  }
  const int d = blockIdx.x;
  const u16* uT = (const u16*)(ws + WS_UT) + (size_t)d * UT_STRIDE + UT_PAD;
  const int r = lane & 31, h = lane >> 5;
  const u16* ap = uT + (r - 8 * h - 7) + 32 * wave;
  {
    const u32x4* src = (const u32x4*)(ws + WS_KIMG + (size_t)d * 8192);
    u32x4 v0 = src[tid], v1 = src[tid + 256];
    for (int i = tid; i < 288; i += 256) *(u32x4*)(smem + i * 16) = (u32x4){0u, 0u, 0u, 0u};
    *(u32x4*)(smem + (32 + (tid >> 3)) * 144 + (tid & 7) * 16) = v0;
    *(u32x4*)(smem + (64 + (tid >> 3)) * 144 + (tid & 7) * 16) = v1;
  }
  f32x16 acc0, acc1;
#pragma unroll
  for (int i = 0; i < 16; i++) { acc0[i] = 0.f; acc1[i] = 0.f; }
  u32x4 af[2][8];
#pragma unroll
  for (int ii = 0; ii < 4; ii++)
#pragma unroll
    for (int s = 0; s < 2; s++) af[0][ii * 2 + s] = ((const U16B*)(ap + 128 * ii - 16 * s))->v;
  __syncthreads();
  const char* bp = smem + (32 + r - wave) * 144 + h * 16;
#pragma unroll
  for (int bi = 0; bi < 4; bi++) {
    if (bi < 3) {
#pragma unroll
      for (int ii = 0; ii < 4; ii++)
#pragma unroll
        for (int s = 0; s < 2; s++) af[(bi + 1) & 1][ii * 2 + s] = ((const U16B*)(ap + 128 * (4 * (bi + 1) + ii) - 16 * s))->v;
    }
#pragma unroll
    for (int ii = 0; ii < 4; ii++) {
      const int dl = 4 * (4 * bi + ii);
#pragma unroll
      for (int s = 0; s < 2; s++) {
        s16x8 a = __builtin_bit_cast(s16x8, af[bi & 1][ii * 2 + s]);
        if (bi < 2) {
          s16x8 bh = *(const s16x8*)(bp - dl * 144 + s * 32);
          s16x8 bl = *(const s16x8*)(bp - dl * 144 + s * 32 + 64);
          acc0 = __builtin_amdgcn_mfma_f32_32x32x16_bf16(a, bh, acc0, 0, 0, 0);
          acc0 = __builtin_amdgcn_mfma_f32_32x32x16_bf16(a, bl, acc0, 0, 0, 0);
        }
        s16x8 bh = *(const s16x8*)(bp + (32 - dl) * 144 + s * 32);
        s16x8 bl = *(const s16x8*)(bp + (32 - dl) * 144 + s * 32 + 64);
        acc1 = __builtin_amdgcn_mfma_f32_32x32x16_bf16(a, bh, acc1, 0, 0, 0);
        acc1 = __builtin_amdgcn_mfma_f32_32x32x16_bf16(a, bl, acc1, 0, 0, 0);
      }
    }
  }
  float* red = (float*)(smem + 96 * 144);
#pragma unroll
  for (int q = 0; q < 4; q++) {
    *(f32x4*)(red + (((wave * 2 + 0) * 4 + q) * 64 + lane) * 4) = (f32x4){acc0[4 * q], acc0[4 * q + 1], acc0[4 * q + 2], acc0[4 * q + 3]};
    *(f32x4*)(red + (((wave * 2 + 1) * 4 + q) * 64 + lane) * 4) = (f32x4){acc1[4 * q], acc1[4 * q + 1], acc1[4 * q + 2], acc1[4 * q + 3]};
  }
  __syncthreads();
  const float dsk = b2f(Dskip[d]);
  u16* aT = (u16*)(ws + WS_AT) + (size_t)d * L;
#pragma unroll
  for (int t = 0; t < 2; t++) {
    f32x4 s = *(const f32x4*)(red + (((0 * 2 + t) * 4 + wave) * 64 + lane) * 4);
    f32x4 s1 = *(const f32x4*)(red + (((1 * 2 + t) * 4 + wave) * 64 + lane) * 4);
    f32x4 s2 = *(const f32x4*)(red + (((2 * 2 + t) * 4 + wave) * 64 + lane) * 4);
    f32x4 s3 = *(const f32x4*)(red + (((3 * 2 + t) * 4 + wave) * 64 + lane) * 4);
    s = (s + s1) + (s2 + s3);
    const int l = 32 * (32 * t + r) + 8 * wave + 4 * h;
    u32x2 uu = *(const u32x2*)(uT + l);
    float o[4];
#pragma unroll
    for (int e = 0; e < 4; e++) {
      float u = b2f((u16)((uu[e >> 1] >> (16 * (e & 1))) & 0xffffu));
      o[e] = rb(s[e]) + rb(u * dsk);
    }
    u32x2 ov; ov[0] = pack2(o[0], o[1]); ov[1] = pack2(o[2], o[3]);
    *(u32x2*)(aT + l) = ov;
  }
}

constexpr int K3_RS = 352;
constexpr int K3_LDS = 4 * 64 * K3_RS + 64 * 176;
__global__ __launch_bounds__(256) void k3(char* __restrict__ ws, u16* __restrict__ out) {
  __shared__ __attribute__((aligned(16))) char smem[K3_LDS];
  const int tid = threadIdx.x, wave = tid >> 6, lane = tid & 63;
  const int xcd = blockIdx.x & 7, idx = blockIdx.x >> 3;
  const int rowblk = xcd * 4 + (idx >> 3), cb = idx & 7;
  const int l0 = rowblk * 64, c0 = cb * 80;
  char* tb = smem + wave * (64 * K3_RS);
  {
    const u16* pq = (const u16*)(ws + WS_PQ) + (size_t)l0 * D + 160 * wave;
    u32x4 v[20];
#pragma unroll
    for (int i = 0; i < 20; i++) { int id = lane + 64 * i; int rr = id / 20, ch = id % 20; v[i] = *(const u32x4*)(pq + (size_t)rr * D + ch * 8); }
#pragma unroll
    for (int i = 0; i < 20; i++) { int id = lane + 64 * i; int rr = id / 20, ch = id % 20; *(u32x4*)(tb + rr * K3_RS + ch * 16) = v[i]; }
    *(u32x4*)(tb + lane * K3_RS + 320) = (u32x4){0u, 0u, 0u, 0u};
    *(u32x4*)(tb + lane * K3_RS + 336) = (u32x4){0u, 0u, 0u, 0u};
  }
  {
    char* at = smem + 4 * 64 * K3_RS;
    const u16* aT = (const u16*)(ws + WS_AT);
    for (int i = tid; i < 640; i += 256) {
      int dd = i >> 3, oc = i & 7;
      u32x4 v = *(const u32x4*)(aT + (size_t)(c0 + dd) * L + l0 + 8 * oc);
#pragma unroll
      for (int e = 0; e < 8; e++) *(u16*)(at + (8 * oc + e) * 176 + dd * 2) = (u16)((v[e >> 1] >> (16 * (e & 1))) & 0xffffu);
    }
  }
  s16x8 wf[5][5];
  {
    const u32x4* wp = (const u32x4*)(ws + WS_WOF) + (size_t)((cb * 4 + wave) * 25) * 64 + lane;
#pragma unroll
    for (int ks = 0; ks < 5; ks++)
#pragma unroll
      for (int ct = 0; ct < 5; ct++) wf[ks][ct] = __builtin_bit_cast(s16x8, wp[(ks * 5 + ct) * 64]);
  }
  __builtin_amdgcn_wave_barrier();
  {
    const int r = lane & 31, h = lane >> 5;
    const u32x4* kvt = (const u32x4*)(ws + WS_KVT) + lane;
#pragma unroll
    for (int hh = 0; hh < 8; hh++) {
      const int hd = wave * 8 + hh;
      s16x8 a0 = __builtin_bit_cast(s16x8, kvt[(hd * 2 + 0) * 64]);
      s16x8 a1 = __builtin_bit_cast(s16x8, kvt[(hd * 2 + 1) * 64]);
#pragma unroll
      for (int t = 0; t < 2; t++) {
        char* rowp = tb + (32 * t + r) * K3_RS + hh * 40;
        u32x2 b00 = *(const u32x2*)(rowp + h * 16), b01 = *(const u32x2*)(rowp + h * 16 + 8);
        u32x2 b10 = *(const u32x2*)(rowp + 32 + h * 16), b11 = *(const u32x2*)(rowp + 32 + h * 16 + 8);
        s16x8 b0 = __builtin_bit_cast(s16x8, ((u32x4){b00[0], b00[1], b01[0], b01[1]}));
        s16x8 b1 = __builtin_bit_cast(s16x8, ((u32x4){b10[0], b10[1], b11[0], b11[1]}));
        f32x16 c;
#pragma unroll
        for (int i = 0; i < 16; i++) c[i] = 0.f;
        c = __builtin_amdgcn_mfma_f32_32x32x16_bf16(a0, b0, c, 0, 0, 0);
        c = __builtin_amdgcn_mfma_f32_32x32x16_bf16(a1, b1, c, 0, 0, 0);
        float den = rb(rb(c[12]) + 1e-6f);
        float inv = __builtin_amdgcn_rcpf(den);
        __builtin_amdgcn_wave_barrier();
#pragma unroll
        for (int q = 0; q < 3; q++) {
          if (q == 2 && h == 1) continue;
          u32x2 o;
          o[0] = pack2(rb(c[4 * q]) * inv, rb(c[4 * q + 1]) * inv);
          o[1] = pack2(rb(c[4 * q + 2]) * inv, rb(c[4 * q + 3]) * inv);
          *(u32x2*)(rowp + (8 * q + 4 * h) * 2) = o;
        }
      }
    }
  }
  __builtin_amdgcn_wave_barrier();
  f32x4 acc[4][5];
#pragma unroll
  for (int m = 0; m < 4; m++)
#pragma unroll
    for (int c = 0; c < 5; c++) acc[m][c] = (f32x4){0.f, 0.f, 0.f, 0.f};
  {
    const char* ap = tb + (lane & 15) * K3_RS + (lane >> 4) * 16;
#pragma unroll
    for (int ks = 0; ks < 5; ks++) {
#pragma unroll
      for (int m = 0; m < 4; m++) {
        s16x8 a = *(const s16x8*)(ap + m * 16 * K3_RS + ks * 64);
#pragma unroll
        for (int c = 0; c < 5; c++) acc[m][c] = __builtin_amdgcn_mfma_f32_16x16x32_bf16(wf[ks][c], a, acc[m][c], 0, 0, 0);
      }
    }
  }
  __builtin_amdgcn_wave_barrier();
  {
    float* pr = (float*)tb;
#pragma unroll
    for (int m = 0; m < 4; m++)
#pragma unroll
      for (int c = 0; c < 5; c++) *(f32x4*)(pr + (16 * m + (lane & 15)) * 84 + 16 * c + 4 * (lane >> 4)) = acc[m][c];
  }
  __syncthreads();
  {
    const float* gate = (const float*)(ws + WS_GATE);
    const char* at = smem + 4 * 64 * K3_RS;
    for (int i = tid; i < 640; i += 256) {
      int rr = i / 10, oc = i % 10;
      float g = gate[l0 + rr]; float g1 = rb(1.f - g);
      f32x4 s0 = {0.f, 0.f, 0.f, 0.f}, s1 = {0.f, 0.f, 0.f, 0.f};
#pragma unroll
      for (int w = 0; w < 4; w++) {
        const float* pr = (const float*)(smem + w * (64 * K3_RS)) + rr * 84 + oc * 8;
        s0 += *(const f32x4*)pr; s1 += *(const f32x4*)(pr + 4);
      }
      u32x4 av = *(const u32x4*)(at + rr * 176 + oc * 16);
      u32x4 ov;
#pragma unroll
      for (int e = 0; e < 4; e++) {
        float b0 = rb(e < 2 ? s0[2 * e] : s1[2 * e - 4]), b1 = rb(e < 2 ? s0[2 * e + 1] : s1[2 * e - 3]);
        float a0 = b2f((u16)(av[e] & 0xffffu)), a1 = b2f((u16)(av[e] >> 16));
        ov[e] = pack2(rb(g * a0) + rb(g1 * b0), rb(g * a1) + rb(g1 * b1));
      }
      *(u32x4*)(out + (size_t)(l0 + rr) * D + c0 + oc * 8) = ov;
    }
  }
}

extern "C" void kernel_launch(void* const* d_in, const int* in_sizes, int n_in, void* d_out, int out_size, void* d_ws, size_t ws_size,
                              hipStream_t stream) {
  const u16* u = (const u16*)d_in[0]; const u16* logA = (const u16*)d_in[1]; const u16* Aim = (const u16*)d_in[2];
  const u16* Cr = (const u16*)d_in[3]; const u16* Ci = (const u16*)d_in[4]; const u16* logdt = (const u16*)d_in[5];
  const u16* Dsk = (const u16*)d_in[6]; const u16* wqkv = (const u16*)d_in[8]; const u16* wo = (const u16*)d_in[9];
  const u16* gw = (const u16*)d_in[10];
  char* ws = (char*)d_ws;
  k1<<<NB_GEMM + NB_TR + NB_WO + NB_KG, 256, 0, stream>>>(u, logA, Aim, Cr, Ci, logdt, wqkv, wo, gw, ws);
  k2<<<D + NH, 256, 0, stream>>>(Dsk, ws);
  k3<<<256, 256, 0, stream>>>(ws, (u16*)d_out);
}
```

```cpp
#include <hip/hip_runtime.h>
#include <hip/hip_bf16.h>
#include <stdint.h>

typedef unsigned short u16;
typedef __attribute__((ext_vector_type(8))) short s16x8;
typedef __attribute__((ext_vector_type(4))) short s16x4;
typedef __attribute__((ext_vector_type(4))) float f32x4;
typedef __attribute__((ext_vector_type(16))) float f32x16;
typedef __attribute__((ext_vector_type(4))) unsigned u32x4;
typedef __attribute__((ext_vector_type(2))) unsigned u32x2;
struct __attribute__((packed, aligned(2))) U16B { u32x4 v; };
typedef __attribute__((address_space(3))) s16x4* lds_s16x4_p;

constexpr int L = 2048, D = 640, NS = 192, NH = 32;
constexpr int UT_PAD = 32, UT_STRIDE = L + UT_PAD;
constexpr size_t WS_UT   = 0;
constexpr size_t WS_KIMG = WS_UT + (size_t)D * UT_STRIDE * 2;
constexpr size_t WS_PQ   = WS_KIMG + (size_t)D * 8192;
constexpr size_t WS_PART = WS_PQ + (size_t)L * D * 2;
constexpr size_t WS_GATE = WS_PART + (size_t)16 * 32 * 420 * 4;
constexpr size_t WS_WOF  = WS_GATE + (size_t)L * 4;
constexpr size_t WS_KVT  = WS_WOF + (size_t)D * D * 2;
constexpr size_t WS_AT   = WS_KVT + (size_t)32 * 2 * 64 * 16;
constexpr size_t WS_END  = WS_AT + (size_t)D * L * 2;

__device__ __forceinline__ float b2f(u16 v) { return __uint_as_float(((unsigned)v) << 16); }
__device__ __forceinline__ u16 f2b(float f) { __hip_bfloat16 h = __float2bfloat16(f); return *reinterpret_cast<u16*>(&h); }
__device__ __forceinline__ float rb(float f) { return b2f(f2b(f)); }
__device__ __forceinline__ float phi_f(float t) {
  float r = t > 0.f ? rb(t + 1.f) : rb(__expf(t));
  return rb(r + 1e-6f);
}
typedef __bf16 bf16x2_t __attribute__((ext_vector_type(2)));
typedef float f32x2 __attribute__((ext_vector_type(2)));
__device__ __forceinline__ unsigned pack2(float a, float b) { f32x2 v = {a, b}; bf16x2_t r = __builtin_convertvector(v, bf16x2_t); return __builtin_bit_cast(unsigned, r); }
__device__ __forceinline__ float lo_f(unsigned u) { return __uint_as_float(u << 16); }
__device__ __forceinline__ float hi_f(unsigned u) { return __uint_as_float(u & 0xffff0000u); }
__device__ __forceinline__ unsigned phi2(float a, float b) {
  unsigned t = pack2(a, b); float t0 = lo_f(t), t1 = hi_f(t);
  float r0 = t0 > 0.f ? t0 + 1.f : __expf(t0), r1 = t1 > 0.f ? t1 + 1.f : __expf(t1);
  unsigned r = pack2(r0, r1);
  return pack2(lo_f(r) + 1e-6f, hi_f(r) + 1e-6f);
}
__device__ __forceinline__ unsigned packhi(float lo, float hi) { return __builtin_amdgcn_perm(__float_as_uint(hi), __float_as_uint(lo), 0x07060302u); }

struct cf { float r, i; };
__device__ __forceinline__ cf cmul(cf a, cf b) { cf o; o.r = a.r * b.r - a.i * b.i; o.i = a.r * b.i + a.i * b.r; return o; }
__device__ __forceinline__ cf zpow(float ar, float ai, float p) {
  float e = expf(ar * p); float s, c; sincosf(ai * p, &s, &c); cf o; o.r = e * c; o.i = e * s; return o;
}

constexpr int K1_LDS = 50688;
constexpr int NB_GEMM = 192, NB_TR = 64, NB_WO = 50, NB_KG = 640;

template <bool ISQ>
__device__ __forceinline__ void gemm_role(char* smem, const u16* __restrict__ x, const u16* __restrict__ W,
                                          u16* __restrict__ pq, float* __restrict__ part, int rowblk, int cb, int rot) {
  const int tid = threadIdx.x, wave = tid >> 6, lane = tid & 63;
  const int row0 = rowblk * 128;
  const int seg0 = ISQ ? 160 * (cb - 8) : 640 + 80 * cb;
  const int seg1 = ISQ ? seg0 + 80 : 1280 + 80 * cb;
  char* sA = smem; char* sB = smem + 20480;
  const u16* ga[4]; int la[4];
#pragma unroll
  for (int i = 0; i < 4; i++) { int id = tid + 256 * i; int r = id >> 3, ch = id & 7; ga[i] = x + (size_t)(row0 + r) * D + ch * 8; la[i] = r * 160 + ch * 16; }
  const u16* gb[5]; int lb[5];
#pragma unroll
  for (int i = 0; i < 5; i++) { int id = tid + 256 * i; int kr = id / 20, ch = id % 20; int col = (ch < 10 ? seg0 + ch * 8 : seg1 + (ch - 10) * 8);
    gb[i] = W + (size_t)kr * 1920 + col; lb[i] = kr * 320 + (kr >> 3) * 32 + ch * 16; }
  u32x4 ra[4], rbv[5];
  f32x4 acc[2][10];
#pragma unroll
  for (int m = 0; m < 2; m++)
#pragma unroll
    for (int c = 0; c < 10; c++) acc[m][c] = (f32x4){0.f, 0.f, 0.f, 0.f};
  int kt = rot;
  {
    int k0 = kt * 64;
#pragma unroll
    for (int i = 0; i < 4; i++) ra[i] = *(const u32x4*)(ga[i] + k0);
#pragma unroll
    for (int i = 0; i < 5; i++) rbv[i] = *(const u32x4*)(gb[i] + (size_t)k0 * 1920);
  }
  const int g = lane >> 4, fr = lane & 15;
  const int aoff = (wave * 32 + fr) * 160 + g * 16;
  const int q4 = (lane & 15) >> 2, p4 = lane & 3;
  const int boff = (8 * g + q4) * 320 + g * 32 + p4 * 8;
  for (int t = 0; t < 10; t++) {
    __syncthreads();
#pragma unroll
    for (int i = 0; i < 4; i++) *(u32x4*)(sA + la[i]) = ra[i];
#pragma unroll
    for (int i = 0; i < 5; i++) *(u32x4*)(sB + lb[i]) = rbv[i];
    if (t < 9) {
      kt = kt + 1; if (kt >= 10) kt = 0;
      int k0 = kt * 64;
#pragma unroll
      for (int i = 0; i < 4; i++) ra[i] = *(const u32x4*)(ga[i] + k0);
#pragma unroll
      for (int i = 0; i < 5; i++) rbv[i] = *(const u32x4*)(gb[i] + (size_t)k0 * 1920);
    }
    __syncthreads();
#pragma unroll
    for (int ks = 0; ks < 2; ks++) {
      s16x8 af[2];
#pragma unroll
      for (int m = 0; m < 2; m++) af[m] = *(const s16x8*)(sA + aoff + m * 16 * 160 + ks * 64);
#pragma unroll
      for (int c = 0; c < 10; c++) {
        const char* bp = sB + boff + ks * (32 * 320 + 4 * 32) + c * 32;
        s16x4 b0 = __builtin_amdgcn_ds_read_tr16_b64_v4i16((lds_s16x4_p)(bp));
        s16x4 b1 = __builtin_amdgcn_ds_read_tr16_b64_v4i16((lds_s16x4_p)(bp + 4 * 320));
        s16x8 bf = __builtin_shufflevector(b0, b1, 0, 1, 2, 3, 4, 5, 6, 7);
#pragma unroll
        for (int m = 0; m < 2; m++) {
          if (ISQ) acc[m][c] = __builtin_amdgcn_mfma_f32_16x16x32_bf16(bf, af[m], acc[m][c], 0, 0, 0);
          else     acc[m][c] = __builtin_amdgcn_mfma_f32_16x16x32_bf16(af[m], bf, acc[m][c], 0, 0, 0);
        }
      }
    }
  }
  if (ISQ) {
    char* pq0 = (char*)pq; char* pq1 = (char*)pq + (size_t)L * 16 * 32 * 2;
#pragma unroll
    for (int m = 0; m < 2; m++) {
      const int row = row0 + wave * 32 + 16 * m + fr;
      const int rb64 = row >> 6, t = (row >> 5) & 1, r = row & 31;
#pragma unroll
      for (int c = 0; c < 10; c++) {
        u32x2 o;
        o[0] = phi2(acc[m][c][0], acc[m][c][1]);
        o[1] = phi2(acc[m][c][2], acc[m][c][3]);
        const int n = seg0 + 16 * c + 4 * g;
        const int hd = n / 20, d0 = n - 20 * hd;
        const int fi = (rb64 * 32 + hd) * 2 + t;
        char* dst = d0 < 16 ? pq0 + ((size_t)fi * 64 + (d0 >> 3) * 32 + r) * 16 + (d0 & 7) * 2 : pq1 + ((size_t)fi * 32 + r) * 8;
        *(u32x2*)dst = o;
      }
    }
  } else {
    s16x8 pk[5], vv[5];
#pragma unroll
    for (int c = 0; c < 5; c++) {
      u32x4 a, b;
      a[0] = phi2(acc[0][c][0], acc[0][c][1]);
      a[1] = phi2(acc[0][c][2], acc[0][c][3]);
      a[2] = phi2(acc[1][c][0], acc[1][c][1]);
      a[3] = phi2(acc[1][c][2], acc[1][c][3]);
      b[0] = pack2(acc[0][5 + c][0], acc[0][5 + c][1]);
      b[1] = pack2(acc[0][5 + c][2], acc[0][5 + c][3]);
      b[2] = pack2(acc[1][5 + c][0], acc[1][5 + c][1]);
      b[3] = pack2(acc[1][5 + c][2], acc[1][5 + c][3]);
      pk[c] = __builtin_bit_cast(s16x8, a); vv[c] = __builtin_bit_cast(s16x8, b);
    }
    const s16x8 ones = {0x3F80, 0x3F80, 0x3F80, 0x3F80, 0x3F80, 0x3F80, 0x3F80, 0x3F80};
    __syncthreads();
    float* red = (float*)smem + wave * 1680;
#pragma unroll
    for (int i = 0; i < 5; i++) {
#pragma unroll
      for (int j = 0; j < 5; j++) {
        if (j - i > 1 || i - j > 1) continue;
        f32x4 r = __builtin_amdgcn_mfma_f32_16x16x32_bf16(pk[i], vv[j], (f32x4){0.f, 0.f, 0.f, 0.f}, 0, 0, 0);
        int ecol = 16 * j + fr; int eh = ecol / 20;
#pragma unroll
        for (int e = 0; e < 4; e++) {
          int dcol = 16 * i + 4 * g + e; int dh = dcol / 20;
          if (dh == eh) red[dh * 420 + (dcol - 20 * dh) * 20 + (ecol - 20 * eh)] = r[e];
        }
      }
      f32x4 r = __builtin_amdgcn_mfma_f32_16x16x32_bf16(pk[i], ones, (f32x4){0.f, 0.f, 0.f, 0.f}, 0, 0, 0);
      if (fr == 0) {
#pragma unroll
        for (int e = 0; e < 4; e++) { int dcol = 16 * i + 4 * g + e; int dh = dcol / 20; red[dh * 420 + 400 + dcol - 20 * dh] = r[e]; }
      }
    }
    __syncthreads();
    const float* rr = (const float*)smem;
    for (int i = tid; i < 1680; i += 256) {
      float s = (rr[i] + rr[1680 + i]) + (rr[3360 + i] + rr[5040 + i]);
      part[((size_t)rowblk * 32 + 4 * cb) * 420 + i] = s;
    }
  }
}

__device__ __forceinline__ void tr_role(char* smem, const u16* __restrict__ x, const u16* __restrict__ gw,
                                        u16* __restrict__ uT, float* __restrict__ gate, int blk) {
  const int tid = threadIdx.x, wave = tid >> 6, lane = tid & 63;
  const int l0 = blk * 32;
  constexpr int RS = 1296;
#pragma unroll
  for (int i = 0; i < 10; i++) { int id = tid + 256 * i; int r = id / 80, ch = id % 80;
    *(u32x4*)(smem + r * RS + ch * 16) = *(const u32x4*)(x + (size_t)(l0 + r) * D + ch * 8); }
  float gwr[10];
#pragma unroll
  for (int i = 0; i < 10; i++) gwr[i] = b2f(gw[lane + 64 * i]);
  __syncthreads();
  for (int rr = 0; rr < 8; rr++) {
    int r = wave * 8 + rr; float s = 0.f;
#pragma unroll
    for (int i = 0; i < 10; i++) s += rb(b2f(*(const u16*)(smem + r * RS + (lane + 64 * i) * 2)) * gwr[i]);
#pragma unroll
    for (int o = 32; o > 0; o >>= 1) s += __shfl_xor(s, o);
    if (lane == 0) { float a = rb(s); gate[l0 + r] = rb(1.f / (1.f + __expf(-a))); }
  }
#pragma unroll
  for (int i = 0; i < 10; i++) {
    int id = tid + 256 * i; int d = id >> 2, oc = id & 3;
    u32x4 v;
#pragma unroll
    for (int e = 0; e < 4; e++) {
      unsigned lo = *(const u16*)(smem + (8 * oc + 2 * e) * RS + d * 2);
      unsigned hi = *(const u16*)(smem + (8 * oc + 2 * e + 1) * RS + d * 2);
      v[e] = lo | (hi << 16);
    }
    *(u32x4*)(uT + (size_t)d * UT_STRIDE + UT_PAD + l0 + 8 * oc) = v;
    if (blk == 0) *(u32x4*)(uT + (size_t)d * UT_STRIDE + 8 * oc) = (u32x4){0u, 0u, 0u, 0u};
  }
}

__device__ __forceinline__ void wo_role(const u16* __restrict__ wo, u16* __restrict__ woF, int blk) {
#pragma unroll
  for (int i = 0; i < 4; i++) {
    int id = blk * 1024 + i * 256 + threadIdx.x;
    int lane = id & 63; int f = id >> 6;
    int ct = f % 5; int ks = (f / 5) % 5; int w = (f / 25) & 3; int cb = f / 100;
    int k = 160 * w + 32 * ks + 8 * (lane >> 4); int n = 80 * cb + 16 * ct + (lane & 15);
    u32x4 v;
#pragma unroll
    for (int e = 0; e < 4; e++) {
      unsigned lo = wo[(size_t)(k + 2 * e) * D + n]; unsigned hi = wo[(size_t)(k + 2 * e + 1) * D + n];
      v[e] = lo | (hi << 16);
    }
    *(u32x4*)(woF + (size_t)id * 8) = v;
  }
}

__device__ __forceinline__ void put_A(char* p, cf w) {
  float r = w.r, i = -w.i;
  float rh = __uint_as_float(__float_as_uint(r) & 0xffff0000u), ih = __uint_as_float(__float_as_uint(i) & 0xffff0000u);
  float rl = r - rh, il = i - ih;
  u32x4 v; v[0] = packhi(r, r); v[1] = packhi(rl, i); v[2] = packhi(i, il); v[3] = packhi(rl, il);
  *(u32x4*)p = v;
}
__device__ __forceinline__ void put_B(char* p, cf w) {
  float r = w.r, i = w.i;
  float rh = __uint_as_float(__float_as_uint(r) & 0xffff0000u), ih = __uint_as_float(__float_as_uint(i) & 0xffff0000u);
  float rl = r - rh, il = i - ih;
  u32x4 v; v[0] = packhi(r, rl); v[1] = packhi(r, i); v[2] = packhi(il, i); v[3] = packhi(rl, il);
  *(u32x4*)p = v;
}
__device__ __forceinline__ void kgen_role(char* smem, const u16* __restrict__ logA, const u16* __restrict__ Aim,
                                          const u16* __restrict__ Cr, const u16* __restrict__ Ci, const u16* __restrict__ logdt,
                                          u16* __restrict__ kimg, int d) {
  const int tid = threadIdx.x, wave = tid >> 6, lane = tid & 63;
  constexpr int RS = 1056;
  f32x4 acc[3];
  if (wave < 3) {
    const int n = wave * 64 + lane;
    const float dt = rb(expf(b2f(logdt[d])));
    const float ar = -rb(expf(b2f(logA[d * NS + n]))) * dt;
    const float ai = b2f(Aim[d * NS + n]) * dt;
    cf C; C.r = b2f(Cr[d * NS + n]) * dt; C.i = b2f(Ci[d * NS + n]) * dt;
    char* tb = smem + wave * (16 * RS);
    char* wp = tb + lane * 16;
    const char* rp = tb + (lane & 15) * RS + (lane >> 4) * 16;
    {
      cf z8 = zpow(ar, ai, 8.f); cf e; e.r = 1.f; e.i = 0.f;
      put_B(wp, e); e = z8; put_B(wp + RS, e);
#pragma unroll
      for (int c = 2; c < 16; c++) { e = cmul(e, z8); put_B(wp + c * RS, e); }
    }
    __builtin_amdgcn_wave_barrier();
    s16x8 bfr[16];
#pragma unroll
    for (int k = 0; k < 16; k++) bfr[k] = *(const s16x8*)(rp + k * 64);
    __builtin_amdgcn_wave_barrier();
    cf S[8];
    S[1] = zpow(ar, ai, 1.f);
#pragma unroll
    for (int s = 2; s < 8; s++) S[s] = cmul(S[s - 1], S[1]);
    const cf z128 = zpow(ar, ai, 128.f);
    cf G = C;
    put_A(wp, G);
#pragma unroll
    for (int s = 1; s < 8; s++) put_A(wp + s * RS, cmul(G, S[s]));
    G = cmul(G, z128);
    put_A(wp + 8 * RS, G);
#pragma unroll
    for (int s = 1; s < 8; s++) put_A(wp + (8 + s) * RS, cmul(G, S[s]));
    __builtin_amdgcn_wave_barrier();
    {
      f32x4 a = {0.f, 0.f, 0.f, 0.f};
#pragma unroll
      for (int k = 0; k < 16; k++) a = __builtin_amdgcn_mfma_f32_16x16x32_bf16(*(const s16x8*)(rp + k * 64), bfr[k], a, 0, 0, 0);
      acc[0] = a;
    }
    __builtin_amdgcn_wave_barrier();
#pragma unroll
    for (int h = 0; h < 2; h++) {
      G = cmul(G, z128);
      put_A(wp + (4 * h) * RS, G);
#pragma unroll
      for (int s = 1; s < 4; s++) put_A(wp + (4 * h + s) * RS, cmul(G, S[2 * s]));
    }
#pragma unroll
    for (int h = 0; h < 4; h++) {
      G = cmul(G, z128);
      put_A(wp + (8 + 2 * h) * RS, G);
      put_A(wp + (9 + 2 * h) * RS, cmul(G, S[4]));
    }
    __builtin_amdgcn_wave_barrier();
    {
      f32x4 a = {0.f, 0.f, 0.f, 0.f};
#pragma unroll
      for (int k = 0; k < 16; k++) a = __builtin_amdgcn_mfma_f32_16x16x32_bf16(*(const s16x8*)(rp + k * 64), bfr[k], a, 0, 0, 0);
      acc[1] = a;
    }
    __builtin_amdgcn_wave_barrier();
#pragma unroll
    for (int h = 0; h < 9; h++) { G = cmul(G, z128); put_A(wp + h * RS, G); }
    __builtin_amdgcn_wave_barrier();
    {
      f32x4 a = {0.f, 0.f, 0.f, 0.f};
#pragma unroll
      for (int k = 0; k < 16; k++) a = __builtin_amdgcn_mfma_f32_16x16x32_bf16(*(const s16x8*)(rp + k * 64), bfr[k], a, 0, 0, 0);
      acc[2] = a;
    }
  }
  __syncthreads();
  float* red = (float*)smem;
  if (wave < 3) {
#pragma unroll
    for (int t = 0; t < 3; t++)
#pragma unroll
      for (int e = 0; e < 4; e++) red[wave * 768 + (16 * t + 4 * (lane >> 4) + e) * 16 + (lane & 15)] = acc[t][e];
  }
  __syncthreads();
  {
    const int j0 = tid * 8;
    u32x4 hi, lo;
    float kv[8];
#pragma unroll
    for (int e = 0; e < 8; e++) {
      int j = j0 + 7 - e;
      int p = (int)rb((float)j);
      int h = p >> 7, c = (p >> 3) & 15, s = p & 7;
      int row = h < 2 ? 8 * h + s : (h < 4 ? 16 + 4 * (h - 2) + (s >> 1) : (h < 8 ? 24 + 2 * (h - 4) + (s >> 2) : 24 + h));
      int idx = row * 16 + c;
      kv[e] = 2.f * ((red[idx] + red[768 + idx]) + red[1536 + idx]);
    }
#pragma unroll
    for (int e = 0; e < 4; e++) {
      float a = kv[2 * e], b = kv[2 * e + 1];
      u16 ah = f2b(a), bh = f2b(b);
      hi[e] = (unsigned)ah | ((unsigned)bh << 16);
      lo[e] = pack2(a - b2f(ah), b - b2f(bh));
    }
    u16* dst = kimg + (size_t)d * 4096 + (j0 >> 5) * 64 + (j0 & 31);
    *(u32x4*)dst = hi;
    *(u32x4*)(dst + 32) = lo;
  }
}

__global__ __launch_bounds__(256, 3) void k1(const u16* __restrict__ x, const u16* __restrict__ logA, const u16* __restrict__ Aim,
                                          const u16* __restrict__ Cr, const u16* __restrict__ Ci, const u16* __restrict__ logdt,
                                          const u16* __restrict__ wqkv, const u16* __restrict__ wo, const u16* __restrict__ gw,
                                          char* __restrict__ ws) {
  __shared__ __attribute__((aligned(16))) char smem[K1_LDS];
  int b = blockIdx.x;
  if (b < NB_GEMM) {
    int xcd = b & 7, idx = b >> 3;
    int rowblk = 2 * xcd + idx / 12, cb = idx % 12;
    int rot = (idx * 3 + xcd) % 10;
    if (cb < 8) gemm_role<false>(smem, x, wqkv, (u16*)(ws + WS_PQ), (float*)(ws + WS_PART), rowblk, cb, rot);
    else        gemm_role<true>(smem, x, wqkv, (u16*)(ws + WS_PQ), (float*)(ws + WS_PART), rowblk, cb, rot);
    return;
  }
  b -= NB_GEMM;
  if (b < NB_TR) { tr_role(smem, x, gw, (u16*)(ws + WS_UT), (float*)(ws + WS_GATE), b); return; }
  b -= NB_TR;
  if (b < NB_WO) { wo_role(wo, (u16*)(ws + WS_WOF), b); return; }
  b -= NB_WO;
  kgen_role(smem, logA, Aim, Cr, Ci, logdt, (u16*)(ws + WS_KIMG), b);
}

constexpr int K2_LDS = 96 * 144 + 32768;
__global__ __launch_bounds__(256, 3) void k2(const u16* __restrict__ Dskip, char* __restrict__ ws) {
  __shared__ __attribute__((aligned(16))) char smem[K2_LDS];
  const int tid = threadIdx.x, wave = tid >> 6, lane = tid & 63;
  if (blockIdx.x >= D) {
    const int hd = blockIdx.x - D;
    const float* part = (const float*)(ws + WS_PART);
    float* s = (float*)smem;
    for (int i = tid; i < 420; i += 256) {
      float a = 0.f;
#pragma unroll
      for (int r = 0; r < 16; r++) a += part[((size_t)r * 32 + hd) * 420 + i];
      s[i] = rb(a);
    }
    __syncthreads();
    if (tid < 128) {
      int ks = tid >> 6, r = lane & 31, h = lane >> 5;
      u32x4 v;
#pragma unroll
      for (int e = 0; e < 4; e++) {
        float f[2];
#pragma unroll
        for (int t = 0; t < 2; t++) { int k = 16 * ks + 8 * h + 2 * e + t; f[t] = k < 20 ? (r < 20 ? s[k * 20 + r] : s[400 + k]) : 0.f; }
        v[e] = pack2(f[0], f[1]);
      }
      *(u32x4*)(ws + WS_KVT + ((size_t)(hd * 2 + ks) * 64 + lane) * 16) = v;
    }
    return;
  }
  const int d = blockIdx.x;
  const u16* uT = (const u16*)(ws + WS_UT) + (size_t)d * UT_STRIDE + UT_PAD;
  const int r = lane & 31, h = lane >> 5;
  const u16* ap = uT + (r - 8 * h - 7) + 32 * wave;
  {
    const u32x4* src = (const u32x4*)(ws + WS_KIMG + (size_t)d * 8192);
    u32x4 v0 = src[tid], v1 = src[tid + 256];
    for (int i = tid; i < 288; i += 256) *(u32x4*)(smem + i * 16) = (u32x4){0u, 0u, 0u, 0u};
    *(u32x4*)(smem + (32 + (tid >> 3)) * 144 + (tid & 7) * 16) = v0;
    *(u32x4*)(smem + (64 + (tid >> 3)) * 144 + (tid & 7) * 16) = v1;
  }
  f32x16 acc0, acc1;
#pragma unroll
  for (int i = 0; i < 16; i++) { acc0[i] = 0.f; acc1[i] = 0.f; }
  u32x4 af[2][4][2][2];
#pragma unroll
  for (int ii = 0; ii < 4; ii++)
#pragma unroll
    for (int w2 = 0; w2 < 2; w2++)
#pragma unroll
      for (int s = 0; s < 2; s++) af[0][ii][w2][s] = ((const U16B*)(ap + 128 * ii + 1024 * w2 - 16 * s))->v;
  __builtin_amdgcn_sched_barrier(0);
  __syncthreads();
#pragma unroll
  for (int ii = 0; ii < 4; ii++)
#pragma unroll
    for (int w2 = 0; w2 < 2; w2++)
#pragma unroll
      for (int s = 0; s < 2; s++) af[1][ii][w2][s] = ((const U16B*)(ap + 128 * (4 + ii) + 1024 * w2 - 16 * s))->v;
  __builtin_amdgcn_sched_barrier(0);
  const char* bp = smem + (32 + r - wave) * 144 + h * 16;
#pragma unroll
  for (int hf = 0; hf < 2; hf++) {
#pragma unroll
    for (int ii = 0; ii < 4; ii++) {
      const int dl = 4 * (4 * hf + ii);
#pragma unroll
      for (int s = 0; s < 2; s++) {
        s16x8 a1 = __builtin_bit_cast(s16x8, af[hf][ii][0][s]);
        s16x8 a2 = __builtin_bit_cast(s16x8, af[hf][ii][1][s]);
        s16x8 b0h = *(const s16x8*)(bp - dl * 144 + s * 32);
        s16x8 b0l = *(const s16x8*)(bp - dl * 144 + s * 32 + 64);
        s16x8 b1h = *(const s16x8*)(bp + (32 - dl) * 144 + s * 32);
        s16x8 b1l = *(const s16x8*)(bp + (32 - dl) * 144 + s * 32 + 64);
        acc0 = __builtin_amdgcn_mfma_f32_32x32x16_bf16(a1, b0h, acc0, 0, 0, 0);
        acc1 = __builtin_amdgcn_mfma_f32_32x32x16_bf16(a2, b0h, acc1, 0, 0, 0);
        acc0 = __builtin_amdgcn_mfma_f32_32x32x16_bf16(a1, b0l, acc0, 0, 0, 0);
        acc1 = __builtin_amdgcn_mfma_f32_32x32x16_bf16(a2, b0l, acc1, 0, 0, 0);
        acc1 = __builtin_amdgcn_mfma_f32_32x32x16_bf16(a1, b1h, acc1, 0, 0, 0);
        acc1 = __builtin_amdgcn_mfma_f32_32x32x16_bf16(a1, b1l, acc1, 0, 0, 0);
      }
    }
  }
  float* red = (float*)(smem + 96 * 144);
#pragma unroll
  for (int q = 0; q < 4; q++) {
    *(f32x4*)(red + (((wave * 2 + 0) * 4 + q) * 64 + lane) * 4) = (f32x4){acc0[4 * q], acc0[4 * q + 1], acc0[4 * q + 2], acc0[4 * q + 3]};
    *(f32x4*)(red + (((wave * 2 + 1) * 4 + q) * 64 + lane) * 4) = (f32x4){acc1[4 * q], acc1[4 * q + 1], acc1[4 * q + 2], acc1[4 * q + 3]};
  }
  __syncthreads();
  const float dsk = b2f(Dskip[d]);
  u16* aT = (u16*)(ws + WS_AT) + (size_t)d * L;
#pragma unroll
  for (int t = 0; t < 2; t++) {
    f32x4 s = *(const f32x4*)(red + (((0 * 2 + t) * 4 + wave) * 64 + lane) * 4);
    f32x4 s1 = *(const f32x4*)(red + (((1 * 2 + t) * 4 + wave) * 64 + lane) * 4);
    f32x4 s2 = *(const f32x4*)(red + (((2 * 2 + t) * 4 + wave) * 64 + lane) * 4);
    f32x4 s3 = *(const f32x4*)(red + (((3 * 2 + t) * 4 + wave) * 64 + lane) * 4);
    s = (s + s1) + (s2 + s3);
    const int l = 32 * (32 * t + r) + 8 * wave + 4 * h;
    u32x2 uu = *(const u32x2*)(uT + l);
    float o[4];
#pragma unroll
    for (int e = 0; e < 4; e++) {
      float u = b2f((u16)((uu[e >> 1] >> (16 * (e & 1))) & 0xffffu));
      o[e] = rb(s[e]) + rb(u * dsk);
    }
    u32x2 ov; ov[0] = pack2(o[0], o[1]); ov[1] = pack2(o[2], o[3]);
    *(u32x2*)(aT + l) = ov;
  }
}

constexpr int K3_RS = 352;
constexpr int K3_LDS = 4 * 64 * K3_RS + 64 * 176;
__global__ __launch_bounds__(256, 1) void k3(char* __restrict__ ws, u16* __restrict__ out) {
  __shared__ __attribute__((aligned(16))) char smem[K3_LDS];
  const int tid = threadIdx.x, wave = tid >> 6, lane = tid & 63;
  const int xcd = blockIdx.x & 7, idx = blockIdx.x >> 3;
  const int rowblk = xcd * 4 + (idx >> 3), cb = idx & 7;
  const int l0 = rowblk * 64, c0 = cb * 80;
  char* tb = smem + wave * (64 * K3_RS);
  const int r = lane & 31, h = lane >> 5;
  u32x4 pf0[8][2]; u32x2 pf1[8][2]; u32x4 kv0[8], kv1[8];
  {
    const u32x4* p0 = (const u32x4*)(ws + WS_PQ) + ((size_t)(rowblk * 32 + wave * 8) * 2) * 64 + lane;
    const u32x2* p1 = (const u32x2*)(ws + WS_PQ + (size_t)L * 16 * 32 * 2) + ((size_t)(rowblk * 32 + wave * 8) * 2) * 32 + r;
    const u32x4* kvt = (const u32x4*)(ws + WS_KVT) + (size_t)(wave * 16) * 64 + lane;
#pragma unroll
    for (int hh = 0; hh < 8; hh++) {
      kv0[hh] = kvt[(hh * 2 + 0) * 64]; kv1[hh] = kvt[(hh * 2 + 1) * 64];
#pragma unroll
      for (int t = 0; t < 2; t++) { pf0[hh][t] = p0[(hh * 2 + t) * 64]; pf1[hh][t] = p1[(hh * 2 + t) * 32]; }
    }
  }
  u32x4 av3[3];
  {
    const u16* aT = (const u16*)(ws + WS_AT);
#pragma unroll
    for (int i3 = 0; i3 < 3; i3++) {
      int i = tid + 256 * i3; if (i > 639) i = 639;
      int dd = i >> 3, oc = i & 7;
      av3[i3] = *(const u32x4*)(aT + (size_t)(c0 + dd) * L + l0 + 8 * oc);
    }
  }
  s16x8 wf[5][5];
  {
    const u32x4* wp = (const u32x4*)(ws + WS_WOF) + (size_t)((cb * 4 + wave) * 25) * 64 + lane;
#pragma unroll
    for (int ks = 0; ks < 5; ks++)
#pragma unroll
      for (int ct = 0; ct < 5; ct++) wf[ks][ct] = __builtin_bit_cast(s16x8, wp[(ks * 5 + ct) * 64]);
  }
  __builtin_amdgcn_sched_barrier(0);
  {
#pragma unroll
    for (int hh = 0; hh < 8; hh++) {
      s16x8 a0 = __builtin_bit_cast(s16x8, kv0[hh]);
      s16x8 a1 = __builtin_bit_cast(s16x8, kv1[hh]);
#pragma unroll
      for (int t = 0; t < 2; t++) {
        char* rowp = tb + (32 * t + r) * K3_RS + hh * 40;
        s16x8 b0 = __builtin_bit_cast(s16x8, pf0[hh][t]);
        u32x4 b1u = {h ? 0u : pf1[hh][t][0], h ? 0u : pf1[hh][t][1], 0u, 0u};
        s16x8 b1 = __builtin_bit_cast(s16x8, b1u);
        f32x16 c;
#pragma unroll
        for (int i = 0; i < 16; i++) c[i] = 0.f;
        c = __builtin_amdgcn_mfma_f32_32x32x16_bf16(a0, b0, c, 0, 0, 0);
        c = __builtin_amdgcn_mfma_f32_32x32x16_bf16(a1, b1, c, 0, 0, 0);
        float den = rb(rb(c[12]) + 1e-6f);
        float inv = __builtin_amdgcn_rcpf(den);
#pragma unroll
        for (int q = 0; q < 3; q++) {
          unsigned n0 = pack2(c[4 * q], c[4 * q + 1]), n1 = pack2(c[4 * q + 2], c[4 * q + 3]);
          u32x2 o;
          o[0] = pack2(lo_f(n0) * inv, hi_f(n0) * inv);
          o[1] = pack2(lo_f(n1) * inv, hi_f(n1) * inv);
          if (q < 2 || h == 0) *(u32x2*)(rowp + (8 * q + 4 * h) * 2) = o;
        }
      }
    }
  }
  {
    char* at = smem + 4 * 64 * K3_RS;
#pragma unroll
    for (int i3 = 0; i3 < 3; i3++) {
      int i = tid + 256 * i3;
      if (i < 640) {
        int dd = i >> 3, oc = i & 7;
#pragma unroll
        for (int e = 0; e < 8; e++) *(u16*)(at + (8 * oc + e) * 176 + dd * 2) = (u16)((av3[i3][e >> 1] >> (16 * (e & 1))) & 0xffffu);
      }
    }
  }
  __builtin_amdgcn_wave_barrier();
  f32x4 acc[4][5];
#pragma unroll
  for (int m = 0; m < 4; m++)
#pragma unroll
    for (int c = 0; c < 5; c++) acc[m][c] = (f32x4){0.f, 0.f, 0.f, 0.f};
  {
    const char* ap = tb + (lane & 15) * K3_RS + (lane >> 4) * 16;
#pragma unroll
    for (int ks = 0; ks < 5; ks++) {
#pragma unroll
      for (int m = 0; m < 4; m++) {
        s16x8 a = *(const s16x8*)(ap + m * 16 * K3_RS + ks * 64);
#pragma unroll
        for (int c = 0; c < 5; c++) acc[m][c] = __builtin_amdgcn_mfma_f32_16x16x32_bf16(wf[ks][c], a, acc[m][c], 0, 0, 0);
      }
    }
  }
  __builtin_amdgcn_wave_barrier();
  {
    float* pr = (float*)tb;
#pragma unroll
    for (int m = 0; m < 4; m++)
#pragma unroll
      for (int c = 0; c < 5; c++) *(f32x4*)(pr + (16 * m + (lane & 15)) * 84 + 16 * c + 4 * (lane >> 4)) = acc[m][c];
  }
  __syncthreads();
  {
    const float* gate = (const float*)(ws + WS_GATE);
    const char* at = smem + 4 * 64 * K3_RS;
    for (int i = tid; i < 640; i += 256) {
      int rr = i / 10, oc = i % 10;
      float g = gate[l0 + rr]; float g1 = rb(1.f - g);
      f32x4 s0 = {0.f, 0.f, 0.f, 0.f}, s1 = {0.f, 0.f, 0.f, 0.f};
#pragma unroll
      for (int w = 0; w < 4; w++) {
        const float* pr = (const float*)(smem + w * (64 * K3_RS)) + rr * 84 + oc * 8;
        s0 += *(const f32x4*)pr; s1 += *(const f32x4*)(pr + 4);
      }
      u32x4 av = *(const u32x4*)(at + rr * 176 + oc * 16);
      u32x4 ov;
#pragma unroll
      for (int e = 0; e < 4; e++) {
        float b0 = rb(e < 2 ? s0[2 * e] : s1[2 * e - 4]), b1 = rb(e < 2 ? s0[2 * e + 1] : s1[2 * e - 3]);
        float a0 = b2f((u16)(av[e] & 0xffffu)), a1 = b2f((u16)(av[e] >> 16));
        ov[e] = pack2(rb(g * a0) + rb(g1 * b0), rb(g * a1) + rb(g1 * b1));
      }
      *(u32x4*)(out + (size_t)(l0 + rr) * D + c0 + oc * 8) = ov;
    }
  }
}

extern "C" void kernel_launch(void* const* d_in, const int* in_sizes, int n_in, void* d_out, int out_size, void* d_ws, size_t ws_size,
                              hipStream_t stream) {
  const u16* u = (const u16*)d_in[0]; const u16* logA = (const u16*)d_in[1]; const u16* Aim = (const u16*)d_in[2];
  const u16* Cr = (const u16*)d_in[3]; const u16* Ci = (const u16*)d_in[4]; const u16* logdt = (const u16*)d_in[5];
  const u16* Dsk = (const u16*)d_in[6]; const u16* wqkv = (const u16*)d_in[8]; const u16* wo = (const u16*)d_in[9];
  const u16* gw = (const u16*)d_in[10];
  char* ws = (char*)d_ws;
  k1<<<NB_GEMM + NB_TR + NB_WO + NB_KG, 256, 0, stream>>>(u, logA, Aim, Cr, Ci, logdt, wqkv, wo, gw, ws);
  k2<<<D + NH, 256, 0, stream>>>(Dsk, ws);
  k3<<<256, 256, 0, stream>>>(ws, (u16*)d_out);
}
```

```cpp
#include <hip/hip_runtime.h>
#include <hip/hip_bf16.h>
#include <stdint.h>

typedef unsigned short u16;
typedef __attribute__((ext_vector_type(8))) short s16x8;
typedef __attribute__((ext_vector_type(4))) short s16x4;
typedef __attribute__((ext_vector_type(4))) float f32x4;
typedef __attribute__((ext_vector_type(16))) float f32x16;
typedef __attribute__((ext_vector_type(4))) unsigned u32x4;
typedef __attribute__((ext_vector_type(2))) unsigned u32x2;
struct __attribute__((packed, aligned(2))) U16B { u32x4 v; };
typedef __attribute__((address_space(3))) s16x4* lds_s16x4_p;

constexpr int L = 2048, D = 640, NS = 192, NH = 32;
constexpr int UT_PAD = 32, UT_STRIDE = L + UT_PAD;
constexpr size_t WS_UT   = 0;
constexpr size_t WS_KIMG = WS_UT + (size_t)D * UT_STRIDE * 2;
constexpr size_t WS_PQ   = WS_KIMG + (size_t)D * 8192;
constexpr size_t WS_PART = WS_PQ + (size_t)L * D * 2;
constexpr size_t WS_GATE = WS_PART + (size_t)16 * 32 * 420 * 4;
constexpr size_t WS_WOF  = WS_GATE + (size_t)L * 4;
constexpr size_t WS_KVT  = WS_WOF + (size_t)D * D * 2;
constexpr size_t WS_AT   = WS_KVT + (size_t)32 * 2 * 64 * 16;
constexpr size_t WS_END  = WS_AT + (size_t)D * L * 2;

__device__ __forceinline__ float b2f(u16 v) { return __uint_as_float(((unsigned)v) << 16); }
__device__ __forceinline__ u16 f2b(float f) { __hip_bfloat16 h = __float2bfloat16(f); return *reinterpret_cast<u16*>(&h); }
__device__ __forceinline__ float rb(float f) { return b2f(f2b(f)); }
__device__ __forceinline__ float phi_f(float t) {
  float r = t > 0.f ? rb(t + 1.f) : rb(__expf(t));
  return rb(r + 1e-6f);
}
typedef __bf16 bf16x2_t __attribute__((ext_vector_type(2)));
typedef float f32x2 __attribute__((ext_vector_type(2)));
__device__ __forceinline__ unsigned pack2(float a, float b) { f32x2 v = {a, b}; bf16x2_t r = __builtin_convertvector(v, bf16x2_t); return __builtin_bit_cast(unsigned, r); }
__device__ __forceinline__ float lo_f(unsigned u) { return __uint_as_float(u << 16); }
__device__ __forceinline__ float hi_f(unsigned u) { return __uint_as_float(u & 0xffff0000u); }
__device__ __forceinline__ unsigned phi2(float a, float b) {
  unsigned t = pack2(a, b); float t0 = lo_f(t), t1 = hi_f(t);
  float r0 = t0 > 0.f ? t0 + 1.f : __expf(t0), r1 = t1 > 0.f ? t1 + 1.f : __expf(t1);
  unsigned r = pack2(r0, r1);
  return pack2(lo_f(r) + 1e-6f, hi_f(r) + 1e-6f);
}
__device__ __forceinline__ unsigned packhi(float lo, float hi) { return __builtin_amdgcn_perm(__float_as_uint(hi), __float_as_uint(lo), 0x07060302u); }

struct cf { float r, i; };
__device__ __forceinline__ float smul(float a, float b) { float o; asm("v_mul_f32 %0, %1, %2" : "=v"(o) : "v"(a), "v"(b)); return o; }
__device__ __forceinline__ float sfma(float a, float b, float c) { float o; asm("v_fma_f32 %0, %1, %2, %3" : "=v"(o) : "v"(a), "v"(b), "v"(c)); return o; }
__device__ __forceinline__ float sfnma(float a, float b, float c) { float o; asm("v_fma_f32 %0, -%1, %2, %3" : "=v"(o) : "v"(a), "v"(b), "v"(c)); return o; }
__device__ __forceinline__ cf cmul(cf a, cf b) { cf o; o.r = sfnma(a.i, b.i, smul(a.r, b.r)); o.i = sfma(a.i, b.r, smul(a.r, b.i)); return o; }
__device__ __forceinline__ cf zpow(float ar, float ai, float p) {
  float e = expf(ar * p); float s, c; sincosf(ai * p, &s, &c); cf o; o.r = e * c; o.i = e * s; return o;
}

constexpr int K1_LDS = 50688;
constexpr int NB_GEMM = 192, NB_TR = 64, NB_WO = 50, NB_KG = 640;

template <bool ISQ>
__device__ __forceinline__ void gemm_role(char* smem, const u16* __restrict__ x, const u16* __restrict__ W,
                                          u16* __restrict__ pq, float* __restrict__ part, int rowblk, int cb, int rot) {
  const int tid = threadIdx.x, wave = tid >> 6, lane = tid & 63;
  const int row0 = rowblk * 128;
  const int seg0 = ISQ ? 160 * (cb - 8) : 640 + 80 * cb;
  const int seg1 = ISQ ? seg0 + 80 : 1280 + 80 * cb;
  char* sA = smem; char* sB = smem + 20480;
  const u16* ga[4]; int la[4];
#pragma unroll
  for (int i = 0; i < 4; i++) { int id = tid + 256 * i; int r = id >> 3, ch = id & 7; ga[i] = x + (size_t)(row0 + r) * D + ch * 8; la[i] = r * 160 + ch * 16; }
  const u16* gb[5]; int lb[5];
#pragma unroll
  for (int i = 0; i < 5; i++) { int id = tid + 256 * i; int kr = id / 20, ch = id % 20; int col = (ch < 10 ? seg0 + ch * 8 : seg1 + (ch - 10) * 8);
    gb[i] = W + (size_t)kr * 1920 + col; lb[i] = kr * 320 + (kr >> 3) * 32 + ch * 16; }
  u32x4 ra[4], rbv[5];
  f32x4 acc[2][10];
#pragma unroll
  for (int m = 0; m < 2; m++)
#pragma unroll
    for (int c = 0; c < 10; c++) acc[m][c] = (f32x4){0.f, 0.f, 0.f, 0.f};
  int kt = rot;
  {
    int k0 = kt * 64;
#pragma unroll
    for (int i = 0; i < 4; i++) ra[i] = *(const u32x4*)(ga[i] + k0);
#pragma unroll
    for (int i = 0; i < 5; i++) rbv[i] = *(const u32x4*)(gb[i] + (size_t)k0 * 1920);
  }
  const int g = lane >> 4, fr = lane & 15;
  const int aoff = (wave * 32 + fr) * 160 + g * 16;
  const int q4 = (lane & 15) >> 2, p4 = lane & 3;
  const int boff = (8 * g + q4) * 320 + g * 32 + p4 * 8;
  for (int t = 0; t < 10; t++) {
    __syncthreads();
#pragma unroll
    for (int i = 0; i < 4; i++) *(u32x4*)(sA + la[i]) = ra[i];
#pragma unroll
    for (int i = 0; i < 5; i++) *(u32x4*)(sB + lb[i]) = rbv[i];
    if (t < 9) {
      kt = kt + 1; if (kt >= 10) kt = 0;
      int k0 = kt * 64;
#pragma unroll
      for (int i = 0; i < 4; i++) ra[i] = *(const u32x4*)(ga[i] + k0);
#pragma unroll
      for (int i = 0; i < 5; i++) rbv[i] = *(const u32x4*)(gb[i] + (size_t)k0 * 1920);
    }
    __syncthreads();
#pragma unroll
    for (int ks = 0; ks < 2; ks++) {
      s16x8 af[2];
#pragma unroll
      for (int m = 0; m < 2; m++) af[m] = *(const s16x8*)(sA + aoff + m * 16 * 160 + ks * 64);
#pragma unroll
      for (int c = 0; c < 10; c++) {
        const char* bp = sB + boff + ks * (32 * 320 + 4 * 32) + c * 32;
        s16x4 b0 = __builtin_amdgcn_ds_read_tr16_b64_v4i16((lds_s16x4_p)(bp));
        s16x4 b1 = __builtin_amdgcn_ds_read_tr16_b64_v4i16((lds_s16x4_p)(bp + 4 * 320));
        s16x8 bf = __builtin_shufflevector(b0, b1, 0, 1, 2, 3, 4, 5, 6, 7);
#pragma unroll
        for (int m = 0; m < 2; m++) {
          if (ISQ) acc[m][c] = __builtin_amdgcn_mfma_f32_16x16x32_bf16(bf, af[m], acc[m][c], 0, 0, 0);
          else     acc[m][c] = __builtin_amdgcn_mfma_f32_16x16x32_bf16(af[m], bf, acc[m][c], 0, 0, 0);
        }
      }
    }
  }
  if (ISQ) {
    char* pq0 = (char*)pq; char* pq1 = (char*)pq + (size_t)L * 16 * 32 * 2;
#pragma unroll
    for (int m = 0; m < 2; m++) {
      const int row = row0 + wave * 32 + 16 * m + fr;
      const int rb64 = row >> 6, t = (row >> 5) & 1, r = row & 31;
#pragma unroll
      for (int c = 0; c < 10; c++) {
        u32x2 o;
        o[0] = phi2(acc[m][c][0], acc[m][c][1]);
        o[1] = phi2(acc[m][c][2], acc[m][c][3]);
        const int n = seg0 + 16 * c + 4 * g;
        const int hd = n / 20, d0 = n - 20 * hd;
        const int fi = (rb64 * 32 + hd) * 2 + t;
        char* dst = d0 < 16 ? pq0 + ((size_t)fi * 64 + (d0 >> 3) * 32 + r) * 16 + (d0 & 7) * 2 : pq1 + ((size_t)fi * 32 + r) * 8;
        *(u32x2*)dst = o;
      }
    }
  } else {
    s16x8 pk[5], vv[5];
#pragma unroll
    for (int c = 0; c < 5; c++) {
      u32x4 a, b;
      a[0] = phi2(acc[0][c][0], acc[0][c][1]);
      a[1] = phi2(acc[0][c][2], acc[0][c][3]);
      a[2] = phi2(acc[1][c][0], acc[1][c][1]);
      a[3] = phi2(acc[1][c][2], acc[1][c][3]);
      b[0] = pack2(acc[0][5 + c][0], acc[0][5 + c][1]);
      b[1] = pack2(acc[0][5 + c][2], acc[0][5 + c][3]);
      b[2] = pack2(acc[1][5 + c][0], acc[1][5 + c][1]);
      b[3] = pack2(acc[1][5 + c][2], acc[1][5 + c][3]);
      pk[c] = __builtin_bit_cast(s16x8, a); vv[c] = __builtin_bit_cast(s16x8, b);
    }
    const s16x8 ones = {0x3F80, 0x3F80, 0x3F80, 0x3F80, 0x3F80, 0x3F80, 0x3F80, 0x3F80};
    __syncthreads();
    float* red = (float*)smem + wave * 1680;
#pragma unroll
    for (int i = 0; i < 5; i++) {
#pragma unroll
      for (int j = 0; j < 5; j++) {
        if (j - i > 1 || i - j > 1) continue;
        f32x4 r = __builtin_amdgcn_mfma_f32_16x16x32_bf16(pk[i], vv[j], (f32x4){0.f, 0.f, 0.f, 0.f}, 0, 0, 0);
        int ecol = 16 * j + fr; int eh = ecol / 20;
#pragma unroll
        for (int e = 0; e < 4; e++) {
          int dcol = 16 * i + 4 * g + e; int dh = dcol / 20;
          if (dh == eh) red[dh * 420 + (dcol - 20 * dh) * 20 + (ecol - 20 * eh)] = r[e];
        }
      }
      f32x4 r = __builtin_amdgcn_mfma_f32_16x16x32_bf16(pk[i], ones, (f32x4){0.f, 0.f, 0.f, 0.f}, 0, 0, 0);
      if (fr == 0) {
#pragma unroll
        for (int e = 0; e < 4; e++) { int dcol = 16 * i + 4 * g + e; int dh = dcol / 20; red[dh * 420 + 400 + dcol - 20 * dh] = r[e]; }
      }
    }
    __syncthreads();
    const float* rr = (const float*)smem;
    for (int i = tid; i < 1680; i += 256) {
      float s = (rr[i] + rr[1680 + i]) + (rr[3360 + i] + rr[5040 + i]);
      part[((size_t)rowblk * 32 + 4 * cb) * 420 + i] = s;
    }
  }
}

__device__ __forceinline__ void tr_role(char* smem, const u16* __restrict__ x, const u16* __restrict__ gw,
                                        u16* __restrict__ uT, float* __restrict__ gate, int blk) {
  const int tid = threadIdx.x, wave = tid >> 6, lane = tid & 63;
  const int l0 = blk * 32;
  constexpr int RS = 1296;
#pragma unroll
  for (int i = 0; i < 10; i++) { int id = tid + 256 * i; int r = id / 80, ch = id % 80;
    *(u32x4*)(smem + r * RS + ch * 16) = *(const u32x4*)(x + (size_t)(l0 + r) * D + ch * 8); }
  float gwr[10];
#pragma unroll
  for (int i = 0; i < 10; i++) gwr[i] = b2f(gw[lane + 64 * i]);
  __syncthreads();
  for (int rr = 0; rr < 8; rr++) {
    int r = wave * 8 + rr; float s = 0.f;
#pragma unroll
    for (int i = 0; i < 10; i++) s += rb(b2f(*(const u16*)(smem + r * RS + (lane + 64 * i) * 2)) * gwr[i]);
#pragma unroll
    for (int o = 32; o > 0; o >>= 1) s += __shfl_xor(s, o);
    if (lane == 0) { float a = rb(s); gate[l0 + r] = rb(1.f / (1.f + __expf(-a))); }
  }
#pragma unroll
  for (int i = 0; i < 10; i++) {
    int id = tid + 256 * i; int d = id >> 2, oc = id & 3;
    u32x4 v;
#pragma unroll
    for (int e = 0; e < 4; e++) {
      unsigned lo = *(const u16*)(smem + (8 * oc + 2 * e) * RS + d * 2);
      unsigned hi = *(const u16*)(smem + (8 * oc + 2 * e + 1) * RS + d * 2);
      v[e] = lo | (hi << 16);
    }
    *(u32x4*)(uT + (size_t)d * UT_STRIDE + UT_PAD + l0 + 8 * oc) = v;
    if (blk == 0) *(u32x4*)(uT + (size_t)d * UT_STRIDE + 8 * oc) = (u32x4){0u, 0u, 0u, 0u};
  }
}

__device__ __forceinline__ void wo_role(const u16* __restrict__ wo, u16* __restrict__ woF, int blk) {
#pragma unroll
  for (int i = 0; i < 4; i++) {
    int id = blk * 1024 + i * 256 + threadIdx.x;
    int lane = id & 63; int f = id >> 6;
    int ct = f % 5; int ks = (f / 5) % 5; int w = (f / 25) & 3; int cb = f / 100;
    int k = 160 * w + 32 * ks + 8 * (lane >> 4); int n = 80 * cb + 16 * ct + (lane & 15);
    u32x4 v;
#pragma unroll
    for (int e = 0; e < 4; e++) {
      unsigned lo = wo[(size_t)(k + 2 * e) * D + n]; unsigned hi = wo[(size_t)(k + 2 * e + 1) * D + n];
      v[e] = lo | (hi << 16);
    }
    *(u32x4*)(woF + (size_t)id * 8) = v;
  }
}

__device__ __forceinline__ void put_A(char* p, cf w) {
  float r = w.r, i = -w.i;
  float rh = __uint_as_float(__float_as_uint(r) & 0xffff0000u), ih = __uint_as_float(__float_as_uint(i) & 0xffff0000u);
  float rl = r - rh, il = i - ih;
  u32x4 v; v[0] = packhi(r, r); v[1] = packhi(rl, i); v[2] = packhi(i, il); v[3] = packhi(rl, il);
  *(u32x4*)p = v;
}
__device__ __forceinline__ void put_B(char* p, cf w) {
  float r = w.r, i = w.i;
  float rh = __uint_as_float(__float_as_uint(r) & 0xffff0000u), ih = __uint_as_float(__float_as_uint(i) & 0xffff0000u);
  float rl = r - rh, il = i - ih;
  u32x4 v; v[0] = packhi(r, rl); v[1] = packhi(r, i); v[2] = packhi(il, i); v[3] = packhi(rl, il);
  *(u32x4*)p = v;
}
__device__ __forceinline__ void kgen_role(char* smem, const u16* __restrict__ logA, const u16* __restrict__ Aim,
                                          const u16* __restrict__ Cr, const u16* __restrict__ Ci, const u16* __restrict__ logdt,
                                          u16* __restrict__ kimg, int d) {
  const int tid = threadIdx.x, wave = tid >> 6, lane = tid & 63;
  constexpr int RS = 1056;
  f32x4 acc[3];
  if (wave < 3) {
    const int n = wave * 64 + lane;
    const float dt = rb(expf(b2f(logdt[d])));
    const float ar = -rb(expf(b2f(logA[d * NS + n]))) * dt;
    const float ai = b2f(Aim[d * NS + n]) * dt;
    cf C; C.r = b2f(Cr[d * NS + n]) * dt; C.i = b2f(Ci[d * NS + n]) * dt;
    char* tb = smem + wave * (16 * RS);
    char* wp = tb + lane * 16;
    const char* rp = tb + (lane & 15) * RS + (lane >> 4) * 16;
    {
      cf z8 = zpow(ar, ai, 8.f); cf e; e.r = 1.f; e.i = 0.f;
      put_B(wp, e); e = z8; put_B(wp + RS, e);
#pragma unroll
      for (int c = 2; c < 16; c++) { e = cmul(e, z8); put_B(wp + c * RS, e); }
    }
    __builtin_amdgcn_wave_barrier();
    s16x8 bfr[16];
#pragma unroll
    for (int k = 0; k < 16; k++) bfr[k] = *(const s16x8*)(rp + k * 64);
    __builtin_amdgcn_wave_barrier();
    cf S[8];
    S[1] = zpow(ar, ai, 1.f);
#pragma unroll
    for (int s = 2; s < 8; s++) S[s] = cmul(S[s - 1], S[1]);
    const cf z128 = zpow(ar, ai, 128.f);
    cf G = C;
    put_A(wp, G);
#pragma unroll
    for (int s = 1; s < 8; s++) put_A(wp + s * RS, cmul(G, S[s]));
    G = cmul(G, z128);
    put_A(wp + 8 * RS, G);
#pragma unroll
    for (int s = 1; s < 8; s++) put_A(wp + (8 + s) * RS, cmul(G, S[s]));
    __builtin_amdgcn_wave_barrier();
    {
      f32x4 a = {0.f, 0.f, 0.f, 0.f};
#pragma unroll
      for (int k = 0; k < 16; k++) a = __builtin_amdgcn_mfma_f32_16x16x32_bf16(*(const s16x8*)(rp + k * 64), bfr[k], a, 0, 0, 0);
      acc[0] = a;
    }
    __builtin_amdgcn_wave_barrier();
#pragma unroll
    for (int h = 0; h < 2; h++) {
      G = cmul(G, z128);
      put_A(wp + (4 * h) * RS, G);
#pragma unroll
      for (int s = 1; s < 4; s++) put_A(wp + (4 * h + s) * RS, cmul(G, S[2 * s]));
    }
#pragma unroll
    for (int h = 0; h < 4; h++) {
      G = cmul(G, z128);
      put_A(wp + (8 + 2 * h) * RS, G);
      put_A(wp + (9 + 2 * h) * RS, cmul(G, S[4]));
    }
    __builtin_amdgcn_wave_barrier();
    {
      f32x4 a = {0.f, 0.f, 0.f, 0.f};
#pragma unroll
      for (int k = 0; k < 16; k++) a = __builtin_amdgcn_mfma_f32_16x16x32_bf16(*(const s16x8*)(rp + k * 64), bfr[k], a, 0, 0, 0);
      acc[1] = a;
    }
    __builtin_amdgcn_wave_barrier();
#pragma unroll
    for (int h = 0; h < 9; h++) { G = cmul(G, z128); put_A(wp + h * RS, G); }
    __builtin_amdgcn_wave_barrier();
    {
      f32x4 a = {0.f, 0.f, 0.f, 0.f};
#pragma unroll
      for (int k = 0; k < 16; k++) a = __builtin_amdgcn_mfma_f32_16x16x32_bf16(*(const s16x8*)(rp + k * 64), bfr[k], a, 0, 0, 0);
      acc[2] = a;
    }
  }
  __syncthreads();
  float* red = (float*)smem;
  if (wave < 3) {
#pragma unroll
    for (int t = 0; t < 3; t++)
#pragma unroll
      for (int e = 0; e < 4; e++) red[wave * 768 + (16 * t + 4 * (lane >> 4) + e) * 16 + (lane & 15)] = acc[t][e];
  }
  __syncthreads();
  {
    const int j0 = tid * 8;
    u32x4 hi, lo;
    float kv[8];
#pragma unroll
    for (int e = 0; e < 8; e++) {
      int j = j0 + 7 - e;
      int p = (int)rb((float)j);
      int h = p >> 7, c = (p >> 3) & 15, s = p & 7;
      int row = h < 2 ? 8 * h + s : (h < 4 ? 16 + 4 * (h - 2) + (s >> 1) : (h < 8 ? 24 + 2 * (h - 4) + (s >> 2) : 24 + h));
      int idx = row * 16 + c;
      kv[e] = 2.f * ((red[idx] + red[768 + idx]) + red[1536 + idx]);
    }
#pragma unroll
    for (int e = 0; e < 4; e++) {
      float a = kv[2 * e], b = kv[2 * e + 1];
      u16 ah = f2b(a), bh = f2b(b);
      hi[e] = (unsigned)ah | ((unsigned)bh << 16);
      lo[e] = pack2(a - b2f(ah), b - b2f(bh));
    }
    u16* dst = kimg + (size_t)d * 4096 + (j0 >> 5) * 64 + (j0 & 31);
    *(u32x4*)dst = hi;
    *(u32x4*)(dst + 32) = lo;
  }
}

__global__ __launch_bounds__(256, 3) void k1(const u16* __restrict__ x, const u16* __restrict__ logA, const u16* __restrict__ Aim,
                                          const u16* __restrict__ Cr, const u16* __restrict__ Ci, const u16* __restrict__ logdt,
                                          const u16* __restrict__ wqkv, const u16* __restrict__ wo, const u16* __restrict__ gw,
                                          char* __restrict__ ws) {
  __shared__ __attribute__((aligned(16))) char smem[K1_LDS];
  int b = blockIdx.x;
  if (b < NB_GEMM) {
    int xcd = b & 7, idx = b >> 3;
    int rowblk = 2 * xcd + idx / 12, cb = idx % 12;
    int rot = (idx * 3 + xcd) % 10;
    if (cb < 8) gemm_role<false>(smem, x, wqkv, (u16*)(ws + WS_PQ), (float*)(ws + WS_PART), rowblk, cb, rot);
    else        gemm_role<true>(smem, x, wqkv, (u16*)(ws + WS_PQ), (float*)(ws + WS_PART), rowblk, cb, rot);
    return;
  }
  b -= NB_GEMM;
  if (b < NB_TR) { tr_role(smem, x, gw, (u16*)(ws + WS_UT), (float*)(ws + WS_GATE), b); return; }
  b -= NB_TR;
  if (b < NB_WO) { wo_role(wo, (u16*)(ws + WS_WOF), b); return; }
  b -= NB_WO;
  kgen_role(smem, logA, Aim, Cr, Ci, logdt, (u16*)(ws + WS_KIMG), b);
}

constexpr int K2_LDS = 96 * 144 + 32768;
__global__ __launch_bounds__(256, 3) void k2(const u16* __restrict__ Dskip, char* __restrict__ ws) {
  __shared__ __attribute__((aligned(16))) char smem[K2_LDS];
  const int tid = threadIdx.x, wave = tid >> 6, lane = tid & 63;
  if (blockIdx.x >= D) {
    const int hd = blockIdx.x - D;
    const float* part = (const float*)(ws + WS_PART);
    float* s = (float*)smem;
    for (int i = tid; i < 420; i += 256) {
      float a = 0.f;
#pragma unroll
      for (int r = 0; r < 16; r++) a += part[((size_t)r * 32 + hd) * 420 + i];
      s[i] = rb(a);
    }
    __syncthreads();
    if (tid < 128) {
      int ks = tid >> 6, r = lane & 31, h = lane >> 5;
      u32x4 v;
#pragma unroll
      for (int e = 0; e < 4; e++) {
        float f[2];
#pragma unroll
        for (int t = 0; t < 2; t++) { int k = 16 * ks + 8 * h + 2 * e + t; f[t] = k < 20 ? (r < 20 ? s[k * 20 + r] : s[400 + k]) : 0.f; }
        v[e] = pack2(f[0], f[1]);
      }
      *(u32x4*)(ws + WS_KVT + ((size_t)(hd * 2 + ks) * 64 + lane) * 16) = v;
    }
    return;
  }
  const int d = blockIdx.x;
  const u16* uT = (const u16*)(ws + WS_UT) + (size_t)d * UT_STRIDE + UT_PAD;
  const int r = lane & 31, h = lane >> 5;
  const u16* ap = uT + (r - 8 * h - 7) + 32 * wave;
  {
    const u32x4* src = (const u32x4*)(ws + WS_KIMG + (size_t)d * 8192);
    u32x4 v0 = src[tid], v1 = src[tid + 256];
    for (int i = tid; i < 288; i += 256) *(u32x4*)(smem + i * 16) = (u32x4){0u, 0u, 0u, 0u};
    *(u32x4*)(smem + (32 + (tid >> 3)) * 144 + (tid & 7) * 16) = v0;
    *(u32x4*)(smem + (64 + (tid >> 3)) * 144 + (tid & 7) * 16) = v1;
  }
  f32x16 acc0, acc1;
#pragma unroll
  for (int i = 0; i < 16; i++) { acc0[i] = 0.f; acc1[i] = 0.f; }
  u32x4 af[2][4][2][2];
#pragma unroll
  for (int ii = 0; ii < 4; ii++)
#pragma unroll
    for (int w2 = 0; w2 < 2; w2++)
#pragma unroll
      for (int s = 0; s < 2; s++) af[0][ii][w2][s] = ((const U16B*)(ap + 128 * ii + 1024 * w2 - 16 * s))->v;
  __builtin_amdgcn_sched_barrier(0);
  __syncthreads();
#pragma unroll
  for (int ii = 0; ii < 4; ii++)
#pragma unroll
    for (int w2 = 0; w2 < 2; w2++)
#pragma unroll
      for (int s = 0; s < 2; s++) af[1][ii][w2][s] = ((const U16B*)(ap + 128 * (4 + ii) + 1024 * w2 - 16 * s))->v;
  __builtin_amdgcn_sched_barrier(0);
  const char* bp = smem + (32 + r - wave) * 144 + h * 16;
#pragma unroll
  for (int hf = 0; hf < 2; hf++) {
#pragma unroll
    for (int ii = 0; ii < 4; ii++) {
      const int dl = 4 * (4 * hf + ii);
#pragma unroll
      for (int s = 0; s < 2; s++) {
        s16x8 a1 = __builtin_bit_cast(s16x8, af[hf][ii][0][s]);
        s16x8 a2 = __builtin_bit_cast(s16x8, af[hf][ii][1][s]);
        s16x8 b0h = *(const s16x8*)(bp - dl * 144 + s * 32);
        s16x8 b0l = *(const s16x8*)(bp - dl * 144 + s * 32 + 64);
        s16x8 b1h = *(const s16x8*)(bp + (32 - dl) * 144 + s * 32);
        s16x8 b1l = *(const s16x8*)(bp + (32 - dl) * 144 + s * 32 + 64);
        acc0 = __builtin_amdgcn_mfma_f32_32x32x16_bf16(a1, b0h, acc0, 0, 0, 0);
        acc1 = __builtin_amdgcn_mfma_f32_32x32x16_bf16(a2, b0h, acc1, 0, 0, 0);
        acc0 = __builtin_amdgcn_mfma_f32_32x32x16_bf16(a1, b0l, acc0, 0, 0, 0);
        acc1 = __builtin_amdgcn_mfma_f32_32x32x16_bf16(a2, b0l, acc1, 0, 0, 0);
        acc1 = __builtin_amdgcn_mfma_f32_32x32x16_bf16(a1, b1h, acc1, 0, 0, 0);
        acc1 = __builtin_amdgcn_mfma_f32_32x32x16_bf16(a1, b1l, acc1, 0, 0, 0);
      }
    }
  }
  float* red = (float*)(smem + 96 * 144);
#pragma unroll
  for (int q = 0; q < 4; q++) {
    *(f32x4*)(red + (((wave * 2 + 0) * 4 + q) * 64 + lane) * 4) = (f32x4){acc0[4 * q], acc0[4 * q + 1], acc0[4 * q + 2], acc0[4 * q + 3]};
    *(f32x4*)(red + (((wave * 2 + 1) * 4 + q) * 64 + lane) * 4) = (f32x4){acc1[4 * q], acc1[4 * q + 1], acc1[4 * q + 2], acc1[4 * q + 3]};
  }
  __syncthreads();
  const float dsk = b2f(Dskip[d]);
  u16* aT = (u16*)(ws + WS_AT) + (size_t)d * L;
#pragma unroll
  for (int t = 0; t < 2; t++) {
    f32x4 s = *(const f32x4*)(red + (((0 * 2 + t) * 4 + wave) * 64 + lane) * 4);
    f32x4 s1 = *(const f32x4*)(red + (((1 * 2 + t) * 4 + wave) * 64 + lane) * 4);
    f32x4 s2 = *(const f32x4*)(red + (((2 * 2 + t) * 4 + wave) * 64 + lane) * 4);
    f32x4 s3 = *(const f32x4*)(red + (((3 * 2 + t) * 4 + wave) * 64 + lane) * 4);
    s = (s + s1) + (s2 + s3);
    const int l = 32 * (32 * t + r) + 8 * wave + 4 * h;
    u32x2 uu = *(const u32x2*)(uT + l);
    float o[4];
#pragma unroll
    for (int e = 0; e < 4; e++) {
      float u = b2f((u16)((uu[e >> 1] >> (16 * (e & 1))) & 0xffffu));
      o[e] = rb(s[e]) + rb(u * dsk);
    }
    u32x2 ov; ov[0] = pack2(o[0], o[1]); ov[1] = pack2(o[2], o[3]);
    *(u32x2*)(aT + l) = ov;
  }
}

constexpr int K3_RS = 352;
constexpr int K3_LDS = 4 * 64 * K3_RS + 64 * 176;
__global__ __launch_bounds__(256, 1) void k3(char* __restrict__ ws, u16* __restrict__ out) {
  __shared__ __attribute__((aligned(16))) char smem[K3_LDS];
  const int tid = threadIdx.x, wave = tid >> 6, lane = tid & 63;
  const int xcd = blockIdx.x & 7, idx = blockIdx.x >> 3;
  const int rowblk = xcd * 4 + (idx >> 3), cb = idx & 7;
  const int l0 = rowblk * 64, c0 = cb * 80;
  char* tb = smem + wave * (64 * K3_RS);
  const int r = lane & 31, h = lane >> 5;
  u32x4 pf0[8][2]; u32x2 pf1[8][2]; u32x4 kv0[8], kv1[8];
  {
    const u32x4* p0 = (const u32x4*)(ws + WS_PQ) + ((size_t)(rowblk * 32 + wave * 8) * 2) * 64 + lane;
    const u32x2* p1 = (const u32x2*)(ws + WS_PQ + (size_t)L * 16 * 32 * 2) + ((size_t)(rowblk * 32 + wave * 8) * 2) * 32 + r;
    const u32x4* kvt = (const u32x4*)(ws + WS_KVT) + (size_t)(wave * 16) * 64 + lane;
#pragma unroll
    for (int hh = 0; hh < 8; hh++) {
      kv0[hh] = kvt[(hh * 2 + 0) * 64]; kv1[hh] = kvt[(hh * 2 + 1) * 64];
#pragma unroll
      for (int t = 0; t < 2; t++) { pf0[hh][t] = p0[(hh * 2 + t) * 64]; pf1[hh][t] = p1[(hh * 2 + t) * 32]; }
    }
  }
  u32x4 av3[3];
  {
    const u16* aT = (const u16*)(ws + WS_AT);
#pragma unroll
    for (int i3 = 0; i3 < 3; i3++) {
      int i = tid + 256 * i3; if (i > 639) i = 639;
      int dd = i >> 3, oc = i & 7;
      av3[i3] = *(const u32x4*)(aT + (size_t)(c0 + dd) * L + l0 + 8 * oc);
    }
  }
  s16x8 wf[5][5];
  {
    const u32x4* wp = (const u32x4*)(ws + WS_WOF) + (size_t)((cb * 4 + wave) * 25) * 64 + lane;
#pragma unroll
    for (int ks = 0; ks < 5; ks++)
#pragma unroll
      for (int ct = 0; ct < 5; ct++) wf[ks][ct] = __builtin_bit_cast(s16x8, wp[(ks * 5 + ct) * 64]);
  }
  __builtin_amdgcn_sched_barrier(0);
  {
#pragma unroll
    for (int hh = 0; hh < 8; hh++) {
      s16x8 a0 = __builtin_bit_cast(s16x8, kv0[hh]);
      s16x8 a1 = __builtin_bit_cast(s16x8, kv1[hh]);
#pragma unroll
      for (int t = 0; t < 2; t++) {
        char* rowp = tb + (32 * t + r) * K3_RS + hh * 40;
        s16x8 b0 = __builtin_bit_cast(s16x8, pf0[hh][t]);
        u32x4 b1u = {h ? 0u : pf1[hh][t][0], h ? 0u : pf1[hh][t][1], 0u, 0u};
        s16x8 b1 = __builtin_bit_cast(s16x8, b1u);
        f32x16 c;
#pragma unroll
        for (int i = 0; i < 16; i++) c[i] = 0.f;
        c = __builtin_amdgcn_mfma_f32_32x32x16_bf16(a0, b0, c, 0, 0, 0);
        c = __builtin_amdgcn_mfma_f32_32x32x16_bf16(a1, b1, c, 0, 0, 0);
        float den = rb(rb(c[12]) + 1e-6f);
        float inv = __builtin_amdgcn_rcpf(den);
#pragma unroll
        for (int q = 0; q < 3; q++) {
          unsigned n0 = pack2(c[4 * q], c[4 * q + 1]), n1 = pack2(c[4 * q + 2], c[4 * q + 3]);
          u32x2 o;
          o[0] = pack2(lo_f(n0) * inv, hi_f(n0) * inv);
          o[1] = pack2(lo_f(n1) * inv, hi_f(n1) * inv);
          if (q < 2 || h == 0) *(u32x2*)(rowp + (8 * q + 4 * h) * 2) = o;
        }
      }
    }
  }
  {
    char* at = smem + 4 * 64 * K3_RS;
#pragma unroll
    for (int i3 = 0; i3 < 3; i3++) {
      int i = tid + 256 * i3;
      if (i < 640) {
        int dd = i >> 3, oc = i & 7;
#pragma unroll
        for (int e = 0; e < 8; e++) *(u16*)(at + (8 * oc + e) * 176 + dd * 2) = (u16)((av3[i3][e >> 1] >> (16 * (e & 1))) & 0xffffu);
      }
    }
  }
  __builtin_amdgcn_wave_barrier();
  f32x4 acc[4][5];
#pragma unroll
  for (int m = 0; m < 4; m++)
#pragma unroll
    for (int c = 0; c < 5; c++) acc[m][c] = (f32x4){0.f, 0.f, 0.f, 0.f};
  {
    const char* ap = tb + (lane & 15) * K3_RS + (lane >> 4) * 16;
#pragma unroll
    for (int ks = 0; ks < 5; ks++) {
#pragma unroll
      for (int m = 0; m < 4; m++) {
        s16x8 a = *(const s16x8*)(ap + m * 16 * K3_RS + ks * 64);
#pragma unroll
        for (int c = 0; c < 5; c++) acc[m][c] = __builtin_amdgcn_mfma_f32_16x16x32_bf16(wf[ks][c], a, acc[m][c], 0, 0, 0);
      }
    }
  }
  __builtin_amdgcn_wave_barrier();
  {
    float* pr = (float*)tb;
#pragma unroll
    for (int m = 0; m < 4; m++)
#pragma unroll
      for (int c = 0; c < 5; c++) *(f32x4*)(pr + (16 * m + (lane & 15)) * 84 + 16 * c + 4 * (lane >> 4)) = acc[m][c];
  }
  __syncthreads();
  {
    const float* gate = (const float*)(ws + WS_GATE);
    const char* at = smem + 4 * 64 * K3_RS;
    for (int i = tid; i < 640; i += 256) {
      int rr = i / 10, oc = i % 10;
      float g = gate[l0 + rr]; float g1 = rb(1.f - g);
      f32x4 s0 = {0.f, 0.f, 0.f, 0.f}, s1 = {0.f, 0.f, 0.f, 0.f};
#pragma unroll
      for (int w = 0; w < 4; w++) {
        const float* pr = (const float*)(smem + w * (64 * K3_RS)) + rr * 84 + oc * 8;
        s0 += *(const f32x4*)pr; s1 += *(const f32x4*)(pr + 4);
      }
      u32x4 av = *(const u32x4*)(at + rr * 176 + oc * 16);
      u32x4 ov;
#pragma unroll
      for (int e = 0; e < 4; e++) {
        float b0 = rb(e < 2 ? s0[2 * e] : s1[2 * e - 4]), b1 = rb(e < 2 ? s0[2 * e + 1] : s1[2 * e - 3]);
        float a0 = b2f((u16)(av[e] & 0xffffu)), a1 = b2f((u16)(av[e] >> 16));
        ov[e] = pack2(rb(g * a0) + rb(g1 * b0), rb(g * a1) + rb(g1 * b1));
      }
      *(u32x4*)(out + (size_t)(l0 + rr) * D + c0 + oc * 8) = ov;
    }
  }
}

extern "C" void kernel_launch(void* const* d_in, const int* in_sizes, int n_in, void* d_out, int out_size, void* d_ws, size_t ws_size,
                              hipStream_t stream) {
  const u16* u = (const u16*)d_in[0]; const u16* logA = (const u16*)d_in[1]; const u16* Aim = (const u16*)d_in[2];
  const u16* Cr = (const u16*)d_in[3]; const u16* Ci = (const u16*)d_in[4]; const u16* logdt = (const u16*)d_in[5];
  const u16* Dsk = (const u16*)d_in[6]; const u16* wqkv = (const u16*)d_in[8]; const u16* wo = (const u16*)d_in[9];
  const u16* gw = (const u16*)d_in[10];
  char* ws = (char*)d_ws;
  k1<<<NB_GEMM + NB_TR + NB_WO + NB_KG, 256, 0, stream>>>(u, logA, Aim, Cr, Ci, logdt, wqkv, wo, gw, ws);
  k2<<<D + NH, 256, 0, stream>>>(Dsk, ws);
  k3<<<256, 256, 0, stream>>>(ws, (u16*)d_out);
}
```

```cpp
#include <hip/hip_runtime.h>
#include <hip/hip_bf16.h>
#include <stdint.h>

typedef unsigned short u16;
typedef __attribute__((ext_vector_type(8))) short s16x8;
typedef __attribute__((ext_vector_type(4))) short s16x4;
typedef __attribute__((ext_vector_type(4))) float f32x4;
typedef __attribute__((ext_vector_type(16))) float f32x16;
typedef __attribute__((ext_vector_type(4))) unsigned u32x4;
typedef __attribute__((ext_vector_type(2))) unsigned u32x2;
typedef __attribute__((address_space(3))) s16x4* lds_s16x4_p;
typedef const __attribute__((address_space(1))) void* gptr_t;
typedef __attribute__((address_space(3))) void* lptr_t;

constexpr int L = 2048, D = 640, NS = 192, NH = 32;
constexpr int UT_PAD = 32, UT_STRIDE = L + UT_PAD;
constexpr size_t WS_UT   = 0;
constexpr size_t WS_KIMG = WS_UT + (size_t)D * UT_STRIDE * 2;
constexpr size_t WS_PQ   = WS_KIMG + (size_t)D * 8192;
constexpr size_t WS_PART = WS_PQ + (size_t)L * D * 2;
constexpr size_t WS_GATE = WS_PART + (size_t)L * 1280 * 2;
constexpr size_t WS_WOF  = WS_GATE + (size_t)L * 4;
constexpr size_t WS_KVT  = WS_WOF + (size_t)D * D * 2;
constexpr size_t WS_AT   = WS_KVT + (size_t)32 * 1280;
constexpr size_t WS_END  = WS_AT + (size_t)D * L * 2;
#define STAMP(kid, slot) do {} while (0)

__device__ __forceinline__ float b2f(u16 v) { return __uint_as_float(((unsigned)v) << 16); }
typedef __bf16 bf16x2_t __attribute__((ext_vector_type(2)));
typedef float f32x2 __attribute__((ext_vector_type(2)));
__device__ __forceinline__ unsigned pack2(float a, float b) { f32x2 v = {a, b}; bf16x2_t r = __builtin_convertvector(v, bf16x2_t); return __builtin_bit_cast(unsigned, r); }
__device__ __forceinline__ float lo_f(unsigned u) { return __uint_as_float(u << 16); }
__device__ __forceinline__ float hi_f(unsigned u) { return __uint_as_float(u & 0xffff0000u); }
__device__ __forceinline__ float rb(float f) { return lo_f(pack2(f, f)); }
__device__ __forceinline__ unsigned phi2(float a, float b) {
  unsigned t = pack2(a, b); float t0 = lo_f(t), t1 = hi_f(t);
  float r0 = t0 > 0.f ? t0 + 1.f : __expf(t0), r1 = t1 > 0.f ? t1 + 1.f : __expf(t1);
  unsigned r = pack2(r0, r1);
  return pack2(lo_f(r) + 1e-6f, hi_f(r) + 1e-6f);
}
#define VMCNT(n) asm volatile("s_waitcnt vmcnt(" #n ")" ::: "memory")
#define RAW_BARRIER() do { asm volatile("s_waitcnt lgkmcnt(0)" ::: "memory"); __builtin_amdgcn_s_barrier(); asm volatile("" ::: "memory"); } while (0)
__device__ __forceinline__ void glds16(const void* g, void* l) { __builtin_amdgcn_global_load_lds((gptr_t)g, (lptr_t)l, 16, 0, 0); }

struct cf { float r, i; };
__device__ __forceinline__ float smul(float a, float b) { float o; asm("v_mul_f32 %0, %1, %2" : "=v"(o) : "v"(a), "v"(b)); return o; }
__device__ __forceinline__ float sfma(float a, float b, float c) { float o; asm("v_fma_f32 %0, %1, %2, %3" : "=v"(o) : "v"(a), "v"(b), "v"(c)); return o; }
__device__ __forceinline__ float sfnma(float a, float b, float c) { float o; asm("v_fma_f32 %0, -%1, %2, %3" : "=v"(o) : "v"(a), "v"(b), "v"(c)); return o; }
__device__ __forceinline__ float ssub(float a, float b) { float o; asm("v_sub_f32 %0, %1, %2" : "=v"(o) : "v"(a), "v"(b)); return o; }
__device__ __forceinline__ cf cmul(cf a, cf b) { cf o; o.r = sfnma(a.i, b.i, smul(a.r, b.r)); o.i = sfma(a.i, b.r, smul(a.r, b.i)); return o; }

constexpr int K1_LDS = 81920;
constexpr int NB_GEMM = 240, NB_TR = 64, NB_KG = 320;

template <bool ISQ>
__device__ __forceinline__ void gemm_role(char* smem, const u16* __restrict__ x, const u16* __restrict__ W, char* __restrict__ ws,
                                          int rowblk, int cb, int rot) {
  const int tid = threadIdx.x, lane = tid & 63;
  const int wave = __builtin_amdgcn_readfirstlane(tid >> 6);
  const int wr = wave & 3, wc = wave >> 2;
  const int row0 = rowblk * 128, col0 = cb * 128;
  char* sA = smem; char* sB = smem + 49152;
  const char* gA = (const char*)x + (size_t)(row0 + 16 * wave + (lane >> 3)) * 1280 + (((lane & 7) ^ (lane >> 3)) << 4);
  const int fB = (lane >> 4) | (((wave >> 1) & 1) << 2);
  const char* gB = (const char*)W + (size_t)(4 * wave + (lane >> 4)) * 3840 + col0 * 2 + (((((lane & 15) >> 1) ^ fB) * 2 + (lane & 1)) << 4);
  auto issueA = [&](int st, int h) {
    int k = st + rot; if (k >= 10) k -= 10;
    glds16(gA + h * (8 * 1280) + k * 128, sA + (st % 3) * 16384 + (2 * wave + h) * 1024);
  };
  auto issueB = [&](int hs) {
    int k = hs + 2 * rot; if (k >= 20) k -= 20;
    glds16(gB + (size_t)k * (32 * 3840), sB + (hs & 3) * 8192 + wave * 1024);
  };
  issueA(0, 0); issueA(0, 1); issueB(0);
  issueA(1, 0); issueB(1);
  issueA(1, 1); issueB(2);
  f32x4 acc[2][4];
#pragma unroll
  for (int m = 0; m < 2; m++)
#pragma unroll
    for (int c = 0; c < 4; c++) acc[m][c] = (f32x4){0.f, 0.f, 0.f, 0.f};
  const int g = lane >> 4, fr = lane & 15;
  const int q4 = fr >> 2, p4 = lane & 3;
  int aoff[2];
#pragma unroll
  for (int ks = 0; ks < 2; ks++) aoff[ks] = (wr * 32 + fr) * 128 + (((4 * ks + g) ^ (fr & 7)) << 4);
  int boff[4];
  {
    const int f = q4 | ((g & 1) << 2);
#pragma unroll
    for (int c = 0; c < 4; c++) boff[c] = (8 * g + q4) * 256 + (((4 * wc + c) ^ f) << 5) + p4 * 8;
  }
#pragma unroll
  for (int s = 0; s < 20; s++) {
    if (s <= 16) VMCNT(4); else if (s == 17) VMCNT(3); else if (s == 18) VMCNT(1); else VMCNT(0);
    RAW_BARRIER();
    if (s < 16) issueA((s >> 1) + 2, s & 1);
    if (s < 17) issueB(s + 3);
    if (s == 0) STAMP(0, 2);
    const char* pa = sA + ((s >> 1) % 3) * 16384 + aoff[s & 1];
    const char* pb = sB + (s & 3) * 8192;
    s16x8 af[2], bf[4];
#pragma unroll
    for (int m = 0; m < 2; m++) af[m] = *(const s16x8*)(pa + m * 2048);
#pragma unroll
    for (int c = 0; c < 4; c++) {
      s16x4 b0 = __builtin_amdgcn_ds_read_tr16_b64_v4i16((lds_s16x4_p)(pb + boff[c]));
      s16x4 b1 = __builtin_amdgcn_ds_read_tr16_b64_v4i16((lds_s16x4_p)(pb + boff[c] + 1024));
      bf[c] = __builtin_shufflevector(b0, b1, 0, 1, 2, 3, 4, 5, 6, 7);
    }
#pragma unroll
    for (int c = 0; c < 4; c++)
#pragma unroll
      for (int m = 0; m < 2; m++) {
        if (ISQ) acc[m][c] = __builtin_amdgcn_mfma_f32_16x16x32_bf16(bf[c], af[m], acc[m][c], 0, 0, 0);
        else     acc[m][c] = __builtin_amdgcn_mfma_f32_16x16x32_bf16(af[m], bf[c], acc[m][c], 0, 0, 0);
      }
  }
  STAMP(0, 3);
  if (ISQ) {
    char* pq0 = ws + WS_PQ; char* pq1 = ws + WS_PQ + (size_t)L * 16 * 32 * 2;
#pragma unroll
    for (int m = 0; m < 2; m++) {
      const int row = row0 + wr * 32 + 16 * m + fr;
      const int rb64 = row >> 6, t = (row >> 5) & 1, r = row & 31;
#pragma unroll
      for (int c = 0; c < 4; c++) {
        u32x2 o;
        o[0] = phi2(acc[m][c][0], acc[m][c][1]);
        o[1] = phi2(acc[m][c][2], acc[m][c][3]);
        const int n = col0 + 64 * wc + 16 * c + 4 * g;
        const int hd = n / 20, d0 = n - 20 * hd;
        const int fi = (rb64 * 32 + hd) * 2 + t;
        char* dst = d0 < 16 ? pq0 + ((size_t)fi * 64 + (d0 >> 3) * 32 + r) * 16 + (d0 & 7) * 2 : pq1 + ((size_t)fi * 32 + r) * 8;
        *(u32x2*)dst = o;
      }
    }
  } else {
    const int rg = rowblk * 4 + wr;
    const bool isk = cb < 10;
    char* dst = ws + WS_PART + ((size_t)(rg * 4 + g) * 1280 + (col0 - 640) + 64 * wc + fr) * 16;
#pragma unroll
    for (int c = 0; c < 4; c++) {
      u32x4 a;
      if (isk) {
        a[0] = phi2(acc[0][c][0], acc[0][c][1]); a[1] = phi2(acc[0][c][2], acc[0][c][3]);
        a[2] = phi2(acc[1][c][0], acc[1][c][1]); a[3] = phi2(acc[1][c][2], acc[1][c][3]);
      } else {
        a[0] = pack2(acc[0][c][0], acc[0][c][1]); a[1] = pack2(acc[0][c][2], acc[0][c][3]);
        a[2] = pack2(acc[1][c][0], acc[1][c][1]); a[3] = pack2(acc[1][c][2], acc[1][c][3]);
      }
      *(u32x4*)(dst + c * 256) = a;
    }
  }
}

__device__ __forceinline__ void tr_role(char* smem, const u16* __restrict__ x, const u16* __restrict__ gw,
                                        u16* __restrict__ uT, float* __restrict__ gate, int blk) {
  const int tid = threadIdx.x, wave = tid >> 6, lane = tid & 63;
  const int l0 = blk * 32;
#pragma unroll
  for (int i = 0; i < 5; i++) { int id = tid + 512 * i; int r = id / 80, ch = id % 80;
    *(u32x4*)(smem + r * 1280 + (r >> 3) * 16 + ch * 16) = *(const u32x4*)(x + (size_t)(l0 + r) * D + ch * 8); }
  unsigned gwr[5];
#pragma unroll
  for (int i = 0; i < 5; i++) gwr[i] = *(const unsigned*)(gw + 2 * lane + 128 * i);
  __syncthreads();
#pragma unroll
  for (int rr = 0; rr < 4; rr++) {
    int r = wave * 4 + rr; float s = 0.f;
#pragma unroll
    for (int i = 0; i < 5; i++) {
      unsigned v = *(const unsigned*)(smem + r * 1280 + (r >> 3) * 16 + (2 * lane + 128 * i) * 2);
      unsigned p = pack2(lo_f(v) * lo_f(gwr[i]), hi_f(v) * hi_f(gwr[i]));
      s += lo_f(p) + hi_f(p);
    }
#pragma unroll
    for (int o = 32; o > 0; o >>= 1) s += __shfl_xor(s, o);
    if (lane == 0) { float a = rb(s); gate[l0 + r] = rb(1.f / (1.f + __expf(-a))); }
  }
#pragma unroll
  for (int i = 0; i < 5; i++) {
    int id = tid + 512 * i; int d = id >> 2, oc = id & 3;
    u32x4 v;
#pragma unroll
    for (int e = 0; e < 4; e++) {
      unsigned lo = *(const u16*)(smem + (8 * oc + 2 * e) * 1280 + oc * 16 + d * 2);
      unsigned hi = *(const u16*)(smem + (8 * oc + 2 * e + 1) * 1280 + oc * 16 + d * 2);
      v[e] = lo | (hi << 16);
    }
    *(u32x4*)(uT + (size_t)d * UT_STRIDE + UT_PAD + l0 + 8 * oc) = v;
    if (blk == 0) *(u32x4*)(uT + (size_t)d * UT_STRIDE + 8 * oc) = (u32x4){0u, 0u, 0u, 0u};
  }
}

__device__ __forceinline__ u32x2 split2(float r, float i) {
  u32x2 v; v[0] = pack2(r, i);
  v[1] = pack2(ssub(r, lo_f(v[0])), ssub(i, hi_f(v[0])));
  return v;
}
__device__ __forceinline__ cf zpow(float th, float tl, float rh, float rl, float p) {
  float e = __builtin_amdgcn_exp2f(th * p); e = fmaf(e, (tl * p) * 0.69314718f, e);
  float t = rh * p; float f = (t - __builtin_rintf(t)) + rl * p;
  cf o; o.r = e * __builtin_amdgcn_cosf(f); o.i = e * __builtin_amdgcn_sinf(f); return o;
}
constexpr int KG_RS = 544;
__device__ __forceinline__ void kgen_role(char* smem, const u16* __restrict__ logA, const u16* __restrict__ Aim,
                                          const u16* __restrict__ Cr, const u16* __restrict__ Ci, const u16* __restrict__ logdt,
                                          u16* __restrict__ kimg, int blk) {
  const int tid = threadIdx.x, lane = tid & 63;
  const int wave = __builtin_amdgcn_readfirstlane(tid >> 6);
  constexpr int RS = KG_RS;
  float* red = (float*)(smem + 6 * 16 * RS);
  if (wave < 6) {
    const int d = blk * 2 + (wave >= 3 ? 1 : 0);
    const int n = (wave >= 3 ? wave - 3 : wave) * 64 + lane;
    const float dt = rb(expf(b2f(logdt[d])));
    float ar;
    {
      const float la = b2f(logA[d * NS + n]);
      float h = la * 1.44269502f; float l = fmaf(la, 1.44269502f, -h) + la * 1.92596299e-8f;
      float e = __builtin_amdgcn_exp2f(h); e = fmaf(e, l * 0.69314718f, e);
      ar = -rb(e) * dt;
    }
    const float ai = b2f(Aim[d * NS + n]) * dt;
    cf C; C.r = b2f(Cr[d * NS + n]) * dt; C.i = b2f(Ci[d * NS + n]) * dt;
    const float th = ar * 1.44269502f, tl = fmaf(ar, 1.44269502f, -th) + ar * 1.92596299e-8f;
    const float rh = ai * 0.159154937f, rl = fmaf(ai, 0.159154937f, -rh) + ai * 6.42063833e-9f;
    char* tb = smem + wave * (16 * RS);
    char* wp = tb + lane * 8;
    const char* rp = tb + (lane & 15) * RS + (lane >> 4) * 16;
    {
      const cf z8 = zpow(th, tl, rh, rl, 8.f); cf e = z8;
      *(u32x2*)(wp) = (u32x2){0x00003F80u, 0u};
      *(u32x2*)(wp + RS) = split2(e.r, -e.i);
#pragma unroll
      for (int c = 2; c < 16; c++) { e = cmul(e, z8); *(u32x2*)(wp + c * RS) = split2(e.r, -e.i); }
    }
    __builtin_amdgcn_wave_barrier();
    s16x8 b1[8], b2[8];
#pragma unroll
    for (int k = 0; k < 8; k++) {
      u32x4 v = *(const u32x4*)(rp + k * 64);
      b1[k] = __builtin_bit_cast(s16x8, (u32x4){v[0], v[0], v[2], v[2]});
      b2[k] = __builtin_bit_cast(s16x8, (u32x4){v[1], v[1], v[3], v[3]});
    }
    __builtin_amdgcn_wave_barrier();
    cf S[8];
    S[1] = zpow(th, tl, rh, rl, 1.f);
    S[2] = cmul(S[1], S[1]); S[3] = cmul(S[2], S[1]); S[4] = cmul(S[2], S[2]);
    S[5] = cmul(S[4], S[1]); S[6] = cmul(S[4], S[2]); S[7] = cmul(S[4], S[3]);
    const cf z128 = zpow(th, tl, rh, rl, 128.f);
    cf G = C;
    auto put = [&](int row, cf w) { *(u32x2*)(wp + row * RS) = split2(w.r, w.i); };
    auto mm = [&](int t) {
      __builtin_amdgcn_wave_barrier();
      f32x4 a = {0.f, 0.f, 0.f, 0.f}, a2 = {0.f, 0.f, 0.f, 0.f};
#pragma unroll
      for (int k = 0; k < 8; k++) {
        s16x8 af = *(const s16x8*)(rp + k * 64);
        a = __builtin_amdgcn_mfma_f32_16x16x32_bf16(af, b1[k], a, 0, 0, 0);
        a2 = __builtin_amdgcn_mfma_f32_16x16x32_bf16(af, b2[k], a2, 0, 0, 0);
      }
      a += a2;
#pragma unroll
      for (int e = 0; e < 4; e++) red[wave * 768 + (16 * t + 4 * (lane >> 4) + e) * 16 + (lane & 15)] = a[e];
      __builtin_amdgcn_wave_barrier();
    };
    put(0, G);
#pragma unroll
    for (int s = 1; s < 8; s++) put(s, cmul(G, S[s]));
    G = cmul(G, z128);
    put(8, G);
#pragma unroll
    for (int s = 1; s < 8; s++) put(8 + s, cmul(G, S[s]));
    mm(0);
#pragma unroll
    for (int h = 0; h < 2; h++) {
      G = cmul(G, z128);
      put(4 * h, G);
#pragma unroll
      for (int s = 1; s < 4; s++) put(4 * h + s, cmul(G, S[2 * s]));
    }
#pragma unroll
    for (int h = 0; h < 4; h++) {
      G = cmul(G, z128);
      put(8 + 2 * h, G);
      put(9 + 2 * h, cmul(G, S[4]));
    }
    mm(1);
#pragma unroll
    for (int h = 0; h < 9; h++) { G = cmul(G, z128); put(h, G); }
    mm(2);
  }
  __syncthreads();
  {
    const int dl = tid >> 8;
    const int j0 = (tid & 255) * 8;
    const float* rd = red + dl * (3 * 768);
    u32x4 hi, lo;
    float kv[8];
#pragma unroll
    for (int e = 0; e < 8; e++) {
      int j = j0 + 7 - e;
      int p = (int)rb((float)j);
      int h = p >> 7, c = (p >> 3) & 15, s = p & 7;
      int row = h < 2 ? 8 * h + s : (h < 4 ? 16 + 4 * (h - 2) + (s >> 1) : (h < 8 ? 24 + 2 * (h - 4) + (s >> 2) : 24 + h));
      int idx = row * 16 + c;
      kv[e] = 2.f * ((rd[idx] + rd[768 + idx]) + rd[1536 + idx]);
    }
#pragma unroll
    for (int e = 0; e < 4; e++) {
      float a = kv[2 * e], b = kv[2 * e + 1];
      hi[e] = pack2(a, b);
      lo[e] = pack2(ssub(a, lo_f(hi[e])), ssub(b, hi_f(hi[e])));
    }
    u16* dst = kimg + (size_t)(blk * 2 + dl) * 4096 + j0;
    *(u32x4*)dst = hi;
    *(u32x4*)(dst + 2048) = lo;
  }
}

__global__ __launch_bounds__(512, 4) void k1(const u16* __restrict__ x, const u16* __restrict__ logA, const u16* __restrict__ Aim,
                                          const u16* __restrict__ Cr, const u16* __restrict__ Ci, const u16* __restrict__ logdt,
                                          const u16* __restrict__ wqkv, const u16* __restrict__ gw, char* __restrict__ ws) {
  __shared__ __attribute__((aligned(16))) char smem[K1_LDS];
  int b = blockIdx.x;
  STAMP(0, 0);
  if (b < NB_GEMM) {
    const int xcd = b & 7, idx = b >> 3;
    const int ti = (xcd & 1) * 30 + idx;
    const int rowblk = (xcd >> 1) * 4 + (ti & 3), cb = ti >> 2;
    const int rot = (cb * 3 + (ti & 3) * 7) % 10;
    if (cb < 5) gemm_role<true>(smem, x, wqkv, ws, rowblk, cb, rot);
    else        gemm_role<false>(smem, x, wqkv, ws, rowblk, cb, rot);
    STAMP(0, 1);
    return;
  }
  b -= NB_GEMM;
  if (b < NB_TR) { tr_role(smem, x, gw, (u16*)(ws + WS_UT), (float*)(ws + WS_GATE), b); STAMP(0, 1); return; }
  b -= NB_TR;
  kgen_role(smem, logA, Aim, Cr, Ci, logdt, (u16*)(ws + WS_KIMG), b);
  STAMP(0, 1);
}

constexpr int K2_CS = 4160;
constexpr int K2_KOFF = 8 * K2_CS;
constexpr int K2_LDS = K2_KOFF + 2 * 4608;
constexpr int NB_KV = 32, NB_WO = 50;
__global__ __launch_bounds__(256, 3) void k2(const u16* __restrict__ Dskip, const u16* __restrict__ wo, char* __restrict__ ws) {
  __shared__ __attribute__((aligned(16))) char smem[K2_LDS];
  const int tid = threadIdx.x, lane = tid & 63;
  const int wave = __builtin_amdgcn_readfirstlane(tid >> 6);
  STAMP(1, 0);
  if (blockIdx.x < NB_KV) {
    const int hd = blockIdx.x;
    const int c = lane & 31, h = lane >> 5;
    const int cc = c < 20 ? c : 19;
    const char* pk = ws + WS_PART + ((size_t)(wave * 16 * 4 + h) * 1280 + 20 * hd + cc) * 16;
    const char* pv = pk + 640 * 16;
    f32x16 acc;
#pragma unroll
    for (int i = 0; i < 16; i++) acc[i] = 0.f;
#pragma unroll
    for (int bt = 0; bt < 2; bt++) {
      u32x4 a[16], v[16];
#pragma unroll
      for (int i = 0; i < 16; i++) {
        a[i] = *(const u32x4*)(pk + (size_t)(bt * 16 + i) * (2 * 1280 * 16));
        v[i] = *(const u32x4*)(pv + (size_t)(bt * 16 + i) * (2 * 1280 * 16));
      }
      __builtin_amdgcn_sched_barrier(0);
#pragma unroll
      for (int i = 0; i < 16; i++) {
        u32x4 vv = v[i];
        if (c == 20) vv = (u32x4){0x3F803F80u, 0x3F803F80u, 0x3F803F80u, 0x3F803F80u};
        acc = __builtin_amdgcn_mfma_f32_32x32x16_bf16(__builtin_bit_cast(s16x8, a[i]), __builtin_bit_cast(s16x8, vv), acc, 0, 0, 0);
      }
    }
    float* red = (float*)smem;
#pragma unroll
    for (int q = 0; q < 3; q++)
#pragma unroll
      for (int e = 0; e < 4; e++) red[(wave * 32 + 8 * q + 4 * h + e) * 33 + c] = acc[4 * q + e];
    __syncthreads();
    float* fin = red + 4 * 32 * 33;
    for (int i = tid; i < 420; i += 256) {
      int dk = i / 21, dv = i - 21 * dk;
      float s = (red[dk * 33 + dv] + red[(32 + dk) * 33 + dv]) + (red[(64 + dk) * 33 + dv] + red[(96 + dk) * 33 + dv]);
      fin[i] = rb(s);
    }
    __syncthreads();
    if (tid < 96) {
      const int m = lane & 31;
      int col = m < 16 ? m : ((m >> 2) == ((hd & 2) ? 5 : 4) ? 16 + (m & 3) : 20);
      if (tid < 64) {
        u32x4 o;
#pragma unroll
        for (int e = 0; e < 4; e++) o[e] = pack2(fin[(8 * h + 2 * e) * 21 + col], fin[(8 * h + 2 * e + 1) * 21 + col]);
        *(u32x4*)(ws + WS_KVT + (size_t)hd * 1280 + lane * 16) = o;
      } else {
        u32x2 o;
#pragma unroll
        for (int e = 0; e < 2; e++) o[e] = pack2(fin[(16 + 2 * e) * 21 + col], fin[(17 + 2 * e) * 21 + col]);
        *(u32x2*)(ws + WS_KVT + (size_t)hd * 1280 + 1024 + m * 8) = o;
      }
    }
    STAMP(1, 1);
    return;
  }
  if (blockIdx.x < NB_KV + NB_WO) {
    const int blk = blockIdx.x - NB_KV;
#pragma unroll
    for (int i = 0; i < 4; i++) {
      const int id = blk * 1024 + i * 256 + tid;
      const int slot = id % 160, f = id / 160;
      const int ks = f % 10, kq = (f / 10) & 3, cb = f / 40;
      int m, h;
      if (slot < 128) { m = (slot >> 6) * 32 + (slot & 31); h = (slot >> 5) & 1; } else { m = 64 + (slot & 15); h = (slot >> 4) & 1; }
      const int n = 80 * cb + m;
      int k0, k1;
      if (ks < 8) { k0 = 20 * (8 * kq + ks) + 4 * h; k1 = k0 + 8; }
      else { k0 = 20 * (8 * kq + 4 * (ks - 8) + 2 * h) + 16; k1 = k0 + 20; }
      u32x4 v;
#pragma unroll
      for (int e = 0; e < 2; e++) {
        unsigned a0 = wo[(size_t)(k0 + 2 * e) * D + n], a1 = wo[(size_t)(k0 + 2 * e + 1) * D + n];
        unsigned b0 = wo[(size_t)(k1 + 2 * e) * D + n], b1 = wo[(size_t)(k1 + 2 * e + 1) * D + n];
        v[e] = a0 | (a1 << 16); v[2 + e] = b0 | (b1 << 16);
      }
      *(u32x4*)(ws + WS_WOF + (size_t)id * 16) = v;
    }
    STAMP(1, 1);
    return;
  }
  const int d = blockIdx.x - (NB_KV + NB_WO);
  const u16* uT = (const u16*)(ws + WS_UT) + (size_t)d * UT_STRIDE;
  {
    const u32x4* ksrc = (const u32x4*)(ws + WS_KIMG + (size_t)d * 8192);
    const u32x4* usrc = (const u32x4*)uT;
    u32x4 kh = ksrc[tid], kl = ksrc[tid + 256];
    u32x4 c0 = usrc[tid + 1], c1 = usrc[tid + 2];
    u32x4 e0, e1;
    if (tid < 2) { e0 = usrc[tid + 257]; e1 = usrc[tid + 258]; }
    if (tid < 64) *(u32x4*)(smem + K2_KOFF + (tid >> 5) * 4608 + (tid & 31) * 16) = (u32x4){0u, 0u, 0u, 0u};
    *(u32x4*)(smem + K2_KOFF + 512 + tid * 16) = kh;
    *(u32x4*)(smem + K2_KOFF + 4608 + 512 + tid * 16) = kl;
    auto put_copies = [&](u32x4 a, u32x4 b, int xch) {
      unsigned w[8] = {a[0], a[1], a[2], a[3], b[0], b[1], b[2], b[3]};
      unsigned o[7];
#pragma unroll
      for (int k = 0; k < 7; k++) o[k] = __builtin_amdgcn_alignbyte(w[k + 1], w[k], 2);
      char* p = smem + xch * 16;
#pragma unroll
      for (int q = 0; q < 4; q++) {
        *(u32x4*)(p + (2 * q) * K2_CS) = (u32x4){o[q], o[q + 1], o[q + 2], o[q + 3]};
        *(u32x4*)(p + (2 * q + 1) * K2_CS) = (u32x4){w[q + 1], w[q + 2], w[q + 3], w[q + 4]};
      }
    };
    put_copies(c0, c1, tid);
    if (tid < 2) put_copies(e0, e1, tid + 256);
  }
  __syncthreads();
  STAMP(1, 2);
  f32x4 acc[8];
#pragma unroll
  for (int t = 0; t < 8; t++) acc[t] = (f32x4){0.f, 0.f, 0.f, 0.f};
  {
    const int i = lane & 15, kg = lane >> 4, e = kg >> 1, bb = kg & 1;
    const char* ap = smem + (i & 7) * K2_CS + (2 * e - bb + 2 + (i >> 3)) * 16;
    const char* bp = smem + K2_KOFF + (16 + i - e) * 32 + bb * 16;
#pragma unroll
    for (int it = 0; it < 2; it++) {
      const int d0 = 2 * (wave + 4 * it);
      s16x8 af[8], bh[8], bl[8];
#pragma unroll
      for (int j = 0; j < 8; j++) af[j] = *(const s16x8*)(ap + (32 * j + 2 * d0) * 16);
#pragma unroll
      for (int m = 0; m < 8; m++) {
        bh[m] = *(const s16x8*)(bp + (16 * m - d0) * 32);
        bl[m] = *(const s16x8*)(bp + (16 * m - d0) * 32 + 4608);
      }
#pragma unroll
      for (int m = 0; m < 8; m++) {
#pragma unroll
        for (int j = 0; j + m < 8; j++) acc[j + m] = __builtin_amdgcn_mfma_f32_16x16x32_bf16(af[j], bh[m], acc[j + m], 0, 0, 0);
#pragma unroll
        for (int j = 0; j + m < 8; j++) acc[j + m] = __builtin_amdgcn_mfma_f32_16x16x32_bf16(af[j], bl[m], acc[j + m], 0, 0, 0);
      }
    }
  }
  STAMP(1, 3);
  u32x2 uu[2];
#pragma unroll
  for (int t = 0; t < 2; t++) uu[t] = *(const u32x2*)(uT + UT_PAD + 256 * (2 * wave + t) + 4 * lane);
  const float dsk = b2f(Dskip[d]);
  __syncthreads();
  float* red = (float*)smem;
#pragma unroll
  for (int t = 0; t < 8; t++) *(f32x4*)(red + ((wave * 8 + t) * 64 + (lane & 15) * 4 + (lane >> 4)) * 4) = acc[t];
  __syncthreads();
  u16* aT = (u16*)(ws + WS_AT) + (size_t)d * L;
#pragma unroll
  for (int t = 0; t < 2; t++) {
    const int tt = 2 * wave + t;
    f32x4 s = *(const f32x4*)(red + ((0 * 8 + tt) * 64 + lane) * 4);
    f32x4 s1 = *(const f32x4*)(red + ((1 * 8 + tt) * 64 + lane) * 4);
    f32x4 s2 = *(const f32x4*)(red + ((2 * 8 + tt) * 64 + lane) * 4);
    f32x4 s3 = *(const f32x4*)(red + ((3 * 8 + tt) * 64 + lane) * 4);
    s = (s + s1) + (s2 + s3);
    u32x2 ov;
#pragma unroll
    for (int e = 0; e < 2; e++) {
      unsigned y = pack2(s[2 * e], s[2 * e + 1]);
      unsigned k = pack2(lo_f(uu[t][e]) * dsk, hi_f(uu[t][e]) * dsk);
      ov[e] = pack2(lo_f(y) + lo_f(k), hi_f(y) + hi_f(k));
    }
    *(u32x2*)(aT + 256 * tt + 4 * lane) = ov;
  }
  STAMP(1, 1);
}

constexpr int K3_WO = 0;
constexpr int K3_KV = 102400;
constexpr int K3_AT = K3_KV + 40960;
constexpr int K3_LDS = K3_AT + 80 * 144;
__global__ __launch_bounds__(512, 2) void k3(char* __restrict__ ws, u16* __restrict__ out) {
  __shared__ __attribute__((aligned(16))) char smem[K3_LDS];
  const int tid = threadIdx.x, lane = tid & 63;
  const int wave = __builtin_amdgcn_readfirstlane(tid >> 6);
  STAMP(2, 0);
  const int xcd = blockIdx.x & 7, idx = blockIdx.x >> 3;
  const int rowblk = xcd * 4 + (idx >> 3), cb = idx & 7;
  const int l0 = rowblk * 64, c0 = cb * 80;
  const int kq = wave & 3, t = wave >> 2;
  const int r = lane & 31, h = lane >> 5;
#pragma unroll
  for (int i = 0; i < 5; i++) glds16(ws + WS_KVT + (size_t)(wave * 5 + i) * 1024 + lane * 16, smem + K3_KV + (wave * 5 + i) * 1024);
  u32x4 pf0[8]; u32x2 pf1[8];
  {
    const char* p0 = ws + WS_PQ + ((size_t)((rowblk * 32 + kq * 8) * 2 + t) * 64 + lane) * 16;
    const char* p1 = ws + WS_PQ + (size_t)L * 16 * 32 * 2 + ((size_t)((rowblk * 32 + kq * 8) * 2 + t) * 32 + r) * 8;
#pragma unroll
    for (int hh = 0; hh < 8; hh++) {
      asm volatile("global_load_dwordx4 %0, %1, off offset:%2" : "=v"(pf0[hh]) : "v"(p0 + (hh >> 1) * 4096), "i"((hh & 1) * 2048));
      asm volatile("global_load_dwordx2 %0, %1, off offset:%2" : "=v"(pf1[hh]) : "v"(p1), "i"(hh * 512));
    }
  }
  {
    const char* src = ws + WS_WOF + (size_t)cb * 102400 + lane * 16;
#pragma unroll
    for (int i = 0; i < 13; i++) { int p = i * 8 + wave; if (p < 100) glds16(src + p * 1024, smem + K3_WO + p * 1024); }
  }
  u32x4 av[2];
  {
    const char* aT = ws + WS_AT;
#pragma unroll
    for (int i = 0; i < 2; i++) {
      int id = tid + 512 * i; if (id > 639) id = 639;
      const char* p = aT + ((size_t)(c0 + (id >> 3)) * L + l0 + 8 * (id & 7)) * 2;
      asm volatile("global_load_dwordx4 %0, %1, off" : "=v"(av[i]) : "v"(p));
    }
  }
  if (wave < 4) VMCNT(15); else VMCNT(14);
  asm volatile("" : "+v"(pf0[0]), "+v"(pf0[1]), "+v"(pf0[2]), "+v"(pf0[3]), "+v"(pf0[4]), "+v"(pf0[5]), "+v"(pf0[6]), "+v"(pf0[7]));
  asm volatile("" : "+v"(pf1[0]), "+v"(pf1[1]), "+v"(pf1[2]), "+v"(pf1[3]), "+v"(pf1[4]), "+v"(pf1[5]), "+v"(pf1[6]), "+v"(pf1[7]));
  RAW_BARRIER();
  STAMP(2, 2);
  u32x4 ob[10];
  {
    unsigned rem[8][2];
#pragma unroll
    for (int hh = 0; hh < 8; hh++) {
      const char* kp = smem + K3_KV + (kq * 8 + hh) * 1280;
      s16x8 a0 = *(const s16x8*)(kp + lane * 16);
      u32x2 a1h = *(const u32x2*)(kp + 1024 + r * 8);
      u32x4 a1u = {a1h[0], a1h[1], 0u, 0u};
      u32x4 b1u = {h ? 0u : pf1[hh][0], h ? 0u : pf1[hh][1], 0u, 0u};
      f32x16 c;
#pragma unroll
      for (int i = 0; i < 16; i++) c[i] = 0.f;
      c = __builtin_amdgcn_mfma_f32_32x32x16_bf16(a0, __builtin_bit_cast(s16x8, pf0[hh]), c, 0, 0, 0);
      c = __builtin_amdgcn_mfma_f32_32x32x16_bf16(__builtin_bit_cast(s16x8, a1u), __builtin_bit_cast(s16x8, b1u), c, 0, 0, 0);
      float den = rb(rb(c[12]) + 1e-6f);
      float inv = __builtin_amdgcn_rcpf(den);
      unsigned o[6];
#pragma unroll
      for (int q = 0; q < 6; q++) {
        unsigned n0 = pack2(c[2 * q], c[2 * q + 1]);
        o[q] = pack2(lo_f(n0) * inv, hi_f(n0) * inv);
      }
      ob[hh] = (u32x4){o[0], o[1], o[2], o[3]};
      rem[hh][0] = o[4]; rem[hh][1] = o[5];
    }
#pragma unroll
    for (int G = 0; G < 2; G++)
      ob[8 + G] = (u32x4){h ? rem[4 * G + 2][0] : rem[4 * G][0], h ? rem[4 * G + 2][1] : rem[4 * G][1],
                          h ? rem[4 * G + 3][0] : rem[4 * G + 1][0], h ? rem[4 * G + 3][1] : rem[4 * G + 1][1]};
  }
  STAMP(2, 3);
  VMCNT(0);
  asm volatile("" : "+v"(av[0]), "+v"(av[1]));
#pragma unroll
  for (int i = 0; i < 2; i++) { int id = tid + 512 * i; if (id < 640) *(u32x4*)(smem + K3_AT + (id >> 3) * 144 + (id & 7) * 16) = av[i]; }
  RAW_BARRIER();
  STAMP(2, 4);
  f32x16 acc[3];
#pragma unroll
  for (int c = 0; c < 3; c++)
#pragma unroll
    for (int i = 0; i < 16; i++) acc[c][i] = 0.f;
  {
    const char* wp = smem + K3_WO + kq * 25600;
    const int o2 = 2048 + (h * 16 + (lane & 15)) * 16;
#pragma unroll
    for (int ks = 0; ks < 10; ks++) {
      s16x8 w0 = *(const s16x8*)(wp + ks * 2560 + lane * 16);
      s16x8 w1 = *(const s16x8*)(wp + ks * 2560 + 1024 + lane * 16);
      s16x8 w2 = *(const s16x8*)(wp + ks * 2560 + o2);
      s16x8 b = __builtin_bit_cast(s16x8, ob[ks]);
      acc[0] = __builtin_amdgcn_mfma_f32_32x32x16_bf16(w0, b, acc[0], 0, 0, 0);
      acc[1] = __builtin_amdgcn_mfma_f32_32x32x16_bf16(w1, b, acc[1], 0, 0, 0);
      acc[2] = __builtin_amdgcn_mfma_f32_32x32x16_bf16(w2, b, acc[2], 0, 0, 0);
    }
  }
  STAMP(2, 5);
  RAW_BARRIER();
  {
    float* pr = (float*)smem + (size_t)(kq * 64 + 32 * t + r) * 84 + 4 * h;
#pragma unroll
    for (int c = 0; c < 3; c++)
#pragma unroll
      for (int q = 0; q < 4; q++)
        if (c < 2 || q < 2) *(f32x4*)(pr + 32 * c + 8 * q) = (f32x4){acc[c][4 * q], acc[c][4 * q + 1], acc[c][4 * q + 2], acc[c][4 * q + 3]};
  }
  RAW_BARRIER();
  {
    const float* gate = (const float*)(ws + WS_GATE);
#pragma unroll
    for (int i2 = 0; i2 < 2; i2++) {
      const int i = tid + 512 * i2;
      if (i < 640) {
        const int rr = i / 10, oc = i - 10 * rr;
        const float g = gate[l0 + rr]; const float g1 = rb(1.f - g);
        f32x4 s0 = {0.f, 0.f, 0.f, 0.f}, s1 = {0.f, 0.f, 0.f, 0.f};
#pragma unroll
        for (int w = 0; w < 4; w++) {
          const float* pr = (const float*)smem + (size_t)(w * 64 + rr) * 84 + oc * 8;
          s0 += *(const f32x4*)pr; s1 += *(const f32x4*)(pr + 4);
        }
        u32x4 ov;
#pragma unroll
        for (int e = 0; e < 4; e++) {
          unsigned bb = e < 2 ? pack2(s0[2 * e], s0[2 * e + 1]) : pack2(s1[2 * e - 4], s1[2 * e - 3]);
          float a0 = b2f(*(const u16*)(smem + K3_AT + (oc * 8 + 2 * e) * 144 + rr * 2));
          float a1 = b2f(*(const u16*)(smem + K3_AT + (oc * 8 + 2 * e + 1) * 144 + rr * 2));
          unsigned ga = pack2(g * a0, g * a1), gb = pack2(g1 * lo_f(bb), g1 * hi_f(bb));
          ov[e] = pack2(lo_f(ga) + lo_f(gb), hi_f(ga) + hi_f(gb));
        }
        *(u32x4*)(out + (size_t)(l0 + rr) * D + c0 + oc * 8) = ov;
      }
    }
  }
  STAMP(2, 1);
}

extern "C" void kernel_launch(void* const* d_in, const int* in_sizes, int n_in, void* d_out, int out_size, void* d_ws, size_t ws_size,
                              hipStream_t stream) {
  const u16* u = (const u16*)d_in[0]; const u16* logA = (const u16*)d_in[1]; const u16* Aim = (const u16*)d_in[2];
  const u16* Cr = (const u16*)d_in[3]; const u16* Ci = (const u16*)d_in[4]; const u16* logdt = (const u16*)d_in[5];
  const u16* Dsk = (const u16*)d_in[6]; const u16* wqkv = (const u16*)d_in[8]; const u16* wo = (const u16*)d_in[9];
  const u16* gw = (const u16*)d_in[10];
  char* ws = (char*)d_ws;
  k1<<<NB_GEMM + NB_TR + NB_KG, 512, 0, stream>>>(u, logA, Aim, Cr, Ci, logdt, wqkv, gw, ws);
  k2<<<NB_KV + NB_WO + D, 256, 0, stream>>>(Dsk, wo, ws);
  k3<<<256, 512, 0, stream>>>(ws, (u16*)d_out);
}
```

```cpp
#include <hip/hip_runtime.h>
#include <hip/hip_bf16.h>
#include <stdint.h>

typedef unsigned short u16;
typedef __attribute__((ext_vector_type(8))) short s16x8;
typedef __attribute__((ext_vector_type(4))) short s16x4;
typedef __attribute__((ext_vector_type(4))) float f32x4;
typedef __attribute__((ext_vector_type(16))) float f32x16;
typedef __attribute__((ext_vector_type(4))) unsigned u32x4;
typedef __attribute__((ext_vector_type(2))) unsigned u32x2;
typedef __attribute__((address_space(3))) s16x4* lds_s16x4_p;
typedef const __attribute__((address_space(1))) void* gptr_t;
typedef __attribute__((address_space(3))) void* lptr_t;

constexpr int L = 2048, D = 640, NS = 192, NH = 32;
constexpr int UT_PAD = 32, UT_STRIDE = L + UT_PAD;
constexpr size_t WS_UT   = 0;
constexpr size_t WS_KIMG = WS_UT + (size_t)D * UT_STRIDE * 2;
constexpr size_t WS_PQ   = WS_KIMG + (size_t)D * 9216;
constexpr size_t WS_PART = WS_PQ + (size_t)L * D * 2;
constexpr size_t WS_GATE = WS_PART + (size_t)L * 1280 * 2;
constexpr size_t WS_WOF  = WS_GATE + (size_t)L * 4;
constexpr size_t WS_KVT  = WS_WOF + (size_t)D * D * 2;
constexpr size_t WS_AT   = WS_KVT + (size_t)32 * 1280;
constexpr size_t WS_END  = WS_AT + (size_t)D * L * 2;
#define STAMP(kid, slot) do {} while (0)

__device__ __forceinline__ float b2f(u16 v) { return __uint_as_float(((unsigned)v) << 16); }
typedef __bf16 bf16x2_t __attribute__((ext_vector_type(2)));
typedef float f32x2 __attribute__((ext_vector_type(2)));
__device__ __forceinline__ unsigned pack2(float a, float b) { f32x2 v = {a, b}; bf16x2_t r = __builtin_convertvector(v, bf16x2_t); return __builtin_bit_cast(unsigned, r); }
__device__ __forceinline__ float lo_f(unsigned u) { return __uint_as_float(u << 16); }
__device__ __forceinline__ float hi_f(unsigned u) { return __uint_as_float(u & 0xffff0000u); }
__device__ __forceinline__ float rb(float f) { return lo_f(pack2(f, f)); }
__device__ __forceinline__ unsigned phi2(float a, float b) {
  unsigned t = pack2(a, b); float t0 = lo_f(t), t1 = hi_f(t);
  float r0 = t0 > 0.f ? t0 + 1.f : __expf(t0), r1 = t1 > 0.f ? t1 + 1.f : __expf(t1);
  unsigned r = pack2(r0, r1);
  return pack2(lo_f(r) + 1e-6f, hi_f(r) + 1e-6f);
}
#define VMCNT(n) asm volatile("s_waitcnt vmcnt(" #n ")" ::: "memory")
#define RAW_BARRIER() do { asm volatile("s_waitcnt lgkmcnt(0)" ::: "memory"); __builtin_amdgcn_s_barrier(); asm volatile("" ::: "memory"); } while (0)
__device__ __forceinline__ void glds16(const void* g, void* l) { __builtin_amdgcn_global_load_lds((gptr_t)g, (lptr_t)l, 16, 0, 0); }

struct cf { float r, i; };
__device__ __forceinline__ float smul(float a, float b) { float o; asm("v_mul_f32 %0, %1, %2" : "=v"(o) : "v"(a), "v"(b)); return o; }
__device__ __forceinline__ float sfma(float a, float b, float c) { float o; asm("v_fma_f32 %0, %1, %2, %3" : "=v"(o) : "v"(a), "v"(b), "v"(c)); return o; }
__device__ __forceinline__ float sfnma(float a, float b, float c) { float o; asm("v_fma_f32 %0, -%1, %2, %3" : "=v"(o) : "v"(a), "v"(b), "v"(c)); return o; }
__device__ __forceinline__ float ssub(float a, float b) { float o; asm("v_sub_f32 %0, %1, %2" : "=v"(o) : "v"(a), "v"(b)); return o; }
__device__ __forceinline__ cf cmul(cf a, cf b) { cf o; o.r = sfnma(a.i, b.i, smul(a.r, b.r)); o.i = sfma(a.i, b.r, smul(a.r, b.i)); return o; }

constexpr int K1_LDS = 81920;
constexpr int NB_GEMM = 240, NB_TR = 64, NB_KG = 240;

template <bool ISQ>
__device__ __forceinline__ void gemm_role(char* smem, const u16* __restrict__ x, const u16* __restrict__ W, char* __restrict__ ws,
                                          int rowblk, int cb, int rot) {
  const int tid = threadIdx.x, lane = tid & 63;
  const int wave = __builtin_amdgcn_readfirstlane(tid >> 6);
  const int wr = wave & 3, wc = wave >> 2;
  const int row0 = rowblk * 128, col0 = cb * 128;
  char* sA = smem; char* sB = smem + 49152;
  const char* gA = (const char*)x + (size_t)(row0 + 16 * wave + (lane >> 3)) * 1280 + (((lane & 7) ^ (lane >> 3)) << 4);
  const int fB = (lane >> 4) | (((wave >> 1) & 1) << 2);
  const char* gB = (const char*)W + (size_t)(4 * wave + (lane >> 4)) * 3840 + col0 * 2 + (((((lane & 15) >> 1) ^ fB) * 2 + (lane & 1)) << 4);
  auto issueA = [&](int st, int h) {
    int k = st + rot; if (k >= 10) k -= 10;
    glds16(gA + h * (8 * 1280) + k * 128, sA + (st % 3) * 16384 + (2 * wave + h) * 1024);
  };
  auto issueB = [&](int hs) {
    int k = hs + 2 * rot; if (k >= 20) k -= 20;
    glds16(gB + (size_t)k * (32 * 3840), sB + (hs & 3) * 8192 + wave * 1024);
  };
  issueA(0, 0);
  issueB(0); issueA(0, 1);
  issueB(1); issueA(1, 0);
  issueB(2); issueA(1, 1);
  issueB(3); issueA(2, 0);
  f32x4 acc[2][4];
#pragma unroll
  for (int m = 0; m < 2; m++)
#pragma unroll
    for (int c = 0; c < 4; c++) acc[m][c] = (f32x4){0.f, 0.f, 0.f, 0.f};
  const int g = lane >> 4, fr = lane & 15;
  const int q4 = fr >> 2, p4 = lane & 3;
  int aoff[2];
#pragma unroll
  for (int ks = 0; ks < 2; ks++) aoff[ks] = (wr * 32 + fr) * 128 + (((4 * ks + g) ^ (fr & 7)) << 4);
  int boff[4];
  {
    const int f = q4 | ((g & 1) << 2);
#pragma unroll
    for (int c = 0; c < 4; c++) boff[c] = (8 * g + q4) * 256 + (((4 * wc + c) ^ f) << 5) + p4 * 8;
  }
  s16x8 af[2][2], bf[2][4];
  auto rdfrag = [&](int s) {
    const char* pa = sA + ((s >> 1) % 3) * 16384 + aoff[s & 1];
    const char* pb = sB + (s & 3) * 8192;
#pragma unroll
    for (int m = 0; m < 2; m++) af[s & 1][m] = *(const s16x8*)(pa + m * 2048);
#pragma unroll
    for (int c = 0; c < 4; c++) {
      s16x4 b0 = __builtin_amdgcn_ds_read_tr16_b64_v4i16((lds_s16x4_p)(pb + boff[c]));
      s16x4 b1 = __builtin_amdgcn_ds_read_tr16_b64_v4i16((lds_s16x4_p)(pb + boff[c] + 1024));
      bf[s & 1][c] = __builtin_shufflevector(b0, b1, 0, 1, 2, 3, 4, 5, 6, 7);
    }
  };
  VMCNT(6);
  RAW_BARRIER();
  STAMP(0, 2);
  rdfrag(0);
#pragma unroll
  for (int s = 0; s < 20; s++) {
    if (s < 19) {
      if (s <= 15) VMCNT(4); else if (s == 16) VMCNT(3); else if (s == 17) VMCNT(1); else VMCNT(0);
      RAW_BARRIER();
      rdfrag(s + 1);
      if (s <= 15) issueB(s + 4);
      if (s <= 14) { if (s & 1) issueA((s - 1) / 2 + 3, 0); else issueA(s / 2 + 2, 1); }
    }
#pragma unroll
    for (int c = 0; c < 4; c++)
#pragma unroll
      for (int m = 0; m < 2; m++) {
        if (ISQ) acc[m][c] = __builtin_amdgcn_mfma_f32_16x16x32_bf16(bf[s & 1][c], af[s & 1][m], acc[m][c], 0, 0, 0);
        else     acc[m][c] = __builtin_amdgcn_mfma_f32_16x16x32_bf16(af[s & 1][m], bf[s & 1][c], acc[m][c], 0, 0, 0);
      }
  }
  STAMP(0, 3);
  if (ISQ) {
    char* pq0 = ws + WS_PQ; char* pq1 = ws + WS_PQ + (size_t)L * 16 * 32 * 2;
#pragma unroll
    for (int m = 0; m < 2; m++) {
      const int row = row0 + wr * 32 + 16 * m + fr;
      const int rb64 = row >> 6, t = (row >> 5) & 1, r = row & 31;
#pragma unroll
      for (int c = 0; c < 4; c++) {
        u32x2 o;
        o[0] = phi2(acc[m][c][0], acc[m][c][1]);
        o[1] = phi2(acc[m][c][2], acc[m][c][3]);
        const int n = col0 + 64 * wc + 16 * c + 4 * g;
        const int hd = n / 20, d0 = n - 20 * hd;
        const int fi = (rb64 * 32 + hd) * 2 + t;
        char* dst = d0 < 16 ? pq0 + ((size_t)fi * 64 + (d0 >> 3) * 32 + r) * 16 + (d0 & 7) * 2 : pq1 + ((size_t)fi * 32 + r) * 8;
        *(u32x2*)dst = o;
      }
    }
  } else {
    const int rg = rowblk * 4 + wr;
    const bool isk = cb < 10;
    char* dst = ws + WS_PART + ((size_t)(rg * 4 + g) * 1280 + (col0 - 640) + 64 * wc + fr) * 16;
#pragma unroll
    for (int c = 0; c < 4; c++) {
      u32x4 a;
      if (isk) {
        a[0] = phi2(acc[0][c][0], acc[0][c][1]); a[1] = phi2(acc[0][c][2], acc[0][c][3]);
        a[2] = phi2(acc[1][c][0], acc[1][c][1]); a[3] = phi2(acc[1][c][2], acc[1][c][3]);
      } else {
        a[0] = pack2(acc[0][c][0], acc[0][c][1]); a[1] = pack2(acc[0][c][2], acc[0][c][3]);
        a[2] = pack2(acc[1][c][0], acc[1][c][1]); a[3] = pack2(acc[1][c][2], acc[1][c][3]);
      }
      *(u32x4*)(dst + c * 256) = a;
    }
  }
}

__device__ __forceinline__ void tr_role(char* smem, const u16* __restrict__ x, const u16* __restrict__ gw,
                                        u16* __restrict__ uT, float* __restrict__ gate, int blk) {
  const int tid = threadIdx.x, wave = tid >> 6, lane = tid & 63;
  const int l0 = blk * 32;
#pragma unroll
  for (int i = 0; i < 5; i++) { int id = tid + 512 * i; int r = id / 80, ch = id % 80;
    *(u32x4*)(smem + r * 1280 + (r >> 3) * 16 + ch * 16) = *(const u32x4*)(x + (size_t)(l0 + r) * D + ch * 8); }
  unsigned gwr[5];
#pragma unroll
  for (int i = 0; i < 5; i++) gwr[i] = *(const unsigned*)(gw + 2 * lane + 128 * i);
  __syncthreads();
#pragma unroll
  for (int rr = 0; rr < 4; rr++) {
    int r = wave * 4 + rr; float s = 0.f;
#pragma unroll
    for (int i = 0; i < 5; i++) {
      unsigned v = *(const unsigned*)(smem + r * 1280 + (r >> 3) * 16 + (2 * lane + 128 * i) * 2);
      unsigned p = pack2(lo_f(v) * lo_f(gwr[i]), hi_f(v) * hi_f(gwr[i]));
      s += lo_f(p) + hi_f(p);
    }
#pragma unroll
    for (int o = 32; o > 0; o >>= 1) s += __shfl_xor(s, o);
    if (lane == 0) { float a = rb(s); gate[l0 + r] = rb(1.f / (1.f + __expf(-a))); }
  }
#pragma unroll
  for (int i = 0; i < 5; i++) {
    int id = tid + 512 * i; int d = id >> 2, oc = id & 3;
    u32x4 v;
#pragma unroll
    for (int e = 0; e < 4; e++) {
      unsigned lo = *(const u16*)(smem + (8 * oc + 2 * e) * 1280 + oc * 16 + d * 2);
      unsigned hi = *(const u16*)(smem + (8 * oc + 2 * e + 1) * 1280 + oc * 16 + d * 2);
      v[e] = lo | (hi << 16);
    }
    *(u32x4*)(uT + (size_t)d * UT_STRIDE + UT_PAD + l0 + 8 * oc) = v;
    if (blk == 0) *(u32x4*)(uT + (size_t)d * UT_STRIDE + 8 * oc) = (u32x4){0u, 0u, 0u, 0u};
  }
}

__device__ __forceinline__ cf zpow(float th, float tl, float rh, float rl, float p) {
  float e = __builtin_amdgcn_exp2f(th * p); e = fmaf(e, (tl * p) * 0.69314718f, e);
  float t = rh * p; float f = (t - __builtin_rintf(t)) + rl * p;
  cf o; o.r = e * __builtin_amdgcn_cosf(f); o.i = e * __builtin_amdgcn_sinf(f); return o;
}
__device__ __forceinline__ void cmul4(cf& o0, cf& o1, cf& o2, cf& o3, cf a0, cf b0, cf a1, cf b1, cf a2, cf b2, cf a3, cf b3) {
  asm("v_mul_f32 %0, %8, %10\n\tv_mul_f32 %1, %8, %11\n\tv_mul_f32 %2, %12, %14\n\tv_mul_f32 %3, %12, %15\n\t"
      "v_mul_f32 %4, %16, %18\n\tv_mul_f32 %5, %16, %19\n\tv_mul_f32 %6, %20, %22\n\tv_mul_f32 %7, %20, %23\n\t"
      "v_fma_f32 %0, -%9, %11, %0\n\tv_fma_f32 %1, %9, %10, %1\n\tv_fma_f32 %2, -%13, %15, %2\n\tv_fma_f32 %3, %13, %14, %3\n\t"
      "v_fma_f32 %4, -%17, %19, %4\n\tv_fma_f32 %5, %17, %18, %5\n\tv_fma_f32 %6, -%21, %23, %6\n\tv_fma_f32 %7, %21, %22, %7"
      : "=&v"(o0.r), "=&v"(o0.i), "=&v"(o1.r), "=&v"(o1.i), "=&v"(o2.r), "=&v"(o2.i), "=&v"(o3.r), "=&v"(o3.i)
      : "v"(a0.r), "v"(a0.i), "v"(b0.r), "v"(b0.i), "v"(a1.r), "v"(a1.i), "v"(b1.r), "v"(b1.i),
        "v"(a2.r), "v"(a2.i), "v"(b2.r), "v"(b2.i), "v"(a3.r), "v"(a3.i), "v"(b3.r), "v"(b3.i));
}
__device__ __forceinline__ void batch4(unsigned* h, unsigned* l, cf b, cf t0, cf t1, cf t2, cf t3) {
  float w0r, w0i, w1r, w1i, w2r, w2i, w3r, w3i; unsigned x0, x1, x2, x3;
  asm("v_mul_f32 %8, %20, %22\n\tv_mul_f32 %9, %20, %23\n\tv_mul_f32 %10, %20, %24\n\tv_mul_f32 %11, %20, %25\n\t"
      "v_mul_f32 %12, %20, %26\n\tv_mul_f32 %13, %20, %27\n\tv_mul_f32 %14, %20, %28\n\tv_mul_f32 %15, %20, %29\n\t"
      "v_fma_f32 %8, -%21, %23, %8\n\tv_fma_f32 %9, %21, %22, %9\n\tv_fma_f32 %10, -%21, %25, %10\n\tv_fma_f32 %11, %21, %24, %11\n\t"
      "v_fma_f32 %12, -%21, %27, %12\n\tv_fma_f32 %13, %21, %26, %13\n\tv_fma_f32 %14, -%21, %29, %14\n\tv_fma_f32 %15, %21, %28, %15\n\t"
      "v_cvt_pk_bf16_f32 %0, %8, %9\n\tv_cvt_pk_bf16_f32 %1, %10, %11\n\tv_cvt_pk_bf16_f32 %2, %12, %13\n\tv_cvt_pk_bf16_f32 %3, %14, %15\n\t"
      "v_lshlrev_b32 %4, 16, %0\n\tv_and_b32 %16, 0xffff0000, %0\n\tv_lshlrev_b32 %5, 16, %1\n\tv_and_b32 %17, 0xffff0000, %1\n\t"
      "v_lshlrev_b32 %6, 16, %2\n\tv_and_b32 %18, 0xffff0000, %2\n\tv_lshlrev_b32 %7, 16, %3\n\tv_and_b32 %19, 0xffff0000, %3\n\t"
      "v_sub_f32 %8, %8, %4\n\tv_sub_f32 %9, %9, %16\n\tv_sub_f32 %10, %10, %5\n\tv_sub_f32 %11, %11, %17\n\t"
      "v_sub_f32 %12, %12, %6\n\tv_sub_f32 %13, %13, %18\n\tv_sub_f32 %14, %14, %7\n\tv_sub_f32 %15, %15, %19\n\t"
      "v_cvt_pk_bf16_f32 %4, %8, %9\n\tv_cvt_pk_bf16_f32 %5, %10, %11\n\tv_cvt_pk_bf16_f32 %6, %12, %13\n\tv_cvt_pk_bf16_f32 %7, %14, %15"
      : "=&v"(h[0]), "=&v"(h[1]), "=&v"(h[2]), "=&v"(h[3]), "=&v"(l[0]), "=&v"(l[1]), "=&v"(l[2]), "=&v"(l[3]),
        "=&v"(w0r), "=&v"(w0i), "=&v"(w1r), "=&v"(w1i), "=&v"(w2r), "=&v"(w2i), "=&v"(w3r), "=&v"(w3i),
        "=&v"(x0), "=&v"(x1), "=&v"(x2), "=&v"(x3)
      : "v"(b.r), "v"(b.i), "v"(t0.r), "v"(t0.i), "v"(t1.r), "v"(t1.i), "v"(t2.r), "v"(t2.i), "v"(t3.r), "v"(t3.i));
}
constexpr int KG_RS = 288, KG_PL = 16 * KG_RS;
__device__ __forceinline__ void kgen_role(char* smem, const u16* __restrict__ logA, const u16* __restrict__ Aim,
                                          const u16* __restrict__ Cr, const u16* __restrict__ Ci, const u16* __restrict__ logdt,
                                          char* __restrict__ ws, int blk) {
  const int tid = threadIdx.x, lane = tid & 63;
  const int wave = __builtin_amdgcn_readfirstlane(tid >> 6);
  constexpr int RS = KG_RS, PL = KG_PL;
  const int task = blk * 8 + wave;
  const int d = task / 3;
  const int pi = task * 64 + lane;
  const float dt = rb(expf(b2f(logdt[d])));
  float ar;
  {
    const float la = b2f(logA[pi]);
    float h = la * 1.44269502f; float l = fmaf(la, 1.44269502f, -h) + la * 1.92596299e-8f;
    float e = __builtin_amdgcn_exp2f(h); e = fmaf(e, l * 0.69314718f, e);
    ar = -rb(e) * dt;
  }
  const float ai = b2f(Aim[pi]) * dt;
  cf C; C.r = 2.f * (b2f(Cr[pi]) * dt); C.i = 2.f * (b2f(Ci[pi]) * dt);
  const float th = ar * 1.44269502f, tl = fmaf(ar, 1.44269502f, -th) + ar * 1.92596299e-8f;
  const float rh = ai * 0.159154937f, rl = fmaf(ai, 0.159154937f, -rh) + ai * 6.42063833e-9f;
  char* tb = smem + wave * (2 * PL);
  char* wp = tb + lane * 4;
  const char* rp = tb + (lane & 15) * RS + (lane >> 4) * 16;
  STAMP(0, 2);
  const cf one = {1.f, 0.f};
  cf S1 = zpow(th, tl, rh, rl, 1.f), Q1 = zpow(th, tl, rh, rl, 8.f), R1 = zpow(th, tl, rh, rl, 128.f);
  Q1.i = -Q1.i;
  cf S2, Q2, R2, G1, S3, S4, Q3, P1, R3, T1, G2, G3, S5, S6, S7, P2, CT1, T2, P3, CS4, CT2, CT3, CT4, dm0;
  cmul4(S2, Q2, R2, G1, S1, S1, Q1, Q1, R1, R1, C, R1);
  cmul4(S3, S4, Q3, P1, S2, S1, S2, S2, Q2, Q1, Q2, Q2);
  cmul4(R3, T1, G2, P2, R2, R1, R2, R2, C, R2, P1, P1);
  cmul4(S5, S6, S7, P3, S4, S1, S4, S2, S4, S3, P2, P1);
  cmul4(G3, CT1, T2, dm0, C, R3, C, T1, T1, T1, C, R1);
  cmul4(CS4, CT2, CT3, dm0, CT1, S4, C, T2, CT1, T2, C, R1);
  CT4 = cmul(CT2, T2);
  auto rows4 = [&](int row, cf b, cf t0, cf t1, cf t2, cf t3) {
    unsigned h[4], l[4];
    batch4(h, l, b, t0, t1, t2, t3);
#pragma unroll
    for (int k = 0; k < 4; k++) { *(unsigned*)(wp + (row + k) * RS) = h[k]; *(unsigned*)(wp + PL + (row + k) * RS) = l[k]; }
  };
  rows4(0, one, one, Q1, Q2, Q3); rows4(4, P1, one, Q1, Q2, Q3); rows4(8, P2, one, Q1, Q2, Q3); rows4(12, P3, one, Q1, Q2, Q3);
  __builtin_amdgcn_wave_barrier();
  s16x8 bh[4], bl[4];
#pragma unroll
  for (int k = 0; k < 4; k++) { bh[k] = *(const s16x8*)(rp + k * 64); bl[k] = *(const s16x8*)(rp + PL + k * 64); }
  __builtin_amdgcn_wave_barrier();
  STAMP(0, 3);
  f32x4* dst = (f32x4*)(ws + WS_KIMG + (size_t)task * 3072) + lane;
  auto mm = [&](int t) {
    __builtin_amdgcn_wave_barrier();
    f32x4 a = {0.f, 0.f, 0.f, 0.f}, a2 = {0.f, 0.f, 0.f, 0.f};
#pragma unroll
    for (int k = 0; k < 4; k++) {
      s16x8 ah = *(const s16x8*)(rp + k * 64), al = *(const s16x8*)(rp + PL + k * 64);
      a = __builtin_amdgcn_mfma_f32_16x16x32_bf16(ah, bh[k], a, 0, 0, 0);
      a2 = __builtin_amdgcn_mfma_f32_16x16x32_bf16(ah, bl[k], a2, 0, 0, 0);
      a2 = __builtin_amdgcn_mfma_f32_16x16x32_bf16(al, bh[k], a2, 0, 0, 0);
    }
    __builtin_amdgcn_wave_barrier();
    dst[t * 64] = a + a2;
  };
  rows4(0, C, one, S1, S2, S3); rows4(4, C, S4, S5, S6, S7); rows4(8, G1, one, S1, S2, S3); rows4(12, G1, S4, S5, S6, S7);
  mm(0);
  STAMP(0, 4);
  rows4(0, G2, one, S2, S4, S6); rows4(4, G3, one, S2, S4, S6);
  {
    unsigned h[4], l[4];
    batch4(h, l, CT1, one, R1, R2, R3);
#pragma unroll
    for (int k = 0; k < 4; k++) { *(unsigned*)(wp + (8 + 2 * k) * RS) = h[k]; *(unsigned*)(wp + PL + (8 + 2 * k) * RS) = l[k]; }
    batch4(h, l, CS4, one, R1, R2, R3);
#pragma unroll
    for (int k = 0; k < 4; k++) { *(unsigned*)(wp + (9 + 2 * k) * RS) = h[k]; *(unsigned*)(wp + PL + (9 + 2 * k) * RS) = l[k]; }
  }
  mm(1);
  rows4(0, CT2, one, R1, R2, R3); rows4(4, CT3, one, R1, R2, R3); rows4(8, CT4, one, one, one, one);
  mm(2);
}

__global__ __launch_bounds__(512, 4) void k1(const u16* __restrict__ x, const u16* __restrict__ logA, const u16* __restrict__ Aim,
                                          const u16* __restrict__ Cr, const u16* __restrict__ Ci, const u16* __restrict__ logdt,
                                          const u16* __restrict__ wqkv, const u16* __restrict__ gw, char* __restrict__ ws) {
  __shared__ __attribute__((aligned(16))) char smem[K1_LDS];
  int b = blockIdx.x;
  STAMP(0, 0);
  if (b < NB_GEMM) {
    const int xcd = b & 7, idx = b >> 3;
    const int ti = (xcd & 1) * 30 + idx;
    const int rowblk = (xcd >> 1) * 4 + (ti & 3), cb = ti >> 2;
#define ROT 0
    const int rot = ROT ? (cb * 3 + (ti & 3) * 7) % 10 : 0;
    if (cb < 5) gemm_role<true>(smem, x, wqkv, ws, rowblk, cb, rot);
    else        gemm_role<false>(smem, x, wqkv, ws, rowblk, cb, rot);
    STAMP(0, 1);
    return;
  }
  b -= NB_GEMM;
  if (b < NB_KG) { kgen_role(smem, logA, Aim, Cr, Ci, logdt, ws, b); STAMP(0, 1); return; }
  b -= NB_KG;
  tr_role(smem, x, gw, (u16*)(ws + WS_UT), (float*)(ws + WS_GATE), b);
  STAMP(0, 1);
}

constexpr int K2_CS = 4160;
constexpr int K2_KOFF = 8 * K2_CS;
constexpr int K2_TAB = K2_KOFF + 2 * 4608;
constexpr int K2_LDS = K2_TAB + 3072;
constexpr int NB_KV = 32, NB_WO = 50;
__global__ __launch_bounds__(256, 3) void k2(const u16* __restrict__ Dskip, const u16* __restrict__ wo, char* __restrict__ ws) {
  __shared__ __attribute__((aligned(16))) char smem[K2_LDS];
  const int tid = threadIdx.x, lane = tid & 63;
  const int wave = __builtin_amdgcn_readfirstlane(tid >> 6);
  STAMP(1, 0);
  if (blockIdx.x < NB_KV) {
    const int hd = blockIdx.x;
    const int c = lane & 31, h = lane >> 5;
    const int cc = c < 20 ? c : 19;
    const char* pk = ws + WS_PART + ((size_t)(wave * 16 * 4 + h) * 1280 + 20 * hd + cc) * 16;
    const char* pv = pk + 640 * 16;
    f32x16 acc;
#pragma unroll
    for (int i = 0; i < 16; i++) acc[i] = 0.f;
#pragma unroll
    for (int bt = 0; bt < 2; bt++) {
      u32x4 a[16], v[16];
#pragma unroll
      for (int i = 0; i < 16; i++) {
        a[i] = *(const u32x4*)(pk + (size_t)(bt * 16 + i) * (2 * 1280 * 16));
        v[i] = *(const u32x4*)(pv + (size_t)(bt * 16 + i) * (2 * 1280 * 16));
      }
      __builtin_amdgcn_sched_barrier(0);
#pragma unroll
      for (int i = 0; i < 16; i++) {
        u32x4 vv = v[i];
        if (c == 20) vv = (u32x4){0x3F803F80u, 0x3F803F80u, 0x3F803F80u, 0x3F803F80u};
        acc = __builtin_amdgcn_mfma_f32_32x32x16_bf16(__builtin_bit_cast(s16x8, a[i]), __builtin_bit_cast(s16x8, vv), acc, 0, 0, 0);
      }
    }
    float* red = (float*)smem;
#pragma unroll
    for (int q = 0; q < 3; q++)
#pragma unroll
      for (int e = 0; e < 4; e++) red[(wave * 32 + 8 * q + 4 * h + e) * 33 + c] = acc[4 * q + e];
    __syncthreads();
    float* fin = red + 4 * 32 * 33;
    for (int i = tid; i < 420; i += 256) {
      int dk = i / 21, dv = i - 21 * dk;
      float s = (red[dk * 33 + dv] + red[(32 + dk) * 33 + dv]) + (red[(64 + dk) * 33 + dv] + red[(96 + dk) * 33 + dv]);
      fin[i] = rb(s);
    }
    __syncthreads();
    if (tid < 96) {
      const int m = lane & 31;
      int col = m < 16 ? m : ((m >> 2) == ((hd & 2) ? 5 : 4) ? 16 + (m & 3) : 20);
      if (tid < 64) {
        u32x4 o;
#pragma unroll
        for (int e = 0; e < 4; e++) o[e] = pack2(fin[(8 * h + 2 * e) * 21 + col], fin[(8 * h + 2 * e + 1) * 21 + col]);
        *(u32x4*)(ws + WS_KVT + (size_t)hd * 1280 + lane * 16) = o;
      } else {
        u32x2 o;
#pragma unroll
        for (int e = 0; e < 2; e++) o[e] = pack2(fin[(16 + 2 * e) * 21 + col], fin[(17 + 2 * e) * 21 + col]);
        *(u32x2*)(ws + WS_KVT + (size_t)hd * 1280 + 1024 + m * 8) = o;
      }
    }
    STAMP(1, 1);
    return;
  }
  if (blockIdx.x < NB_KV + NB_WO) {
    const int blk = blockIdx.x - NB_KV;
#pragma unroll
    for (int i = 0; i < 4; i++) {
      const int id = blk * 1024 + i * 256 + tid;
      const int slot = id % 160, f = id / 160;
      const int ks = f % 10, kq = (f / 10) & 3, cb = f / 40;
      int m, h;
      if (slot < 128) { m = (slot >> 6) * 32 + (slot & 31); h = (slot >> 5) & 1; } else { m = 64 + (slot & 15); h = (slot >> 4) & 1; }
      const int n = 80 * cb + m;
      int k0, k1;
      if (ks < 8) { k0 = 20 * (8 * kq + ks) + 4 * h; k1 = k0 + 8; }
      else { k0 = 20 * (8 * kq + 4 * (ks - 8) + 2 * h) + 16; k1 = k0 + 20; }
      u32x4 v;
#pragma unroll
      for (int e = 0; e < 2; e++) {
        unsigned a0 = wo[(size_t)(k0 + 2 * e) * D + n], a1 = wo[(size_t)(k0 + 2 * e + 1) * D + n];
        unsigned b0 = wo[(size_t)(k1 + 2 * e) * D + n], b1 = wo[(size_t)(k1 + 2 * e + 1) * D + n];
        v[e] = a0 | (a1 << 16); v[2 + e] = b0 | (b1 << 16);
      }
      *(u32x4*)(ws + WS_WOF + (size_t)id * 16) = v;
    }
    STAMP(1, 1);
    return;
  }
  const int d = blockIdx.x - (NB_KV + NB_WO);
  const u16* uT = (const u16*)(ws + WS_UT) + (size_t)d * UT_STRIDE;
  {
    const f32x4* ksrc = (const f32x4*)(ws + WS_KIMG + (size_t)d * 9216) + (tid < 192 ? tid : 191);
    const u32x4* usrc = (const u32x4*)uT;
    f32x4 p0 = ksrc[0], p1 = ksrc[192], p2 = ksrc[384];
    u32x4 c0 = usrc[tid + 1], c1 = usrc[tid + 2];
    u32x4 e0, e1;
    if (tid < 2) { e0 = usrc[tid + 257]; e1 = usrc[tid + 258]; }
    if (tid < 64) *(u32x4*)(smem + K2_KOFF + (tid >> 5) * 4608 + (tid & 31) * 16) = (u32x4){0u, 0u, 0u, 0u};
    if (tid < 192) *(f32x4*)(smem + K2_TAB + tid * 16) = (p0 + p1) + p2;
    auto put_copies = [&](u32x4 a, u32x4 b, int xch) {
      unsigned w[8] = {a[0], a[1], a[2], a[3], b[0], b[1], b[2], b[3]};
      unsigned o[7];
#pragma unroll
      for (int k = 0; k < 7; k++) o[k] = __builtin_amdgcn_alignbyte(w[k + 1], w[k], 2);
      char* p = smem + xch * 16;
#pragma unroll
      for (int q = 0; q < 4; q++) {
        *(u32x4*)(p + (2 * q) * K2_CS) = (u32x4){o[q], o[q + 1], o[q + 2], o[q + 3]};
        *(u32x4*)(p + (2 * q + 1) * K2_CS) = (u32x4){w[q + 1], w[q + 2], w[q + 3], w[q + 4]};
      }
    };
    put_copies(c0, c1, tid);
    if (tid < 2) put_copies(e0, e1, tid + 256);
  }
  __syncthreads();
  {
    const float* tab = (const float*)(smem + K2_TAB);
    float kv[8];
#pragma unroll
    for (int e = 0; e < 8; e++) {
      const int j = tid * 8 + 7 - e;
      const int p = (int)rb((float)j);
      const int h = p >> 7, c = (p >> 3) & 15;
      int sh = 31 - __builtin_clz(h | 1); sh = sh > 3 ? 3 : sh;
      const int row = 8 * sh + (((h << 3) | (p & 7)) >> sh);
      kv[e] = tab[((row >> 2) * 16 + c) * 4 + (row & 3)];
    }
    u32x4 hi, lo;
#pragma unroll
    for (int e = 0; e < 4; e++) {
      hi[e] = pack2(kv[2 * e], kv[2 * e + 1]);
      lo[e] = pack2(ssub(kv[2 * e], lo_f(hi[e])), ssub(kv[2 * e + 1], hi_f(hi[e])));
    }
    *(u32x4*)(smem + K2_KOFF + 512 + tid * 16) = hi;
    *(u32x4*)(smem + K2_KOFF + 4608 + 512 + tid * 16) = lo;
  }
  __syncthreads();
  STAMP(1, 2);
  f32x4 acc[8];
#pragma unroll
  for (int t = 0; t < 8; t++) acc[t] = (f32x4){0.f, 0.f, 0.f, 0.f};
  {
    const int i = lane & 15, kg = lane >> 4, e = kg >> 1, bb = kg & 1;
    const char* ap = smem + (i & 7) * K2_CS + (2 * e - bb + 2 + (i >> 3)) * 16;
    const char* bp = smem + K2_KOFF + (16 + i - e) * 32 + bb * 16;
#pragma unroll
    for (int it = 0; it < 2; it++) {
      const int d0 = 2 * (wave + 4 * it);
      s16x8 af[8], bh[8], bl[8];
#pragma unroll
      for (int j = 0; j < 8; j++) af[j] = *(const s16x8*)(ap + (32 * j + 2 * d0) * 16);
#pragma unroll
      for (int m = 0; m < 8; m++) {
        bh[m] = *(const s16x8*)(bp + (16 * m - d0) * 32);
        bl[m] = *(const s16x8*)(bp + (16 * m - d0) * 32 + 4608);
      }
#pragma unroll
      for (int m = 0; m < 8; m++) {
#pragma unroll
        for (int j = 0; j + m < 8; j++) acc[j + m] = __builtin_amdgcn_mfma_f32_16x16x32_bf16(af[j], bh[m], acc[j + m], 0, 0, 0);
#pragma unroll
        for (int j = 0; j + m < 8; j++) acc[j + m] = __builtin_amdgcn_mfma_f32_16x16x32_bf16(af[j], bl[m], acc[j + m], 0, 0, 0);
      }
    }
  }
  STAMP(1, 3);
  u32x2 uu[2];
#pragma unroll
  for (int t = 0; t < 2; t++) uu[t] = *(const u32x2*)(uT + UT_PAD + 256 * (2 * wave + t) + 4 * lane);
  const float dsk = b2f(Dskip[d]);
  __syncthreads();
  float* red = (float*)smem;
#pragma unroll
  for (int t = 0; t < 8; t++) *(f32x4*)(red + ((wave * 8 + t) * 64 + (lane & 15) * 4 + (lane >> 4)) * 4) = acc[t];
  __syncthreads();
  u16* aT = (u16*)(ws + WS_AT) + (size_t)d * L;
#pragma unroll
  for (int t = 0; t < 2; t++) {
    const int tt = 2 * wave + t;
    f32x4 s = *(const f32x4*)(red + ((0 * 8 + tt) * 64 + lane) * 4);
    f32x4 s1 = *(const f32x4*)(red + ((1 * 8 + tt) * 64 + lane) * 4);
    f32x4 s2 = *(const f32x4*)(red + ((2 * 8 + tt) * 64 + lane) * 4);
    f32x4 s3 = *(const f32x4*)(red + ((3 * 8 + tt) * 64 + lane) * 4);
    s = (s + s1) + (s2 + s3);
    u32x2 ov;
#pragma unroll
    for (int e = 0; e < 2; e++) {
      unsigned y = pack2(s[2 * e], s[2 * e + 1]);
      unsigned k = pack2(lo_f(uu[t][e]) * dsk, hi_f(uu[t][e]) * dsk);
      ov[e] = pack2(lo_f(y) + lo_f(k), hi_f(y) + hi_f(k));
    }
    *(u32x2*)(aT + 256 * tt + 4 * lane) = ov;
  }
  STAMP(1, 1);
}

constexpr int K3_WO = 0;
constexpr int K3_KV = 102400;
constexpr int K3_AT = K3_KV + 40960;
constexpr int K3_LDS = K3_AT + 80 * 144;
__global__ __launch_bounds__(512, 2) void k3(char* __restrict__ ws, u16* __restrict__ out) {
  __shared__ __attribute__((aligned(16))) char smem[K3_LDS];
  const int tid = threadIdx.x, lane = tid & 63;
  const int wave = __builtin_amdgcn_readfirstlane(tid >> 6);
  STAMP(2, 0);
  const int xcd = blockIdx.x & 7, idx = blockIdx.x >> 3;
  const int rowblk = xcd * 4 + (idx >> 3), cb = idx & 7;
  const int l0 = rowblk * 64, c0 = cb * 80;
  const int kq = wave & 3, t = wave >> 2;
  const int r = lane & 31, h = lane >> 5;
#pragma unroll
  for (int i = 0; i < 5; i++) glds16(ws + WS_KVT + (size_t)(wave * 5 + i) * 1024 + lane * 16, smem + K3_KV + (wave * 5 + i) * 1024);
  u32x4 pf0[8]; u32x2 pf1[8];
  {
    const char* p0 = ws + WS_PQ + ((size_t)((rowblk * 32 + kq * 8) * 2 + t) * 64 + lane) * 16;
    const char* p1 = ws + WS_PQ + (size_t)L * 16 * 32 * 2 + ((size_t)((rowblk * 32 + kq * 8) * 2 + t) * 32 + r) * 8;
#pragma unroll
    for (int hh = 0; hh < 8; hh++) {
      asm volatile("global_load_dwordx4 %0, %1, off offset:%2" : "=v"(pf0[hh]) : "v"(p0 + (hh >> 1) * 4096), "i"((hh & 1) * 2048));
      asm volatile("global_load_dwordx2 %0, %1, off offset:%2" : "=v"(pf1[hh]) : "v"(p1), "i"(hh * 512));
    }
  }
  {
    const char* src = ws + WS_WOF + (size_t)cb * 102400 + lane * 16;
#pragma unroll
    for (int i = 0; i < 13; i++) { int p = i * 8 + wave; if (p < 100) glds16(src + p * 1024, smem + K3_WO + p * 1024); }
  }
  u32x4 av[2];
  {
    const char* aT = ws + WS_AT;
#pragma unroll
    for (int i = 0; i < 2; i++) {
      int id = tid + 512 * i; if (id > 639) id = 639;
      const char* p = aT + ((size_t)(c0 + (id >> 3)) * L + l0 + 8 * (id & 7)) * 2;
      asm volatile("global_load_dwordx4 %0, %1, off" : "=v"(av[i]) : "v"(p));
    }
  }
  float gt[2];
  {
    const float* gate = (const float*)(ws + WS_GATE) + l0;
#pragma unroll
    for (int i = 0; i < 2; i++) {
      int id = tid + 512 * i; if (id > 639) id = 639;
      asm volatile("global_load_dword %0, %1, off" : "=v"(gt[i]) : "v"(gate + id / 10));
    }
  }
  if (wave < 4) VMCNT(17); else VMCNT(16);
  asm volatile("" : "+v"(pf0[0]), "+v"(pf0[1]), "+v"(pf0[2]), "+v"(pf0[3]), "+v"(pf0[4]), "+v"(pf0[5]), "+v"(pf0[6]), "+v"(pf0[7]));
  asm volatile("" : "+v"(pf1[0]), "+v"(pf1[1]), "+v"(pf1[2]), "+v"(pf1[3]), "+v"(pf1[4]), "+v"(pf1[5]), "+v"(pf1[6]), "+v"(pf1[7]));
  RAW_BARRIER();
  STAMP(2, 2);
  u32x4 ob[10];
  {
    unsigned rem[8][2];
#pragma unroll
    for (int hh = 0; hh < 8; hh++) {
      const char* kp = smem + K3_KV + (kq * 8 + hh) * 1280;
      s16x8 a0 = *(const s16x8*)(kp + lane * 16);
      u32x2 a1h = *(const u32x2*)(kp + 1024 + r * 8);
      u32x4 a1u = {a1h[0], a1h[1], 0u, 0u};
      u32x4 b1u = {h ? 0u : pf1[hh][0], h ? 0u : pf1[hh][1], 0u, 0u};
      f32x16 c;
#pragma unroll
      for (int i = 0; i < 16; i++) c[i] = 0.f;
      c = __builtin_amdgcn_mfma_f32_32x32x16_bf16(a0, __builtin_bit_cast(s16x8, pf0[hh]), c, 0, 0, 0);
      c = __builtin_amdgcn_mfma_f32_32x32x16_bf16(__builtin_bit_cast(s16x8, a1u), __builtin_bit_cast(s16x8, b1u), c, 0, 0, 0);
      float den = rb(rb(c[12]) + 1e-6f);
      float inv = __builtin_amdgcn_rcpf(den);
      unsigned o[6];
#pragma unroll
      for (int q = 0; q < 6; q++) {
        unsigned n0 = pack2(c[2 * q], c[2 * q + 1]);
        o[q] = pack2(lo_f(n0) * inv, hi_f(n0) * inv);
      }
      ob[hh] = (u32x4){o[0], o[1], o[2], o[3]};
      rem[hh][0] = o[4]; rem[hh][1] = o[5];
    }
#pragma unroll
    for (int G = 0; G < 2; G++)
      ob[8 + G] = (u32x4){h ? rem[4 * G + 2][0] : rem[4 * G][0], h ? rem[4 * G + 2][1] : rem[4 * G][1],
                          h ? rem[4 * G + 3][0] : rem[4 * G + 1][0], h ? rem[4 * G + 3][1] : rem[4 * G + 1][1]};
  }
  STAMP(2, 3);
  VMCNT(0);
  asm volatile("" : "+v"(av[0]), "+v"(av[1]), "+v"(gt[0]), "+v"(gt[1]));
#pragma unroll
  for (int i = 0; i < 2; i++) { int id = tid + 512 * i; if (id < 640) *(u32x4*)(smem + K3_AT + (id >> 3) * 144 + (id & 7) * 16) = av[i]; }
  RAW_BARRIER();
  STAMP(2, 4);
  f32x16 acc[3];
#pragma unroll
  for (int c = 0; c < 3; c++)
#pragma unroll
    for (int i = 0; i < 16; i++) acc[c][i] = 0.f;
  {
    const char* wp = smem + K3_WO + kq * 25600;
    const int o2 = 2048 + (h * 16 + (lane & 15)) * 16;
#pragma unroll
    for (int ks = 0; ks < 10; ks++) {
      s16x8 w0 = *(const s16x8*)(wp + ks * 2560 + lane * 16);
      s16x8 w1 = *(const s16x8*)(wp + ks * 2560 + 1024 + lane * 16);
      s16x8 w2 = *(const s16x8*)(wp + ks * 2560 + o2);
      s16x8 b = __builtin_bit_cast(s16x8, ob[ks]);
      acc[0] = __builtin_amdgcn_mfma_f32_32x32x16_bf16(w0, b, acc[0], 0, 0, 0);
      acc[1] = __builtin_amdgcn_mfma_f32_32x32x16_bf16(w1, b, acc[1], 0, 0, 0);
      acc[2] = __builtin_amdgcn_mfma_f32_32x32x16_bf16(w2, b, acc[2], 0, 0, 0);
    }
  }
  STAMP(2, 5);
  RAW_BARRIER();
  {
    float* pr = (float*)smem + (size_t)(kq * 64 + 32 * t + r) * 84 + 4 * h;
#pragma unroll
    for (int c = 0; c < 3; c++)
#pragma unroll
      for (int q = 0; q < 4; q++)
        if (c < 2 || q < 2) *(f32x4*)(pr + 32 * c + 8 * q) = (f32x4){acc[c][4 * q], acc[c][4 * q + 1], acc[c][4 * q + 2], acc[c][4 * q + 3]};
  }
  RAW_BARRIER();
  {
#pragma unroll
    for (int i2 = 0; i2 < 2; i2++) {
      const int i = tid + 512 * i2;
      if (i < 640) {
        const int rr = i / 10, oc = i - 10 * rr;
        const float g = gt[i2]; const float g1 = rb(1.f - g);
        f32x4 s0 = {0.f, 0.f, 0.f, 0.f}, s1 = {0.f, 0.f, 0.f, 0.f};
#pragma unroll
        for (int w = 0; w < 4; w++) {
          const float* pr = (const float*)smem + (size_t)(w * 64 + rr) * 84 + oc * 8;
          s0 += *(const f32x4*)pr; s1 += *(const f32x4*)(pr + 4);
        }
        u32x4 ov;
#pragma unroll
        for (int e = 0; e < 4; e++) {
          unsigned bb = e < 2 ? pack2(s0[2 * e], s0[2 * e + 1]) : pack2(s1[2 * e - 4], s1[2 * e - 3]);
          float a0 = b2f(*(const u16*)(smem + K3_AT + (oc * 8 + 2 * e) * 144 + rr * 2));
          float a1 = b2f(*(const u16*)(smem + K3_AT + (oc * 8 + 2 * e + 1) * 144 + rr * 2));
          unsigned ga = pack2(g * a0, g * a1), gb = pack2(g1 * lo_f(bb), g1 * hi_f(bb));
          ov[e] = pack2(lo_f(ga) + lo_f(gb), hi_f(ga) + hi_f(gb));
        }
        *(u32x4*)(out + (size_t)(l0 + rr) * D + c0 + oc * 8) = ov;
      }
    }
  }
  STAMP(2, 1);
}

extern "C" void kernel_launch(void* const* d_in, const int* in_sizes, int n_in, void* d_out, int out_size, void* d_ws, size_t ws_size,
                              hipStream_t stream) {
  const u16* u = (const u16*)d_in[0]; const u16* logA = (const u16*)d_in[1]; const u16* Aim = (const u16*)d_in[2];
  const u16* Cr = (const u16*)d_in[3]; const u16* Ci = (const u16*)d_in[4]; const u16* logdt = (const u16*)d_in[5];
  const u16* Dsk = (const u16*)d_in[6]; const u16* wqkv = (const u16*)d_in[8]; const u16* wo = (const u16*)d_in[9];
  const u16* gw = (const u16*)d_in[10];
  char* ws = (char*)d_ws;
  k1<<<NB_GEMM + NB_TR + NB_KG, 512, 0, stream>>>(u, logA, Aim, Cr, Ci, logdt, wqkv, gw, ws);
  k2<<<NB_KV + NB_WO + D, 256, 0, stream>>>(Dsk, wo, ws);
  k3<<<256, 512, 0, stream>>>(ws, (u16*)d_out);
}
```

```cpp
#include <hip/hip_runtime.h>
#include <hip/hip_bf16.h>
#include <stdint.h>

typedef unsigned short u16;
typedef __attribute__((ext_vector_type(8))) short s16x8;
typedef __attribute__((ext_vector_type(4))) short s16x4;
typedef __attribute__((ext_vector_type(4))) float f32x4;
typedef __attribute__((ext_vector_type(16))) float f32x16;
typedef __attribute__((ext_vector_type(4))) unsigned u32x4;
typedef __attribute__((ext_vector_type(2))) unsigned u32x2;
typedef __attribute__((address_space(3))) s16x4* lds_s16x4_p;
typedef const __attribute__((address_space(1))) void* gptr_t;
typedef __attribute__((address_space(3))) void* lptr_t;

constexpr int L = 2048, D = 640, NS = 192, NH = 32;
constexpr int UT_PAD = 32, UT_STRIDE = L + UT_PAD;
constexpr size_t WS_UT   = 0;
constexpr size_t WS_KIMG = WS_UT + (size_t)D * UT_STRIDE * 2;
constexpr size_t WS_PQ   = WS_KIMG + (size_t)D * 6144;
constexpr size_t WS_PART = WS_PQ + (size_t)L * D * 2;
constexpr size_t WS_GATE = WS_PART + (size_t)L * 1280 * 2;
constexpr size_t WS_WOF  = WS_GATE + (size_t)L * 4;
constexpr size_t WS_KVT  = WS_WOF + (size_t)D * D * 2;
constexpr size_t WS_AT   = WS_KVT + (size_t)32 * 1280;
constexpr size_t WS_END  = WS_AT + (size_t)D * L * 2;
#define STAMP(kid, slot) do {} while (0)

__device__ __forceinline__ float b2f(u16 v) { return __uint_as_float(((unsigned)v) << 16); }
typedef __bf16 bf16x2_t __attribute__((ext_vector_type(2)));
typedef float f32x2 __attribute__((ext_vector_type(2)));
__device__ __forceinline__ unsigned pack2(float a, float b) { f32x2 v = {a, b}; bf16x2_t r = __builtin_convertvector(v, bf16x2_t); return __builtin_bit_cast(unsigned, r); }
__device__ __forceinline__ float lo_f(unsigned u) { return __uint_as_float(u << 16); }
__device__ __forceinline__ float hi_f(unsigned u) { return __uint_as_float(u & 0xffff0000u); }
__device__ __forceinline__ float rb(float f) { return lo_f(pack2(f, f)); }
__device__ __forceinline__ unsigned phi2(float a, float b) {
  unsigned t = pack2(a, b); float t0 = lo_f(t), t1 = hi_f(t);
  float r0 = t0 > 0.f ? t0 + 1.f : __expf(t0), r1 = t1 > 0.f ? t1 + 1.f : __expf(t1);
  unsigned r = pack2(r0, r1);
  return pack2(lo_f(r) + 1e-6f, hi_f(r) + 1e-6f);
}
#define VMCNT(n) asm volatile("s_waitcnt vmcnt(" #n ")" ::: "memory")
#define RAW_BARRIER() do { asm volatile("s_waitcnt lgkmcnt(0)" ::: "memory"); __builtin_amdgcn_s_barrier(); asm volatile("" ::: "memory"); } while (0)
__device__ __forceinline__ void glds16(const void* g, void* l) { __builtin_amdgcn_global_load_lds((gptr_t)g, (lptr_t)l, 16, 0, 0); }

#define OPQ_S(v) asm volatile("" : "+s"(v))
struct cf { float r, i; };
__device__ __forceinline__ float smul(float a, float b) { float o; asm("v_mul_f32 %0, %1, %2" : "=v"(o) : "v"(a), "v"(b)); return o; }
__device__ __forceinline__ float sfma(float a, float b, float c) { float o; asm("v_fma_f32 %0, %1, %2, %3" : "=v"(o) : "v"(a), "v"(b), "v"(c)); return o; }
__device__ __forceinline__ float sfnma(float a, float b, float c) { float o; asm("v_fma_f32 %0, -%1, %2, %3" : "=v"(o) : "v"(a), "v"(b), "v"(c)); return o; }
__device__ __forceinline__ float ssub(float a, float b) { float o; asm("v_sub_f32 %0, %1, %2" : "=v"(o) : "v"(a), "v"(b)); return o; }
__device__ __forceinline__ cf cmul(cf a, cf b) { cf o; o.r = sfnma(a.i, b.i, smul(a.r, b.r)); o.i = sfma(a.i, b.r, smul(a.r, b.i)); return o; }

constexpr int K1_LDS = 81920;
constexpr int NB_GEMM = 240, NB_TR = 32, NB_KG = 240;
#define WT 1
__device__ __forceinline__ void st16(void* p, u32x4 v) {
  asm volatile("global_store_dwordx4 %0, %1, off sc1\n\ts_nop 2" :: "v"(p), "v"(v) : "memory");
}

#define BAR_ONLY() do { asm volatile("" ::: "memory"); __builtin_amdgcn_s_barrier(); asm volatile("" ::: "memory"); } while (0)
template <bool ISQ>
__device__ __forceinline__ void gemm_role(char* smem, const u16* __restrict__ x, const u16* __restrict__ W, char* __restrict__ ws,
                                          int rowblk, int cb) {
  const int tid = threadIdx.x, lane = tid & 63;
  const int wave = __builtin_amdgcn_readfirstlane(tid >> 6);
  const int row0 = rowblk * 128, col0 = cb * 128;
  char* sA = smem; char* sB = smem + 49152;
  if (wave >= 4) {
    const int lw = wave - 4;
    const char* pA = (const char*)x + (size_t)(row0 + 32 * lw + (lane >> 3)) * 1280 + (((lane & 7) ^ (lane >> 3)) << 4);
    const int fB = (lane >> 4) | ((lw & 1) << 2);
    const char* pB = (const char*)W + (size_t)(8 * lw + (lane >> 4)) * 3840 + col0 * 2 + (((((lane & 15) >> 1) ^ fB) * 2 + (lane & 1)) << 4);
    auto issA = [&](int slot_off, int h) {
      glds16(pA + (16 * h) * 1280, sA + slot_off + (4 * lw + 2 * h) * 1024);
      glds16(pA + (16 * h + 8) * 1280, sA + slot_off + (4 * lw + 2 * h + 1) * 1024);
    };
    auto issB = [&](int slot_off) {
      glds16(pB, sB + slot_off + (2 * lw) * 1024);
      glds16(pB + 4 * 3840, sB + slot_off + (2 * lw + 1) * 1024);
      pB += (size_t)32 * 3840;
    };
    issA(0, 0); issB(0); issA(0, 1); pA += 128;
    issB(8192); issA(16384, 0); issB(16384); issA(16384, 1); pA += 128;
    VMCNT(8);
    BAR_ONLY();
    int ao = 32768, bo = 24576;
#pragma unroll 1
    for (int j = 0; j < 10; j++) {
      if (j < 9) VMCNT(6); else VMCNT(0);
      BAR_ONLY();
      if (j < 9) { issB(bo); bo = (bo + 8192) & 32767; }
      if (j < 8) issA(ao, 0);
      if (j < 8) VMCNT(4); else VMCNT(2);
      BAR_ONLY();
      if (j < 8) { issB(bo); bo = (bo + 8192) & 32767; issA(ao, 1); pA += 128; ao = ao == 32768 ? 0 : ao + 16384; }
    }
    return;
  }
  const int wr = wave & 1, wc = wave >> 1;
  f32x4 acc[4][4];
#pragma unroll
  for (int m = 0; m < 4; m++)
#pragma unroll
    for (int c = 0; c < 4; c++) acc[m][c] = (f32x4){0.f, 0.f, 0.f, 0.f};
  const int g = lane >> 4, fr = lane & 15;
  const int q4 = fr >> 2, p4 = lane & 3;
  int aoff[2];
#pragma unroll
  for (int ks = 0; ks < 2; ks++) aoff[ks] = (wr * 64 + fr) * 128 + (((4 * ks + g) ^ (fr & 7)) << 4);
  int boff[4];
  {
    const int f = q4 | ((g & 1) << 2);
#pragma unroll
    for (int c = 0; c < 4; c++) boff[c] = (8 * g + q4) * 256 + (((4 * wc + c) ^ f) << 5) + p4 * 8;
  }
  {
    s16x8 af[2][4], bf[4];
    auto rdA = [&](int buf, int m, const char* pa) { af[buf][m] = *(const s16x8*)(pa + m * 2048); };
    auto rdB = [&](int c, const char* pb) {
      s16x4 b0 = __builtin_amdgcn_ds_read_tr16_b64_v4i16((lds_s16x4_p)(pb + boff[c]));
      s16x4 b1 = __builtin_amdgcn_ds_read_tr16_b64_v4i16((lds_s16x4_p)(pb + boff[c] + 1024));
      bf[c] = __builtin_shufflevector(b0, b1, 0, 1, 2, 3, 4, 5, 6, 7);
    };
    BAR_ONLY();
    STAMP(0, 2);
#pragma unroll
    for (int c = 0; c < 4; c++) { rdA(0, c, sA + aoff[0]); rdB(c, sB); }
    int ao0 = 0, ao1 = 16384, bo = 0;
#pragma unroll 1
    for (int j = 0; j < 10; j++) {
#pragma unroll
      for (int hs = 0; hs < 2; hs++) {
        BAR_ONLY();
        __builtin_amdgcn_sched_barrier(0);
        const char* pa = sA + (hs == 0 ? ao0 + aoff[1] : ao1 + aoff[0]);
        const char* pb = sB + (hs == 0 ? bo + 8192 : (bo ^ 16384));
#pragma unroll
        for (int c = 0; c < 4; c++) {
#pragma unroll
          for (int m = 0; m < 4; m++) {
            if (ISQ) acc[m][c] = __builtin_amdgcn_mfma_f32_16x16x32_bf16(bf[c], af[hs][m], acc[m][c], 0, 0, 0);
            else     acc[m][c] = __builtin_amdgcn_mfma_f32_16x16x32_bf16(af[hs][m], bf[c], acc[m][c], 0, 0, 0);
            if (m == 1) { __builtin_amdgcn_sched_barrier(0); rdA(hs ^ 1, c, pa); __builtin_amdgcn_sched_barrier(0); }
          }
          __builtin_amdgcn_sched_barrier(0);
          rdB(c, pb);
          __builtin_amdgcn_sched_barrier(0);
        }
      }
      ao0 = ao1; ao1 = ao1 == 32768 ? 0 : ao1 + 16384; bo ^= 16384;
    }
    STAMP(0, 3);
  }
  if (ISQ) {
    char* pq0 = ws + WS_PQ; char* pq1 = ws + WS_PQ + (size_t)L * 16 * 32 * 2;
#pragma unroll
    for (int c = 0; c < 4; c++) {
      const int n = col0 + 64 * wc + 16 * c + 4 * g;
      const int hd = n / 20, d0 = n - 20 * hd;
#pragma unroll
      for (int m = 0; m < 4; m++) {
        const int row = row0 + wr * 64 + 16 * m + fr;
        const int rb64 = row >> 6, t = (row >> 5) & 1, r = row & 31;
        u32x2 o;
        o[0] = phi2(acc[m][c][0], acc[m][c][1]);
        o[1] = phi2(acc[m][c][2], acc[m][c][3]);
        const int fi = (rb64 * 32 + hd) * 2 + t;
        char* dst = d0 < 16 ? pq0 + ((size_t)fi * 64 + (d0 >> 3) * 32 + r) * 16 + (d0 & 7) * 2 : pq1 + ((size_t)fi * 32 + r) * 8;
        *(u32x2*)dst = o;
      }
    }
  } else {
    const bool isk = cb < 10;
#pragma unroll
    for (int mm = 0; mm < 2; mm++) {
      const int rg = rowblk * 4 + wr * 2 + mm;
      char* dst = ws + WS_PART + ((size_t)(rg * 4 + g) * 1280 + (col0 - 640) + 64 * wc + fr) * 16;
#pragma unroll
      for (int c = 0; c < 4; c++) {
        u32x4 a;
        if (isk) {
          a[0] = phi2(acc[2 * mm][c][0], acc[2 * mm][c][1]); a[1] = phi2(acc[2 * mm][c][2], acc[2 * mm][c][3]);
          a[2] = phi2(acc[2 * mm + 1][c][0], acc[2 * mm + 1][c][1]); a[3] = phi2(acc[2 * mm + 1][c][2], acc[2 * mm + 1][c][3]);
        } else {
          a[0] = pack2(acc[2 * mm][c][0], acc[2 * mm][c][1]); a[1] = pack2(acc[2 * mm][c][2], acc[2 * mm][c][3]);
          a[2] = pack2(acc[2 * mm + 1][c][0], acc[2 * mm + 1][c][1]); a[3] = pack2(acc[2 * mm + 1][c][2], acc[2 * mm + 1][c][3]);
        }
        *(u32x4*)(dst + c * 256) = a;
      }
    }
  }
}

__device__ __forceinline__ void tr_role(char* smem, const u16* __restrict__ x, const u16* __restrict__ gw,
                                        u16* __restrict__ uT, float* __restrict__ gate, int blk) {
  const int tid = threadIdx.x, wave = tid >> 6, lane = tid & 63;
  const int l0 = blk * 64;
  {
    u32x4 v[10];
#pragma unroll
    for (int i = 0; i < 10; i++) v[i] = *(const u32x4*)(x + (size_t)l0 * D + (size_t)(tid + 512 * i) * 8);
#pragma unroll
    for (int i = 0; i < 10; i++) { int id = tid + 512 * i; int r = id / 80, ch = id - 80 * r;
      *(u32x4*)(smem + r * 1280 + ((ch ^ (r >> 3)) << 4)) = v[i]; }
  }
  unsigned gwr[5];
#pragma unroll
  for (int i = 0; i < 5; i++) gwr[i] = *(const unsigned*)(gw + 2 * lane + 128 * i);
  __syncthreads();
#pragma unroll 1
  for (int i0 = 0; i0 < 10; i0 += 5) {
    u32x4 v[5];
#pragma unroll
    for (int i = 0; i < 5; i++) {
      int id = tid + 512 * (i0 + i); int d = id >> 3, oc = id & 7;
      const char* p = smem + (8 * oc) * 1280 + (((d >> 3) ^ oc) << 4) + (d & 7) * 2;
#pragma unroll
      for (int e = 0; e < 4; e++) {
        unsigned lo = *(const u16*)(p + (2 * e) * 1280);
        unsigned hi = *(const u16*)(p + (2 * e + 1) * 1280);
        v[i][e] = lo | (hi << 16);
      }
    }
#pragma unroll
    for (int i = 0; i < 5; i++) {
      int id = tid + 512 * (i0 + i); int d = id >> 3, oc = id & 7;
      st16(uT + (size_t)d * UT_STRIDE + UT_PAD + l0 + 8 * oc, v[i]);
      if (blk == 0 && oc < 4) st16(uT + (size_t)d * UT_STRIDE + 8 * oc, (u32x4){0u, 0u, 0u, 0u});
    }
  }
  {
    float s[8];
#pragma unroll
    for (int rr = 0; rr < 8; rr++) {
      int r = wave * 8 + rr; s[rr] = 0.f;
#pragma unroll
      for (int i = 0; i < 5; i++) {
        const int ch = (lane >> 2) + 16 * i;
        unsigned v = *(const unsigned*)(smem + r * 1280 + ((ch ^ wave) << 4) + (lane & 3) * 4);
        unsigned p = pack2(lo_f(v) * lo_f(gwr[i]), hi_f(v) * hi_f(gwr[i]));
        s[rr] += lo_f(p) + hi_f(p);
      }
    }
#pragma unroll
    for (int o = 32; o > 0; o >>= 1)
#pragma unroll
      for (int rr = 0; rr < 8; rr++) s[rr] += __shfl_xor(s[rr], o);
    float sv = s[0];
#pragma unroll
    for (int rr = 1; rr < 8; rr++) sv = lane == rr ? s[rr] : sv;
    if (lane < 8) { float a = rb(sv); gate[l0 + wave * 8 + lane] = rb(1.f / (1.f + __expf(-a))); }
  }
}

__device__ __forceinline__ cf zpow(float th, float tl, float rh, float rl, float p) {
  float e = __builtin_amdgcn_exp2f(th * p); e = fmaf(e, (tl * p) * 0.69314718f, e);
  float t = rh * p; float f = (t - __builtin_rintf(t)) + rl * p;
  cf o; o.r = e * __builtin_amdgcn_cosf(f); o.i = e * __builtin_amdgcn_sinf(f); return o;
}
__device__ __forceinline__ void cmul4(cf& o0, cf& o1, cf& o2, cf& o3, cf a0, cf b0, cf a1, cf b1, cf a2, cf b2, cf a3, cf b3) {
  asm("v_mul_f32 %0, %8, %10\n\tv_mul_f32 %1, %8, %11\n\tv_mul_f32 %2, %12, %14\n\tv_mul_f32 %3, %12, %15\n\t"
      "v_mul_f32 %4, %16, %18\n\tv_mul_f32 %5, %16, %19\n\tv_mul_f32 %6, %20, %22\n\tv_mul_f32 %7, %20, %23\n\t"
      "v_fma_f32 %0, -%9, %11, %0\n\tv_fma_f32 %1, %9, %10, %1\n\tv_fma_f32 %2, -%13, %15, %2\n\tv_fma_f32 %3, %13, %14, %3\n\t"
      "v_fma_f32 %4, -%17, %19, %4\n\tv_fma_f32 %5, %17, %18, %5\n\tv_fma_f32 %6, -%21, %23, %6\n\tv_fma_f32 %7, %21, %22, %7"
      : "=&v"(o0.r), "=&v"(o0.i), "=&v"(o1.r), "=&v"(o1.i), "=&v"(o2.r), "=&v"(o2.i), "=&v"(o3.r), "=&v"(o3.i)
      : "v"(a0.r), "v"(a0.i), "v"(b0.r), "v"(b0.i), "v"(a1.r), "v"(a1.i), "v"(b1.r), "v"(b1.i),
        "v"(a2.r), "v"(a2.i), "v"(b2.r), "v"(b2.i), "v"(a3.r), "v"(a3.i), "v"(b3.r), "v"(b3.i));
}
__device__ __forceinline__ void batch4(unsigned* h, unsigned* l, cf b, cf t0, cf t1, cf t2, cf t3) {
  float w0r, w0i, w1r, w1i, w2r, w2i, w3r, w3i; unsigned x0, x1, x2, x3;
  asm("v_mul_f32 %8, %20, %22\n\tv_mul_f32 %9, %20, %23\n\tv_mul_f32 %10, %20, %24\n\tv_mul_f32 %11, %20, %25\n\t"
      "v_mul_f32 %12, %20, %26\n\tv_mul_f32 %13, %20, %27\n\tv_mul_f32 %14, %20, %28\n\tv_mul_f32 %15, %20, %29\n\t"
      "v_fma_f32 %8, -%21, %23, %8\n\tv_fma_f32 %9, %21, %22, %9\n\tv_fma_f32 %10, -%21, %25, %10\n\tv_fma_f32 %11, %21, %24, %11\n\t"
      "v_fma_f32 %12, -%21, %27, %12\n\tv_fma_f32 %13, %21, %26, %13\n\tv_fma_f32 %14, -%21, %29, %14\n\tv_fma_f32 %15, %21, %28, %15\n\t"
      "v_cvt_pk_bf16_f32 %0, %8, %9\n\tv_cvt_pk_bf16_f32 %1, %10, %11\n\tv_cvt_pk_bf16_f32 %2, %12, %13\n\tv_cvt_pk_bf16_f32 %3, %14, %15\n\t"
      "v_lshlrev_b32 %4, 16, %0\n\tv_and_b32 %16, 0xffff0000, %0\n\tv_lshlrev_b32 %5, 16, %1\n\tv_and_b32 %17, 0xffff0000, %1\n\t"
      "v_lshlrev_b32 %6, 16, %2\n\tv_and_b32 %18, 0xffff0000, %2\n\tv_lshlrev_b32 %7, 16, %3\n\tv_and_b32 %19, 0xffff0000, %3\n\t"
      "v_sub_f32 %8, %8, %4\n\tv_sub_f32 %9, %9, %16\n\tv_sub_f32 %10, %10, %5\n\tv_sub_f32 %11, %11, %17\n\t"
      "v_sub_f32 %12, %12, %6\n\tv_sub_f32 %13, %13, %18\n\tv_sub_f32 %14, %14, %7\n\tv_sub_f32 %15, %15, %19\n\t"
      "v_cvt_pk_bf16_f32 %4, %8, %9\n\tv_cvt_pk_bf16_f32 %5, %10, %11\n\tv_cvt_pk_bf16_f32 %6, %12, %13\n\tv_cvt_pk_bf16_f32 %7, %14, %15"
      : "=&v"(h[0]), "=&v"(h[1]), "=&v"(h[2]), "=&v"(h[3]), "=&v"(l[0]), "=&v"(l[1]), "=&v"(l[2]), "=&v"(l[3]),
        "=&v"(w0r), "=&v"(w0i), "=&v"(w1r), "=&v"(w1i), "=&v"(w2r), "=&v"(w2i), "=&v"(w3r), "=&v"(w3i),
        "=&v"(x0), "=&v"(x1), "=&v"(x2), "=&v"(x3)
      : "v"(b.r), "v"(b.i), "v"(t0.r), "v"(t0.i), "v"(t1.r), "v"(t1.i), "v"(t2.r), "v"(t2.i), "v"(t3.r), "v"(t3.i));
}
constexpr int KG_RS = 288, KG_PL = 16 * KG_RS;
__device__ __forceinline__ void kgen_role(char* smem, const u16* __restrict__ logA, const u16* __restrict__ Aim,
                                          const u16* __restrict__ Cr, const u16* __restrict__ Ci, const u16* __restrict__ logdt,
                                          const u16* __restrict__ x, const u16* __restrict__ W, const u16* __restrict__ wo,
                                          char* __restrict__ ws, int blk) {
  const int tid = threadIdx.x, lane = tid & 63;
  const int wave = __builtin_amdgcn_readfirstlane(tid >> 6);
  constexpr int RS = KG_RS, PL = KG_PL;
  const int task = blk * 8 + wave;
  const int d = task / 3;
  const int pi = task * 64 + lane;
#define PF 1
  unsigned pfv = 0; const char* pp;
  {
    const int xcd = blk & 7, i = (blk >> 3) * 64 + lane;
    if (i < 1280) pp = (const char*)W + (size_t)(80 * wave + (i >> 4)) * 3840 + ((xcd & 1) ? 7 * 256 : 0) + (i & 15) * 128;
    else { const int id = wave * 640 + (i - 1280); pp = (const char*)x + (size_t)((xcd >> 1) * 512 + (id & 511)) * 1280 + (id >> 9) * 128; }
  }
  const int pfpt = wave == 0 ? 9 : (wave <= 2 ? 0 : (wave <= 5 ? 1 : 2));
#define PFLOAD(pt) do { __builtin_amdgcn_sched_barrier(0); if (pfpt == (pt)) asm volatile("global_load_dword %0, %1, off" : "+v"(pfv) : "v"(pp)); __builtin_amdgcn_sched_barrier(0); } while (0)
  PFLOAD(0);
  const int wid = min(blk * 216 + (tid < 216 ? tid : 215), 51199);
  u32x4 wv;
  {
    const int slot = wid % 160, f = wid / 160;
    const int ks = f % 10, kq = (f / 10) & 3, cb = f / 40;
    int m, h;
    if (slot < 128) { m = (slot >> 6) * 32 + (slot & 31); h = (slot >> 5) & 1; } else { m = 64 + (slot & 15); h = (slot >> 4) & 1; }
    const int n = 80 * cb + m;
    int k0, k1;
    if (ks < 8) { k0 = 20 * (8 * kq + ks) + 4 * h; k1 = k0 + 8; }
    else { k0 = 20 * (8 * kq + 4 * (ks - 8) + 2 * h) + 16; k1 = k0 + 20; }
    if (wave < 4) {
#pragma unroll
      for (int e = 0; e < 2; e++) {
        unsigned a0 = wo[(size_t)(k0 + 2 * e) * D + n], a1 = wo[(size_t)(k0 + 2 * e + 1) * D + n];
        unsigned b0 = wo[(size_t)(k1 + 2 * e) * D + n], b1 = wo[(size_t)(k1 + 2 * e + 1) * D + n];
        wv[e] = a0 | (a1 << 16); wv[2 + e] = b0 | (b1 << 16);
      }
    }
  }
  const float dt = rb(expf(b2f(logdt[d])));
  float ar;
  {
    const float la = b2f(logA[pi]);
    float h = la * 1.44269502f; float l = fmaf(la, 1.44269502f, -h) + la * 1.92596299e-8f;
    float e = __builtin_amdgcn_exp2f(h); e = fmaf(e, l * 0.69314718f, e);
    ar = -rb(e) * dt;
  }
  const float ai = b2f(Aim[pi]) * dt;
  cf C; C.r = 2.f * (b2f(Cr[pi]) * dt); C.i = 2.f * (b2f(Ci[pi]) * dt);
  const float th = ar * 1.44269502f, tl = fmaf(ar, 1.44269502f, -th) + ar * 1.92596299e-8f;
  const float rh = ai * 0.159154937f, rl = fmaf(ai, 0.159154937f, -rh) + ai * 6.42063833e-9f;
  char* tb = smem + wave * (2 * PL);
  char* wp = tb + lane * 4;
  const char* rp = tb + (lane & 15) * RS + (lane >> 4) * 16;
  STAMP(0, 2);
  PFLOAD(1);
  const cf one = {1.f, 0.f};
  cf S1 = zpow(th, tl, rh, rl, 1.f), Q1 = zpow(th, tl, rh, rl, 8.f), R1 = zpow(th, tl, rh, rl, 128.f);
  Q1.i = -Q1.i;
  cf S2, Q2, R2, G1, S3, S4, Q3, P1, R3, T1, G2, G3, S5, S6, S7, P2, CT1, T2, P3, CS4, CT2, CT3, CT4, dm0;
  cmul4(S2, Q2, R2, G1, S1, S1, Q1, Q1, R1, R1, C, R1);
  cmul4(S3, S4, Q3, P1, S2, S1, S2, S2, Q2, Q1, Q2, Q2);
  cmul4(R3, T1, G2, P2, R2, R1, R2, R2, C, R2, P1, P1);
  cmul4(S5, S6, S7, P3, S4, S1, S4, S2, S4, S3, P2, P1);
  cmul4(G3, CT1, T2, dm0, C, R3, C, T1, T1, T1, C, R1);
  cmul4(CS4, CT2, CT3, dm0, CT1, S4, C, T2, CT1, T2, C, R1);
  CT4 = cmul(CT2, T2);
  PFLOAD(2);
  auto rows4 = [&](int row, cf b, cf t0, cf t1, cf t2, cf t3) {
    unsigned h[4], l[4];
    batch4(h, l, b, t0, t1, t2, t3);
#pragma unroll
    for (int k = 0; k < 4; k++) { *(unsigned*)(wp + (row + k) * RS) = h[k]; *(unsigned*)(wp + PL + (row + k) * RS) = l[k]; }
  };
  rows4(0, one, one, Q1, Q2, Q3); rows4(4, P1, one, Q1, Q2, Q3); rows4(8, P2, one, Q1, Q2, Q3); rows4(12, P3, one, Q1, Q2, Q3);
  __builtin_amdgcn_wave_barrier();
  s16x8 bh[4], bl[4];
#pragma unroll
  for (int k = 0; k < 4; k++) { bh[k] = *(const s16x8*)(rp + k * 64); bl[k] = *(const s16x8*)(rp + PL + k * 64); }
  __builtin_amdgcn_wave_barrier();
  STAMP(0, 3);
  f32x4 res[3];
  auto mm = [&](int t) {
    __builtin_amdgcn_wave_barrier();
    f32x4 a = {0.f, 0.f, 0.f, 0.f}, a2 = {0.f, 0.f, 0.f, 0.f};
#pragma unroll
    for (int k = 0; k < 4; k++) {
      s16x8 ah = *(const s16x8*)(rp + k * 64), al = *(const s16x8*)(rp + PL + k * 64);
      a = __builtin_amdgcn_mfma_f32_16x16x32_bf16(ah, bh[k], a, 0, 0, 0);
      a2 = __builtin_amdgcn_mfma_f32_16x16x32_bf16(ah, bl[k], a2, 0, 0, 0);
      a2 = __builtin_amdgcn_mfma_f32_16x16x32_bf16(al, bh[k], a2, 0, 0, 0);
    }
    __builtin_amdgcn_wave_barrier();
    res[t] = a + a2;
  };
  rows4(0, C, one, S1, S2, S3); rows4(4, C, S4, S5, S6, S7); rows4(8, G1, one, S1, S2, S3); rows4(12, G1, S4, S5, S6, S7);
  mm(0);
  STAMP(0, 4);
  rows4(0, G2, one, S2, S4, S6); rows4(4, G3, one, S2, S4, S6);
  {
    unsigned h[4], l[4];
    batch4(h, l, CT1, one, R1, R2, R3);
#pragma unroll
    for (int k = 0; k < 4; k++) { *(unsigned*)(wp + (8 + 2 * k) * RS) = h[k]; *(unsigned*)(wp + PL + (8 + 2 * k) * RS) = l[k]; }
    batch4(h, l, CS4, one, R1, R2, R3);
#pragma unroll
    for (int k = 0; k < 4; k++) { *(unsigned*)(wp + (9 + 2 * k) * RS) = h[k]; *(unsigned*)(wp + PL + (9 + 2 * k) * RS) = l[k]; }
  }
  mm(1);
  rows4(0, CT2, one, R1, R2, R3); rows4(4, CT3, one, R1, R2, R3); rows4(8, CT4, one, one, one, one);
  mm(2);
#pragma unroll
  for (int t = 0; t < 3; t++) *(f32x4*)(tb + t * 1024 + lane * 16) = res[t];
  if (tid < 216 && blk * 216 + tid < 51200) st16(ws + WS_WOF + (size_t)wid * 16, wv);
  __syncthreads();
  {
    const int d0 = (blk * 8) / 3;
#pragma unroll
    for (int it = 0; it < 2; it++) {
      const int e = tid + 512 * it;
      const int seg = e / 192, ent = e - 192 * seg;
      const int ds = d0 + seg;
      const int wlo = max(0, 3 * ds - 8 * blk), whi = min(8, 3 * ds + 3 - 8 * blk);
      if (seg < 4 && wlo < 8) {
        f32x4 a = *(const f32x4*)(smem + wlo * (2 * PL) + ent * 16);
        for (int w = wlo + 1; w < whi; w++) a += *(const f32x4*)(smem + w * (2 * PL) + ent * 16);
        st16(ws + WS_KIMG + (size_t)ds * 6144 + (3 * ds < 8 * blk ? 3072 : 0) + ent * 16, __builtin_bit_cast(u32x4, a));
      }
    }
  }
  asm volatile("s_waitcnt vmcnt(0)" :: "v"(pfv) : "memory");
}

__global__ __launch_bounds__(512, 4) void k1(const u16* __restrict__ x, const u16* __restrict__ logA, const u16* __restrict__ Aim,
                                          const u16* __restrict__ Cr, const u16* __restrict__ Ci, const u16* __restrict__ logdt,
                                          const u16* __restrict__ wqkv, const u16* __restrict__ gw, const u16* __restrict__ wo,
                                          char* __restrict__ ws) {
  __shared__ __attribute__((aligned(16))) char smem[K1_LDS];
  int b = blockIdx.x;
  STAMP(0, 0);
  if (b < NB_GEMM) {
    const int xcd = b & 7, idx = b >> 3;
    const int ti = (xcd & 1) * 30 + idx;
    const int rowblk = (xcd >> 1) * 4 + (ti & 3), cb = ti >> 2;
    if (cb < 5) gemm_role<true>(smem, x, wqkv, ws, rowblk, cb);
    else        gemm_role<false>(smem, x, wqkv, ws, rowblk, cb);
    STAMP(0, 1);
    return;
  }
  b -= NB_GEMM;
  if (b < NB_KG) { kgen_role(smem, logA, Aim, Cr, Ci, logdt, x, wqkv, wo, ws, b); STAMP(0, 1); return; }
  b -= NB_KG;
  tr_role(smem, x, gw, (u16*)(ws + WS_UT), (float*)(ws + WS_GATE), b);
  STAMP(0, 1);
}

constexpr int K2_CS = 4160;
constexpr int K2_KOFF = 8 * K2_CS;
constexpr int K2_TAB = K2_KOFF + 2 * 4608;
constexpr int K2_LDS = K2_TAB + 3072;
constexpr int NB_KV = 32, NB_WO = 0;
__global__ __launch_bounds__(256, 3) void k2(const u16* __restrict__ Dskip, char* __restrict__ ws) {
  __shared__ __attribute__((aligned(16))) char smem[K2_LDS];
  const int tid = threadIdx.x, lane = tid & 63;
  const int wave = __builtin_amdgcn_readfirstlane(tid >> 6);
  STAMP(1, 0);
  if (blockIdx.x < NB_KV) {
    const int hd = blockIdx.x;
    const int c = lane & 31, h = lane >> 5;
    const int cc = c < 20 ? c : 19;
    const char* pk = ws + WS_PART + ((size_t)(wave * 16 * 4 + h) * 1280 + 20 * hd + cc) * 16;
    const char* pv = pk + 640 * 16;
    f32x16 acc;
#pragma unroll
    for (int i = 0; i < 16; i++) acc[i] = 0.f;
#pragma unroll
    for (int bt = 0; bt < 2; bt++) {
      u32x4 a[16], v[16];
#pragma unroll
      for (int i = 0; i < 16; i++) {
        a[i] = *(const u32x4*)(pk + (size_t)(bt * 16 + i) * (2 * 1280 * 16));
        v[i] = *(const u32x4*)(pv + (size_t)(bt * 16 + i) * (2 * 1280 * 16));
      }
      __builtin_amdgcn_sched_barrier(0);
#pragma unroll
      for (int i = 0; i < 16; i++) {
        u32x4 vv = v[i];
        if (c == 20) vv = (u32x4){0x3F803F80u, 0x3F803F80u, 0x3F803F80u, 0x3F803F80u};
        acc = __builtin_amdgcn_mfma_f32_32x32x16_bf16(__builtin_bit_cast(s16x8, a[i]), __builtin_bit_cast(s16x8, vv), acc, 0, 0, 0);
      }
    }
    float* red = (float*)smem;
#pragma unroll
    for (int q = 0; q < 3; q++)
#pragma unroll
      for (int e = 0; e < 4; e++) red[(wave * 32 + 8 * q + 4 * h + e) * 33 + c] = acc[4 * q + e];
    __syncthreads();
    float* fin = red + 4 * 32 * 33;
    for (int i = tid; i < 420; i += 256) {
      int dk = i / 21, dv = i - 21 * dk;
      float s = (red[dk * 33 + dv] + red[(32 + dk) * 33 + dv]) + (red[(64 + dk) * 33 + dv] + red[(96 + dk) * 33 + dv]);
      fin[i] = rb(s);
    }
    __syncthreads();
    if (tid < 96) {
      const int m = lane & 31;
      int col = m < 16 ? m : ((m >> 2) == ((hd & 2) ? 5 : 4) ? 16 + (m & 3) : 20);
      if (tid < 64) {
        u32x4 o;
#pragma unroll
        for (int e = 0; e < 4; e++) o[e] = pack2(fin[(8 * h + 2 * e) * 21 + col], fin[(8 * h + 2 * e + 1) * 21 + col]);
        *(u32x4*)(ws + WS_KVT + (size_t)hd * 1280 + lane * 16) = o;
      } else {
        u32x2 o;
#pragma unroll
        for (int e = 0; e < 2; e++) o[e] = pack2(fin[(16 + 2 * e) * 21 + col], fin[(17 + 2 * e) * 21 + col]);
        *(u32x2*)(ws + WS_KVT + (size_t)hd * 1280 + 1024 + m * 8) = o;
      }
    }
    STAMP(1, 1);
    return;
  }
  const int d = blockIdx.x - (NB_KV + NB_WO);
  const u16* uT = (const u16*)(ws + WS_UT) + (size_t)d * UT_STRIDE;
  {
    const f32x4* ksrc = (const f32x4*)(ws + WS_KIMG + (size_t)d * 6144) + (tid < 192 ? tid : 191);
    const u32x4* usrc = (const u32x4*)uT;
    f32x4 p0 = ksrc[0];
    if ((d & 7) == 2 || (d & 7) == 5) p0 += ksrc[192];
    u32x4 c0 = usrc[tid + 1], c1 = usrc[tid + 2];
    u32x4 e0, e1;
    if (tid < 2) { e0 = usrc[tid + 257]; e1 = usrc[tid + 258]; }
    if (tid < 64) *(u32x4*)(smem + K2_KOFF + (tid >> 5) * 4608 + (tid & 31) * 16) = (u32x4){0u, 0u, 0u, 0u};
    if (tid < 192) *(f32x4*)(smem + K2_TAB + tid * 16) = p0;
    auto put_copies = [&](u32x4 a, u32x4 b, int xch) {
      unsigned w[8] = {a[0], a[1], a[2], a[3], b[0], b[1], b[2], b[3]};
      unsigned o[7];
#pragma unroll
      for (int k = 0; k < 7; k++) o[k] = __builtin_amdgcn_alignbyte(w[k + 1], w[k], 2);
      char* p = smem + xch * 16;
#pragma unroll
      for (int q = 0; q < 4; q++) {
        *(u32x4*)(p + (2 * q) * K2_CS) = (u32x4){o[q], o[q + 1], o[q + 2], o[q + 3]};
        *(u32x4*)(p + (2 * q + 1) * K2_CS) = (u32x4){w[q + 1], w[q + 2], w[q + 3], w[q + 4]};
      }
    };
    put_copies(c0, c1, tid);
    if (tid < 2) put_copies(e0, e1, tid + 256);
  }
  __syncthreads();
  {
    const float* tab = (const float*)(smem + K2_TAB);
    float kv[8];
#pragma unroll
    for (int e = 0; e < 8; e++) {
      const int j = tid * 8 + 7 - e;
      const int p = (int)rb((float)j);
      const int h = p >> 7, c = (p >> 3) & 15;
      int sh = 31 - __builtin_clz(h | 1); sh = sh > 3 ? 3 : sh;
      const int row = 8 * sh + (((h << 3) | (p & 7)) >> sh);
      kv[e] = tab[((row >> 2) * 16 + c) * 4 + (row & 3)];
    }
    u32x4 hi, lo;
#pragma unroll
    for (int e = 0; e < 4; e++) {
      hi[e] = pack2(kv[2 * e], kv[2 * e + 1]);
      lo[e] = pack2(ssub(kv[2 * e], lo_f(hi[e])), ssub(kv[2 * e + 1], hi_f(hi[e])));
    }
    *(u32x4*)(smem + K2_KOFF + 512 + tid * 16) = hi;
    *(u32x4*)(smem + K2_KOFF + 4608 + 512 + tid * 16) = lo;
  }
  __syncthreads();
  STAMP(1, 2);
  f32x4 acc[8];
#pragma unroll
  for (int t = 0; t < 8; t++) acc[t] = (f32x4){0.f, 0.f, 0.f, 0.f};
  {
    const int i = lane & 15, kg = lane >> 4, e = kg >> 1, bb = kg & 1;
    const char* ap = smem + (i & 7) * K2_CS + (2 * e - bb + 2 + (i >> 3)) * 16;
    const char* bp = smem + K2_KOFF + (16 + i - e) * 32 + bb * 16;
#pragma unroll
    for (int it = 0; it < 2; it++) {
      const int d0 = 2 * (wave + 4 * it);
      s16x8 af[8], bh[8], bl[8];
#pragma unroll
      for (int j = 0; j < 8; j++) af[j] = *(const s16x8*)(ap + (32 * j + 2 * d0) * 16);
#pragma unroll
      for (int m = 0; m < 8; m++) {
        bh[m] = *(const s16x8*)(bp + (16 * m - d0) * 32);
        bl[m] = *(const s16x8*)(bp + (16 * m - d0) * 32 + 4608);
      }
#pragma unroll
      for (int m = 0; m < 8; m++) {
#pragma unroll
        for (int j = 0; j + m < 8; j++) acc[j + m] = __builtin_amdgcn_mfma_f32_16x16x32_bf16(af[j], bh[m], acc[j + m], 0, 0, 0);
#pragma unroll
        for (int j = 0; j + m < 8; j++) acc[j + m] = __builtin_amdgcn_mfma_f32_16x16x32_bf16(af[j], bl[m], acc[j + m], 0, 0, 0);
      }
    }
  }
  STAMP(1, 3);
  u32x2 uu[2];
#pragma unroll
  for (int t = 0; t < 2; t++) uu[t] = *(const u32x2*)(uT + UT_PAD + 256 * (2 * wave + t) + 4 * lane);
  const float dsk = b2f(Dskip[d]);
  __syncthreads();
  float* red = (float*)smem;
#pragma unroll
  for (int t = 0; t < 8; t++) *(f32x4*)(red + ((wave * 8 + t) * 64 + (lane & 15) * 4 + (lane >> 4)) * 4) = acc[t];
  __syncthreads();
  u16* aT = (u16*)(ws + WS_AT) + (size_t)d * L;
#pragma unroll
  for (int t = 0; t < 2; t++) {
    const int tt = 2 * wave + t;
    f32x4 s = *(const f32x4*)(red + ((0 * 8 + tt) * 64 + lane) * 4);
    f32x4 s1 = *(const f32x4*)(red + ((1 * 8 + tt) * 64 + lane) * 4);
    f32x4 s2 = *(const f32x4*)(red + ((2 * 8 + tt) * 64 + lane) * 4);
    f32x4 s3 = *(const f32x4*)(red + ((3 * 8 + tt) * 64 + lane) * 4);
    s = (s + s1) + (s2 + s3);
    u32x2 ov;
#pragma unroll
    for (int e = 0; e < 2; e++) {
      unsigned y = pack2(s[2 * e], s[2 * e + 1]);
      unsigned k = pack2(lo_f(uu[t][e]) * dsk, hi_f(uu[t][e]) * dsk);
      ov[e] = pack2(lo_f(y) + lo_f(k), hi_f(y) + hi_f(k));
    }
    *(u32x2*)(aT + 256 * tt + 4 * lane) = ov;
  }
  STAMP(1, 1);
}

constexpr int K3_WO = 0;
constexpr int K3_KV = 102400;
constexpr int K3_AT = K3_KV + 40960;
constexpr int K3_LDS = K3_AT + 80 * 144;
__global__ __launch_bounds__(512, 2) void k3(char* __restrict__ ws, u16* __restrict__ out) {
  __shared__ __attribute__((aligned(16))) char smem[K3_LDS];
  const int tid = threadIdx.x, lane = tid & 63;
  const int wave = __builtin_amdgcn_readfirstlane(tid >> 6);
  STAMP(2, 0);
  const int xcd = blockIdx.x & 7, idx = blockIdx.x >> 3;
  const int rowblk = xcd * 4 + (idx >> 3), cb = idx & 7;
  const int l0 = rowblk * 64, c0 = cb * 80;
  const int kq = wave & 3, t = wave >> 2;
  const int r = lane & 31, h = lane >> 5;
#pragma unroll
  for (int i = 0; i < 5; i++) glds16(ws + WS_KVT + (size_t)(wave * 5 + i) * 1024 + lane * 16, smem + K3_KV + (wave * 5 + i) * 1024);
  u32x4 pf0[8]; u32x2 pf1[8];
  {
    const char* p0 = ws + WS_PQ + ((size_t)((rowblk * 32 + kq * 8) * 2 + t) * 64 + lane) * 16;
    const char* p1 = ws + WS_PQ + (size_t)L * 16 * 32 * 2 + ((size_t)((rowblk * 32 + kq * 8) * 2 + t) * 32 + r) * 8;
#pragma unroll
    for (int hh = 0; hh < 8; hh++) {
      asm volatile("global_load_dwordx4 %0, %1, off offset:%2" : "=v"(pf0[hh]) : "v"(p0 + (hh >> 1) * 4096), "i"((hh & 1) * 2048));
      asm volatile("global_load_dwordx2 %0, %1, off offset:%2" : "=v"(pf1[hh]) : "v"(p1), "i"(hh * 512));
    }
  }
  {
    const char* src = ws + WS_WOF + (size_t)cb * 102400 + lane * 16;
#pragma unroll
    for (int i = 0; i < 13; i++) { int p = i * 8 + wave; if (p < 100) glds16(src + p * 1024, smem + K3_WO + p * 1024); }
  }
  u32x4 av[2];
  {
    const char* aT = ws + WS_AT;
#pragma unroll
    for (int i = 0; i < 2; i++) {
      int id = tid + 512 * i; if (id > 639) id = 639;
      const char* p = aT + ((size_t)(c0 + (id >> 3)) * L + l0 + 8 * (id & 7)) * 2;
      asm volatile("global_load_dwordx4 %0, %1, off" : "=v"(av[i]) : "v"(p));
    }
  }
  float gt[2];
  {
    const float* gate = (const float*)(ws + WS_GATE) + l0;
#pragma unroll
    for (int i = 0; i < 2; i++) {
      int id = tid + 512 * i; if (id > 639) id = 639;
      asm volatile("global_load_dword %0, %1, off" : "=v"(gt[i]) : "v"(gate + id / 10));
    }
  }
  if (wave < 4) VMCNT(17); else VMCNT(16);
  asm volatile("" : "+v"(pf0[0]), "+v"(pf0[1]), "+v"(pf0[2]), "+v"(pf0[3]), "+v"(pf0[4]), "+v"(pf0[5]), "+v"(pf0[6]), "+v"(pf0[7]));
  asm volatile("" : "+v"(pf1[0]), "+v"(pf1[1]), "+v"(pf1[2]), "+v"(pf1[3]), "+v"(pf1[4]), "+v"(pf1[5]), "+v"(pf1[6]), "+v"(pf1[7]));
  RAW_BARRIER();
  STAMP(2, 2);
  u32x4 ob[10];
  {
    unsigned rem[8][2];
#pragma unroll
    for (int hh = 0; hh < 8; hh++) {
      const char* kp = smem + K3_KV + (kq * 8 + hh) * 1280;
      s16x8 a0 = *(const s16x8*)(kp + lane * 16);
      u32x2 a1h = *(const u32x2*)(kp + 1024 + r * 8);
      u32x4 a1u = {a1h[0], a1h[1], 0u, 0u};
      u32x4 b1u = {h ? 0u : pf1[hh][0], h ? 0u : pf1[hh][1], 0u, 0u};
      f32x16 c;
#pragma unroll
      for (int i = 0; i < 16; i++) c[i] = 0.f;
      c = __builtin_amdgcn_mfma_f32_32x32x16_bf16(a0, __builtin_bit_cast(s16x8, pf0[hh]), c, 0, 0, 0);
      c = __builtin_amdgcn_mfma_f32_32x32x16_bf16(__builtin_bit_cast(s16x8, a1u), __builtin_bit_cast(s16x8, b1u), c, 0, 0, 0);
      float den = rb(rb(c[12]) + 1e-6f);
      float inv = __builtin_amdgcn_rcpf(den);
      unsigned o[6];
#pragma unroll
      for (int q = 0; q < 6; q++) {
        unsigned n0 = pack2(c[2 * q], c[2 * q + 1]);
        o[q] = pack2(lo_f(n0) * inv, hi_f(n0) * inv);
      }
      ob[hh] = (u32x4){o[0], o[1], o[2], o[3]};
      rem[hh][0] = o[4]; rem[hh][1] = o[5];
    }
#pragma unroll
    for (int G = 0; G < 2; G++)
      ob[8 + G] = (u32x4){h ? rem[4 * G + 2][0] : rem[4 * G][0], h ? rem[4 * G + 2][1] : rem[4 * G][1],
                          h ? rem[4 * G + 3][0] : rem[4 * G + 1][0], h ? rem[4 * G + 3][1] : rem[4 * G + 1][1]};
  }
  STAMP(2, 3);
  VMCNT(0);
  asm volatile("" : "+v"(av[0]), "+v"(av[1]), "+v"(gt[0]), "+v"(gt[1]));
#pragma unroll
  for (int i = 0; i < 2; i++) { int id = tid + 512 * i; if (id < 640) *(u32x4*)(smem + K3_AT + (id >> 3) * 144 + (id & 7) * 16) = av[i]; }
  RAW_BARRIER();
  STAMP(2, 4);
  f32x16 acc[3];
#pragma unroll
  for (int c = 0; c < 3; c++)
#pragma unroll
    for (int i = 0; i < 16; i++) acc[c][i] = 0.f;
  {
    const char* wp = smem + K3_WO + kq * 25600;
    const int o2 = 2048 + (h * 16 + (lane & 15)) * 16;
#pragma unroll
    for (int ks = 0; ks < 10; ks++) {
      s16x8 w0 = *(const s16x8*)(wp + ks * 2560 + lane * 16);
      s16x8 w1 = *(const s16x8*)(wp + ks * 2560 + 1024 + lane * 16);
      s16x8 w2 = *(const s16x8*)(wp + ks * 2560 + o2);
      s16x8 b = __builtin_bit_cast(s16x8, ob[ks]);
      acc[0] = __builtin_amdgcn_mfma_f32_32x32x16_bf16(w0, b, acc[0], 0, 0, 0);
      acc[1] = __builtin_amdgcn_mfma_f32_32x32x16_bf16(w1, b, acc[1], 0, 0, 0);
      acc[2] = __builtin_amdgcn_mfma_f32_32x32x16_bf16(w2, b, acc[2], 0, 0, 0);
    }
  }
  STAMP(2, 5);
  RAW_BARRIER();
  {
    float* pr = (float*)smem + (size_t)(kq * 64 + 32 * t + r) * 84 + 4 * h;
#pragma unroll
    for (int c = 0; c < 3; c++)
#pragma unroll
      for (int q = 0; q < 4; q++)
        if (c < 2 || q < 2) *(f32x4*)(pr + 32 * c + 8 * q) = (f32x4){acc[c][4 * q], acc[c][4 * q + 1], acc[c][4 * q + 2], acc[c][4 * q + 3]};
  }
  RAW_BARRIER();
  {
#pragma unroll
    for (int i2 = 0; i2 < 2; i2++) {
      const int i = tid + 512 * i2;
      if (i < 640) {
        const int rr = i / 10, oc = i - 10 * rr;
        const float g = gt[i2]; const float g1 = rb(1.f - g);
        f32x4 s0 = {0.f, 0.f, 0.f, 0.f}, s1 = {0.f, 0.f, 0.f, 0.f};
#pragma unroll
        for (int w = 0; w < 4; w++) {
          const float* pr = (const float*)smem + (size_t)(w * 64 + rr) * 84 + oc * 8;
          s0 += *(const f32x4*)pr; s1 += *(const f32x4*)(pr + 4);
        }
        u32x4 ov;
#pragma unroll
        for (int e = 0; e < 4; e++) {
          unsigned bb = e < 2 ? pack2(s0[2 * e], s0[2 * e + 1]) : pack2(s1[2 * e - 4], s1[2 * e - 3]);
          float a0 = b2f(*(const u16*)(smem + K3_AT + (oc * 8 + 2 * e) * 144 + rr * 2));
          float a1 = b2f(*(const u16*)(smem + K3_AT + (oc * 8 + 2 * e + 1) * 144 + rr * 2));
          unsigned ga = pack2(g * a0, g * a1), gb = pack2(g1 * lo_f(bb), g1 * hi_f(bb));
          ov[e] = pack2(lo_f(ga) + lo_f(gb), hi_f(ga) + hi_f(gb));
        }
        *(u32x4*)(out + (size_t)(l0 + rr) * D + c0 + oc * 8) = ov;
      }
    }
  }
  STAMP(2, 1);
}

extern "C" void kernel_launch(void* const* d_in, const int* in_sizes, int n_in, void* d_out, int out_size, void* d_ws, size_t ws_size,
                              hipStream_t stream) {
  const u16* u = (const u16*)d_in[0]; const u16* logA = (const u16*)d_in[1]; const u16* Aim = (const u16*)d_in[2];
  const u16* Cr = (const u16*)d_in[3]; const u16* Ci = (const u16*)d_in[4]; const u16* logdt = (const u16*)d_in[5];
  const u16* Dsk = (const u16*)d_in[6]; const u16* wqkv = (const u16*)d_in[8]; const u16* wo = (const u16*)d_in[9];
  const u16* gw = (const u16*)d_in[10];
  char* ws = (char*)d_ws;
  k1<<<NB_GEMM + NB_TR + NB_KG, 512, 0, stream>>>(u, logA, Aim, Cr, Ci, logdt, wqkv, gw, wo, ws);
  k2<<<NB_KV + NB_WO + D, 256, 0, stream>>>(Dsk, ws);
  k3<<<256, 512, 0, stream>>>(ws, (u16*)d_out);
}
```

```cpp
#include <hip/hip_runtime.h>
#include <hip/hip_bf16.h>
#include <stdint.h>

typedef unsigned short u16;
typedef __attribute__((ext_vector_type(8))) short s16x8;
typedef __attribute__((ext_vector_type(4))) short s16x4;
typedef __attribute__((ext_vector_type(4))) float f32x4;
typedef __attribute__((ext_vector_type(16))) float f32x16;
typedef __attribute__((ext_vector_type(4))) unsigned u32x4;
typedef __attribute__((ext_vector_type(2))) unsigned u32x2;
typedef __attribute__((address_space(3))) s16x4* lds_s16x4_p;
typedef const __attribute__((address_space(1))) void* gptr_t;
typedef __attribute__((address_space(3))) void* lptr_t;

constexpr int L = 2048, D = 640, NS = 192, NH = 32;
constexpr int UT_PAD = 32, UT_STRIDE = L + UT_PAD;
constexpr size_t WS_UT   = 0;
constexpr size_t WS_KIMG = WS_UT + (size_t)D * UT_STRIDE * 2;
constexpr size_t WS_PQ   = WS_KIMG + (size_t)D * 6144;
constexpr size_t WS_PART = WS_PQ + (size_t)L * D * 2;
constexpr size_t WS_GATE = WS_PART + (size_t)32 * 16 * 1680;
constexpr size_t WS_WOF  = WS_GATE + (size_t)L * 4;
constexpr size_t WS_KVT  = WS_WOF + (size_t)D * D * 2;
constexpr size_t WS_AT   = WS_KVT + (size_t)32 * 1280;
constexpr size_t WS_END  = WS_AT + (size_t)D * L * 2;
#define STAMP(kid, slot) do {} while (0)

__device__ __forceinline__ float b2f(u16 v) { return __uint_as_float(((unsigned)v) << 16); }
typedef __bf16 bf16x2_t __attribute__((ext_vector_type(2)));
typedef float f32x2 __attribute__((ext_vector_type(2)));
__device__ __forceinline__ unsigned pack2(float a, float b) { f32x2 v = {a, b}; bf16x2_t r = __builtin_convertvector(v, bf16x2_t); return __builtin_bit_cast(unsigned, r); }
__device__ __forceinline__ float lo_f(unsigned u) { return __uint_as_float(u << 16); }
__device__ __forceinline__ float hi_f(unsigned u) { return __uint_as_float(u & 0xffff0000u); }
__device__ __forceinline__ float rb(float f) { return lo_f(pack2(f, f)); }
__device__ __forceinline__ unsigned phi2(float a, float b) {
  unsigned t = pack2(a, b); float t0 = lo_f(t), t1 = hi_f(t);
  float r0 = t0 > 0.f ? t0 + 1.f : __expf(t0), r1 = t1 > 0.f ? t1 + 1.f : __expf(t1);
  unsigned r = pack2(r0, r1);
  return pack2(lo_f(r) + 1e-6f, hi_f(r) + 1e-6f);
}
#define VMCNT(n) asm volatile("s_waitcnt vmcnt(" #n ")" ::: "memory")
#define RAW_BARRIER() do { asm volatile("s_waitcnt lgkmcnt(0)" ::: "memory"); __builtin_amdgcn_s_barrier(); asm volatile("" ::: "memory"); } while (0)
__device__ __forceinline__ void glds16(const void* g, void* l) { __builtin_amdgcn_global_load_lds((gptr_t)g, (lptr_t)l, 16, 0, 0); }

#define OPQ_S(v) asm volatile("" : "+s"(v))
struct cf { float r, i; };
__device__ __forceinline__ float smul(float a, float b) { float o; asm("v_mul_f32 %0, %1, %2" : "=v"(o) : "v"(a), "v"(b)); return o; }
__device__ __forceinline__ float sfma(float a, float b, float c) { float o; asm("v_fma_f32 %0, %1, %2, %3" : "=v"(o) : "v"(a), "v"(b), "v"(c)); return o; }
__device__ __forceinline__ float sfnma(float a, float b, float c) { float o; asm("v_fma_f32 %0, -%1, %2, %3" : "=v"(o) : "v"(a), "v"(b), "v"(c)); return o; }
__device__ __forceinline__ float ssub(float a, float b) { float o; asm("v_sub_f32 %0, %1, %2" : "=v"(o) : "v"(a), "v"(b)); return o; }
__device__ __forceinline__ cf cmul(cf a, cf b) { cf o; o.r = sfnma(a.i, b.i, smul(a.r, b.r)); o.i = sfma(a.i, b.r, smul(a.r, b.i)); return o; }

constexpr int K1_LDS = 81920;
constexpr int NB_GEMM = 256, NB_GATE = 16, NB_KG = 240;
#define WT 1
__device__ __forceinline__ void st16(void* p, u32x4 v) {
  asm volatile("global_store_dwordx4 %0, %1, off sc1\n\ts_nop 2" :: "v"(p), "v"(v) : "memory");
}

#define BAR_ONLY() do { asm volatile("" ::: "memory"); __builtin_amdgcn_s_barrier(); asm volatile("" ::: "memory"); } while (0)
#define SB() __builtin_amdgcn_sched_barrier(0)
template <bool ISQ>
__device__ __forceinline__ void gemm_role(char* smem, const u16* __restrict__ x, const u16* __restrict__ W, char* __restrict__ ws,
                                          int rowblk, int cb) {
  const int tid = threadIdx.x, lane = tid & 63;
  const int wave = __builtin_amdgcn_readfirstlane(tid >> 6);
  const int row0 = rowblk * 128, col0 = cb * 128;
  const int kvj = cb - 5;
  char* sB = smem; char* sA = smem + 32768;
  const int wr = wave & 1, wc = (wave >> 1) & 1, hf = wave >> 2;
  f32x4 ep[2][4];
  if (wave >= 4) {
    const int lw = wave - 4;
    const char* pA = (const char*)x + (size_t)(row0 + 32 * lw + (lane >> 3)) * 1280 + (((lane & 7) ^ (lane >> 3)) << 4);
    const int fB = (lane >> 4) | ((lw & 1) << 2);
    const int c16 = (((lane & 15) >> 1) ^ fB) * 2 + (lane & 1);
    int bcol = col0 + 8 * c16;
    if (!ISQ) bcol = min(640 + 640 * (c16 >> 3) + ((60 * kvj) & ~7) + 8 * (c16 & 7), 1912);
    const char* pB = (const char*)W + (size_t)(8 * lw + (lane >> 4)) * 3840 + bcol * 2;
    u32x2 tr[8];
    int tro;
    {
      const int fr = lane & 15, g = lane >> 4, p4 = lane & 3, r = 8 * g + (fr >> 2);
      tro = 32768 + r * 128 + (((2 * lw + (p4 >> 1)) ^ (r & 7)) << 4) + (p4 & 1) * 8;
    }
    auto issA = [&](int slot_off, int h) {
      glds16(pA + (16 * h) * 1280, sA + slot_off + (4 * lw + 2 * h) * 1024);
      glds16(pA + (16 * h + 8) * 1280, sA + slot_off + (4 * lw + 2 * h + 1) * 1024);
    };
    auto issB = [&](int slot_off) {
      glds16(pB, sB + slot_off + (2 * lw) * 1024);
      glds16(pB + 4 * 3840, sB + slot_off + (2 * lw + 1) * 1024);
      pB += (size_t)32 * 3840;
    };
    issA(0, 0); issB(0); issA(0, 1); pA += 128;
    issB(8192); issA(16384, 0); issB(16384); issA(16384, 1); pA += 128;
    VMCNT(8);
    BAR_ONLY();
    int ao = 32768, bo = 24576;
#pragma unroll 1
    for (int j = 0; j < 10; j++) {
      if (j < 9) VMCNT(6); else VMCNT(0);
      BAR_ONLY();
      if (j == cb) {
        const int a0 = (int)(size_t)(lptr_t)smem + (ao == 32768 ? 0 : ao + 16384);
        const int a = a0 + tro, a2 = a0 + (tro ^ 64) + 512;
        asm volatile("ds_read_b64_tr_b16 %0, %8\n\tds_read_b64_tr_b16 %1, %9\n\tds_read_b64_tr_b16 %2, %8 offset:4096\n\tds_read_b64_tr_b16 %3, %9 offset:4096\n\t"
                     "ds_read_b64_tr_b16 %4, %8 offset:8192\n\tds_read_b64_tr_b16 %5, %9 offset:8192\n\tds_read_b64_tr_b16 %6, %8 offset:12288\n\tds_read_b64_tr_b16 %7, %9 offset:12288\n\t"
                     "s_waitcnt lgkmcnt(0)"
                     : "=&v"(tr[0]), "=&v"(tr[1]), "=&v"(tr[2]), "=&v"(tr[3]), "=&v"(tr[4]), "=&v"(tr[5]), "=&v"(tr[6]), "=&v"(tr[7])
                     : "v"(a), "v"(a2) : "memory");
      }
      if (j < 9) { issB(bo); bo = (bo + 8192) & 32767; }
      if (j < 8) issA(ao, 0);
      if (j < 8) VMCNT(4); else VMCNT(2);
      BAR_ONLY();
      if (j < 8) { issB(bo); bo = (bo + 8192) & 32767; issA(ao, 1); pA += 128; }
      ao = ao == 32768 ? 0 : ao + 16384;
    }
    if (cb < 10) {
      u16* dst = (u16*)(ws + WS_UT) + (size_t)(64 * cb + 16 * lw + (lane & 15)) * UT_STRIDE + 8 * (lane >> 4);
#pragma unroll
      for (int i = 0; i < 4; i++) st16(dst + UT_PAD + row0 + 32 * i, (u32x4){tr[2 * i][0], tr[2 * i][1], tr[2 * i + 1][0], tr[2 * i + 1][1]});
      if (rowblk == 0) st16(dst, (u32x4){0u, 0u, 0u, 0u});
    }
    BAR_ONLY();
    char* xch = smem + (wave & 3) * 8192 + lane * 16;
#pragma unroll
    for (int i = 0; i < 8; i++) ep[i >> 2][i & 3] = *(const f32x4*)(xch + i * 1024);
  } else {
    const int g = lane >> 4, fr = lane & 15;
    f32x4 acc[4][4];
#pragma unroll
    for (int m = 0; m < 4; m++)
#pragma unroll
      for (int c = 0; c < 4; c++) acc[m][c] = (f32x4){0.f, 0.f, 0.f, 0.f};
    const int q4 = fr >> 2, p4 = lane & 3;
    int aoff[2];
#pragma unroll
    for (int ks = 0; ks < 2; ks++) aoff[ks] = (wr * 64 + fr) * 128 + (((4 * ks + g) ^ (fr & 7)) << 4);
    int boff[4];
    {
      const int f = q4 | ((g & 1) << 2);
#pragma unroll
      for (int c = 0; c < 4; c++) boff[c] = (8 * g + q4) * 256 + (((4 * wc + c) ^ f) << 5) + p4 * 8;
    }
    s16x8 af[2][4]; s16x4 bl[4], bh[4];
    auto rdA = [&](int buf, int m, const char* pa) { af[buf][m] = *(const s16x8*)(pa + m * 2048); };
    auto rdBl = [&](int c, const char* pb) { bl[c] = __builtin_amdgcn_ds_read_tr16_b64_v4i16((lds_s16x4_p)(pb + boff[c])); };
    auto rdBh = [&](int c, const char* pb) { bh[c] = __builtin_amdgcn_ds_read_tr16_b64_v4i16((lds_s16x4_p)(pb + boff[c] + 1024)); };
    BAR_ONLY();
    STAMP(0, 2);
#pragma unroll
    for (int c = 0; c < 4; c++) { rdA(0, c, sA + aoff[0]); rdBl(c, sB); rdBh(c, sB); }
    int ao0 = 0, ao1 = 16384;
#pragma unroll 1
    for (int j2 = 0; j2 < 5; j2++) {
#pragma unroll
      for (int st = 0; st < 4; st++) {
        const int hs = st & 1;
        BAR_ONLY();
        SB();
        const char* pa = sA + (hs == 0 ? ao0 + aoff[1] : ao1 + aoff[0]);
        const char* pb = sB + ((st + 1) & 3) * 8192;
#pragma unroll
        for (int c = 0; c < 4; c++) {
          const s16x8 bf = __builtin_shufflevector(bl[c], bh[c], 0, 1, 2, 3, 4, 5, 6, 7);
#pragma unroll
          for (int m = 0; m < 4; m++) {
            if (ISQ) acc[m][c] = __builtin_amdgcn_mfma_f32_16x16x32_bf16(bf, af[hs][m], acc[m][c], 0, 0, 0);
            else     acc[m][c] = __builtin_amdgcn_mfma_f32_16x16x32_bf16(af[hs][m], bf, acc[m][c], 0, 0, 0);
            SB();
            if (m == 0 && c > 0) rdBh(c - 1, pb);
            if (m == 1) rdA(hs ^ 1, c, pa);
            if (m == 3) { rdBl(c, pb); if (c == 3) rdBh(3, pb); }
            SB();
          }
        }
        if (hs == 1) { ao0 = ao1; ao1 = ao1 == 32768 ? 0 : ao1 + 16384; }
      }
    }
    STAMP(0, 3);
    int l3 = threadIdx.x; asm volatile("" : "+v"(l3));
    char* xch = smem + (l3 >> 6) * 8192 + (l3 & 63) * 16;
#pragma unroll
    for (int i = 0; i < 8; i++) *(f32x4*)(xch + i * 1024) = acc[2 + (i >> 2)][i & 3];
#pragma unroll
    for (int i = 0; i < 8; i++) ep[i >> 2][i & 3] = acc[i >> 2][i & 3];
    RAW_BARRIER();
  }
  int l2 = threadIdx.x; asm volatile("" : "+v"(l2));
  const int g = (l2 >> 4) & 3, fr = l2 & 15;
  if (ISQ) {
    char* pq0 = ws + WS_PQ; char* pq1 = ws + WS_PQ + (size_t)L * 16 * 32 * 2;
#pragma unroll
    for (int c = 0; c < 4; c++) {
      const int n = col0 + 64 * wc + 16 * c + 4 * g;
      const int hd = n / 20, d0 = n - 20 * hd;
#pragma unroll
      for (int m = 0; m < 2; m++) {
        const int rb64 = rowblk * 2 + wr, r = 16 * m + fr;
        u32x2 o;
        o[0] = phi2(ep[m][c][0], ep[m][c][1]);
        o[1] = phi2(ep[m][c][2], ep[m][c][3]);
        const int fi = (rb64 * 32 + hd) * 2 + hf;
        char* dst = d0 < 16 ? pq0 + ((size_t)fi * 64 + (d0 >> 3) * 32 + r) * 16 + (d0 & 7) * 2 : pq1 + ((size_t)fi * 32 + r) * 8;
        *(u32x2*)dst = o;
      }
    }
  } else {
    const int o = (60 * kvj) & 7, zc = o ? 0 : 60;
    const int lane2 = l2 & 63;
    char* fb = smem + 32768;
#pragma unroll
    for (int c = 0; c < 4; c++) {
      u32x4 a;
      if (wc == 0) {
        a[0] = phi2(ep[0][c][0], ep[0][c][1]); a[1] = phi2(ep[0][c][2], ep[0][c][3]);
        a[2] = phi2(ep[1][c][0], ep[1][c][1]); a[3] = phi2(ep[1][c][2], ep[1][c][3]);
      } else {
        a[0] = pack2(ep[0][c][0], ep[0][c][1]); a[1] = pack2(ep[0][c][2], ep[0][c][3]);
        a[2] = pack2(ep[1][c][0], ep[1][c][1]); a[3] = pack2(ep[1][c][2], ep[1][c][3]);
        if (16 * c + fr == zc) a = (u32x4){0x3F803F80u, 0x3F803F80u, 0x3F803F80u, 0x3F803F80u};
      }
      *(u32x4*)(fb + ((((wc * 4 + wr * 2 + hf) * 4 + c) * 64 + lane2) << 4)) = a;
    }
    RAW_BARRIER();
    const int np = wave < 4 ? 1 : 2;
    for (int i = 0; i < np; i++) {
      const int p = wave < 4 ? wave : wave * 2 - 4 + i;
      int ta, tb;
      if (p < 10) { ta = (int)((0x3322211100ull >> (4 * p)) & 15); tb = (int)((0x3232121010ull >> (4 * p)) & 15); }
      else { ta = (o ? 2 : 0) + (p - 10); tb = o ? 0 : 3; }
      const char* pk = fb + ((ta * 64 + lane2) << 4);
      const char* pv = fb + 16384 + ((tb * 64 + lane2) << 4);
      f32x4 dd = {0.f, 0.f, 0.f, 0.f};
#pragma unroll
      for (int rg = 0; rg < 4; rg++)
        dd = __builtin_amdgcn_mfma_f32_16x16x32_bf16(*(const s16x8*)(pk + rg * 4096), *(const s16x8*)(pv + rg * 4096), dd, 0, 0, 0);
      const int kc0 = 16 * ta + 4 * g - o;
      const int hk = kc0 / 20, dk0 = kc0 - 20 * hk;
      const int vcol = 16 * tb + fr;
      const int dv = vcol == zc ? 20 : vcol - o - 20 * hk;
      if (kc0 >= 0 && kc0 < (kvj == 10 ? 40 : 60) && dv >= 0 && (dv < 20 || vcol == zc))
        *(f32x4*)(ws + WS_PART + (size_t)(((3 * kvj + hk) * 16 + rowblk) * 21 + dv) * 80 + dk0 * 4) = dd;
    }
  }
}

__device__ __forceinline__ void gate_role(const u16* __restrict__ x, const u16* __restrict__ gw, float* __restrict__ gate, int blk) {
  const int tid = threadIdx.x, wave = tid >> 6, lane = tid & 63;
  unsigned gwr[5];
#pragma unroll
  for (int i = 0; i < 5; i++) gwr[i] = *(const unsigned*)(gw + 2 * lane + 128 * i);
#pragma unroll 1
  for (int h = 0; h < 2; h++) {
    const int r0 = blk * 128 + wave * 16 + h * 8;
    unsigned v[8][5];
#pragma unroll
    for (int rr = 0; rr < 8; rr++)
#pragma unroll
      for (int i = 0; i < 5; i++) v[rr][i] = *(const unsigned*)(x + (size_t)(r0 + rr) * D + 2 * lane + 128 * i);
    float s[8];
#pragma unroll
    for (int rr = 0; rr < 8; rr++) {
      s[rr] = 0.f;
#pragma unroll
      for (int i = 0; i < 5; i++) {
        unsigned p = pack2(lo_f(v[rr][i]) * lo_f(gwr[i]), hi_f(v[rr][i]) * hi_f(gwr[i]));
        s[rr] += lo_f(p) + hi_f(p);
      }
    }
#pragma unroll
    for (int o = 32; o > 0; o >>= 1)
#pragma unroll
      for (int rr = 0; rr < 8; rr++) s[rr] += __shfl_xor(s[rr], o);
    float sv = s[0];
#pragma unroll
    for (int rr = 1; rr < 8; rr++) sv = lane == rr ? s[rr] : sv;
    if (lane < 8) { float a = rb(sv); gate[r0 + lane] = rb(1.f / (1.f + __expf(-a))); }
  }
}

__device__ __forceinline__ cf zpow(float th, float tl, float rh, float rl, float p) {
  float e = __builtin_amdgcn_exp2f(th * p); e = fmaf(e, (tl * p) * 0.69314718f, e);
  float t = rh * p; float f = (t - __builtin_rintf(t)) + rl * p;
  cf o; o.r = e * __builtin_amdgcn_cosf(f); o.i = e * __builtin_amdgcn_sinf(f); return o;
}
__device__ __forceinline__ void cmul4(cf& o0, cf& o1, cf& o2, cf& o3, cf a0, cf b0, cf a1, cf b1, cf a2, cf b2, cf a3, cf b3) {
  asm("v_mul_f32 %0, %8, %10\n\tv_mul_f32 %1, %8, %11\n\tv_mul_f32 %2, %12, %14\n\tv_mul_f32 %3, %12, %15\n\t"
      "v_mul_f32 %4, %16, %18\n\tv_mul_f32 %5, %16, %19\n\tv_mul_f32 %6, %20, %22\n\tv_mul_f32 %7, %20, %23\n\t"
      "v_fma_f32 %0, -%9, %11, %0\n\tv_fma_f32 %1, %9, %10, %1\n\tv_fma_f32 %2, -%13, %15, %2\n\tv_fma_f32 %3, %13, %14, %3\n\t"
      "v_fma_f32 %4, -%17, %19, %4\n\tv_fma_f32 %5, %17, %18, %5\n\tv_fma_f32 %6, -%21, %23, %6\n\tv_fma_f32 %7, %21, %22, %7"
      : "=&v"(o0.r), "=&v"(o0.i), "=&v"(o1.r), "=&v"(o1.i), "=&v"(o2.r), "=&v"(o2.i), "=&v"(o3.r), "=&v"(o3.i)
      : "v"(a0.r), "v"(a0.i), "v"(b0.r), "v"(b0.i), "v"(a1.r), "v"(a1.i), "v"(b1.r), "v"(b1.i),
        "v"(a2.r), "v"(a2.i), "v"(b2.r), "v"(b2.i), "v"(a3.r), "v"(a3.i), "v"(b3.r), "v"(b3.i));
}
__device__ __forceinline__ void batch4(unsigned* h, unsigned* l, cf b, cf t0, cf t1, cf t2, cf t3) {
  float w0r, w0i, w1r, w1i, w2r, w2i, w3r, w3i; unsigned x0, x1, x2, x3;
  asm("v_mul_f32 %8, %20, %22\n\tv_mul_f32 %9, %20, %23\n\tv_mul_f32 %10, %20, %24\n\tv_mul_f32 %11, %20, %25\n\t"
      "v_mul_f32 %12, %20, %26\n\tv_mul_f32 %13, %20, %27\n\tv_mul_f32 %14, %20, %28\n\tv_mul_f32 %15, %20, %29\n\t"
      "v_fma_f32 %8, -%21, %23, %8\n\tv_fma_f32 %9, %21, %22, %9\n\tv_fma_f32 %10, -%21, %25, %10\n\tv_fma_f32 %11, %21, %24, %11\n\t"
      "v_fma_f32 %12, -%21, %27, %12\n\tv_fma_f32 %13, %21, %26, %13\n\tv_fma_f32 %14, -%21, %29, %14\n\tv_fma_f32 %15, %21, %28, %15\n\t"
      "v_cvt_pk_bf16_f32 %0, %8, %9\n\tv_cvt_pk_bf16_f32 %1, %10, %11\n\tv_cvt_pk_bf16_f32 %2, %12, %13\n\tv_cvt_pk_bf16_f32 %3, %14, %15\n\t"
      "v_lshlrev_b32 %4, 16, %0\n\tv_and_b32 %16, 0xffff0000, %0\n\tv_lshlrev_b32 %5, 16, %1\n\tv_and_b32 %17, 0xffff0000, %1\n\t"
      "v_lshlrev_b32 %6, 16, %2\n\tv_and_b32 %18, 0xffff0000, %2\n\tv_lshlrev_b32 %7, 16, %3\n\tv_and_b32 %19, 0xffff0000, %3\n\t"
      "v_sub_f32 %8, %8, %4\n\tv_sub_f32 %9, %9, %16\n\tv_sub_f32 %10, %10, %5\n\tv_sub_f32 %11, %11, %17\n\t"
      "v_sub_f32 %12, %12, %6\n\tv_sub_f32 %13, %13, %18\n\tv_sub_f32 %14, %14, %7\n\tv_sub_f32 %15, %15, %19\n\t"
      "v_cvt_pk_bf16_f32 %4, %8, %9\n\tv_cvt_pk_bf16_f32 %5, %10, %11\n\tv_cvt_pk_bf16_f32 %6, %12, %13\n\tv_cvt_pk_bf16_f32 %7, %14, %15"
      : "=&v"(h[0]), "=&v"(h[1]), "=&v"(h[2]), "=&v"(h[3]), "=&v"(l[0]), "=&v"(l[1]), "=&v"(l[2]), "=&v"(l[3]),
        "=&v"(w0r), "=&v"(w0i), "=&v"(w1r), "=&v"(w1i), "=&v"(w2r), "=&v"(w2i), "=&v"(w3r), "=&v"(w3i),
        "=&v"(x0), "=&v"(x1), "=&v"(x2), "=&v"(x3)
      : "v"(b.r), "v"(b.i), "v"(t0.r), "v"(t0.i), "v"(t1.r), "v"(t1.i), "v"(t2.r), "v"(t2.i), "v"(t3.r), "v"(t3.i));
}
constexpr int KG_RS = 288, KG_PL = 16 * KG_RS;
__device__ __forceinline__ void kgen_role(char* smem, const u16* __restrict__ logA, const u16* __restrict__ Aim,
                                          const u16* __restrict__ Cr, const u16* __restrict__ Ci, const u16* __restrict__ logdt,
                                          const u16* __restrict__ x, const u16* __restrict__ W, const u16* __restrict__ wo,
                                          char* __restrict__ ws, int blk) {
  const int tid = threadIdx.x, lane = tid & 63;
  const int wave = __builtin_amdgcn_readfirstlane(tid >> 6);
  constexpr int RS = KG_RS, PL = KG_PL;
  const int task = blk * 8 + wave;
  const int d = task / 3;
  const int pi = task * 64 + lane;
#define PF 1
  unsigned pfv = 0; const char* pp;
  {
    const int xcd = blk & 7, i = (blk >> 3) * 64 + lane;
    const int q = i & 15;
    const int ln = (xcd & 1) ? (q < 8 ? 12 + q : 14 + q) : (q < 13 ? q : 7 + q);
    if (i < 1280) pp = (const char*)W + (size_t)(80 * wave + (i >> 4)) * 3840 + ln * 128;
    else { const int id = wave * 640 + (i - 1280); pp = (const char*)x + (size_t)((xcd >> 1) * 512 + (id & 511)) * 1280 + (id >> 9) * 128; }
  }
  const int pfpt = wave == 0 ? 9 : (wave <= 2 ? 0 : (wave <= 5 ? 1 : 2));
#define PFLOAD(pt) do { __builtin_amdgcn_sched_barrier(0); if (pfpt == (pt)) asm volatile("global_load_dword %0, %1, off" : "+v"(pfv) : "v"(pp)); __builtin_amdgcn_sched_barrier(0); } while (0)
  PFLOAD(0);
  const int wid = min(blk * 216 + (tid < 216 ? tid : 215), 51199);
  u32x4 wv;
  {
    const int slot = wid % 160, f = wid / 160;
    const int ks = f % 10, kq = (f / 10) & 3, cb = f / 40;
    int m, h;
    if (slot < 128) { m = (slot >> 6) * 32 + (slot & 31); h = (slot >> 5) & 1; } else { m = 64 + (slot & 15); h = (slot >> 4) & 1; }
    const int n = 80 * cb + m;
    int k0, k1;
    if (ks < 8) { k0 = 20 * (8 * kq + ks) + 4 * h; k1 = k0 + 8; }
    else { k0 = 20 * (8 * kq + 4 * (ks - 8) + 2 * h) + 16; k1 = k0 + 20; }
    if (wave < 4) {
#pragma unroll
      for (int e = 0; e < 2; e++) {
        unsigned a0 = wo[(size_t)(k0 + 2 * e) * D + n], a1 = wo[(size_t)(k0 + 2 * e + 1) * D + n];
        unsigned b0 = wo[(size_t)(k1 + 2 * e) * D + n], b1 = wo[(size_t)(k1 + 2 * e + 1) * D + n];
        wv[e] = a0 | (a1 << 16); wv[2 + e] = b0 | (b1 << 16);
      }
    }
  }
  const float dt = rb(expf(b2f(logdt[d])));
  float ar;
  {
    const float la = b2f(logA[pi]);
    float h = la * 1.44269502f; float l = fmaf(la, 1.44269502f, -h) + la * 1.92596299e-8f;
    float e = __builtin_amdgcn_exp2f(h); e = fmaf(e, l * 0.69314718f, e);
    ar = -rb(e) * dt;
  }
  const float ai = b2f(Aim[pi]) * dt;
  cf C; C.r = 2.f * (b2f(Cr[pi]) * dt); C.i = 2.f * (b2f(Ci[pi]) * dt);
  const float th = ar * 1.44269502f, tl = fmaf(ar, 1.44269502f, -th) + ar * 1.92596299e-8f;
  const float rh = ai * 0.159154937f, rl = fmaf(ai, 0.159154937f, -rh) + ai * 6.42063833e-9f;
  char* tb = smem + wave * (2 * PL);
  char* wp = tb + lane * 4;
  const char* rp = tb + (lane & 15) * RS + (lane >> 4) * 16;
  STAMP(0, 2);
  PFLOAD(1);
  const cf one = {1.f, 0.f};
  cf S1 = zpow(th, tl, rh, rl, 1.f), Q1 = zpow(th, tl, rh, rl, 8.f), R1 = zpow(th, tl, rh, rl, 128.f);
  Q1.i = -Q1.i;
  cf S2, Q2, R2, G1, S3, S4, Q3, P1, R3, T1, G2, G3, S5, S6, S7, P2, CT1, T2, P3, CS4, CT2, CT3, CT4, dm0;
  cmul4(S2, Q2, R2, G1, S1, S1, Q1, Q1, R1, R1, C, R1);
  cmul4(S3, S4, Q3, P1, S2, S1, S2, S2, Q2, Q1, Q2, Q2);
  cmul4(R3, T1, G2, P2, R2, R1, R2, R2, C, R2, P1, P1);
  cmul4(S5, S6, S7, P3, S4, S1, S4, S2, S4, S3, P2, P1);
  cmul4(G3, CT1, T2, dm0, C, R3, C, T1, T1, T1, C, R1);
  cmul4(CS4, CT2, CT3, dm0, CT1, S4, C, T2, CT1, T2, C, R1);
  CT4 = cmul(CT2, T2);
  PFLOAD(2);
  auto rows4 = [&](int row, cf b, cf t0, cf t1, cf t2, cf t3) {
    unsigned h[4], l[4];
    batch4(h, l, b, t0, t1, t2, t3);
#pragma unroll
    for (int k = 0; k < 4; k++) { *(unsigned*)(wp + (row + k) * RS) = h[k]; *(unsigned*)(wp + PL + (row + k) * RS) = l[k]; }
  };
  rows4(0, one, one, Q1, Q2, Q3); rows4(4, P1, one, Q1, Q2, Q3); rows4(8, P2, one, Q1, Q2, Q3); rows4(12, P3, one, Q1, Q2, Q3);
  __builtin_amdgcn_wave_barrier();
  s16x8 bh[4], bl[4];
#pragma unroll
  for (int k = 0; k < 4; k++) { bh[k] = *(const s16x8*)(rp + k * 64); bl[k] = *(const s16x8*)(rp + PL + k * 64); }
  __builtin_amdgcn_wave_barrier();
  STAMP(0, 3);
  f32x4 res[3];
  auto mm = [&](int t) {
    __builtin_amdgcn_wave_barrier();
    f32x4 a = {0.f, 0.f, 0.f, 0.f}, a2 = {0.f, 0.f, 0.f, 0.f};
#pragma unroll
    for (int k = 0; k < 4; k++) {
      s16x8 ah = *(const s16x8*)(rp + k * 64), al = *(const s16x8*)(rp + PL + k * 64);
      a = __builtin_amdgcn_mfma_f32_16x16x32_bf16(ah, bh[k], a, 0, 0, 0);
      a2 = __builtin_amdgcn_mfma_f32_16x16x32_bf16(ah, bl[k], a2, 0, 0, 0);
      a2 = __builtin_amdgcn_mfma_f32_16x16x32_bf16(al, bh[k], a2, 0, 0, 0);
    }
    __builtin_amdgcn_wave_barrier();
    res[t] = a + a2;
  };
  rows4(0, C, one, S1, S2, S3); rows4(4, C, S4, S5, S6, S7); rows4(8, G1, one, S1, S2, S3); rows4(12, G1, S4, S5, S6, S7);
  mm(0);
  STAMP(0, 4);
  rows4(0, G2, one, S2, S4, S6); rows4(4, G3, one, S2, S4, S6);
  {
    unsigned h[4], l[4];
    batch4(h, l, CT1, one, R1, R2, R3);
#pragma unroll
    for (int k = 0; k < 4; k++) { *(unsigned*)(wp + (8 + 2 * k) * RS) = h[k]; *(unsigned*)(wp + PL + (8 + 2 * k) * RS) = l[k]; }
    batch4(h, l, CS4, one, R1, R2, R3);
#pragma unroll
    for (int k = 0; k < 4; k++) { *(unsigned*)(wp + (9 + 2 * k) * RS) = h[k]; *(unsigned*)(wp + PL + (9 + 2 * k) * RS) = l[k]; }
  }
  mm(1);
  rows4(0, CT2, one, R1, R2, R3); rows4(4, CT3, one, R1, R2, R3); rows4(8, CT4, one, one, one, one);
  mm(2);
#pragma unroll
  for (int t = 0; t < 3; t++) *(f32x4*)(tb + t * 1024 + lane * 16) = res[t];
  if (tid < 216 && blk * 216 + tid < 51200) st16(ws + WS_WOF + (size_t)wid * 16, wv);
  __syncthreads();
  {
    const int d0 = (blk * 8) / 3;
#pragma unroll
    for (int it = 0; it < 2; it++) {
      const int e = tid + 512 * it;
      const int seg = e / 192, ent = e - 192 * seg;
      const int ds = d0 + seg;
      const int wlo = max(0, 3 * ds - 8 * blk), whi = min(8, 3 * ds + 3 - 8 * blk);
      if (seg < 4 && wlo < 8) {
        f32x4 a = *(const f32x4*)(smem + wlo * (2 * PL) + ent * 16);
        for (int w = wlo + 1; w < whi; w++) a += *(const f32x4*)(smem + w * (2 * PL) + ent * 16);
        st16(ws + WS_KIMG + (size_t)ds * 6144 + (3 * ds < 8 * blk ? 3072 : 0) + ent * 16, __builtin_bit_cast(u32x4, a));
      }
    }
  }
  asm volatile("s_waitcnt vmcnt(0)" :: "v"(pfv) : "memory");
}

__global__ __launch_bounds__(512, 4) void k1(const u16* __restrict__ x, const u16* __restrict__ logA, const u16* __restrict__ Aim,
                                          const u16* __restrict__ Cr, const u16* __restrict__ Ci, const u16* __restrict__ logdt,
                                          const u16* __restrict__ wqkv, const u16* __restrict__ gw, const u16* __restrict__ wo,
                                          char* __restrict__ ws) {
  __shared__ __attribute__((aligned(16))) char smem[K1_LDS];
  int b = blockIdx.x;
  STAMP(0, 0);
  if (b < NB_GEMM) {
    const int xcd = b & 7, idx = b >> 3;
    const int rowblk = (xcd >> 1) * 4 + (idx & 3), cb = (xcd & 1) * 8 + (idx >> 2);
    if (cb < 5) gemm_role<true>(smem, x, wqkv, ws, rowblk, cb);
    else        gemm_role<false>(smem, x, wqkv, ws, rowblk, cb);
    STAMP(0, 1);
    return;
  }
  b -= NB_GEMM;
  if (b < NB_KG) { kgen_role(smem, logA, Aim, Cr, Ci, logdt, x, wqkv, wo, ws, b); STAMP(0, 1); return; }
  b -= NB_KG;
  gate_role(x, gw, (float*)(ws + WS_GATE), b);
  STAMP(0, 1);
}

constexpr int K2_CS = 4160;
constexpr int K2_KOFF = 8 * K2_CS;
constexpr int K2_TAB = K2_KOFF + 2 * 4608;
constexpr int K2_LDS = K2_TAB + 3072;
constexpr int NB_KV = 32, NB_WO = 0;
__global__ __launch_bounds__(256, 3) void k2(const u16* __restrict__ Dskip, char* __restrict__ ws) {
  __shared__ __attribute__((aligned(16))) char smem[K2_LDS];
  const int tid = threadIdx.x, lane = tid & 63;
  const int wave = __builtin_amdgcn_readfirstlane(tid >> 6);
  STAMP(1, 0);
  if (blockIdx.x < NB_KV) {
    const int hd = blockIdx.x;
    const int h = lane >> 5;
    float* fin = (float*)smem;
    if (tid < 105) {
      const f32x4* src = (const f32x4*)(ws + WS_PART) + (size_t)hd * 16 * 105 + tid;
      f32x4 v[16];
#pragma unroll
      for (int i = 0; i < 16; i++) v[i] = src[i * 105];
#pragma unroll
      for (int st = 1; st < 16; st <<= 1)
#pragma unroll
        for (int i = 0; i < 16; i += 2 * st) v[i] += v[i + st];
      *(f32x4*)(fin + tid * 4) = (f32x4){rb(v[0][0]), rb(v[0][1]), rb(v[0][2]), rb(v[0][3])};
    }
    __syncthreads();
    if (tid < 96) {
      const int m = lane & 31;
      int col = m < 16 ? m : ((m >> 2) == ((hd & 2) ? 5 : 4) ? 16 + (m & 3) : 20);
      if (tid < 64) {
        u32x4 o;
#pragma unroll
        for (int e = 0; e < 4; e++) o[e] = pack2(fin[col * 20 + 8 * h + 2 * e], fin[col * 20 + 8 * h + 2 * e + 1]);
        *(u32x4*)(ws + WS_KVT + (size_t)hd * 1280 + lane * 16) = o;
      } else {
        u32x2 o;
#pragma unroll
        for (int e = 0; e < 2; e++) o[e] = pack2(fin[col * 20 + 16 + 2 * e], fin[col * 20 + 17 + 2 * e]);
        *(u32x2*)(ws + WS_KVT + (size_t)hd * 1280 + 1024 + m * 8) = o;
      }
    }
    STAMP(1, 1);
    return;
  }
  const int d = blockIdx.x - (NB_KV + NB_WO);
  const u16* uT = (const u16*)(ws + WS_UT) + (size_t)d * UT_STRIDE;
  {
    const f32x4* ksrc = (const f32x4*)(ws + WS_KIMG + (size_t)d * 6144) + (tid < 192 ? tid : 191);
    const u32x4* usrc = (const u32x4*)uT;
    f32x4 p0 = ksrc[0];
    if ((d & 7) == 2 || (d & 7) == 5) p0 += ksrc[192];
    u32x4 c0 = usrc[tid + 1], c1 = usrc[tid + 2];
    u32x4 e0, e1;
    if (tid < 2) { e0 = usrc[tid + 257]; e1 = usrc[tid + 258]; }
    if (tid < 64) *(u32x4*)(smem + K2_KOFF + (tid >> 5) * 4608 + (tid & 31) * 16) = (u32x4){0u, 0u, 0u, 0u};
    if (tid < 192) *(f32x4*)(smem + K2_TAB + tid * 16) = p0;
    auto put_copies = [&](u32x4 a, u32x4 b, int xch) {
      unsigned w[8] = {a[0], a[1], a[2], a[3], b[0], b[1], b[2], b[3]};
      unsigned o[7];
#pragma unroll
      for (int k = 0; k < 7; k++) o[k] = __builtin_amdgcn_alignbyte(w[k + 1], w[k], 2);
      char* p = smem + xch * 16;
#pragma unroll
      for (int q = 0; q < 4; q++) {
        *(u32x4*)(p + (2 * q) * K2_CS) = (u32x4){o[q], o[q + 1], o[q + 2], o[q + 3]};
        *(u32x4*)(p + (2 * q + 1) * K2_CS) = (u32x4){w[q + 1], w[q + 2], w[q + 3], w[q + 4]};
      }
    };
    put_copies(c0, c1, tid);
    if (tid < 2) put_copies(e0, e1, tid + 256);
  }
  __syncthreads();
  {
    const float* tab = (const float*)(smem + K2_TAB);
    float kv[8];
#pragma unroll
    for (int e = 0; e < 8; e++) {
      const int j = tid * 8 + 7 - e;
      const int p = (int)rb((float)j);
      const int h = p >> 7, c = (p >> 3) & 15;
      int sh = 31 - __builtin_clz(h | 1); sh = sh > 3 ? 3 : sh;
      const int row = 8 * sh + (((h << 3) | (p & 7)) >> sh);
      kv[e] = tab[((row >> 2) * 16 + c) * 4 + (row & 3)];
    }
    u32x4 hi, lo;
#pragma unroll
    for (int e = 0; e < 4; e++) {
      hi[e] = pack2(kv[2 * e], kv[2 * e + 1]);
      lo[e] = pack2(ssub(kv[2 * e], lo_f(hi[e])), ssub(kv[2 * e + 1], hi_f(hi[e])));
    }
    *(u32x4*)(smem + K2_KOFF + 512 + tid * 16) = hi;
    *(u32x4*)(smem + K2_KOFF + 4608 + 512 + tid * 16) = lo;
  }
  __syncthreads();
  STAMP(1, 2);
  f32x4 acc[8];
#pragma unroll
  for (int t = 0; t < 8; t++) acc[t] = (f32x4){0.f, 0.f, 0.f, 0.f};
  {
    const int i = lane & 15, kg = lane >> 4, e = kg >> 1, bb = kg & 1;
    const char* ap = smem + (i & 7) * K2_CS + (2 * e - bb + 2 + (i >> 3)) * 16;
    const char* bp = smem + K2_KOFF + (16 + i - e) * 32 + bb * 16;
#pragma unroll
    for (int it = 0; it < 2; it++) {
      const int d0 = 2 * (wave + 4 * it);
      s16x8 af[8], bh[8], bl[8];
#pragma unroll
      for (int j = 0; j < 8; j++) af[j] = *(const s16x8*)(ap + (32 * j + 2 * d0) * 16);
#pragma unroll
      for (int m = 0; m < 8; m++) {
        bh[m] = *(const s16x8*)(bp + (16 * m - d0) * 32);
        bl[m] = *(const s16x8*)(bp + (16 * m - d0) * 32 + 4608);
      }
#pragma unroll
      for (int m = 0; m < 8; m++) {
#pragma unroll
        for (int j = 0; j + m < 8; j++) acc[j + m] = __builtin_amdgcn_mfma_f32_16x16x32_bf16(af[j], bh[m], acc[j + m], 0, 0, 0);
#pragma unroll
        for (int j = 0; j + m < 8; j++) acc[j + m] = __builtin_amdgcn_mfma_f32_16x16x32_bf16(af[j], bl[m], acc[j + m], 0, 0, 0);
      }
    }
  }
  STAMP(1, 3);
  u32x2 uu[2];
#pragma unroll
  for (int t = 0; t < 2; t++) uu[t] = *(const u32x2*)(uT + UT_PAD + 256 * (2 * wave + t) + 4 * lane);
  const float dsk = b2f(Dskip[d]);
  __syncthreads();
  float* red = (float*)smem;
#pragma unroll
  for (int t = 0; t < 8; t++) *(f32x4*)(red + ((wave * 8 + t) * 64 + (lane & 15) * 4 + (lane >> 4)) * 4) = acc[t];
  __syncthreads();
  u16* aT = (u16*)(ws + WS_AT) + (size_t)d * L;
#pragma unroll
  for (int t = 0; t < 2; t++) {
    const int tt = 2 * wave + t;
    f32x4 s = *(const f32x4*)(red + ((0 * 8 + tt) * 64 + lane) * 4);
    f32x4 s1 = *(const f32x4*)(red + ((1 * 8 + tt) * 64 + lane) * 4);
    f32x4 s2 = *(const f32x4*)(red + ((2 * 8 + tt) * 64 + lane) * 4);
    f32x4 s3 = *(const f32x4*)(red + ((3 * 8 + tt) * 64 + lane) * 4);
    s = (s + s1) + (s2 + s3);
    u32x2 ov;
#pragma unroll
    for (int e = 0; e < 2; e++) {
      unsigned y = pack2(s[2 * e], s[2 * e + 1]);
      unsigned k = pack2(lo_f(uu[t][e]) * dsk, hi_f(uu[t][e]) * dsk);
      ov[e] = pack2(lo_f(y) + lo_f(k), hi_f(y) + hi_f(k));
    }
    *(u32x2*)(aT + 256 * tt + 4 * lane) = ov;
  }
  STAMP(1, 1);
}

constexpr int K3_WO = 0;
constexpr int K3_KV = 102400;
constexpr int K3_AT = K3_KV + 40960;
constexpr int K3_LDS = K3_AT + 80 * 144;
__global__ __launch_bounds__(512, 2) void k3(char* __restrict__ ws, u16* __restrict__ out) {
  __shared__ __attribute__((aligned(16))) char smem[K3_LDS];
  const int tid = threadIdx.x, lane = tid & 63;
  const int wave = __builtin_amdgcn_readfirstlane(tid >> 6);
  STAMP(2, 0);
  const int xcd = blockIdx.x & 7, idx = blockIdx.x >> 3;
  const int rowblk = xcd * 4 + (idx >> 3), cb = idx & 7;
  const int l0 = rowblk * 64, c0 = cb * 80;
  const int kq = wave & 3, t = wave >> 2;
  const int r = lane & 31, h = lane >> 5;
#pragma unroll
  for (int i = 0; i < 5; i++) glds16(ws + WS_KVT + (size_t)(wave * 5 + i) * 1024 + lane * 16, smem + K3_KV + (wave * 5 + i) * 1024);
  u32x4 pf0[8]; u32x2 pf1[8];
  {
    const char* p0 = ws + WS_PQ + ((size_t)((rowblk * 32 + kq * 8) * 2 + t) * 64 + lane) * 16;
    const char* p1 = ws + WS_PQ + (size_t)L * 16 * 32 * 2 + ((size_t)((rowblk * 32 + kq * 8) * 2 + t) * 32 + r) * 8;
#pragma unroll
    for (int hh = 0; hh < 8; hh++) {
      asm volatile("global_load_dwordx4 %0, %1, off offset:%2" : "=v"(pf0[hh]) : "v"(p0 + (hh >> 1) * 4096), "i"((hh & 1) * 2048));
      asm volatile("global_load_dwordx2 %0, %1, off offset:%2" : "=v"(pf1[hh]) : "v"(p1), "i"(hh * 512));
    }
  }
  {
    const char* src = ws + WS_WOF + (size_t)cb * 102400 + lane * 16;
#pragma unroll
    for (int i = 0; i < 13; i++) { int p = i * 8 + wave; if (p < 100) glds16(src + p * 1024, smem + K3_WO + p * 1024); }
  }
  u32x4 av[2];
  {
    const char* aT = ws + WS_AT;
#pragma unroll
    for (int i = 0; i < 2; i++) {
      int id = tid + 512 * i; if (id > 639) id = 639;
      const char* p = aT + ((size_t)(c0 + (id >> 3)) * L + l0 + 8 * (id & 7)) * 2;
      asm volatile("global_load_dwordx4 %0, %1, off" : "=v"(av[i]) : "v"(p));
    }
  }
  float gt[2];
  {
    const float* gate = (const float*)(ws + WS_GATE) + l0;
#pragma unroll
    for (int i = 0; i < 2; i++) {
      int id = tid + 512 * i; if (id > 639) id = 639;
      asm volatile("global_load_dword %0, %1, off" : "=v"(gt[i]) : "v"(gate + id / 10));
    }
  }
  if (wave < 4) VMCNT(17); else VMCNT(16);
  asm volatile("" : "+v"(pf0[0]), "+v"(pf0[1]), "+v"(pf0[2]), "+v"(pf0[3]), "+v"(pf0[4]), "+v"(pf0[5]), "+v"(pf0[6]), "+v"(pf0[7]));
  asm volatile("" : "+v"(pf1[0]), "+v"(pf1[1]), "+v"(pf1[2]), "+v"(pf1[3]), "+v"(pf1[4]), "+v"(pf1[5]), "+v"(pf1[6]), "+v"(pf1[7]));
  RAW_BARRIER();
  STAMP(2, 2);
  u32x4 ob[10];
  {
    unsigned rem[8][2];
#pragma unroll
    for (int hh = 0; hh < 8; hh++) {
      const char* kp = smem + K3_KV + (kq * 8 + hh) * 1280;
      s16x8 a0 = *(const s16x8*)(kp + lane * 16);
      u32x2 a1h = *(const u32x2*)(kp + 1024 + r * 8);
      u32x4 a1u = {a1h[0], a1h[1], 0u, 0u};
      u32x4 b1u = {h ? 0u : pf1[hh][0], h ? 0u : pf1[hh][1], 0u, 0u};
      f32x16 c;
#pragma unroll
      for (int i = 0; i < 16; i++) c[i] = 0.f;
      c = __builtin_amdgcn_mfma_f32_32x32x16_bf16(a0, __builtin_bit_cast(s16x8, pf0[hh]), c, 0, 0, 0);
      c = __builtin_amdgcn_mfma_f32_32x32x16_bf16(__builtin_bit_cast(s16x8, a1u), __builtin_bit_cast(s16x8, b1u), c, 0, 0, 0);
      float den = rb(rb(c[12]) + 1e-6f);
      float inv = __builtin_amdgcn_rcpf(den);
      unsigned o[6];
#pragma unroll
      for (int q = 0; q < 6; q++) {
        unsigned n0 = pack2(c[2 * q], c[2 * q + 1]);
        o[q] = pack2(lo_f(n0) * inv, hi_f(n0) * inv);
      }
      ob[hh] = (u32x4){o[0], o[1], o[2], o[3]};
      rem[hh][0] = o[4]; rem[hh][1] = o[5];
    }
#pragma unroll
    for (int G = 0; G < 2; G++)
      ob[8 + G] = (u32x4){h ? rem[4 * G + 2][0] : rem[4 * G][0], h ? rem[4 * G + 2][1] : rem[4 * G][1],
                          h ? rem[4 * G + 3][0] : rem[4 * G + 1][0], h ? rem[4 * G + 3][1] : rem[4 * G + 1][1]};
  }
  STAMP(2, 3);
  VMCNT(0);
  asm volatile("" : "+v"(av[0]), "+v"(av[1]), "+v"(gt[0]), "+v"(gt[1]));
#pragma unroll
  for (int i = 0; i < 2; i++) { int id = tid + 512 * i; if (id < 640) *(u32x4*)(smem + K3_AT + (id >> 3) * 144 + (id & 7) * 16) = av[i]; }
  RAW_BARRIER();
  STAMP(2, 4);
  f32x16 acc[3];
#pragma unroll
  for (int c = 0; c < 3; c++)
#pragma unroll
    for (int i = 0; i < 16; i++) acc[c][i] = 0.f;
  {
    const char* wp = smem + K3_WO + kq * 25600;
    const int o2 = 2048 + (h * 16 + (lane & 15)) * 16;
#pragma unroll
    for (int ks = 0; ks < 10; ks++) {
      s16x8 w0 = *(const s16x8*)(wp + ks * 2560 + lane * 16);
      s16x8 w1 = *(const s16x8*)(wp + ks * 2560 + 1024 + lane * 16);
      s16x8 w2 = *(const s16x8*)(wp + ks * 2560 + o2);
      s16x8 b = __builtin_bit_cast(s16x8, ob[ks]);
      acc[0] = __builtin_amdgcn_mfma_f32_32x32x16_bf16(w0, b, acc[0], 0, 0, 0);
      acc[1] = __builtin_amdgcn_mfma_f32_32x32x16_bf16(w1, b, acc[1], 0, 0, 0);
      acc[2] = __builtin_amdgcn_mfma_f32_32x32x16_bf16(w2, b, acc[2], 0, 0, 0);
    }
  }
  STAMP(2, 5);
  RAW_BARRIER();
  {
    float* pr = (float*)smem + (size_t)(kq * 64 + 32 * t + r) * 84 + 4 * h;
#pragma unroll
    for (int c = 0; c < 3; c++)
#pragma unroll
      for (int q = 0; q < 4; q++)
        if (c < 2 || q < 2) *(f32x4*)(pr + 32 * c + 8 * q) = (f32x4){acc[c][4 * q], acc[c][4 * q + 1], acc[c][4 * q + 2], acc[c][4 * q + 3]};
  }
  RAW_BARRIER();
  {
#pragma unroll
    for (int i2 = 0; i2 < 2; i2++) {
      const int i = tid + 512 * i2;
      if (i < 640) {
        const int rr = i / 10, oc = i - 10 * rr;
        const float g = gt[i2]; const float g1 = rb(1.f - g);
        f32x4 s0 = {0.f, 0.f, 0.f, 0.f}, s1 = {0.f, 0.f, 0.f, 0.f};
#pragma unroll
        for (int w = 0; w < 4; w++) {
          const float* pr = (const float*)smem + (size_t)(w * 64 + rr) * 84 + oc * 8;
          s0 += *(const f32x4*)pr; s1 += *(const f32x4*)(pr + 4);
        }
        u32x4 ov;
#pragma unroll
        for (int e = 0; e < 4; e++) {
          unsigned bb = e < 2 ? pack2(s0[2 * e], s0[2 * e + 1]) : pack2(s1[2 * e - 4], s1[2 * e - 3]);
          float a0 = b2f(*(const u16*)(smem + K3_AT + (oc * 8 + 2 * e) * 144 + rr * 2));
          float a1 = b2f(*(const u16*)(smem + K3_AT + (oc * 8 + 2 * e + 1) * 144 + rr * 2));
          unsigned ga = pack2(g * a0, g * a1), gb = pack2(g1 * lo_f(bb), g1 * hi_f(bb));
          ov[e] = pack2(lo_f(ga) + lo_f(gb), hi_f(ga) + hi_f(gb));
        }
        *(u32x4*)(out + (size_t)(l0 + rr) * D + c0 + oc * 8) = ov;
      }
    }
  }
  STAMP(2, 1);
}

extern "C" void kernel_launch(void* const* d_in, const int* in_sizes, int n_in, void* d_out, int out_size, void* d_ws, size_t ws_size,
                              hipStream_t stream) {
  const u16* u = (const u16*)d_in[0]; const u16* logA = (const u16*)d_in[1]; const u16* Aim = (const u16*)d_in[2];
  const u16* Cr = (const u16*)d_in[3]; const u16* Ci = (const u16*)d_in[4]; const u16* logdt = (const u16*)d_in[5];
  const u16* Dsk = (const u16*)d_in[6]; const u16* wqkv = (const u16*)d_in[8]; const u16* wo = (const u16*)d_in[9];
  const u16* gw = (const u16*)d_in[10];
  char* ws = (char*)d_ws;
  k1<<<NB_GEMM + NB_GATE + NB_KG, 512, 0, stream>>>(u, logA, Aim, Cr, Ci, logdt, wqkv, gw, wo, ws);
  k2<<<NB_KV + NB_WO + D, 256, 0, stream>>>(Dsk, ws);
  k3<<<256, 512, 0, stream>>>(ws, (u16*)d_out);
}
```

```cpp
#include <hip/hip_runtime.h>
#include <hip/hip_bf16.h>
#include <stdint.h>

typedef unsigned short u16;
typedef __attribute__((ext_vector_type(8))) short s16x8;
typedef __attribute__((ext_vector_type(4))) short s16x4;
typedef __attribute__((ext_vector_type(4))) float f32x4;
typedef __attribute__((ext_vector_type(16))) float f32x16;
typedef __attribute__((ext_vector_type(4))) unsigned u32x4;
typedef __attribute__((ext_vector_type(2))) unsigned u32x2;
typedef __attribute__((address_space(3))) s16x4* lds_s16x4_p;
typedef const __attribute__((address_space(1))) void* gptr_t;
typedef __attribute__((address_space(3))) void* lptr_t;

constexpr int L = 2048, D = 640, NS = 192, NH = 32;
constexpr int UT_PAD = 32, UT_STRIDE = L + UT_PAD;
constexpr size_t WS_UT   = 0;
constexpr size_t WS_KIMG = WS_UT + (size_t)D * UT_STRIDE * 2;
constexpr size_t WS_PQ   = WS_KIMG + (size_t)D * 6144;
constexpr size_t WS_PART = WS_PQ + (size_t)L * D * 2;
constexpr size_t WS_GATE = WS_PART + (size_t)32 * 16 * 1680;
constexpr size_t WS_WOF  = WS_GATE + (size_t)L * 4;
constexpr size_t WS_KVT  = WS_WOF + (size_t)D * D * 2;
constexpr size_t WS_AT   = WS_KVT + (size_t)32 * 1280;
constexpr size_t WS_END  = WS_AT + (size_t)D * L * 2;
#define STAMP(kid, slot) do {} while (0)

__device__ __forceinline__ float b2f(u16 v) { return __uint_as_float(((unsigned)v) << 16); }
typedef __bf16 bf16x2_t __attribute__((ext_vector_type(2)));
typedef float f32x2 __attribute__((ext_vector_type(2)));
__device__ __forceinline__ unsigned pack2(float a, float b) { f32x2 v = {a, b}; bf16x2_t r = __builtin_convertvector(v, bf16x2_t); return __builtin_bit_cast(unsigned, r); }
__device__ __forceinline__ float lo_f(unsigned u) { return __uint_as_float(u << 16); }
__device__ __forceinline__ float hi_f(unsigned u) { return __uint_as_float(u & 0xffff0000u); }
__device__ __forceinline__ float rb(float f) { return lo_f(pack2(f, f)); }
__device__ __forceinline__ unsigned phi2(float a, float b) {
  unsigned t = pack2(a, b); float t0 = lo_f(t), t1 = hi_f(t);
  float e0 = __builtin_amdgcn_fmed3f(__builtin_amdgcn_exp2f(t0 * 1.44269504f), 0.f, 1.f);
  float e1 = __builtin_amdgcn_fmed3f(__builtin_amdgcn_exp2f(t1 * 1.44269504f), 0.f, 1.f);
  unsigned r = pack2(__builtin_fmaxf(t0, 0.f) + e0, __builtin_fmaxf(t1, 0.f) + e1);
  return pack2(lo_f(r) + 1e-6f, hi_f(r) + 1e-6f);
}
#define VMCNT(n) asm volatile("s_waitcnt vmcnt(" #n ")" ::: "memory")
#define RAW_BARRIER() do { asm volatile("s_waitcnt lgkmcnt(0)" ::: "memory"); __builtin_amdgcn_s_barrier(); asm volatile("" ::: "memory"); } while (0)
__device__ __forceinline__ void glds16(const void* g, void* l) { __builtin_amdgcn_global_load_lds((gptr_t)g, (lptr_t)l, 16, 0, 0); }

#define OPQ_S(v) asm volatile("" : "+s"(v))
typedef f32x2 cf;
__device__ __forceinline__ float smul(float a, float b) { float o; asm("v_mul_f32 %0, %1, %2" : "=v"(o) : "v"(a), "v"(b)); return o; }
__device__ __forceinline__ float sfma(float a, float b, float c) { float o; asm("v_fma_f32 %0, %1, %2, %3" : "=v"(o) : "v"(a), "v"(b), "v"(c)); return o; }
__device__ __forceinline__ float sfnma(float a, float b, float c) { float o; asm("v_fma_f32 %0, -%1, %2, %3" : "=v"(o) : "v"(a), "v"(b), "v"(c)); return o; }
__device__ __forceinline__ float ssub(float a, float b) { float o; asm("v_sub_f32 %0, %1, %2" : "=v"(o) : "v"(a), "v"(b)); return o; }
__device__ __forceinline__ cf cmul(cf a, cf b) {
  cf o;
  asm("v_pk_mul_f32 %0, %1, %2 op_sel:[0,0] op_sel_hi:[0,1]\n\t"
      "v_pk_fma_f32 %0, %1, %2, %0 op_sel:[1,1,0] op_sel_hi:[1,0,1] neg_lo:[1,0,0]" : "=&v"(o) : "v"(a), "v"(b));
  return o;
}

constexpr int K1_LDS = 81920;
constexpr int NB_GEMM = 256, NB_GATE = 16, NB_KG = 240;
#define WT 1
__device__ __forceinline__ void st16(void* p, u32x4 v) {
  asm volatile("global_store_dwordx4 %0, %1, off sc1\n\ts_nop 2" :: "v"(p), "v"(v) : "memory");
}

#define BAR_ONLY() do { asm volatile("" ::: "memory"); __builtin_amdgcn_s_barrier(); asm volatile("" ::: "memory"); } while (0)
#define SB() __builtin_amdgcn_sched_barrier(0)
template <bool ISQ>
__device__ __forceinline__ void gemm_role(char* smem, const u16* __restrict__ x, const u16* __restrict__ W, char* __restrict__ ws,
                                          int rowblk, int cb) {
  const int tid = threadIdx.x, lane = tid & 63;
  const int wave = __builtin_amdgcn_readfirstlane(tid >> 6);
  const int row0 = rowblk * 128, col0 = cb * 128;
  const int kvj = cb - 5;
  char* sB = smem; char* sA = smem + 32768;
  const int wr = wave & 1, wc = (wave >> 1) & 1, hf = wave >> 2;
  f32x4 ep[2][4];
  if (wave >= 4) {
    const int lw = wave - 4;
    const char* pA = (const char*)x + (size_t)(row0 + 32 * lw + (lane >> 3)) * 1280 + (((lane & 7) ^ (lane >> 3)) << 4);
    const int fB = (lane >> 4) | ((lw & 1) << 2);
    const int c16 = (((lane & 15) >> 1) ^ fB) * 2 + (lane & 1);
    int bcol = col0 + 8 * c16;
    if (!ISQ) bcol = min(640 + 640 * (c16 >> 3) + ((60 * kvj) & ~7) + 8 * (c16 & 7), 1912);
    const char* pB = (const char*)W + (size_t)(8 * lw + (lane >> 4)) * 3840 + bcol * 2;
    u32x2 tr[8];
    int tro;
    {
      const int fr = lane & 15, g = lane >> 4, p4 = lane & 3, r = 8 * g + (fr >> 2);
      tro = 32768 + r * 128 + (((2 * lw + (p4 >> 1)) ^ (r & 7)) << 4) + (p4 & 1) * 8;
    }
    auto issA = [&](int slot_off, int h) {
      glds16(pA + (16 * h) * 1280, sA + slot_off + (4 * lw + 2 * h) * 1024);
      glds16(pA + (16 * h + 8) * 1280, sA + slot_off + (4 * lw + 2 * h + 1) * 1024);
    };
    auto issB = [&](int slot_off) {
      glds16(pB, sB + slot_off + (2 * lw) * 1024);
      glds16(pB + 4 * 3840, sB + slot_off + (2 * lw + 1) * 1024);
      pB += (size_t)32 * 3840;
    };
    issA(0, 0); issB(0); issA(0, 1); pA += 128;
    issB(8192); issA(16384, 0); issB(16384); issA(16384, 1); pA += 128;
    VMCNT(8);
    BAR_ONLY();
    int ao = 32768, bo = 24576;
#pragma unroll 1
    for (int j = 0; j < 10; j++) {
      if (j < 9) VMCNT(6); else VMCNT(0);
      BAR_ONLY();
      if (j == cb) {
        const int a0 = (int)(size_t)(lptr_t)smem + (ao == 32768 ? 0 : ao + 16384);
        const int a = a0 + tro, a2 = a0 + (tro ^ 64) + 512;
        asm volatile("ds_read_b64_tr_b16 %0, %8\n\tds_read_b64_tr_b16 %1, %9\n\tds_read_b64_tr_b16 %2, %8 offset:4096\n\tds_read_b64_tr_b16 %3, %9 offset:4096\n\t"
                     "ds_read_b64_tr_b16 %4, %8 offset:8192\n\tds_read_b64_tr_b16 %5, %9 offset:8192\n\tds_read_b64_tr_b16 %6, %8 offset:12288\n\tds_read_b64_tr_b16 %7, %9 offset:12288\n\t"
                     "s_waitcnt lgkmcnt(0)"
                     : "=&v"(tr[0]), "=&v"(tr[1]), "=&v"(tr[2]), "=&v"(tr[3]), "=&v"(tr[4]), "=&v"(tr[5]), "=&v"(tr[6]), "=&v"(tr[7])
                     : "v"(a), "v"(a2) : "memory");
      }
      if (j < 9) { issB(bo); bo = (bo + 8192) & 32767; }
      if (j < 8) issA(ao, 0);
      if (j < 8) VMCNT(4); else VMCNT(2);
      BAR_ONLY();
      if (j < 8) { issB(bo); bo = (bo + 8192) & 32767; issA(ao, 1); pA += 128; }
      ao = ao == 32768 ? 0 : ao + 16384;
    }
    if (cb < 10) {
      u16* dst = (u16*)(ws + WS_UT) + (size_t)(64 * cb + 16 * lw + (lane & 15)) * UT_STRIDE + 8 * (lane >> 4);
#pragma unroll
      for (int i = 0; i < 4; i++) st16(dst + UT_PAD + row0 + 32 * i, (u32x4){tr[2 * i][0], tr[2 * i][1], tr[2 * i + 1][0], tr[2 * i + 1][1]});
      if (rowblk == 0) st16(dst, (u32x4){0u, 0u, 0u, 0u});
    }
    BAR_ONLY();
    char* xch = smem + (wave & 3) * 8192 + lane * 16;
#pragma unroll
    for (int i = 0; i < 8; i++) ep[i >> 2][i & 3] = *(const f32x4*)(xch + i * 1024);
  } else {
    const int g = lane >> 4, fr = lane & 15;
    f32x4 acc[4][4];
#pragma unroll
    for (int m = 0; m < 4; m++)
#pragma unroll
      for (int c = 0; c < 4; c++) acc[m][c] = (f32x4){0.f, 0.f, 0.f, 0.f};
    const int q4 = fr >> 2, p4 = lane & 3;
    int aoff[2];
#pragma unroll
    for (int ks = 0; ks < 2; ks++) aoff[ks] = (wr * 64 + fr) * 128 + (((4 * ks + g) ^ (fr & 7)) << 4);
    int boff[4];
    {
      const int f = q4 | ((g & 1) << 2);
#pragma unroll
      for (int c = 0; c < 4; c++) boff[c] = (8 * g + q4) * 256 + (((4 * wc + c) ^ f) << 5) + p4 * 8;
    }
    s16x8 af[2][4]; s16x4 bl[4], bh[4];
    auto rdA = [&](int buf, int m, const char* pa) { af[buf][m] = *(const s16x8*)(pa + m * 2048); };
    auto rdBl = [&](int c, const char* pb) { bl[c] = __builtin_amdgcn_ds_read_tr16_b64_v4i16((lds_s16x4_p)(pb + boff[c])); };
    auto rdBh = [&](int c, const char* pb) { bh[c] = __builtin_amdgcn_ds_read_tr16_b64_v4i16((lds_s16x4_p)(pb + boff[c] + 1024)); };
    BAR_ONLY();
    STAMP(0, 2);
#pragma unroll
    for (int c = 0; c < 4; c++) { rdA(0, c, sA + aoff[0]); rdBl(c, sB); rdBh(c, sB); }
    int ao0 = 0, ao1 = 16384;
#pragma unroll 1
    for (int j2 = 0; j2 < 5; j2++) {
#pragma unroll
      for (int st = 0; st < 4; st++) {
        const int hs = st & 1;
        BAR_ONLY();
        SB();
        const char* pa = sA + (hs == 0 ? ao0 + aoff[1] : ao1 + aoff[0]);
        const char* pb = sB + ((st + 1) & 3) * 8192;
#pragma unroll
        for (int c = 0; c < 4; c++) {
          const s16x8 bf = __builtin_shufflevector(bl[c], bh[c], 0, 1, 2, 3, 4, 5, 6, 7);
#pragma unroll
          for (int m = 0; m < 4; m++) {
            if (ISQ) acc[m][c] = __builtin_amdgcn_mfma_f32_16x16x32_bf16(bf, af[hs][m], acc[m][c], 0, 0, 0);
            else     acc[m][c] = __builtin_amdgcn_mfma_f32_16x16x32_bf16(af[hs][m], bf, acc[m][c], 0, 0, 0);
            SB();
            if (m == 0 && c > 0) rdBh(c - 1, pb);
            if (m == 1) rdA(hs ^ 1, c, pa);
            if (m == 3) { rdBl(c, pb); if (c == 3) rdBh(3, pb); }
            SB();
          }
        }
        if (hs == 1) { ao0 = ao1; ao1 = ao1 == 32768 ? 0 : ao1 + 16384; }
      }
    }
    STAMP(0, 3);
    int l3 = threadIdx.x; asm volatile("" : "+v"(l3));
    char* xch = smem + (l3 >> 6) * 8192 + (l3 & 63) * 16;
#pragma unroll
    for (int i = 0; i < 8; i++) *(f32x4*)(xch + i * 1024) = acc[2 + (i >> 2)][i & 3];
#pragma unroll
    for (int i = 0; i < 8; i++) ep[i >> 2][i & 3] = acc[i >> 2][i & 3];
    RAW_BARRIER();
  }
  int l2 = threadIdx.x; asm volatile("" : "+v"(l2));
  const int g = (l2 >> 4) & 3, fr = l2 & 15;
  if (ISQ) {
    char* pq0 = ws + WS_PQ; char* pq1 = ws + WS_PQ + (size_t)L * 16 * 32 * 2;
#pragma unroll
    for (int c = 0; c < 4; c++) {
      const int n = col0 + 64 * wc + 16 * c + 4 * g;
      const int hd = n / 20, d0 = n - 20 * hd;
#pragma unroll
      for (int m = 0; m < 2; m++) {
        const int rb64 = rowblk * 2 + wr, r = 16 * m + fr;
        u32x2 o;
        o[0] = phi2(ep[m][c][0], ep[m][c][1]);
        o[1] = phi2(ep[m][c][2], ep[m][c][3]);
        const int fi = (rb64 * 32 + hd) * 2 + hf;
        char* dst = d0 < 16 ? pq0 + ((size_t)fi * 64 + (d0 >> 3) * 32 + r) * 16 + (d0 & 7) * 2 : pq1 + ((size_t)fi * 32 + r) * 8;
        *(u32x2*)dst = o;
      }
    }
  } else {
    const int o = (60 * kvj) & 7, zc = o ? 0 : 60;
    const int lane2 = l2 & 63;
    char* fb = smem + 32768;
#pragma unroll
    for (int c = 0; c < 4; c++) {
      u32x4 a;
      if (wc == 0) {
        a[0] = phi2(ep[0][c][0], ep[0][c][1]); a[1] = phi2(ep[0][c][2], ep[0][c][3]);
        a[2] = phi2(ep[1][c][0], ep[1][c][1]); a[3] = phi2(ep[1][c][2], ep[1][c][3]);
      } else {
        a[0] = pack2(ep[0][c][0], ep[0][c][1]); a[1] = pack2(ep[0][c][2], ep[0][c][3]);
        a[2] = pack2(ep[1][c][0], ep[1][c][1]); a[3] = pack2(ep[1][c][2], ep[1][c][3]);
        if (16 * c + fr == zc) a = (u32x4){0x3F803F80u, 0x3F803F80u, 0x3F803F80u, 0x3F803F80u};
      }
      *(u32x4*)(fb + ((((wc * 4 + wr * 2 + hf) * 4 + c) * 64 + lane2) << 4)) = a;
    }
    RAW_BARRIER();
    const int np = wave < 4 ? 1 : 2;
    for (int i = 0; i < np; i++) {
      const int p = wave < 4 ? wave : wave * 2 - 4 + i;
      int ta, tb;
      if (p < 10) { ta = (int)((0x3322211100ull >> (4 * p)) & 15); tb = (int)((0x3232121010ull >> (4 * p)) & 15); }
      else { ta = (o ? 2 : 0) + (p - 10); tb = o ? 0 : 3; }
      const char* pk = fb + ((ta * 64 + lane2) << 4);
      const char* pv = fb + 16384 + ((tb * 64 + lane2) << 4);
      f32x4 dd = {0.f, 0.f, 0.f, 0.f};
#pragma unroll
      for (int rg = 0; rg < 4; rg++)
        dd = __builtin_amdgcn_mfma_f32_16x16x32_bf16(*(const s16x8*)(pk + rg * 4096), *(const s16x8*)(pv + rg * 4096), dd, 0, 0, 0);
      const int kc0 = 16 * ta + 4 * g - o;
      const int hk = kc0 / 20, dk0 = kc0 - 20 * hk;
      const int vcol = 16 * tb + fr;
      const int dv = vcol == zc ? 20 : vcol - o - 20 * hk;
      if (kc0 >= 0 && kc0 < (kvj == 10 ? 40 : 60) && dv >= 0 && (dv < 20 || vcol == zc))
        *(f32x4*)(ws + WS_PART + (size_t)(((3 * kvj + hk) * 16 + rowblk) * 21 + dv) * 80 + dk0 * 4) = dd;
    }
  }
}

__device__ __forceinline__ void gate_role(const u16* __restrict__ x, const u16* __restrict__ gw, float* __restrict__ gate, int blk) {
  const int tid = threadIdx.x, wave = tid >> 6, lane = tid & 63;
  unsigned gwr[5];
#pragma unroll
  for (int i = 0; i < 5; i++) gwr[i] = *(const unsigned*)(gw + 2 * lane + 128 * i);
#pragma unroll 1
  for (int h = 0; h < 2; h++) {
    const int r0 = blk * 128 + wave * 16 + h * 8;
    unsigned v[8][5];
#pragma unroll
    for (int rr = 0; rr < 8; rr++)
#pragma unroll
      for (int i = 0; i < 5; i++) v[rr][i] = *(const unsigned*)(x + (size_t)(r0 + rr) * D + 2 * lane + 128 * i);
    float s[8];
#pragma unroll
    for (int rr = 0; rr < 8; rr++) {
      s[rr] = 0.f;
#pragma unroll
      for (int i = 0; i < 5; i++) {
        unsigned p = pack2(lo_f(v[rr][i]) * lo_f(gwr[i]), hi_f(v[rr][i]) * hi_f(gwr[i]));
        s[rr] += lo_f(p) + hi_f(p);
      }
    }
#pragma unroll
    for (int o = 32; o > 0; o >>= 1)
#pragma unroll
      for (int rr = 0; rr < 8; rr++) s[rr] += __shfl_xor(s[rr], o);
    float sv = s[0];
#pragma unroll
    for (int rr = 1; rr < 8; rr++) sv = lane == rr ? s[rr] : sv;
    if (lane < 8) { float a = rb(sv); gate[r0 + lane] = rb(1.f / (1.f + __expf(-a))); }
  }
}

__device__ __forceinline__ cf zpow(float th, float tl, float rh, float rl, float p) {
  float e = __builtin_amdgcn_exp2f(th * p); e = fmaf(e, (tl * p) * 0.69314718f, e);
  float t = rh * p; float f = (t - __builtin_rintf(t)) + rl * p;
  cf o; o[0] = e * __builtin_amdgcn_cosf(f); o[1] = e * __builtin_amdgcn_sinf(f); return o;
}
#define PKM(o, a, b) "v_pk_mul_f32 " o ", " a ", " b " op_sel:[0,0] op_sel_hi:[0,1]\n\t"
#define PKF(o, a, b) "v_pk_fma_f32 " o ", " a ", " b ", " o " op_sel:[1,1,0] op_sel_hi:[1,0,1] neg_lo:[1,0,0]\n\t"
__device__ __forceinline__ void cmul4(cf& o0, cf& o1, cf& o2, cf& o3, cf a0, cf b0, cf a1, cf b1, cf a2, cf b2, cf a3, cf b3) {
  asm(PKM("%0", "%4", "%5") PKM("%1", "%6", "%7") PKM("%2", "%8", "%9") PKM("%3", "%10", "%11")
      PKF("%0", "%4", "%5") PKF("%1", "%6", "%7") PKF("%2", "%8", "%9") PKF("%3", "%10", "%11")
      : "=&v"(o0), "=&v"(o1), "=&v"(o2), "=&v"(o3)
      : "v"(a0), "v"(b0), "v"(a1), "v"(b1), "v"(a2), "v"(b2), "v"(a3), "v"(b3));
}
__device__ __forceinline__ void batch4(unsigned* h, unsigned* l, cf b, cf t0, cf t1, cf t2, cf t3) {
  cf w0, w1, w2, w3;
  asm(PKM("%0", "%4", "%5") PKM("%1", "%4", "%6") PKM("%2", "%4", "%7") PKM("%3", "%4", "%8")
      PKF("%0", "%4", "%5") PKF("%1", "%4", "%6") PKF("%2", "%4", "%7") PKF("%3", "%4", "%8")
      : "=&v"(w0), "=&v"(w1), "=&v"(w2), "=&v"(w3) : "v"(b), "v"(t0), "v"(t1), "v"(t2), "v"(t3));
  cf w[4] = {w0, w1, w2, w3};
#pragma unroll
  for (int k = 0; k < 4; k++) h[k] = pack2(w[k][0], w[k][1]);
#pragma unroll
  for (int k = 0; k < 4; k++) {
    cf x = {lo_f(h[k]), hi_f(h[k])};
    asm("v_pk_add_f32 %0, %1, %2 neg_lo:[0,1] neg_hi:[0,1]" : "=v"(w[k]) : "v"(w[k]), "v"(x));
  }
#pragma unroll
  for (int k = 0; k < 4; k++) l[k] = pack2(w[k][0], w[k][1]);
}
constexpr int KG_RS = 288, KG_PL = 16 * KG_RS;
__device__ __forceinline__ void kgen_role(char* smem, const u16* __restrict__ logA, const u16* __restrict__ Aim,
                                          const u16* __restrict__ Cr, const u16* __restrict__ Ci, const u16* __restrict__ logdt,
                                          const u16* __restrict__ x, const u16* __restrict__ W, const u16* __restrict__ wo,
                                          char* __restrict__ ws, int blk) {
  const int tid = threadIdx.x, lane = tid & 63;
  const int wave = __builtin_amdgcn_readfirstlane(tid >> 6);
  constexpr int RS = KG_RS, PL = KG_PL;
  const int task = blk * 8 + wave;
  const int d = task / 3;
  const int pi = task * 64 + lane;
#define PF 1
  unsigned pfv = 0; const char* pp;
  {
    const int xcd = blk & 7, i = (blk >> 3) * 64 + lane;
    const int q = i & 15;
    const int ln = (xcd & 1) ? (q < 8 ? 12 + q : 14 + q) : (q < 13 ? q : 7 + q);
    if (i < 1280) pp = (const char*)W + (size_t)(80 * wave + (i >> 4)) * 3840 + ln * 128;
    else { const int id = wave * 640 + (i - 1280); pp = (const char*)x + (size_t)((xcd >> 1) * 512 + (id & 511)) * 1280 + (id >> 9) * 128; }
  }
  const int pfpt = wave == 0 ? 9 : (wave <= 2 ? 0 : (wave <= 5 ? 1 : 2));
#define PFLOAD(pt) do { __builtin_amdgcn_sched_barrier(0); if (pfpt == (pt)) asm volatile("global_load_dword %0, %1, off" : "+v"(pfv) : "v"(pp)); __builtin_amdgcn_sched_barrier(0); } while (0)
  PFLOAD(0);
  const int wid = min(blk * 216 + (tid < 216 ? tid : 215), 51199);
  u32x4 wv;
  {
    const int slot = wid % 160, f = wid / 160;
    const int ks = f % 10, kq = (f / 10) & 3, cb = f / 40;
    int m, h;
    if (slot < 128) { m = (slot >> 6) * 32 + (slot & 31); h = (slot >> 5) & 1; } else { m = 64 + (slot & 15); h = (slot >> 4) & 1; }
    const int n = 80 * cb + m;
    int k0, k1;
    if (ks < 8) { k0 = 20 * (8 * kq + ks) + 4 * h; k1 = k0 + 8; }
    else { k0 = 20 * (8 * kq + 4 * (ks - 8) + 2 * h) + 16; k1 = k0 + 20; }
    if (wave < 4) {
#pragma unroll
      for (int e = 0; e < 2; e++) {
        unsigned a0 = wo[(size_t)(k0 + 2 * e) * D + n], a1 = wo[(size_t)(k0 + 2 * e + 1) * D + n];
        unsigned b0 = wo[(size_t)(k1 + 2 * e) * D + n], b1 = wo[(size_t)(k1 + 2 * e + 1) * D + n];
        wv[e] = a0 | (a1 << 16); wv[2 + e] = b0 | (b1 << 16);
      }
    }
  }
  const float dt = rb(expf(b2f(logdt[d])));
  float ar;
  {
    const float la = b2f(logA[pi]);
    float h = la * 1.44269502f; float l = fmaf(la, 1.44269502f, -h) + la * 1.92596299e-8f;
    float e = __builtin_amdgcn_exp2f(h); e = fmaf(e, l * 0.69314718f, e);
    ar = -rb(e) * dt;
  }
  const float ai = b2f(Aim[pi]) * dt;
  cf C; C[0] = 2.f * (b2f(Cr[pi]) * dt); C[1] = 2.f * (b2f(Ci[pi]) * dt);
  const float th = ar * 1.44269502f, tl = fmaf(ar, 1.44269502f, -th) + ar * 1.92596299e-8f;
  const float rh = ai * 0.159154937f, rl = fmaf(ai, 0.159154937f, -rh) + ai * 6.42063833e-9f;
  char* tb = smem + wave * (2 * PL);
  char* wp = tb + lane * 4;
  const char* rp = tb + (lane & 15) * RS + (lane >> 4) * 16;
  STAMP(0, 2);
  PFLOAD(1);
  const cf one = {1.f, 0.f};
  cf S1 = zpow(th, tl, rh, rl, 1.f), Q1 = zpow(th, tl, rh, rl, 8.f), R1 = zpow(th, tl, rh, rl, 128.f);
  Q1[1] = -Q1[1];
  cf S2, Q2, R2, G1, S3, S4, Q3, P1, R3, T1, G2, G3, S5, S6, S7, P2, CT1, T2, P3, CS4, CT2, CT3, CT4, dm0;
  cmul4(S2, Q2, R2, G1, S1, S1, Q1, Q1, R1, R1, C, R1);
  cmul4(S3, S4, Q3, P1, S2, S1, S2, S2, Q2, Q1, Q2, Q2);
  cmul4(R3, T1, G2, P2, R2, R1, R2, R2, C, R2, P1, P1);
  cmul4(S5, S6, S7, P3, S4, S1, S4, S2, S4, S3, P2, P1);
  cmul4(G3, CT1, T2, dm0, C, R3, C, T1, T1, T1, C, R1);
  cmul4(CS4, CT2, CT3, dm0, CT1, S4, C, T2, CT1, T2, C, R1);
  CT4 = cmul(CT2, T2);
  PFLOAD(2);
  auto rows4 = [&](int row, cf b, cf t0, cf t1, cf t2, cf t3) {
    unsigned h[4], l[4];
    batch4(h, l, b, t0, t1, t2, t3);
#pragma unroll
    for (int k = 0; k < 4; k++) { *(unsigned*)(wp + (row + k) * RS) = h[k]; *(unsigned*)(wp + PL + (row + k) * RS) = l[k]; }
  };
  rows4(0, one, one, Q1, Q2, Q3); rows4(4, P1, one, Q1, Q2, Q3); rows4(8, P2, one, Q1, Q2, Q3); rows4(12, P3, one, Q1, Q2, Q3);
  __builtin_amdgcn_wave_barrier();
  s16x8 bh[4], bl[4];
#pragma unroll
  for (int k = 0; k < 4; k++) { bh[k] = *(const s16x8*)(rp + k * 64); bl[k] = *(const s16x8*)(rp + PL + k * 64); }
  __builtin_amdgcn_wave_barrier();
  STAMP(0, 3);
  f32x4 res[3];
  auto mm = [&](int t) {
    __builtin_amdgcn_wave_barrier();
    f32x4 a = {0.f, 0.f, 0.f, 0.f}, a2 = {0.f, 0.f, 0.f, 0.f};
#pragma unroll
    for (int k = 0; k < 4; k++) {
      s16x8 ah = *(const s16x8*)(rp + k * 64), al = *(const s16x8*)(rp + PL + k * 64);
      a = __builtin_amdgcn_mfma_f32_16x16x32_bf16(ah, bh[k], a, 0, 0, 0);
      a2 = __builtin_amdgcn_mfma_f32_16x16x32_bf16(ah, bl[k], a2, 0, 0, 0);
      a2 = __builtin_amdgcn_mfma_f32_16x16x32_bf16(al, bh[k], a2, 0, 0, 0);
    }
    __builtin_amdgcn_wave_barrier();
    res[t] = a + a2;
  };
  rows4(0, C, one, S1, S2, S3); rows4(4, C, S4, S5, S6, S7); rows4(8, G1, one, S1, S2, S3); rows4(12, G1, S4, S5, S6, S7);
  mm(0);
  STAMP(0, 4);
  rows4(0, G2, one, S2, S4, S6); rows4(4, G3, one, S2, S4, S6);
  {
    unsigned h[4], l[4];
    batch4(h, l, CT1, one, R1, R2, R3);
#pragma unroll
    for (int k = 0; k < 4; k++) { *(unsigned*)(wp + (8 + 2 * k) * RS) = h[k]; *(unsigned*)(wp + PL + (8 + 2 * k) * RS) = l[k]; }
    batch4(h, l, CS4, one, R1, R2, R3);
#pragma unroll
    for (int k = 0; k < 4; k++) { *(unsigned*)(wp + (9 + 2 * k) * RS) = h[k]; *(unsigned*)(wp + PL + (9 + 2 * k) * RS) = l[k]; }
  }
  mm(1);
  rows4(0, CT2, one, R1, R2, R3); rows4(4, CT3, one, R1, R2, R3); rows4(8, CT4, one, one, one, one);
  mm(2);
#pragma unroll
  for (int t = 0; t < 3; t++) *(f32x4*)(tb + t * 1024 + lane * 16) = res[t];
  if (tid < 216 && blk * 216 + tid < 51200) st16(ws + WS_WOF + (size_t)wid * 16, wv);
  __syncthreads();
  {
    const int d0 = (blk * 8) / 3;
#pragma unroll
    for (int it = 0; it < 2; it++) {
      const int e = tid + 512 * it;
      const int seg = e / 192, ent = e - 192 * seg;
      const int ds = d0 + seg;
      const int wlo = max(0, 3 * ds - 8 * blk), whi = min(8, 3 * ds + 3 - 8 * blk);
      if (seg < 4 && wlo < 8) {
        f32x4 a = *(const f32x4*)(smem + wlo * (2 * PL) + ent * 16);
        for (int w = wlo + 1; w < whi; w++) a += *(const f32x4*)(smem + w * (2 * PL) + ent * 16);
        st16(ws + WS_KIMG + (size_t)ds * 6144 + (3 * ds < 8 * blk ? 3072 : 0) + ent * 16, __builtin_bit_cast(u32x4, a));
      }
    }
  }
  asm volatile("s_waitcnt vmcnt(0)" :: "v"(pfv) : "memory");
}

__global__ __launch_bounds__(512, 4) void k1(const u16* __restrict__ x, const u16* __restrict__ logA, const u16* __restrict__ Aim,
                                          const u16* __restrict__ Cr, const u16* __restrict__ Ci, const u16* __restrict__ logdt,
                                          const u16* __restrict__ wqkv, const u16* __restrict__ gw, const u16* __restrict__ wo,
                                          char* __restrict__ ws) {
  __shared__ __attribute__((aligned(16))) char smem[K1_LDS];
  int b = blockIdx.x;
  STAMP(0, 0);
  if (b < NB_GEMM) {
    const int xcd = b & 7, idx = b >> 3;
    const int rowblk = (xcd >> 1) * 4 + (idx & 3), cb = (xcd & 1) * 8 + (idx >> 2);
    if (cb < 5) gemm_role<true>(smem, x, wqkv, ws, rowblk, cb);
    else        gemm_role<false>(smem, x, wqkv, ws, rowblk, cb);
    STAMP(0, 1);
    return;
  }
  b -= NB_GEMM;
  if (b < NB_KG) { kgen_role(smem, logA, Aim, Cr, Ci, logdt, x, wqkv, wo, ws, b); STAMP(0, 1); return; }
  b -= NB_KG;
  gate_role(x, gw, (float*)(ws + WS_GATE), b);
  STAMP(0, 1);
}

constexpr int K2_CS = 4160;
constexpr int K2_KOFF = 8 * K2_CS;
constexpr int K2_TAB = K2_KOFF + 2 * 4608;
constexpr int K2_LDS = K2_TAB + 3072;
constexpr int NB_KV = 32, NB_WO = 0;
__global__ __launch_bounds__(256, 3) void k2(const u16* __restrict__ Dskip, char* __restrict__ ws) {
  __shared__ __attribute__((aligned(16))) char smem[K2_LDS];
  const int tid = threadIdx.x, lane = tid & 63;
  const int wave = __builtin_amdgcn_readfirstlane(tid >> 6);
  STAMP(1, 0);
  if (blockIdx.x < NB_KV) {
    const int hd = blockIdx.x;
    const int h = lane >> 5;
    float* fin = (float*)smem;
    if (tid < 105) {
      const f32x4* src = (const f32x4*)(ws + WS_PART) + (size_t)hd * 16 * 105 + tid;
      f32x4 v[16];
#pragma unroll
      for (int i = 0; i < 16; i++) v[i] = src[i * 105];
#pragma unroll
      for (int st = 1; st < 16; st <<= 1)
#pragma unroll
        for (int i = 0; i < 16; i += 2 * st) v[i] += v[i + st];
      *(f32x4*)(fin + tid * 4) = (f32x4){rb(v[0][0]), rb(v[0][1]), rb(v[0][2]), rb(v[0][3])};
    }
    __syncthreads();
    if (tid < 96) {
      const int m = lane & 31;
      int col = m < 16 ? m : ((m >> 2) == ((hd & 2) ? 5 : 4) ? 16 + (m & 3) : 20);
      if (tid < 64) {
        u32x4 o;
#pragma unroll
        for (int e = 0; e < 4; e++) o[e] = pack2(fin[col * 20 + 8 * h + 2 * e], fin[col * 20 + 8 * h + 2 * e + 1]);
        *(u32x4*)(ws + WS_KVT + (size_t)hd * 1280 + lane * 16) = o;
      } else {
        u32x2 o;
#pragma unroll
        for (int e = 0; e < 2; e++) o[e] = pack2(fin[col * 20 + 16 + 2 * e], fin[col * 20 + 17 + 2 * e]);
        *(u32x2*)(ws + WS_KVT + (size_t)hd * 1280 + 1024 + m * 8) = o;
      }
    }
    STAMP(1, 1);
    return;
  }
  const int d = blockIdx.x - (NB_KV + NB_WO);
  const u16* uT = (const u16*)(ws + WS_UT) + (size_t)d * UT_STRIDE;
  {
    const f32x4* ksrc = (const f32x4*)(ws + WS_KIMG + (size_t)d * 6144) + (tid < 192 ? tid : 191);
    const u32x4* usrc = (const u32x4*)uT;
    f32x4 p0 = ksrc[0];
    if ((d & 7) == 2 || (d & 7) == 5) p0 += ksrc[192];
    u32x4 c0 = usrc[tid + 1], c1 = usrc[tid + 2];
    u32x4 e0, e1;
    if (tid < 2) { e0 = usrc[tid + 257]; e1 = usrc[tid + 258]; }
    if (tid < 64) *(u32x4*)(smem + K2_KOFF + (tid >> 5) * 4608 + (tid & 31) * 16) = (u32x4){0u, 0u, 0u, 0u};
    if (tid < 192) *(f32x4*)(smem + K2_TAB + tid * 16) = p0;
    auto put_copies = [&](u32x4 a, u32x4 b, int xch) {
      unsigned w[8] = {a[0], a[1], a[2], a[3], b[0], b[1], b[2], b[3]};
      unsigned o[7];
#pragma unroll
      for (int k = 0; k < 7; k++) o[k] = __builtin_amdgcn_alignbyte(w[k + 1], w[k], 2);
      char* p = smem + xch * 16;
#pragma unroll
      for (int q = 0; q < 4; q++) {
        *(u32x4*)(p + (2 * q) * K2_CS) = (u32x4){o[q], o[q + 1], o[q + 2], o[q + 3]};
        *(u32x4*)(p + (2 * q + 1) * K2_CS) = (u32x4){w[q + 1], w[q + 2], w[q + 3], w[q + 4]};
      }
    };
    put_copies(c0, c1, tid);
    if (tid < 2) put_copies(e0, e1, tid + 256);
  }
  __syncthreads();
  {
    const float* tab = (const float*)(smem + K2_TAB);
    float kv[8];
#pragma unroll
    for (int e = 0; e < 8; e++) {
      const int j = tid * 8 + 7 - e;
      const int p = (int)rb((float)j);
      const int h = p >> 7, c = (p >> 3) & 15;
      int sh = 31 - __builtin_clz(h | 1); sh = sh > 3 ? 3 : sh;
      const int row = 8 * sh + (((h << 3) | (p & 7)) >> sh);
      kv[e] = tab[((row >> 2) * 16 + c) * 4 + (row & 3)];
    }
    u32x4 hi, lo;
#pragma unroll
    for (int e = 0; e < 4; e++) {
      hi[e] = pack2(kv[2 * e], kv[2 * e + 1]);
      lo[e] = pack2(ssub(kv[2 * e], lo_f(hi[e])), ssub(kv[2 * e + 1], hi_f(hi[e])));
    }
    *(u32x4*)(smem + K2_KOFF + 512 + tid * 16) = hi;
    *(u32x4*)(smem + K2_KOFF + 4608 + 512 + tid * 16) = lo;
  }
  __syncthreads();
  STAMP(1, 2);
  f32x4 acc[8];
#pragma unroll
  for (int t = 0; t < 8; t++) acc[t] = (f32x4){0.f, 0.f, 0.f, 0.f};
  {
    const int i = lane & 15, kg = lane >> 4, e = kg >> 1, bb = kg & 1;
    const char* ap = smem + (i & 7) * K2_CS + (2 * e - bb + 2 + (i >> 3)) * 16;
    const char* bp = smem + K2_KOFF + (16 + i - e) * 32 + bb * 16;
#pragma unroll
    for (int it = 0; it < 2; it++) {
      const int d0 = 2 * (wave + 4 * it);
      s16x8 af[8], bh[8], bl[8];
#pragma unroll
      for (int j = 0; j < 8; j++) af[j] = *(const s16x8*)(ap + (32 * j + 2 * d0) * 16);
#pragma unroll
      for (int m = 0; m < 8; m++) {
        bh[m] = *(const s16x8*)(bp + (16 * m - d0) * 32);
        bl[m] = *(const s16x8*)(bp + (16 * m - d0) * 32 + 4608);
      }
#pragma unroll
      for (int m = 0; m < 8; m++) {
#pragma unroll
        for (int j = 0; j + m < 8; j++) acc[j + m] = __builtin_amdgcn_mfma_f32_16x16x32_bf16(af[j], bh[m], acc[j + m], 0, 0, 0);
#pragma unroll
        for (int j = 0; j + m < 8; j++) acc[j + m] = __builtin_amdgcn_mfma_f32_16x16x32_bf16(af[j], bl[m], acc[j + m], 0, 0, 0);
      }
    }
  }
  STAMP(1, 3);
  u32x2 uu[2];
#pragma unroll
  for (int t = 0; t < 2; t++) uu[t] = *(const u32x2*)(uT + UT_PAD + 256 * (2 * wave + t) + 4 * lane);
  const float dsk = b2f(Dskip[d]);
  __syncthreads();
  float* red = (float*)smem;
#pragma unroll
  for (int t = 0; t < 8; t++) *(f32x4*)(red + ((wave * 8 + t) * 64 + (lane & 15) * 4 + (lane >> 4)) * 4) = acc[t];
  __syncthreads();
  u16* aT = (u16*)(ws + WS_AT) + (size_t)d * L;
#pragma unroll
  for (int t = 0; t < 2; t++) {
    const int tt = 2 * wave + t;
    f32x4 s = *(const f32x4*)(red + ((0 * 8 + tt) * 64 + lane) * 4);
    f32x4 s1 = *(const f32x4*)(red + ((1 * 8 + tt) * 64 + lane) * 4);
    f32x4 s2 = *(const f32x4*)(red + ((2 * 8 + tt) * 64 + lane) * 4);
    f32x4 s3 = *(const f32x4*)(red + ((3 * 8 + tt) * 64 + lane) * 4);
    s = (s + s1) + (s2 + s3);
    u32x2 ov;
#pragma unroll
    for (int e = 0; e < 2; e++) {
      unsigned y = pack2(s[2 * e], s[2 * e + 1]);
      unsigned k = pack2(lo_f(uu[t][e]) * dsk, hi_f(uu[t][e]) * dsk);
      ov[e] = pack2(lo_f(y) + lo_f(k), hi_f(y) + hi_f(k));
    }
    *(u32x2*)(aT + 256 * tt + 4 * lane) = ov;
  }
  STAMP(1, 1);
}

constexpr int K3_WO = 0;
constexpr int K3_KV = 102400;
constexpr int K3_AT = K3_KV + 40960;
constexpr int K3_LDS = K3_AT + 80 * 144;
__global__ __launch_bounds__(512, 2) void k3(char* __restrict__ ws, u16* __restrict__ out) {
  __shared__ __attribute__((aligned(16))) char smem[K3_LDS];
  const int tid = threadIdx.x, lane = tid & 63;
  const int wave = __builtin_amdgcn_readfirstlane(tid >> 6);
  STAMP(2, 0);
  const int xcd = blockIdx.x & 7, idx = blockIdx.x >> 3;
  const int rowblk = xcd * 4 + (idx >> 3), cb = idx & 7;
  const int l0 = rowblk * 64, c0 = cb * 80;
  const int kq = wave & 3, t = wave >> 2;
  const int r = lane & 31, h = lane >> 5;
#pragma unroll
  for (int i = 0; i < 5; i++) glds16(ws + WS_KVT + (size_t)(wave * 5 + i) * 1024 + lane * 16, smem + K3_KV + (wave * 5 + i) * 1024);
  u32x4 pf0[8]; u32x2 pf1[8];
  {
    const char* p0 = ws + WS_PQ + ((size_t)((rowblk * 32 + kq * 8) * 2 + t) * 64 + lane) * 16;
    const char* p1 = ws + WS_PQ + (size_t)L * 16 * 32 * 2 + ((size_t)((rowblk * 32 + kq * 8) * 2 + t) * 32 + r) * 8;
#pragma unroll
    for (int hh = 0; hh < 8; hh++) {
      asm volatile("global_load_dwordx4 %0, %1, off offset:%2" : "=v"(pf0[hh]) : "v"(p0 + (hh >> 1) * 4096), "i"((hh & 1) * 2048));
      asm volatile("global_load_dwordx2 %0, %1, off offset:%2" : "=v"(pf1[hh]) : "v"(p1), "i"(hh * 512));
    }
  }
  {
    const char* src = ws + WS_WOF + (size_t)cb * 102400 + lane * 16;
#pragma unroll
    for (int i = 0; i < 13; i++) { int p = i * 8 + wave; if (p < 100) glds16(src + p * 1024, smem + K3_WO + p * 1024); }
  }
  u32x4 av[2];
  {
    const char* aT = ws + WS_AT;
#pragma unroll
    for (int i = 0; i < 2; i++) {
      int id = tid + 512 * i; if (id > 639) id = 639;
      const char* p = aT + ((size_t)(c0 + (id >> 3)) * L + l0 + 8 * (id & 7)) * 2;
      asm volatile("global_load_dwordx4 %0, %1, off" : "=v"(av[i]) : "v"(p));
    }
  }
  float gt[2];
  {
    const float* gate = (const float*)(ws + WS_GATE) + l0;
#pragma unroll
    for (int i = 0; i < 2; i++) {
      int id = tid + 512 * i; if (id > 639) id = 639;
      asm volatile("global_load_dword %0, %1, off" : "=v"(gt[i]) : "v"(gate + id / 10));
    }
  }
  if (wave < 4) VMCNT(17); else VMCNT(16);
  asm volatile("" : "+v"(pf0[0]), "+v"(pf0[1]), "+v"(pf0[2]), "+v"(pf0[3]), "+v"(pf0[4]), "+v"(pf0[5]), "+v"(pf0[6]), "+v"(pf0[7]));
  asm volatile("" : "+v"(pf1[0]), "+v"(pf1[1]), "+v"(pf1[2]), "+v"(pf1[3]), "+v"(pf1[4]), "+v"(pf1[5]), "+v"(pf1[6]), "+v"(pf1[7]));
  RAW_BARRIER();
  STAMP(2, 2);
  u32x4 ob[10];
  {
    unsigned rem[8][2];
#pragma unroll
    for (int hh = 0; hh < 8; hh++) {
      const char* kp = smem + K3_KV + (kq * 8 + hh) * 1280;
      s16x8 a0 = *(const s16x8*)(kp + lane * 16);
      u32x2 a1h = *(const u32x2*)(kp + 1024 + r * 8);
      u32x4 a1u = {a1h[0], a1h[1], 0u, 0u};
      u32x4 b1u = {h ? 0u : pf1[hh][0], h ? 0u : pf1[hh][1], 0u, 0u};
      f32x16 c;
#pragma unroll
      for (int i = 0; i < 16; i++) c[i] = 0.f;
      c = __builtin_amdgcn_mfma_f32_32x32x16_bf16(a0, __builtin_bit_cast(s16x8, pf0[hh]), c, 0, 0, 0);
      c = __builtin_amdgcn_mfma_f32_32x32x16_bf16(__builtin_bit_cast(s16x8, a1u), __builtin_bit_cast(s16x8, b1u), c, 0, 0, 0);
      float den = rb(rb(c[12]) + 1e-6f);
      float inv = __builtin_amdgcn_rcpf(den);
      unsigned o[6];
#pragma unroll
      for (int q = 0; q < 6; q++) {
        unsigned n0 = pack2(c[2 * q], c[2 * q + 1]);
        o[q] = pack2(lo_f(n0) * inv, hi_f(n0) * inv);
      }
      ob[hh] = (u32x4){o[0], o[1], o[2], o[3]};
      rem[hh][0] = o[4]; rem[hh][1] = o[5];
    }
#pragma unroll
    for (int G = 0; G < 2; G++)
      ob[8 + G] = (u32x4){h ? rem[4 * G + 2][0] : rem[4 * G][0], h ? rem[4 * G + 2][1] : rem[4 * G][1],
                          h ? rem[4 * G + 3][0] : rem[4 * G + 1][0], h ? rem[4 * G + 3][1] : rem[4 * G + 1][1]};
  }
  STAMP(2, 3);
  VMCNT(0);
  asm volatile("" : "+v"(av[0]), "+v"(av[1]), "+v"(gt[0]), "+v"(gt[1]));
#pragma unroll
  for (int i = 0; i < 2; i++) { int id = tid + 512 * i; if (id < 640) *(u32x4*)(smem + K3_AT + (id >> 3) * 144 + (id & 7) * 16) = av[i]; }
  RAW_BARRIER();
  STAMP(2, 4);
  f32x16 acc[3];
#pragma unroll
  for (int c = 0; c < 3; c++)
#pragma unroll
    for (int i = 0; i < 16; i++) acc[c][i] = 0.f;
  {
    const char* wp = smem + K3_WO + kq * 25600;
    const int o2 = 2048 + (h * 16 + (lane & 15)) * 16;
#pragma unroll
    for (int ks = 0; ks < 10; ks++) {
      s16x8 w0 = *(const s16x8*)(wp + ks * 2560 + lane * 16);
      s16x8 w1 = *(const s16x8*)(wp + ks * 2560 + 1024 + lane * 16);
      s16x8 w2 = *(const s16x8*)(wp + ks * 2560 + o2);
      s16x8 b = __builtin_bit_cast(s16x8, ob[ks]);
      acc[0] = __builtin_amdgcn_mfma_f32_32x32x16_bf16(w0, b, acc[0], 0, 0, 0);
      acc[1] = __builtin_amdgcn_mfma_f32_32x32x16_bf16(w1, b, acc[1], 0, 0, 0);
      acc[2] = __builtin_amdgcn_mfma_f32_32x32x16_bf16(w2, b, acc[2], 0, 0, 0);
    }
  }
  STAMP(2, 5);
  RAW_BARRIER();
  {
    float* pr = (float*)smem + (size_t)(kq * 64 + 32 * t + r) * 84 + 4 * h;
#pragma unroll
    for (int c = 0; c < 3; c++)
#pragma unroll
      for (int q = 0; q < 4; q++)
        if (c < 2 || q < 2) *(f32x4*)(pr + 32 * c + 8 * q) = (f32x4){acc[c][4 * q], acc[c][4 * q + 1], acc[c][4 * q + 2], acc[c][4 * q + 3]};
  }
  RAW_BARRIER();
  {
#pragma unroll
    for (int i2 = 0; i2 < 2; i2++) {
      const int i = tid + 512 * i2;
      if (i < 640) {
        const int rr = i / 10, oc = i - 10 * rr;
        const float g = gt[i2]; const float g1 = rb(1.f - g);
        f32x4 s0 = {0.f, 0.f, 0.f, 0.f}, s1 = {0.f, 0.f, 0.f, 0.f};
#pragma unroll
        for (int w = 0; w < 4; w++) {
          const float* pr = (const float*)smem + (size_t)(w * 64 + rr) * 84 + oc * 8;
          s0 += *(const f32x4*)pr; s1 += *(const f32x4*)(pr + 4);
        }
        u32x4 ov;
#pragma unroll
        for (int e = 0; e < 4; e++) {
          unsigned bb = e < 2 ? pack2(s0[2 * e], s0[2 * e + 1]) : pack2(s1[2 * e - 4], s1[2 * e - 3]);
          float a0 = b2f(*(const u16*)(smem + K3_AT + (oc * 8 + 2 * e) * 144 + rr * 2));
          float a1 = b2f(*(const u16*)(smem + K3_AT + (oc * 8 + 2 * e + 1) * 144 + rr * 2));
          unsigned ga = pack2(g * a0, g * a1), gb = pack2(g1 * lo_f(bb), g1 * hi_f(bb));
          ov[e] = pack2(lo_f(ga) + lo_f(gb), hi_f(ga) + hi_f(gb));
        }
        *(u32x4*)(out + (size_t)(l0 + rr) * D + c0 + oc * 8) = ov;
      }
    }
  }
  STAMP(2, 1);
}

extern "C" void kernel_launch(void* const* d_in, const int* in_sizes, int n_in, void* d_out, int out_size, void* d_ws, size_t ws_size,
                              hipStream_t stream) {
  const u16* u = (const u16*)d_in[0]; const u16* logA = (const u16*)d_in[1]; const u16* Aim = (const u16*)d_in[2];
  const u16* Cr = (const u16*)d_in[3]; const u16* Ci = (const u16*)d_in[4]; const u16* logdt = (const u16*)d_in[5];
  const u16* Dsk = (const u16*)d_in[6]; const u16* wqkv = (const u16*)d_in[8]; const u16* wo = (const u16*)d_in[9];
  const u16* gw = (const u16*)d_in[10];
  char* ws = (char*)d_ws;
  k1<<<NB_GEMM + NB_GATE + NB_KG, 512, 0, stream>>>(u, logA, Aim, Cr, Ci, logdt, wqkv, gw, wo, ws);
  k2<<<NB_KV + NB_WO + D, 256, 0, stream>>>(Dsk, ws);
  k3<<<256, 512, 0, stream>>>(ws, (u16*)d_out);
}
```

```cpp
#include <hip/hip_runtime.h>
#include <hip/hip_bf16.h>
#include <stdint.h>

typedef unsigned short u16;
typedef __attribute__((ext_vector_type(8))) short s16x8;
typedef __attribute__((ext_vector_type(4))) short s16x4;
typedef __attribute__((ext_vector_type(4))) float f32x4;
typedef __attribute__((ext_vector_type(16))) float f32x16;
typedef __attribute__((ext_vector_type(4))) unsigned u32x4;
typedef __attribute__((ext_vector_type(2))) unsigned u32x2;
typedef __attribute__((address_space(3))) s16x4* lds_s16x4_p;
typedef const __attribute__((address_space(1))) void* gptr_t;
typedef __attribute__((address_space(3))) void* lptr_t;

constexpr int L = 2048, D = 640, NS = 192, NH = 32;
constexpr int UT_PAD = 32, UT_STRIDE = L + UT_PAD;
constexpr size_t WS_UT   = 0;
constexpr size_t WS_KIMG = WS_UT + (size_t)D * UT_STRIDE * 2;
constexpr size_t WS_PQ   = WS_KIMG + (size_t)D * 6144;
constexpr size_t WS_PART = WS_PQ + (size_t)L * D * 2;
constexpr size_t WS_GATE = WS_PART + (size_t)32 * 16 * 1680;
constexpr size_t WS_WOF  = WS_GATE + (size_t)L * 4;
constexpr size_t WS_KVT  = WS_WOF + (size_t)D * D * 2;
constexpr size_t WS_AT   = WS_KVT + (size_t)32 * 1280;
constexpr size_t WS_CNT  = WS_AT + (size_t)D * L * 2;
constexpr size_t WS_END  = WS_CNT + 4096;
#define STAMP(kid, slot) do {} while (0)

__device__ __forceinline__ float b2f(u16 v) { return __uint_as_float(((unsigned)v) << 16); }
typedef __bf16 bf16x2_t __attribute__((ext_vector_type(2)));
typedef float f32x2 __attribute__((ext_vector_type(2)));
__device__ __forceinline__ unsigned pack2(float a, float b) { f32x2 v = {a, b}; bf16x2_t r = __builtin_convertvector(v, bf16x2_t); return __builtin_bit_cast(unsigned, r); }
__device__ __forceinline__ float lo_f(unsigned u) { return __uint_as_float(u << 16); }
__device__ __forceinline__ float hi_f(unsigned u) { return __uint_as_float(u & 0xffff0000u); }
__device__ __forceinline__ float rb(float f) { return lo_f(pack2(f, f)); }
__device__ __forceinline__ unsigned phi2(float a, float b) {
  unsigned t = pack2(a, b); float t0 = lo_f(t), t1 = hi_f(t);
  float e0 = __builtin_amdgcn_fmed3f(__builtin_amdgcn_exp2f(t0 * 1.44269504f), 0.f, 1.f);
  float e1 = __builtin_amdgcn_fmed3f(__builtin_amdgcn_exp2f(t1 * 1.44269504f), 0.f, 1.f);
  unsigned r = pack2(__builtin_fmaxf(t0, 0.f) + e0, __builtin_fmaxf(t1, 0.f) + e1);
  return pack2(lo_f(r) + 1e-6f, hi_f(r) + 1e-6f);
}
#define VMCNT(n) asm volatile("s_waitcnt vmcnt(" #n ")" ::: "memory")
#define RAW_BARRIER() do { asm volatile("s_waitcnt lgkmcnt(0)" ::: "memory"); __builtin_amdgcn_s_barrier(); asm volatile("" ::: "memory"); } while (0)
__device__ __forceinline__ void glds16(const void* g, void* l) { __builtin_amdgcn_global_load_lds((gptr_t)g, (lptr_t)l, 16, 0, 0); }

#define OPQ_S(v) asm volatile("" : "+s"(v))
typedef f32x2 cf;
__device__ __forceinline__ float smul(float a, float b) { float o; asm("v_mul_f32 %0, %1, %2" : "=v"(o) : "v"(a), "v"(b)); return o; }
__device__ __forceinline__ float sfma(float a, float b, float c) { float o; asm("v_fma_f32 %0, %1, %2, %3" : "=v"(o) : "v"(a), "v"(b), "v"(c)); return o; }
__device__ __forceinline__ float sfnma(float a, float b, float c) { float o; asm("v_fma_f32 %0, -%1, %2, %3" : "=v"(o) : "v"(a), "v"(b), "v"(c)); return o; }
__device__ __forceinline__ float ssub(float a, float b) { float o; asm("v_sub_f32 %0, %1, %2" : "=v"(o) : "v"(a), "v"(b)); return o; }
__device__ __forceinline__ cf cmul(cf a, cf b) {
  cf o;
  asm("v_pk_mul_f32 %0, %1, %2 op_sel:[0,0] op_sel_hi:[0,1]\n\t"
      "v_pk_fma_f32 %0, %1, %2, %0 op_sel:[1,1,0] op_sel_hi:[1,0,1] neg_lo:[1,0,0]" : "=&v"(o) : "v"(a), "v"(b));
  return o;
}

constexpr int K1_LDS = 81920;
constexpr int NB_GEMM = 256, NB_GATE = 16, NB_KG = 240;
#define WT 1
__device__ __forceinline__ void st16(void* p, u32x4 v) {
  asm volatile("global_store_dwordx4 %0, %1, off sc1\n\ts_nop 2" :: "v"(p), "v"(v) : "memory");
}

#define BAR_ONLY() do { asm volatile("" ::: "memory"); __builtin_amdgcn_s_barrier(); asm volatile("" ::: "memory"); } while (0)
#define SB() __builtin_amdgcn_sched_barrier(0)
template <bool ISQ>
__device__ __forceinline__ void gemm_role(char* smem, const u16* __restrict__ x, const u16* __restrict__ W, char* __restrict__ ws,
                                          int rowblk, int cb) {
  const int tid = threadIdx.x, lane = tid & 63;
  const int wave = __builtin_amdgcn_readfirstlane(tid >> 6);
  const int row0 = rowblk * 128, col0 = cb * 128;
  const int kvj = cb - 5;
  char* sB = smem; char* sA = smem + 32768;
  const int wr = wave & 1, wc = (wave >> 1) & 1, hf = wave >> 2;
  f32x4 ep[2][4];
  if (wave >= 4) {
    const int lw = wave - 4;
    const char* pA = (const char*)x + (size_t)(row0 + 32 * lw + (lane >> 3)) * 1280 + (((lane & 7) ^ (lane >> 3)) << 4);
    const int fB = (lane >> 4) | ((lw & 1) << 2);
    const int c16 = (((lane & 15) >> 1) ^ fB) * 2 + (lane & 1);
    int bcol = col0 + 8 * c16;
    if (!ISQ) bcol = min(640 + 640 * (c16 >> 3) + ((60 * kvj) & ~7) + 8 * (c16 & 7), 1912);
    const char* pB = (const char*)W + (size_t)(8 * lw + (lane >> 4)) * 3840 + bcol * 2;
    u32x2 tr[8];
    int tro;
    {
      const int fr = lane & 15, g = lane >> 4, p4 = lane & 3, r = 8 * g + (fr >> 2);
      tro = 32768 + r * 128 + (((2 * lw + (p4 >> 1)) ^ (r & 7)) << 4) + (p4 & 1) * 8;
    }
    auto issA = [&](int slot_off, int h) {
      glds16(pA + (16 * h) * 1280, sA + slot_off + (4 * lw + 2 * h) * 1024);
      glds16(pA + (16 * h + 8) * 1280, sA + slot_off + (4 * lw + 2 * h + 1) * 1024);
    };
    auto issB = [&](int slot_off) {
      glds16(pB, sB + slot_off + (2 * lw) * 1024);
      glds16(pB + 4 * 3840, sB + slot_off + (2 * lw + 1) * 1024);
      pB += (size_t)32 * 3840;
    };
    issA(0, 0); issB(0); issA(0, 1); pA += 128;
    issB(8192); issA(16384, 0); issB(16384); issA(16384, 1); pA += 128;
    VMCNT(8);
    BAR_ONLY();
    int ao = 32768, bo = 24576;
#pragma unroll 1
    for (int j = 0; j < 10; j++) {
      if (j < 9) VMCNT(6); else VMCNT(0);
      BAR_ONLY();
      if (j == cb) {
        const int a0 = (int)(size_t)(lptr_t)smem + (ao == 32768 ? 0 : ao + 16384);
        const int a = a0 + tro, a2 = a0 + (tro ^ 64) + 512;
        asm volatile("ds_read_b64_tr_b16 %0, %8\n\tds_read_b64_tr_b16 %1, %9\n\tds_read_b64_tr_b16 %2, %8 offset:4096\n\tds_read_b64_tr_b16 %3, %9 offset:4096\n\t"
                     "ds_read_b64_tr_b16 %4, %8 offset:8192\n\tds_read_b64_tr_b16 %5, %9 offset:8192\n\tds_read_b64_tr_b16 %6, %8 offset:12288\n\tds_read_b64_tr_b16 %7, %9 offset:12288\n\t"
                     "s_waitcnt lgkmcnt(0)"
                     : "=&v"(tr[0]), "=&v"(tr[1]), "=&v"(tr[2]), "=&v"(tr[3]), "=&v"(tr[4]), "=&v"(tr[5]), "=&v"(tr[6]), "=&v"(tr[7])
                     : "v"(a), "v"(a2) : "memory");
      }
      if (j < 9) { issB(bo); bo = (bo + 8192) & 32767; }
      if (j < 8) issA(ao, 0);
      if (j < 8) VMCNT(4); else VMCNT(2);
      BAR_ONLY();
      if (j < 8) { issB(bo); bo = (bo + 8192) & 32767; issA(ao, 1); pA += 128; }
      ao = ao == 32768 ? 0 : ao + 16384;
    }
    if (cb < 10) {
      u16* dst = (u16*)(ws + WS_UT) + (size_t)(64 * cb + 16 * lw + (lane & 15)) * UT_STRIDE + 8 * (lane >> 4);
#pragma unroll
      for (int i = 0; i < 4; i++) st16(dst + UT_PAD + row0 + 32 * i, (u32x4){tr[2 * i][0], tr[2 * i][1], tr[2 * i + 1][0], tr[2 * i + 1][1]});
      if (rowblk == 0) st16(dst, (u32x4){0u, 0u, 0u, 0u});
    }
    BAR_ONLY();
    char* xch = smem + (wave & 3) * 8192 + lane * 16;
#pragma unroll
    for (int i = 0; i < 8; i++) ep[i >> 2][i & 3] = *(const f32x4*)(xch + i * 1024);
  } else {
    const int g = lane >> 4, fr = lane & 15;
    f32x4 acc[4][4];
#pragma unroll
    for (int m = 0; m < 4; m++)
#pragma unroll
      for (int c = 0; c < 4; c++) acc[m][c] = (f32x4){0.f, 0.f, 0.f, 0.f};
    const int q4 = fr >> 2, p4 = lane & 3;
    int aoff[2];
#pragma unroll
    for (int ks = 0; ks < 2; ks++) aoff[ks] = (wr * 64 + fr) * 128 + (((4 * ks + g) ^ (fr & 7)) << 4);
    int boff[4];
    {
      const int f = q4 | ((g & 1) << 2);
#pragma unroll
      for (int c = 0; c < 4; c++) boff[c] = (8 * g + q4) * 256 + (((4 * wc + c) ^ f) << 5) + p4 * 8;
    }
    s16x8 af[2][4]; s16x4 bl[4], bh[4];
    auto rdA = [&](int buf, int m, const char* pa) { af[buf][m] = *(const s16x8*)(pa + m * 2048); };
    auto rdBl = [&](int c, const char* pb) { bl[c] = __builtin_amdgcn_ds_read_tr16_b64_v4i16((lds_s16x4_p)(pb + boff[c])); };
    auto rdBh = [&](int c, const char* pb) { bh[c] = __builtin_amdgcn_ds_read_tr16_b64_v4i16((lds_s16x4_p)(pb + boff[c] + 1024)); };
    BAR_ONLY();
    STAMP(0, 2);
#pragma unroll
    for (int c = 0; c < 4; c++) { rdA(0, c, sA + aoff[0]); rdBl(c, sB); rdBh(c, sB); }
    int ao0 = 0, ao1 = 16384;
#pragma unroll 1
    for (int j2 = 0; j2 < 5; j2++) {
#pragma unroll
      for (int st = 0; st < 4; st++) {
        const int hs = st & 1;
        BAR_ONLY();
        SB();
        const char* pa = sA + (hs == 0 ? ao0 + aoff[1] : ao1 + aoff[0]);
        const char* pb = sB + ((st + 1) & 3) * 8192;
#pragma unroll
        for (int c = 0; c < 4; c++) {
          const s16x8 bf = __builtin_shufflevector(bl[c], bh[c], 0, 1, 2, 3, 4, 5, 6, 7);
#pragma unroll
          for (int m = 0; m < 4; m++) {
            if (ISQ) acc[m][c] = __builtin_amdgcn_mfma_f32_16x16x32_bf16(bf, af[hs][m], acc[m][c], 0, 0, 0);
            else     acc[m][c] = __builtin_amdgcn_mfma_f32_16x16x32_bf16(af[hs][m], bf, acc[m][c], 0, 0, 0);
            SB();
            if (m == 0 && c > 0) rdBh(c - 1, pb);
            if (m == 1) rdA(hs ^ 1, c, pa);
            if (m == 3) { rdBl(c, pb); if (c == 3) rdBh(3, pb); }
            SB();
          }
        }
        if (hs == 1) { ao0 = ao1; ao1 = ao1 == 32768 ? 0 : ao1 + 16384; }
      }
    }
    STAMP(0, 3);
    int l3 = threadIdx.x; asm volatile("" : "+v"(l3));
    char* xch = smem + (l3 >> 6) * 8192 + (l3 & 63) * 16;
#pragma unroll
    for (int i = 0; i < 8; i++) *(f32x4*)(xch + i * 1024) = acc[2 + (i >> 2)][i & 3];
#pragma unroll
    for (int i = 0; i < 8; i++) ep[i >> 2][i & 3] = acc[i >> 2][i & 3];
    RAW_BARRIER();
  }
  int l2 = threadIdx.x; asm volatile("" : "+v"(l2));
  const int g = (l2 >> 4) & 3, fr = l2 & 15;
  if (ISQ) {
    char* pq0 = ws + WS_PQ; char* pq1 = ws + WS_PQ + (size_t)L * 16 * 32 * 2;
#pragma unroll
    for (int c = 0; c < 4; c++) {
      const int n = col0 + 64 * wc + 16 * c + 4 * g;
      const int hd = n / 20, d0 = n - 20 * hd;
#pragma unroll
      for (int m = 0; m < 2; m++) {
        const int rb64 = rowblk * 2 + wr, r = 16 * m + fr;
        u32x2 o;
        o[0] = phi2(ep[m][c][0], ep[m][c][1]);
        o[1] = phi2(ep[m][c][2], ep[m][c][3]);
        const int fi = (rb64 * 32 + hd) * 2 + hf;
        char* dst = d0 < 16 ? pq0 + ((size_t)fi * 64 + (d0 >> 3) * 32 + r) * 16 + (d0 & 7) * 2 : pq1 + ((size_t)fi * 32 + r) * 8;
        *(u32x2*)dst = o;
      }
    }
  } else {
    const int o = (60 * kvj) & 7, zc = o ? 0 : 60;
    const int lane2 = l2 & 63;
    char* fb = smem + 32768;
#pragma unroll
    for (int c = 0; c < 4; c++) {
      u32x4 a;
      if (wc == 0) {
        a[0] = phi2(ep[0][c][0], ep[0][c][1]); a[1] = phi2(ep[0][c][2], ep[0][c][3]);
        a[2] = phi2(ep[1][c][0], ep[1][c][1]); a[3] = phi2(ep[1][c][2], ep[1][c][3]);
      } else {
        a[0] = pack2(ep[0][c][0], ep[0][c][1]); a[1] = pack2(ep[0][c][2], ep[0][c][3]);
        a[2] = pack2(ep[1][c][0], ep[1][c][1]); a[3] = pack2(ep[1][c][2], ep[1][c][3]);
        if (16 * c + fr == zc) a = (u32x4){0x3F803F80u, 0x3F803F80u, 0x3F803F80u, 0x3F803F80u};
      }
      *(u32x4*)(fb + ((((wc * 4 + wr * 2 + hf) * 4 + c) * 64 + lane2) << 4)) = a;
    }
    RAW_BARRIER();
    const int np = wave < 4 ? 1 : 2;
    for (int i = 0; i < np; i++) {
      const int p = wave < 4 ? wave : wave * 2 - 4 + i;
      int ta, tb;
      if (p < 10) { ta = (int)((0x3322211100ull >> (4 * p)) & 15); tb = (int)((0x3232121010ull >> (4 * p)) & 15); }
      else { ta = (o ? 2 : 0) + (p - 10); tb = o ? 0 : 3; }
      const char* pk = fb + ((ta * 64 + lane2) << 4);
      const char* pv = fb + 16384 + ((tb * 64 + lane2) << 4);
      f32x4 dd = {0.f, 0.f, 0.f, 0.f};
#pragma unroll
      for (int rg = 0; rg < 4; rg++)
        dd = __builtin_amdgcn_mfma_f32_16x16x32_bf16(*(const s16x8*)(pk + rg * 4096), *(const s16x8*)(pv + rg * 4096), dd, 0, 0, 0);
      const int kc0 = 16 * ta + 4 * g - o;
      const int hk = kc0 / 20, dk0 = kc0 - 20 * hk;
      const int vcol = 16 * tb + fr;
      const int dv = vcol == zc ? 20 : vcol - o - 20 * hk;
      if (kc0 >= 0 && kc0 < (kvj == 10 ? 40 : 60) && dv >= 0 && (dv < 20 || vcol == zc))
        *(f32x4*)(ws + WS_PART + (size_t)(((3 * kvj + hk) * 16 + rowblk) * 21 + dv) * 80 + dk0 * 4) = dd;
    }
  }
}

__device__ __forceinline__ void gate_role(const u16* __restrict__ x, const u16* __restrict__ gw, float* __restrict__ gate, int blk) {
  const int tid = threadIdx.x, wave = tid >> 6, lane = tid & 63;
  unsigned gwr[5];
#pragma unroll
  for (int i = 0; i < 5; i++) gwr[i] = *(const unsigned*)(gw + 2 * lane + 128 * i);
#pragma unroll 1
  for (int h = 0; h < 2; h++) {
    const int r0 = blk * 128 + wave * 16 + h * 8;
    unsigned v[8][5];
#pragma unroll
    for (int rr = 0; rr < 8; rr++)
#pragma unroll
      for (int i = 0; i < 5; i++) v[rr][i] = *(const unsigned*)(x + (size_t)(r0 + rr) * D + 2 * lane + 128 * i);
    float s[8];
#pragma unroll
    for (int rr = 0; rr < 8; rr++) {
      s[rr] = 0.f;
#pragma unroll
      for (int i = 0; i < 5; i++) {
        unsigned p = pack2(lo_f(v[rr][i]) * lo_f(gwr[i]), hi_f(v[rr][i]) * hi_f(gwr[i]));
        s[rr] += lo_f(p) + hi_f(p);
      }
    }
#pragma unroll
    for (int o = 32; o > 0; o >>= 1)
#pragma unroll
      for (int rr = 0; rr < 8; rr++) s[rr] += __shfl_xor(s[rr], o);
    float sv = s[0];
#pragma unroll
    for (int rr = 1; rr < 8; rr++) sv = lane == rr ? s[rr] : sv;
    if (lane < 8) { float a = rb(sv); gate[r0 + lane] = rb(1.f / (1.f + __expf(-a))); }
  }
}

__device__ __forceinline__ cf zpow(float th, float tl, float rh, float rl, float p) {
  float e = __builtin_amdgcn_exp2f(th * p); e = fmaf(e, (tl * p) * 0.69314718f, e);
  float t = rh * p; float f = (t - __builtin_rintf(t)) + rl * p;
  cf o; o[0] = e * __builtin_amdgcn_cosf(f); o[1] = e * __builtin_amdgcn_sinf(f); return o;
}
#define PKM(o, a, b) "v_pk_mul_f32 " o ", " a ", " b " op_sel:[0,0] op_sel_hi:[0,1]\n\t"
#define PKF(o, a, b) "v_pk_fma_f32 " o ", " a ", " b ", " o " op_sel:[1,1,0] op_sel_hi:[1,0,1] neg_lo:[1,0,0]\n\t"
__device__ __forceinline__ void cmul4(cf& o0, cf& o1, cf& o2, cf& o3, cf a0, cf b0, cf a1, cf b1, cf a2, cf b2, cf a3, cf b3) {
  asm(PKM("%0", "%4", "%5") PKM("%1", "%6", "%7") PKM("%2", "%8", "%9") PKM("%3", "%10", "%11")
      PKF("%0", "%4", "%5") PKF("%1", "%6", "%7") PKF("%2", "%8", "%9") PKF("%3", "%10", "%11")
      : "=&v"(o0), "=&v"(o1), "=&v"(o2), "=&v"(o3)
      : "v"(a0), "v"(b0), "v"(a1), "v"(b1), "v"(a2), "v"(b2), "v"(a3), "v"(b3));
}
__device__ __forceinline__ void batch4(unsigned* h, unsigned* l, cf b, cf t0, cf t1, cf t2, cf t3) {
  cf w0, w1, w2, w3;
  asm(PKM("%0", "%4", "%5") PKM("%1", "%4", "%6") PKM("%2", "%4", "%7") PKM("%3", "%4", "%8")
      PKF("%0", "%4", "%5") PKF("%1", "%4", "%6") PKF("%2", "%4", "%7") PKF("%3", "%4", "%8")
      : "=&v"(w0), "=&v"(w1), "=&v"(w2), "=&v"(w3) : "v"(b), "v"(t0), "v"(t1), "v"(t2), "v"(t3));
  cf w[4] = {w0, w1, w2, w3};
#pragma unroll
  for (int k = 0; k < 4; k++) h[k] = pack2(w[k][0], w[k][1]);
#pragma unroll
  for (int k = 0; k < 4; k++) {
    cf x = {lo_f(h[k]), hi_f(h[k])};
    asm("v_pk_add_f32 %0, %1, %2 neg_lo:[0,1] neg_hi:[0,1]" : "=v"(w[k]) : "v"(w[k]), "v"(x));
  }
#pragma unroll
  for (int k = 0; k < 4; k++) l[k] = pack2(w[k][0], w[k][1]);
}
constexpr int KG_RS = 288, KG_PL = 16 * KG_RS;
__device__ __forceinline__ void kgen_role(char* smem, const u16* __restrict__ logA, const u16* __restrict__ Aim,
                                          const u16* __restrict__ Cr, const u16* __restrict__ Ci, const u16* __restrict__ logdt,
                                          const u16* __restrict__ x, const u16* __restrict__ W, const u16* __restrict__ wo,
                                          char* __restrict__ ws, int blk) {
  const int tid = threadIdx.x, lane = tid & 63;
  const int wave = __builtin_amdgcn_readfirstlane(tid >> 6);
  constexpr int RS = KG_RS, PL = KG_PL;
  const int task = blk * 8 + wave;
  const int d = task / 3;
  const int pi = task * 64 + lane;
#define PF 1
  unsigned pfv = 0; const char* pp;
  {
    const int xcd = blk & 7, i = (blk >> 3) * 64 + lane;
    const int q = i & 15;
    const int ln = (xcd & 1) ? (q < 8 ? 12 + q : 14 + q) : (q < 13 ? q : 7 + q);
    if (i < 1280) pp = (const char*)W + (size_t)(80 * wave + (i >> 4)) * 3840 + ln * 128;
    else { const int id = wave * 640 + (i - 1280); pp = (const char*)x + (size_t)((xcd >> 1) * 512 + (id & 511)) * 1280 + (id >> 9) * 128; }
  }
  const int pfpt = wave == 0 ? 9 : (wave <= 2 ? 0 : (wave <= 5 ? 1 : 2));
#define PFLOAD(pt) do { __builtin_amdgcn_sched_barrier(0); if (pfpt == (pt)) asm volatile("global_load_dword %0, %1, off" : "+v"(pfv) : "v"(pp)); __builtin_amdgcn_sched_barrier(0); } while (0)
  PFLOAD(0);
  const float dt = rb(expf(b2f(logdt[d])));
  float ar;
  {
    const float la = b2f(logA[pi]);
    float h = la * 1.44269502f; float l = fmaf(la, 1.44269502f, -h) + la * 1.92596299e-8f;
    float e = __builtin_amdgcn_exp2f(h); e = fmaf(e, l * 0.69314718f, e);
    ar = -rb(e) * dt;
  }
  const float ai = b2f(Aim[pi]) * dt;
  cf C; C[0] = 2.f * (b2f(Cr[pi]) * dt); C[1] = 2.f * (b2f(Ci[pi]) * dt);
  const float th = ar * 1.44269502f, tl = fmaf(ar, 1.44269502f, -th) + ar * 1.92596299e-8f;
  const float rh = ai * 0.159154937f, rl = fmaf(ai, 0.159154937f, -rh) + ai * 6.42063833e-9f;
  char* tb = smem + wave * (2 * PL);
  char* wp = tb + lane * 4;
  const char* rp = tb + (lane & 15) * RS + (lane >> 4) * 16;
  STAMP(0, 2);
  PFLOAD(1);
  const cf one = {1.f, 0.f};
  cf S1 = zpow(th, tl, rh, rl, 1.f), Q1 = zpow(th, tl, rh, rl, 8.f), R1 = zpow(th, tl, rh, rl, 128.f);
  Q1[1] = -Q1[1];
  cf S2, Q2, R2, G1, S3, S4, Q3, P1, R3, T1, G2, G3, S5, S6, S7, P2, CT1, T2, P3, CS4, CT2, CT3, CT4, dm0;
  cmul4(S2, Q2, R2, G1, S1, S1, Q1, Q1, R1, R1, C, R1);
  cmul4(S3, S4, Q3, P1, S2, S1, S2, S2, Q2, Q1, Q2, Q2);
  cmul4(R3, T1, G2, P2, R2, R1, R2, R2, C, R2, P1, P1);
  cmul4(S5, S6, S7, P3, S4, S1, S4, S2, S4, S3, P2, P1);
  cmul4(G3, CT1, T2, dm0, C, R3, C, T1, T1, T1, C, R1);
  cmul4(CS4, CT2, CT3, dm0, CT1, S4, C, T2, CT1, T2, C, R1);
  CT4 = cmul(CT2, T2);
  PFLOAD(2);
  const int wid = min(blk * 216 + (tid < 216 ? tid : 215), 51199);
  unsigned wr_[8];
  {
    const int slot = wid & 127, f = wid >> 7;
    const int s5 = f % 5, hb = 4 * ((f / 5) & 7), cb = f / 40;
    const int m = (slot >> 6) * 32 + (slot & 31), h = (slot >> 5) & 1;
    const int n = 64 * cb + m;
    int k0, k1;
    if (s5 < 4) { k0 = 20 * (hb + s5) + 4 * h; k1 = k0 + 8; }
    else { k0 = 20 * (hb + 2 * h) + 16; k1 = k0 + 20; }
    if (wave < 4) {
      const u16* p0 = wo + (size_t)k0 * D + n; const u16* p1 = wo + (size_t)k1 * D + n;
#pragma unroll
      for (int e = 0; e < 4; e++) {
        asm volatile("global_load_ushort %0, %1, off offset:%2" : "=v"(wr_[e]) : "v"(p0), "i"(e * 1280));
        asm volatile("global_load_ushort %0, %1, off offset:%2" : "=v"(wr_[4 + e]) : "v"(p1), "i"(e * 1280));
      }
    }
  }
  auto rows4 = [&](int row, cf b, cf t0, cf t1, cf t2, cf t3) {
    unsigned h[4], l[4];
    batch4(h, l, b, t0, t1, t2, t3);
#pragma unroll
    for (int k = 0; k < 4; k++) { *(unsigned*)(wp + (row + k) * RS) = h[k]; *(unsigned*)(wp + PL + (row + k) * RS) = l[k]; }
  };
  rows4(0, one, one, Q1, Q2, Q3); rows4(4, P1, one, Q1, Q2, Q3); rows4(8, P2, one, Q1, Q2, Q3); rows4(12, P3, one, Q1, Q2, Q3);
  __builtin_amdgcn_wave_barrier();
  s16x8 bh[4], bl[4];
#pragma unroll
  for (int k = 0; k < 4; k++) { bh[k] = *(const s16x8*)(rp + k * 64); bl[k] = *(const s16x8*)(rp + PL + k * 64); }
  __builtin_amdgcn_wave_barrier();
  STAMP(0, 3);
  f32x4 res[3];
  auto mm = [&](int t) {
    __builtin_amdgcn_wave_barrier();
    f32x4 a = {0.f, 0.f, 0.f, 0.f}, a2 = {0.f, 0.f, 0.f, 0.f};
#pragma unroll
    for (int k = 0; k < 4; k++) {
      s16x8 ah = *(const s16x8*)(rp + k * 64), al = *(const s16x8*)(rp + PL + k * 64);
      a = __builtin_amdgcn_mfma_f32_16x16x32_bf16(ah, bh[k], a, 0, 0, 0);
      a2 = __builtin_amdgcn_mfma_f32_16x16x32_bf16(ah, bl[k], a2, 0, 0, 0);
      a2 = __builtin_amdgcn_mfma_f32_16x16x32_bf16(al, bh[k], a2, 0, 0, 0);
    }
    __builtin_amdgcn_wave_barrier();
    res[t] = a + a2;
  };
  rows4(0, C, one, S1, S2, S3); rows4(4, C, S4, S5, S6, S7); rows4(8, G1, one, S1, S2, S3); rows4(12, G1, S4, S5, S6, S7);
  mm(0);
  if (wave < 4) {
    asm volatile("s_waitcnt vmcnt(0)" : "+v"(wr_[0]), "+v"(wr_[1]), "+v"(wr_[2]), "+v"(wr_[3]), "+v"(wr_[4]), "+v"(wr_[5]), "+v"(wr_[6]), "+v"(wr_[7]) :: "memory");
    u32x4 wv = {wr_[0] | (wr_[1] << 16), wr_[2] | (wr_[3] << 16), wr_[4] | (wr_[5] << 16), wr_[6] | (wr_[7] << 16)};
    if (tid < 216 && blk * 216 + tid < 51200) st16(ws + WS_WOF + (size_t)wid * 16, wv);
  }
  STAMP(0, 4);
  rows4(0, G2, one, S2, S4, S6); rows4(4, G3, one, S2, S4, S6);
  {
    unsigned h[4], l[4];
    batch4(h, l, CT1, one, R1, R2, R3);
#pragma unroll
    for (int k = 0; k < 4; k++) { *(unsigned*)(wp + (8 + 2 * k) * RS) = h[k]; *(unsigned*)(wp + PL + (8 + 2 * k) * RS) = l[k]; }
    batch4(h, l, CS4, one, R1, R2, R3);
#pragma unroll
    for (int k = 0; k < 4; k++) { *(unsigned*)(wp + (9 + 2 * k) * RS) = h[k]; *(unsigned*)(wp + PL + (9 + 2 * k) * RS) = l[k]; }
  }
  mm(1);
  rows4(0, CT2, one, R1, R2, R3); rows4(4, CT3, one, R1, R2, R3); rows4(8, CT4, one, one, one, one);
  mm(2);
#pragma unroll
  for (int t = 0; t < 3; t++) *(f32x4*)(tb + t * 1024 + lane * 16) = res[t];
  __syncthreads();
  {
    const int d0 = (blk * 8) / 3;
#pragma unroll
    for (int it = 0; it < 2; it++) {
      const int e = tid + 512 * it;
      const int seg = e / 192, ent = e - 192 * seg;
      const int ds = d0 + seg;
      const int wlo = max(0, 3 * ds - 8 * blk), whi = min(8, 3 * ds + 3 - 8 * blk);
      if (seg < 4 && wlo < 8) {
        f32x4 a = *(const f32x4*)(smem + wlo * (2 * PL) + ent * 16);
        for (int w = wlo + 1; w < whi; w++) a += *(const f32x4*)(smem + w * (2 * PL) + ent * 16);
        st16(ws + WS_KIMG + (size_t)ds * 6144 + (3 * ds < 8 * blk ? 3072 : 0) + ent * 16, __builtin_bit_cast(u32x4, a));
      }
    }
  }
  asm volatile("s_waitcnt vmcnt(0)" :: "v"(pfv) : "memory");
}

__global__ __launch_bounds__(512, 4) void k1(const u16* __restrict__ x, const u16* __restrict__ logA, const u16* __restrict__ Aim,
                                          const u16* __restrict__ Cr, const u16* __restrict__ Ci, const u16* __restrict__ logdt,
                                          const u16* __restrict__ wqkv, const u16* __restrict__ gw, const u16* __restrict__ wo,
                                          char* __restrict__ ws) {
  __shared__ __attribute__((aligned(16))) char smem[K1_LDS];
  int b = blockIdx.x;
  STAMP(0, 0);
  b = b < NB_KG ? b + NB_GEMM : (b < NB_KG + NB_GEMM ? b - NB_KG : b);
  if (b < NB_GEMM) {
    const int xcd = b & 7, idx = b >> 3;
    const int rowblk = (xcd >> 1) * 4 + (idx & 3), cb = (xcd & 1) * 8 + (idx >> 2);
    if (cb < 5) gemm_role<true>(smem, x, wqkv, ws, rowblk, cb);
    else        gemm_role<false>(smem, x, wqkv, ws, rowblk, cb);
    STAMP(0, 1);
    return;
  }
  b -= NB_GEMM;
  if (b < NB_KG) { kgen_role(smem, logA, Aim, Cr, Ci, logdt, x, wqkv, wo, ws, b); STAMP(0, 1); return; }
  b -= NB_KG;
  if (b == 0) ((unsigned*)(ws + WS_CNT))[threadIdx.x] = 0u, ((unsigned*)(ws + WS_CNT))[threadIdx.x + 512] = 0u;
  gate_role(x, gw, (float*)(ws + WS_GATE), b);
  STAMP(0, 1);
}

constexpr int K2_CS = 4160;
constexpr int K2_KOFF = 4 * K2_CS;
constexpr int K2_TAB = K2_KOFF + 2 * 4608;
constexpr int K2_LDS = 40960;
constexpr int NB_KV = 32, NB_PROJ = 320;
typedef __attribute__((address_space(1))) unsigned gu32;
__device__ __forceinline__ void wait_count(char* p, unsigned target) {
  gu32* c = (gu32*)(size_t)p;
  for (int n = 0; n < (1 << 22); n++) {
    if (__hip_atomic_load(c, __ATOMIC_RELAXED, __HIP_MEMORY_SCOPE_AGENT) >= target) break;
    __builtin_amdgcn_s_sleep(4);
  }
}
#define GLD4(dst, voff, sbase, pol) asm volatile("global_load_dwordx4 %0, %1, %2" pol : "=v"(dst) : "v"(voff), "s"(sbase))
#define GLD2(dst, voff, sbase, pol) asm volatile("global_load_dwordx2 %0, %1, %2" pol : "=v"(dst) : "v"(voff), "s"(sbase))
#define VMW(n, i) asm volatile("s_waitcnt vmcnt(" #n ")" : "+v"(pf0[i]), "+v"(pf1[i]), "+v"(ka[i]), "+v"(kb[i]) :: "memory")
__device__ __forceinline__ void proj_role(char* smem, char* __restrict__ ws, u16* __restrict__ out, int pb) {
  const int tid = threadIdx.x, lane = tid & 63;
  const int wave = __builtin_amdgcn_readfirstlane(tid >> 6);
  const int t = wave & 1, kh = wave >> 1;
  const int r = lane & 31, h = lane >> 5;
  const int xcd = pb & 7, idx = pb >> 3;
  const int rowblk = xcd * 4 + idx / 10, cb = idx % 10;
  const int l0 = rowblk * 64, c0 = cb * 64;
  const int lds0 = (int)(size_t)(lptr_t)smem;
  const char* wsrc = ws + WS_WOF + (size_t)(cb * 2 + kh) * 40960 + t * 5120;
  const int l16 = lane * 16, r8 = r * 8;
  auto dma = [&](int G) {
    const char* sp = wsrc + G * 10240;
    const int lb = lds0 + (G & 1) * 20480 + kh * 10240 + t * 5120;
    asm volatile("s_mov_b32 m0, %2\n\ts_nop 0\n\tglobal_load_lds_dwordx4 %0, %1\n\tglobal_load_lds_dwordx4 %0, %1 offset:1024\n\t"
                 "global_load_lds_dwordx4 %0, %1 offset:2048\n\tglobal_load_lds_dwordx4 %0, %1 offset:3072"
                 :: "v"(l16), "s"(sp), "s"(lb) : "memory", "m0");
    asm volatile("s_mov_b32 m0, %2\n\ts_nop 0\n\tglobal_load_lds_dwordx4 %0, %1" :: "v"(l16), "s"(sp + 4096), "s"(lb + 4096) : "memory", "m0");
  };
  const char* p0 = ws + WS_PQ + (size_t)((rowblk * 32 + kh * 16) * 2 + t) * 1024;
  const char* p1 = ws + WS_PQ + (size_t)L * 16 * 32 * 2 + (size_t)((rowblk * 32 + kh * 16) * 2 + t) * 256;
  const char* kp = ws + WS_KVT + (size_t)kh * 16 * 1280;
  u32x4 pf0[4], ka[4]; u32x2 pf1[4], kb[4];
  dma(0); dma(1);
#pragma unroll
  for (int i = 0; i < 4; i++) { GLD4(pf0[i], l16, p0 + i * 2048, ""); GLD2(pf1[i], r8, p1 + i * 512, ""); }
  float g_r; float g_f[2];
  {
    const float* gate = (const float*)(ws + WS_GATE) + l0;
    asm volatile("global_load_dword %0, %1, off" : "=v"(g_r) : "v"(gate + 32 * t + r));
    asm volatile("global_load_dword %0, %1, off" : "=v"(g_f[0]) : "v"(gate + (tid >> 3)));
    asm volatile("global_load_dword %0, %1, off offset:128" : "=v"(g_f[1]) : "v"(gate + (tid >> 3)));
  }
  if (tid == 0) wait_count(ws + WS_CNT + xcd * 128, NB_KV);
  BAR_ONLY();
  STAMP(1, 2);
#pragma unroll
  for (int i = 0; i < 4; i++) { GLD4(ka[i], l16, kp + i * 1280, " sc1"); GLD2(kb[i], r8, kp + i * 1280 + 1024, " sc1"); }
  f32x16 acc[2];
#pragma unroll
  for (int c = 0; c < 2; c++)
#pragma unroll
    for (int i = 0; i < 16; i++) acc[c][i] = 0.f;
  u32x4 rm = {0u, 0u, 0u, 0u};
  const char* wl = smem + kh * 10240 + l16;
#pragma unroll
  for (int i = 0; i < 16; i++) {
    const int j = i & 3, G = i >> 2;
    switch (i) {
      case 0: VMW(6, 0); break;  case 1: VMW(8, 1); break;  case 2: VMW(10, 2); break; case 3: VMW(12, 3); break;
      case 4: VMW(12, 0); break; case 5: VMW(17, 1); break; case 6: VMW(17, 2); break; case 7: VMW(17, 3); break;
      case 8: VMW(17, 0); break; case 9: VMW(17, 1); break; case 10: VMW(17, 2); break; case 11: VMW(17, 3); break;
      case 12: VMW(17, 0); break; case 13: VMW(8, 1); break; case 14: VMW(4, 2); break; default: VMW(0, 3); break;
    }
    u32x4 a1u = {kb[j][0], kb[j][1], 0u, 0u};
    u32x4 b1u = {h ? 0u : pf1[j][0], h ? 0u : pf1[j][1], 0u, 0u};
    f32x16 c;
#pragma unroll
    for (int q = 0; q < 16; q++) c[q] = 0.f;
    c = __builtin_amdgcn_mfma_f32_32x32x16_bf16(__builtin_bit_cast(s16x8, ka[j]), __builtin_bit_cast(s16x8, pf0[j]), c, 0, 0, 0);
    c = __builtin_amdgcn_mfma_f32_32x32x16_bf16(__builtin_bit_cast(s16x8, a1u), __builtin_bit_cast(s16x8, b1u), c, 0, 0, 0);
    const float den = rb(rb(c[12]) + 1e-6f);
    const float inv = __builtin_amdgcn_rcpf(den);
    unsigned o[6];
#pragma unroll
    for (int q = 0; q < 6; q++) {
      unsigned n0 = pack2(c[2 * q], c[2 * q + 1]);
      o[q] = pack2(lo_f(n0) * inv, hi_f(n0) * inv);
    }
    u32x4 ob = {o[0], o[1], o[2], o[3]};
    if ((j >> 1) == h) { rm[2 * (j & 1)] = o[4]; rm[2 * (j & 1) + 1] = o[5]; }
    asm volatile("" : "+v"(ob), "+v"(rm));
    if (i + 4 < 16) {
      GLD4(pf0[j], l16, p0 + (i + 4) * 2048, ""); GLD2(pf1[j], r8, p1 + (i + 4) * 512, "");
      GLD4(ka[j], l16, kp + (i + 4) * 1280, " sc1"); GLD2(kb[j], r8, kp + (i + 4) * 1280 + 1024, " sc1");
    }
    if (j == 0) {
      if (G == 2) VMCNT(16);
      if (G == 3) VMCNT(12);
      RAW_BARRIER();
      if (G == 1 || G == 2) dma(G + 1);
    }
    const char* wp = wl + (G & 1) * 20480;
    {
      s16x8 w0 = *(const s16x8*)(wp + j * 2048), w1 = *(const s16x8*)(wp + j * 2048 + 1024);
      acc[0] = __builtin_amdgcn_mfma_f32_32x32x16_bf16(w0, __builtin_bit_cast(s16x8, ob), acc[0], 0, 0, 0);
      acc[1] = __builtin_amdgcn_mfma_f32_32x32x16_bf16(w1, __builtin_bit_cast(s16x8, ob), acc[1], 0, 0, 0);
    }
    if (j == 3) {
      s16x8 w0 = *(const s16x8*)(wp + 8192), w1 = *(const s16x8*)(wp + 9216);
      acc[0] = __builtin_amdgcn_mfma_f32_32x32x16_bf16(w0, __builtin_bit_cast(s16x8, rm), acc[0], 0, 0, 0);
      acc[1] = __builtin_amdgcn_mfma_f32_32x32x16_bf16(w1, __builtin_bit_cast(s16x8, rm), acc[1], 0, 0, 0);
    }
  }
  asm volatile("" : "+v"(g_r), "+v"(g_f[0]), "+v"(g_f[1]));
  RAW_BARRIER();
  STAMP(1, 3);
  {
    char* xw = smem + ((t * 2 + (1 - kh)) * 4) * 1024 + l16;
#pragma unroll
    for (int q = 0; q < 4; q++) {
      f32x4 v = kh ? (f32x4){acc[0][4 * q], acc[0][4 * q + 1], acc[0][4 * q + 2], acc[0][4 * q + 3]}
                   : (f32x4){acc[1][4 * q], acc[1][4 * q + 1], acc[1][4 * q + 2], acc[1][4 * q + 3]};
      *(f32x4*)(xw + q * 1024) = v;
    }
  }
  RAW_BARRIER();
  {
    const char* xr = smem + ((t * 2 + kh) * 4) * 1024 + l16;
    const float g1 = rb(1.f - g_r);
    char* gbp = smem + 16384 + (32 * t + r) * 144 + (32 * kh + 4 * h) * 2;
#pragma unroll
    for (int q = 0; q < 4; q++) {
      f32x4 v = *(const f32x4*)(xr + q * 1024);
      f32x4 m = kh ? (f32x4){acc[1][4 * q], acc[1][4 * q + 1], acc[1][4 * q + 2], acc[1][4 * q + 3]}
                   : (f32x4){acc[0][4 * q], acc[0][4 * q + 1], acc[0][4 * q + 2], acc[0][4 * q + 3]};
      v += m;
      unsigned b0 = pack2(v[0], v[1]), b1 = pack2(v[2], v[3]);
      *(u32x2*)(gbp + q * 16) = (u32x2){pack2(g1 * lo_f(b0), g1 * hi_f(b0)), pack2(g1 * lo_f(b1), g1 * hi_f(b1))};
    }
  }
  STAMP(1, 4);
  if (tid == 0) wait_count(ws + WS_CNT + (8 + cb) * 128, 64);
  RAW_BARRIER();
  STAMP(1, 5);
  {
    u32x4 av[2];
    const char* ap = ws + WS_AT + ((size_t)(c0 + (tid >> 3)) * L + l0 + 8 * (tid & 7)) * 2;
    asm volatile("global_load_dwordx4 %0, %2, off sc1\n\tglobal_load_dwordx4 %1, %3, off sc1\n\ts_waitcnt vmcnt(0)"
                 : "=&v"(av[0]), "=&v"(av[1]) : "v"(ap), "v"(ap + (size_t)32 * L * 2) : "memory");
#pragma unroll
    for (int i = 0; i < 2; i++) *(u32x4*)(smem + 25600 + ((tid >> 3) + 32 * i) * 144 + (tid & 7) * 16) = av[i];
  }
  RAW_BARRIER();
#pragma unroll
  for (int i = 0; i < 2; i++) {
    const int rr = (tid >> 3) + 32 * i, oc = tid & 7;
    const float g = g_f[i];
    u32x4 gb = *(const u32x4*)(smem + 16384 + rr * 144 + oc * 16);
    u32x4 ov;
#pragma unroll
    for (int e = 0; e < 4; e++) {
      float a0 = b2f(*(const u16*)(smem + 25600 + (oc * 8 + 2 * e) * 144 + rr * 2));
      float a1 = b2f(*(const u16*)(smem + 25600 + (oc * 8 + 2 * e + 1) * 144 + rr * 2));
      unsigned ga = pack2(g * a0, g * a1);
      ov[e] = pack2(lo_f(ga) + lo_f(gb[e]), hi_f(ga) + hi_f(gb[e]));
    }
    *(u32x4*)(out + (size_t)(l0 + rr) * D + c0 + oc * 8) = ov;
  }
}

__global__ __launch_bounds__(256, 4) void k2(const u16* __restrict__ Dskip, char* __restrict__ ws, u16* __restrict__ out) {
  __shared__ __attribute__((aligned(16))) char smem[K2_LDS];
  const int tid = threadIdx.x, lane = tid & 63;
  const int wave = __builtin_amdgcn_readfirstlane(tid >> 6);
  STAMP(1, 0);
  if (blockIdx.x >= NB_KV + D) { proj_role(smem, ws, out, blockIdx.x - (NB_KV + D)); STAMP(1, 1); return; }
  if (blockIdx.x < NB_KV) {
    const int hd = blockIdx.x;
    const int h = lane >> 5;
    float* fin = (float*)smem;
    if (tid < 105) {
      const f32x4* src = (const f32x4*)(ws + WS_PART) + (size_t)hd * 16 * 105 + tid;
      f32x4 v[16];
#pragma unroll
      for (int i = 0; i < 16; i++) v[i] = src[i * 105];
#pragma unroll
      for (int st = 1; st < 16; st <<= 1)
#pragma unroll
        for (int i = 0; i < 16; i += 2 * st) v[i] += v[i + st];
      *(f32x4*)(fin + tid * 4) = (f32x4){rb(v[0][0]), rb(v[0][1]), rb(v[0][2]), rb(v[0][3])};
    }
    __syncthreads();
    if (wave == 0) {
      const int m = lane & 31;
      int col = m < 16 ? m : ((m >> 2) == ((hd & 2) ? 5 : 4) ? 16 + (m & 3) : 20);
      u32x4 o;
#pragma unroll
      for (int e = 0; e < 4; e++) o[e] = pack2(fin[col * 20 + 8 * h + 2 * e], fin[col * 20 + 8 * h + 2 * e + 1]);
      st16(ws + WS_KVT + (size_t)hd * 1280 + lane * 16, o);
      const int m0 = 2 * (lane & 15), m1 = m0 + 1;
      int ca = m0 < 16 ? m0 : ((m0 >> 2) == ((hd & 2) ? 5 : 4) ? 16 + (m0 & 3) : 20);
      int cb = m1 < 16 ? m1 : ((m1 >> 2) == ((hd & 2) ? 5 : 4) ? 16 + (m1 & 3) : 20);
      u32x4 o2 = {pack2(fin[ca * 20 + 16], fin[ca * 20 + 17]), pack2(fin[ca * 20 + 18], fin[ca * 20 + 19]),
                  pack2(fin[cb * 20 + 16], fin[cb * 20 + 17]), pack2(fin[cb * 20 + 18], fin[cb * 20 + 19])};
      if (lane < 16) st16(ws + WS_KVT + (size_t)hd * 1280 + 1024 + lane * 16, o2);
      VMCNT(0);
      if (lane < 8) __hip_atomic_fetch_add((gu32*)(size_t)(ws + WS_CNT + lane * 128), 1u, __ATOMIC_RELAXED, __HIP_MEMORY_SCOPE_AGENT);
    }
    STAMP(1, 1);
    return;
  }
  const int d = blockIdx.x - NB_KV;
  const u16* uT = (const u16*)(ws + WS_UT) + (size_t)d * UT_STRIDE;
  u32x4 uu;
  {
    const f32x4* ksrc = (const f32x4*)(ws + WS_KIMG + (size_t)d * 6144) + (tid < 192 ? tid : 191);
    const u32x4* usrc = (const u32x4*)uT;
    f32x4 p0 = ksrc[0];
    if ((d & 7) == 2 || (d & 7) == 5) p0 += ksrc[192];
    u32x4 c0 = usrc[tid + 1], c1 = usrc[tid + 2];
    uu = usrc[tid + 4];
    u32x4 e0, e1;
    if (tid < 3) { e0 = usrc[min(tid + 257, 259)]; e1 = usrc[min(tid + 258, 259)]; }
    if (tid < 64) *(u32x4*)(smem + K2_KOFF + (tid >> 5) * 4608 + (tid & 31) * 16) = (u32x4){0u, 0u, 0u, 0u};
    if (tid < 192) *(f32x4*)(smem + K2_TAB + tid * 16) = p0;
    auto put_copies = [&](u32x4 a, u32x4 b, int xch) {
      unsigned w[6] = {a[0], a[1], a[2], a[3], b[0], b[1]};
      unsigned o[5];
#pragma unroll
      for (int k = 0; k < 5; k++) o[k] = __builtin_amdgcn_alignbyte(w[k + 1], w[k], 2);
      char* p = smem + xch * 16;
#pragma unroll
      for (int q = 0; q < 2; q++) {
        *(u32x4*)(p + (2 * q) * K2_CS) = (u32x4){o[q], o[q + 1], o[q + 2], o[q + 3]};
        *(u32x4*)(p + (2 * q + 1) * K2_CS) = (u32x4){w[q + 1], w[q + 2], w[q + 3], w[q + 4]};
      }
    };
    put_copies(c0, c1, tid);
    if (tid < 3) put_copies(e0, e1, tid + 256);
  }
  __syncthreads();
  {
    const float* tab = (const float*)(smem + K2_TAB);
    float kv[8];
#pragma unroll
    for (int e = 0; e < 8; e++) {
      const int j = tid * 8 + 7 - e;
      const int p = (int)rb((float)j);
      const int h = p >> 7, c = (p >> 3) & 15;
      int sh = 31 - __builtin_clz(h | 1); sh = sh > 3 ? 3 : sh;
      const int row = 8 * sh + (((h << 3) | (p & 7)) >> sh);
      kv[e] = tab[((row >> 2) * 16 + c) * 4 + (row & 3)];
    }
    u32x4 hi, lo;
#pragma unroll
    for (int e = 0; e < 4; e++) {
      hi[e] = pack2(kv[2 * e], kv[2 * e + 1]);
      lo[e] = pack2(ssub(kv[2 * e], lo_f(hi[e])), ssub(kv[2 * e + 1], hi_f(hi[e])));
    }
    *(u32x4*)(smem + K2_KOFF + 512 + tid * 16) = hi;
    *(u32x4*)(smem + K2_KOFF + 4608 + 512 + tid * 16) = lo;
  }
  __syncthreads();
  STAMP(1, 2);
  f32x4 acc[8];
#pragma unroll
  for (int t = 0; t < 8; t++) acc[t] = (f32x4){0.f, 0.f, 0.f, 0.f};
  {
    const int i = lane & 15, kg = lane >> 4, e = kg >> 1, bb = kg & 1;
    const char* ap = smem + (i & 3) * K2_CS + (2 * e - bb + 2) * 16 + (i >> 2) * 8;
    const char* bp = smem + K2_KOFF + (16 + i - e) * 32 + bb * 16;
#pragma unroll 1
    for (int it = 0; it < 2; it++) {
      const int d0 = 2 * (wave + 4 * it);
      s16x8 af[8];
#pragma unroll
      for (int j = 0; j < 8; j++) {
        u32x2 a0 = *(const volatile u32x2*)(ap + (32 * j + 2 * d0) * 16);
        u32x2 a1 = *(const volatile u32x2*)(ap + (32 * j + 2 * d0) * 16 + 8);
        af[j] = __builtin_bit_cast(s16x8, (u32x4){a0[0], a0[1], a1[0], a1[1]});
      }
#pragma unroll
      for (int mh = 0; mh < 2; mh++) {
        s16x8 bh[4], bl[4];
#pragma unroll
        for (int m = 0; m < 4; m++) {
          bh[m] = *(const s16x8*)(bp + (16 * (4 * mh + m) - d0) * 32);
          bl[m] = *(const s16x8*)(bp + (16 * (4 * mh + m) - d0) * 32 + 4608);
        }
#pragma unroll
        for (int m = 0; m < 4; m++) {
#pragma unroll
          for (int j = 0; j + 4 * mh + m < 8; j++) acc[j + 4 * mh + m] = __builtin_amdgcn_mfma_f32_16x16x32_bf16(af[j], bh[m], acc[j + 4 * mh + m], 0, 0, 0);
#pragma unroll
          for (int j = 0; j + 4 * mh + m < 8; j++) acc[j + 4 * mh + m] = __builtin_amdgcn_mfma_f32_16x16x32_bf16(af[j], bl[m], acc[j + 4 * mh + m], 0, 0, 0);
        }
      }
    }
  }
  STAMP(1, 3);
  const float dsk = b2f(Dskip[d]);
  __syncthreads();
  float* red = (float*)smem;
#pragma unroll
  for (int t = 0; t < 8; t++) *(f32x4*)(red + ((wave * 8 + t) * 64 + (lane & 15) * 4 + (lane >> 4)) * 4) = acc[t];
  __syncthreads();
  {
    u32x4 ov;
#pragma unroll
    for (int hf = 0; hf < 2; hf++) {
      const float* rp = red + (tid * 2 + hf) * 4;
      f32x4 s = *(const f32x4*)rp, s1 = *(const f32x4*)(rp + 2048), s2 = *(const f32x4*)(rp + 4096), s3 = *(const f32x4*)(rp + 6144);
      s = (s + s1) + (s2 + s3);
#pragma unroll
      for (int e = 0; e < 2; e++) {
        unsigned y = pack2(s[2 * e], s[2 * e + 1]);
        unsigned k = pack2(lo_f(uu[2 * hf + e]) * dsk, hi_f(uu[2 * hf + e]) * dsk);
        ov[2 * hf + e] = pack2(lo_f(y) + lo_f(k), hi_f(y) + hi_f(k));
      }
    }
    st16(ws + WS_AT + ((size_t)d * L + 8 * tid) * 2, ov);
  }
  VMCNT(0);
  BAR_ONLY();
  if (tid == 0) __hip_atomic_fetch_add((gu32*)(size_t)(ws + WS_CNT + (8 + (d >> 6)) * 128), 1u, __ATOMIC_RELAXED, __HIP_MEMORY_SCOPE_AGENT);
  STAMP(1, 1);
}

extern "C" void kernel_launch(void* const* d_in, const int* in_sizes, int n_in, void* d_out, int out_size, void* d_ws, size_t ws_size,
                              hipStream_t stream) {
  const u16* u = (const u16*)d_in[0]; const u16* logA = (const u16*)d_in[1]; const u16* Aim = (const u16*)d_in[2];
  const u16* Cr = (const u16*)d_in[3]; const u16* Ci = (const u16*)d_in[4]; const u16* logdt = (const u16*)d_in[5];
  const u16* Dsk = (const u16*)d_in[6]; const u16* wqkv = (const u16*)d_in[8]; const u16* wo = (const u16*)d_in[9];
  const u16* gw = (const u16*)d_in[10];
  char* ws = (char*)d_ws;
  k1<<<NB_GEMM + NB_GATE + NB_KG, 512, 0, stream>>>(u, logA, Aim, Cr, Ci, logdt, wqkv, gw, wo, ws);
  k2<<<NB_KV + D + NB_PROJ, 256, 0, stream>>>(Dsk, ws, (u16*)d_out);
}
```

```cpp
#include <hip/hip_runtime.h>
#include <hip/hip_bf16.h>
#include <stdint.h>

typedef unsigned short u16;
typedef __attribute__((ext_vector_type(8))) short s16x8;
typedef __attribute__((ext_vector_type(4))) short s16x4;
typedef __attribute__((ext_vector_type(4))) float f32x4;
typedef __attribute__((ext_vector_type(16))) float f32x16;
typedef __attribute__((ext_vector_type(4))) unsigned u32x4;
typedef __attribute__((ext_vector_type(2))) unsigned u32x2;
typedef __attribute__((address_space(3))) s16x4* lds_s16x4_p;
typedef const __attribute__((address_space(1))) void* gptr_t;
typedef __attribute__((address_space(3))) void* lptr_t;

constexpr int L = 2048, D = 640, NS = 192, NH = 32;
constexpr int UT_PAD = 32, UT_STRIDE = L + UT_PAD;
constexpr size_t WS_UT   = 0;
constexpr size_t WS_KIMG = WS_UT + (size_t)D * UT_STRIDE * 2;
constexpr size_t WS_PQ   = WS_KIMG + (size_t)D * 6144;
constexpr size_t WS_PART = WS_PQ + (size_t)L * D * 2;
constexpr size_t WS_GATE = WS_PART + (size_t)32 * 16 * 1680;
constexpr size_t WS_WOF  = WS_GATE + (size_t)L * 4;
constexpr size_t WS_KVT  = WS_WOF + (size_t)D * D * 2;
constexpr size_t WS_AT   = WS_KVT + (size_t)32 * 1280;
constexpr size_t WS_END  = WS_AT + (size_t)D * L * 2;
#define STAMP(kid, slot) do {} while (0)

__device__ __forceinline__ float b2f(u16 v) { return __uint_as_float(((unsigned)v) << 16); }
typedef __bf16 bf16x2_t __attribute__((ext_vector_type(2)));
typedef float f32x2 __attribute__((ext_vector_type(2)));
__device__ __forceinline__ unsigned pack2(float a, float b) { f32x2 v = {a, b}; bf16x2_t r = __builtin_convertvector(v, bf16x2_t); return __builtin_bit_cast(unsigned, r); }
__device__ __forceinline__ float lo_f(unsigned u) { return __uint_as_float(u << 16); }
__device__ __forceinline__ float hi_f(unsigned u) { return __uint_as_float(u & 0xffff0000u); }
__device__ __forceinline__ float rb(float f) { return lo_f(pack2(f, f)); }
__device__ __forceinline__ unsigned phi2(float a, float b) {
  unsigned t = pack2(a, b); float t0 = lo_f(t), t1 = hi_f(t);
  float e0 = __builtin_amdgcn_fmed3f(__builtin_amdgcn_exp2f(t0 * 1.44269504f), 0.f, 1.f);
  float e1 = __builtin_amdgcn_fmed3f(__builtin_amdgcn_exp2f(t1 * 1.44269504f), 0.f, 1.f);
  unsigned r = pack2(__builtin_fmaxf(t0, 0.f) + e0, __builtin_fmaxf(t1, 0.f) + e1);
  return pack2(lo_f(r) + 1e-6f, hi_f(r) + 1e-6f);
}
#define VMCNT(n) asm volatile("s_waitcnt vmcnt(" #n ")" ::: "memory")
#define RAW_BARRIER() do { asm volatile("s_waitcnt lgkmcnt(0)" ::: "memory"); __builtin_amdgcn_s_barrier(); asm volatile("" ::: "memory"); } while (0)
__device__ __forceinline__ void glds16(const void* g, void* l) { __builtin_amdgcn_global_load_lds((gptr_t)g, (lptr_t)l, 16, 0, 0); }

#define OPQ_S(v) asm volatile("" : "+s"(v))
typedef f32x2 cf;
__device__ __forceinline__ float smul(float a, float b) { float o; asm("v_mul_f32 %0, %1, %2" : "=v"(o) : "v"(a), "v"(b)); return o; }
__device__ __forceinline__ float sfma(float a, float b, float c) { float o; asm("v_fma_f32 %0, %1, %2, %3" : "=v"(o) : "v"(a), "v"(b), "v"(c)); return o; }
__device__ __forceinline__ float sfnma(float a, float b, float c) { float o; asm("v_fma_f32 %0, -%1, %2, %3" : "=v"(o) : "v"(a), "v"(b), "v"(c)); return o; }
__device__ __forceinline__ float ssub(float a, float b) { float o; asm("v_sub_f32 %0, %1, %2" : "=v"(o) : "v"(a), "v"(b)); return o; }
__device__ __forceinline__ cf cmul(cf a, cf b) {
  cf o;
  asm("v_pk_mul_f32 %0, %1, %2 op_sel:[0,0] op_sel_hi:[0,1]\n\t"
      "v_pk_fma_f32 %0, %1, %2, %0 op_sel:[1,1,0] op_sel_hi:[1,0,1] neg_lo:[1,0,0]" : "=&v"(o) : "v"(a), "v"(b));
  return o;
}

constexpr int K1_LDS = 81920;
constexpr int NB_GEMM = 256, NB_GATE = 16, NB_KG = 240;
#define WT 1
__device__ __forceinline__ void st16(void* p, u32x4 v) {
  asm volatile("global_store_dwordx4 %0, %1, off sc1\n\ts_nop 2" :: "v"(p), "v"(v) : "memory");
}

#define BAR_ONLY() do { asm volatile("" ::: "memory"); __builtin_amdgcn_s_barrier(); asm volatile("" ::: "memory"); } while (0)
#define SB() __builtin_amdgcn_sched_barrier(0)
template <bool ISQ>
__device__ __forceinline__ void gemm_role(char* smem, const u16* __restrict__ x, const u16* __restrict__ W, char* __restrict__ ws,
                                          int rowblk, int cb) {
  const int tid = threadIdx.x, lane = tid & 63;
  const int wave = __builtin_amdgcn_readfirstlane(tid >> 6);
  const int row0 = rowblk * 128, col0 = cb * 128;
  const int kvj = cb - 5;
  char* sB = smem; char* sA = smem + 32768;
  const int wr = wave & 1, wc = (wave >> 1) & 1, hf = wave >> 2;
  f32x4 ep[2][4];
  if (wave >= 4) {
    const int lw = wave - 4;
    const char* pA = (const char*)x + (size_t)(row0 + 32 * lw + (lane >> 3)) * 1280 + (((lane & 7) ^ (lane >> 3)) << 4);
    const int fB = (lane >> 4) | ((lw & 1) << 2);
    const int c16 = (((lane & 15) >> 1) ^ fB) * 2 + (lane & 1);
    int bcol = col0 + 8 * c16;
    if (!ISQ) bcol = min(640 + 640 * (c16 >> 3) + ((60 * kvj) & ~7) + 8 * (c16 & 7), 1912);
    const char* pB = (const char*)W + (size_t)(8 * lw + (lane >> 4)) * 3840 + bcol * 2;
    u32x2 tr[8];
    int tro;
    {
      const int fr = lane & 15, g = lane >> 4, p4 = lane & 3, r = 8 * g + (fr >> 2);
      tro = 32768 + r * 128 + (((2 * lw + (p4 >> 1)) ^ (r & 7)) << 4) + (p4 & 1) * 8;
    }
    auto issA = [&](int slot_off, int h) {
      glds16(pA + (16 * h) * 1280, sA + slot_off + (4 * lw + 2 * h) * 1024);
      glds16(pA + (16 * h + 8) * 1280, sA + slot_off + (4 * lw + 2 * h + 1) * 1024);
    };
    auto issB = [&](int slot_off) {
      glds16(pB, sB + slot_off + (2 * lw) * 1024);
      glds16(pB + 4 * 3840, sB + slot_off + (2 * lw + 1) * 1024);
      pB += (size_t)32 * 3840;
    };
    issA(0, 0); issB(0); issA(0, 1); pA += 128;
    issB(8192); issA(16384, 0); issB(16384); issA(16384, 1); pA += 128;
    VMCNT(8);
    BAR_ONLY();
    int ao = 32768, bo = 24576;
#pragma unroll 1
    for (int j = 0; j < 10; j++) {
      if (j < 9) VMCNT(6); else VMCNT(0);
      BAR_ONLY();
      if (j == cb) {
        const int a0 = (int)(size_t)(lptr_t)smem + (ao == 32768 ? 0 : ao + 16384);
        const int a = a0 + tro, a2 = a0 + (tro ^ 64) + 512;
        asm volatile("ds_read_b64_tr_b16 %0, %8\n\tds_read_b64_tr_b16 %1, %9\n\tds_read_b64_tr_b16 %2, %8 offset:4096\n\tds_read_b64_tr_b16 %3, %9 offset:4096\n\t"
                     "ds_read_b64_tr_b16 %4, %8 offset:8192\n\tds_read_b64_tr_b16 %5, %9 offset:8192\n\tds_read_b64_tr_b16 %6, %8 offset:12288\n\tds_read_b64_tr_b16 %7, %9 offset:12288\n\t"
                     "s_waitcnt lgkmcnt(0)"
                     : "=&v"(tr[0]), "=&v"(tr[1]), "=&v"(tr[2]), "=&v"(tr[3]), "=&v"(tr[4]), "=&v"(tr[5]), "=&v"(tr[6]), "=&v"(tr[7])
                     : "v"(a), "v"(a2) : "memory");
      }
      if (j < 9) { issB(bo); bo = (bo + 8192) & 32767; }
      if (j < 8) issA(ao, 0);
      if (j < 8) VMCNT(4); else VMCNT(2);
      BAR_ONLY();
      if (j < 8) { issB(bo); bo = (bo + 8192) & 32767; issA(ao, 1); pA += 128; }
      ao = ao == 32768 ? 0 : ao + 16384;
    }
    if (cb < 10) {
      u16* dst = (u16*)(ws + WS_UT) + (size_t)(64 * cb + 16 * lw + (lane & 15)) * UT_STRIDE + 8 * (lane >> 4);
#pragma unroll
      for (int i = 0; i < 4; i++) st16(dst + UT_PAD + row0 + 32 * i, (u32x4){tr[2 * i][0], tr[2 * i][1], tr[2 * i + 1][0], tr[2 * i + 1][1]});
      if (rowblk == 0) st16(dst, (u32x4){0u, 0u, 0u, 0u});
    }
    BAR_ONLY();
    char* xch = smem + (wave & 3) * 8192 + lane * 16;
#pragma unroll
    for (int i = 0; i < 8; i++) ep[i >> 2][i & 3] = *(const f32x4*)(xch + i * 1024);
  } else {
    const int g = lane >> 4, fr = lane & 15;
    f32x4 acc[4][4];
#pragma unroll
    for (int m = 0; m < 4; m++)
#pragma unroll
      for (int c = 0; c < 4; c++) acc[m][c] = (f32x4){0.f, 0.f, 0.f, 0.f};
    const int q4 = fr >> 2, p4 = lane & 3;
    int aoff[2];
#pragma unroll
    for (int ks = 0; ks < 2; ks++) aoff[ks] = (wr * 64 + fr) * 128 + (((4 * ks + g) ^ (fr & 7)) << 4);
    int boff[4];
    {
      const int f = q4 | ((g & 1) << 2);
#pragma unroll
      for (int c = 0; c < 4; c++) boff[c] = (8 * g + q4) * 256 + (((4 * wc + c) ^ f) << 5) + p4 * 8;
    }
    s16x8 af[2][4]; s16x4 bl[4], bh[4];
    auto rdA = [&](int buf, int m, const char* pa) { af[buf][m] = *(const s16x8*)(pa + m * 2048); };
    auto rdBl = [&](int c, const char* pb) { bl[c] = __builtin_amdgcn_ds_read_tr16_b64_v4i16((lds_s16x4_p)(pb + boff[c])); };
    auto rdBh = [&](int c, const char* pb) { bh[c] = __builtin_amdgcn_ds_read_tr16_b64_v4i16((lds_s16x4_p)(pb + boff[c] + 1024)); };
    BAR_ONLY();
    STAMP(0, 2);
#pragma unroll
    for (int c = 0; c < 4; c++) { rdA(0, c, sA + aoff[0]); rdBl(c, sB); rdBh(c, sB); }
    int ao0 = 0, ao1 = 16384;
#pragma unroll 1
    for (int j2 = 0; j2 < 5; j2++) {
#pragma unroll
      for (int st = 0; st < 4; st++) {
        const int hs = st & 1;
        BAR_ONLY();
        SB();
        const char* pa = sA + (hs == 0 ? ao0 + aoff[1] : ao1 + aoff[0]);
        const char* pb = sB + ((st + 1) & 3) * 8192;
#pragma unroll
        for (int c = 0; c < 4; c++) {
          const s16x8 bf = __builtin_shufflevector(bl[c], bh[c], 0, 1, 2, 3, 4, 5, 6, 7);
#pragma unroll
          for (int m = 0; m < 4; m++) {
            if (ISQ) acc[m][c] = __builtin_amdgcn_mfma_f32_16x16x32_bf16(bf, af[hs][m], acc[m][c], 0, 0, 0);
            else     acc[m][c] = __builtin_amdgcn_mfma_f32_16x16x32_bf16(af[hs][m], bf, acc[m][c], 0, 0, 0);
            SB();
            if (m == 0 && c > 0) rdBh(c - 1, pb);
            if (m == 1) rdA(hs ^ 1, c, pa);
            if (m == 3) { rdBl(c, pb); if (c == 3) rdBh(3, pb); }
            SB();
          }
        }
        if (hs == 1) { ao0 = ao1; ao1 = ao1 == 32768 ? 0 : ao1 + 16384; }
      }
    }
    STAMP(0, 3);
    int l3 = threadIdx.x; asm volatile("" : "+v"(l3));
    char* xch = smem + (l3 >> 6) * 8192 + (l3 & 63) * 16;
#pragma unroll
    for (int i = 0; i < 8; i++) *(f32x4*)(xch + i * 1024) = acc[2 + (i >> 2)][i & 3];
#pragma unroll
    for (int i = 0; i < 8; i++) ep[i >> 2][i & 3] = acc[i >> 2][i & 3];
    RAW_BARRIER();
  }
  int l2 = threadIdx.x; asm volatile("" : "+v"(l2));
  const int g = (l2 >> 4) & 3, fr = l2 & 15;
  if (ISQ) {
    char* pq0 = ws + WS_PQ; char* pq1 = ws + WS_PQ + (size_t)L * 16 * 32 * 2;
#pragma unroll
    for (int c = 0; c < 4; c++) {
      const int n = col0 + 64 * wc + 16 * c + 4 * g;
      const int hd = n / 20, d0 = n - 20 * hd;
#pragma unroll
      for (int m = 0; m < 2; m++) {
        const int rb64 = rowblk * 2 + wr, r = 16 * m + fr;
        u32x2 o;
        o[0] = phi2(ep[m][c][0], ep[m][c][1]);
        o[1] = phi2(ep[m][c][2], ep[m][c][3]);
        const int fi = (rb64 * 32 + hd) * 2 + hf;
        char* dst = d0 < 16 ? pq0 + ((size_t)fi * 64 + (d0 >> 3) * 32 + r) * 16 + (d0 & 7) * 2 : pq1 + ((size_t)fi * 32 + r) * 8;
        *(u32x2*)dst = o;
      }
    }
  } else {
    const int o = (60 * kvj) & 7, zc = o ? 0 : 60;
    const int lane2 = l2 & 63;
    char* fb = smem + 32768;
#pragma unroll
    for (int c = 0; c < 4; c++) {
      u32x4 a;
      if (wc == 0) {
        a[0] = phi2(ep[0][c][0], ep[0][c][1]); a[1] = phi2(ep[0][c][2], ep[0][c][3]);
        a[2] = phi2(ep[1][c][0], ep[1][c][1]); a[3] = phi2(ep[1][c][2], ep[1][c][3]);
      } else {
        a[0] = pack2(ep[0][c][0], ep[0][c][1]); a[1] = pack2(ep[0][c][2], ep[0][c][3]);
        a[2] = pack2(ep[1][c][0], ep[1][c][1]); a[3] = pack2(ep[1][c][2], ep[1][c][3]);
        if (16 * c + fr == zc) a = (u32x4){0x3F803F80u, 0x3F803F80u, 0x3F803F80u, 0x3F803F80u};
      }
      *(u32x4*)(fb + ((((wc * 4 + wr * 2 + hf) * 4 + c) * 64 + lane2) << 4)) = a;
    }
    RAW_BARRIER();
    const int np = wave < 4 ? 1 : 2;
    for (int i = 0; i < np; i++) {
      const int p = wave < 4 ? wave : wave * 2 - 4 + i;
      int ta, tb;
      if (p < 10) { ta = (int)((0x3322211100ull >> (4 * p)) & 15); tb = (int)((0x3232121010ull >> (4 * p)) & 15); }
      else { ta = (o ? 2 : 0) + (p - 10); tb = o ? 0 : 3; }
      const char* pk = fb + ((ta * 64 + lane2) << 4);
      const char* pv = fb + 16384 + ((tb * 64 + lane2) << 4);
      f32x4 dd = {0.f, 0.f, 0.f, 0.f};
#pragma unroll
      for (int rg = 0; rg < 4; rg++)
        dd = __builtin_amdgcn_mfma_f32_16x16x32_bf16(*(const s16x8*)(pk + rg * 4096), *(const s16x8*)(pv + rg * 4096), dd, 0, 0, 0);
      const int kc0 = 16 * ta + 4 * g - o;
      const int hk = kc0 / 20, dk0 = kc0 - 20 * hk;
      const int vcol = 16 * tb + fr;
      const int dv = vcol == zc ? 20 : vcol - o - 20 * hk;
      if (kc0 >= 0 && kc0 < (kvj == 10 ? 40 : 60) && dv >= 0 && (dv < 20 || vcol == zc))
        *(f32x4*)(ws + WS_PART + (size_t)(((3 * kvj + hk) * 16 + rowblk) * 21 + dv) * 80 + dk0 * 4) = dd;
    }
  }
}

__device__ __forceinline__ void gate_role(const u16* __restrict__ x, const u16* __restrict__ gw, float* __restrict__ gate, int blk) {
  const int tid = threadIdx.x, wave = tid >> 6, lane = tid & 63;
  unsigned gwr[5];
#pragma unroll
  for (int i = 0; i < 5; i++) gwr[i] = *(const unsigned*)(gw + 2 * lane + 128 * i);
#pragma unroll 1
  for (int h = 0; h < 2; h++) {
    const int r0 = blk * 128 + wave * 16 + h * 8;
    unsigned v[8][5];
#pragma unroll
    for (int rr = 0; rr < 8; rr++)
#pragma unroll
      for (int i = 0; i < 5; i++) v[rr][i] = *(const unsigned*)(x + (size_t)(r0 + rr) * D + 2 * lane + 128 * i);
    float s[8];
#pragma unroll
    for (int rr = 0; rr < 8; rr++) {
      s[rr] = 0.f;
#pragma unroll
      for (int i = 0; i < 5; i++) {
        unsigned p = pack2(lo_f(v[rr][i]) * lo_f(gwr[i]), hi_f(v[rr][i]) * hi_f(gwr[i]));
        s[rr] += lo_f(p) + hi_f(p);
      }
    }
#pragma unroll
    for (int o = 32; o > 0; o >>= 1)
#pragma unroll
      for (int rr = 0; rr < 8; rr++) s[rr] += __shfl_xor(s[rr], o);
    float sv = s[0];
#pragma unroll
    for (int rr = 1; rr < 8; rr++) sv = lane == rr ? s[rr] : sv;
    if (lane < 8) { float a = rb(sv); gate[r0 + lane] = rb(1.f / (1.f + __expf(-a))); }
  }
}

__device__ __forceinline__ cf zpow(float th, float tl, float rh, float rl, float p) {
  float e = __builtin_amdgcn_exp2f(th * p); e = fmaf(e, (tl * p) * 0.69314718f, e);
  float t = rh * p; float f = (t - __builtin_rintf(t)) + rl * p;
  cf o; o[0] = e * __builtin_amdgcn_cosf(f); o[1] = e * __builtin_amdgcn_sinf(f); return o;
}
#define PKM(o, a, b) "v_pk_mul_f32 " o ", " a ", " b " op_sel:[0,0] op_sel_hi:[0,1]\n\t"
#define PKF(o, a, b) "v_pk_fma_f32 " o ", " a ", " b ", " o " op_sel:[1,1,0] op_sel_hi:[1,0,1] neg_lo:[1,0,0]\n\t"
__device__ __forceinline__ void cmul4(cf& o0, cf& o1, cf& o2, cf& o3, cf a0, cf b0, cf a1, cf b1, cf a2, cf b2, cf a3, cf b3) {
  asm(PKM("%0", "%4", "%5") PKM("%1", "%6", "%7") PKM("%2", "%8", "%9") PKM("%3", "%10", "%11")
      PKF("%0", "%4", "%5") PKF("%1", "%6", "%7") PKF("%2", "%8", "%9") PKF("%3", "%10", "%11")
      : "=&v"(o0), "=&v"(o1), "=&v"(o2), "=&v"(o3)
      : "v"(a0), "v"(b0), "v"(a1), "v"(b1), "v"(a2), "v"(b2), "v"(a3), "v"(b3));
}
__device__ __forceinline__ void batch4(unsigned* h, unsigned* l, cf b, cf t0, cf t1, cf t2, cf t3) {
  cf w0, w1, w2, w3;
  asm(PKM("%0", "%4", "%5") PKM("%1", "%4", "%6") PKM("%2", "%4", "%7") PKM("%3", "%4", "%8")
      PKF("%0", "%4", "%5") PKF("%1", "%4", "%6") PKF("%2", "%4", "%7") PKF("%3", "%4", "%8")
      : "=&v"(w0), "=&v"(w1), "=&v"(w2), "=&v"(w3) : "v"(b), "v"(t0), "v"(t1), "v"(t2), "v"(t3));
  cf w[4] = {w0, w1, w2, w3};
#pragma unroll
  for (int k = 0; k < 4; k++) h[k] = pack2(w[k][0], w[k][1]);
#pragma unroll
  for (int k = 0; k < 4; k++) {
    cf x = {lo_f(h[k]), hi_f(h[k])};
    asm("v_pk_add_f32 %0, %1, %2 neg_lo:[0,1] neg_hi:[0,1]" : "=v"(w[k]) : "v"(w[k]), "v"(x));
  }
#pragma unroll
  for (int k = 0; k < 4; k++) l[k] = pack2(w[k][0], w[k][1]);
}
constexpr int KG_RS = 288, KG_PL = 16 * KG_RS;
__device__ __forceinline__ void kgen_role(char* smem, const u16* __restrict__ logA, const u16* __restrict__ Aim,
                                          const u16* __restrict__ Cr, const u16* __restrict__ Ci, const u16* __restrict__ logdt,
                                          const u16* __restrict__ x, const u16* __restrict__ W, const u16* __restrict__ wo,
                                          char* __restrict__ ws, int blk) {
  const int tid = threadIdx.x, lane = tid & 63;
  const int wave = __builtin_amdgcn_readfirstlane(tid >> 6);
  constexpr int RS = KG_RS, PL = KG_PL;
  const int task = blk * 8 + wave;
  const int d = task / 3;
  const int pi = task * 64 + lane;
#define PF 1
  unsigned pfv = 0; const char* pp;
  {
    const int xcd = blk & 7, i = (blk >> 3) * 64 + lane;
    const int q = i & 15;
    const int ln = (xcd & 1) ? (q < 8 ? 12 + q : 14 + q) : (q < 13 ? q : 7 + q);
    if (i < 1280) pp = (const char*)W + (size_t)(80 * wave + (i >> 4)) * 3840 + ln * 128;
    else { const int id = wave * 640 + (i - 1280); pp = (const char*)x + (size_t)((xcd >> 1) * 512 + (id & 511)) * 1280 + (id >> 9) * 128; }
  }
  const int pfpt = wave == 0 ? 9 : (wave <= 2 ? 0 : (wave <= 5 ? 1 : 2));
#define PFLOAD(pt) do { __builtin_amdgcn_sched_barrier(0); if (pfpt == (pt)) asm volatile("global_load_dword %0, %1, off" : "+v"(pfv) : "v"(pp)); __builtin_amdgcn_sched_barrier(0); } while (0)
  PFLOAD(0);
  const float dt = rb(expf(b2f(logdt[d])));
  float ar;
  {
    const float la = b2f(logA[pi]);
    float h = la * 1.44269502f; float l = fmaf(la, 1.44269502f, -h) + la * 1.92596299e-8f;
    float e = __builtin_amdgcn_exp2f(h); e = fmaf(e, l * 0.69314718f, e);
    ar = -rb(e) * dt;
  }
  const float ai = b2f(Aim[pi]) * dt;
  cf C; C[0] = 2.f * (b2f(Cr[pi]) * dt); C[1] = 2.f * (b2f(Ci[pi]) * dt);
  const float th = ar * 1.44269502f, tl = fmaf(ar, 1.44269502f, -th) + ar * 1.92596299e-8f;
  const float rh = ai * 0.159154937f, rl = fmaf(ai, 0.159154937f, -rh) + ai * 6.42063833e-9f;
  char* tb = smem + wave * (2 * PL);
  char* wp = tb + lane * 4;
  const char* rp = tb + (lane & 15) * RS + (lane >> 4) * 16;
  STAMP(0, 2);
  PFLOAD(1);
  const cf one = {1.f, 0.f};
  cf S1 = zpow(th, tl, rh, rl, 1.f), Q1 = zpow(th, tl, rh, rl, 8.f), R1 = zpow(th, tl, rh, rl, 128.f);
  Q1[1] = -Q1[1];
  cf S2, Q2, R2, G1, S3, S4, Q3, P1, R3, T1, G2, G3, S5, S6, S7, P2, CT1, T2, P3, CS4, CT2, CT3, CT4, dm0;
  cmul4(S2, Q2, R2, G1, S1, S1, Q1, Q1, R1, R1, C, R1);
  cmul4(S3, S4, Q3, P1, S2, S1, S2, S2, Q2, Q1, Q2, Q2);
  cmul4(R3, T1, G2, P2, R2, R1, R2, R2, C, R2, P1, P1);
  cmul4(S5, S6, S7, P3, S4, S1, S4, S2, S4, S3, P2, P1);
  cmul4(G3, CT1, T2, dm0, C, R3, C, T1, T1, T1, C, R1);
  cmul4(CS4, CT2, CT3, dm0, CT1, S4, C, T2, CT1, T2, C, R1);
  CT4 = cmul(CT2, T2);
  PFLOAD(2);
  const int wid = min(blk * 216 + (tid < 216 ? tid : 215), 51199);
  unsigned wr_[8];
  {
    const int slot = wid % 160, f = wid / 160;
    const int ks = f % 10, kq = (f / 10) & 3, cb = f / 40;
    int m, h;
    if (slot < 128) { m = (slot >> 6) * 32 + (slot & 31); h = (slot >> 5) & 1; } else { m = 64 + (slot & 15); h = (slot >> 4) & 1; }
    const int n = 80 * cb + m;
    int k0, k1;
    if (ks < 8) { k0 = 20 * (8 * kq + ks) + 4 * h; k1 = k0 + 8; }
    else { k0 = 20 * (8 * kq + 4 * (ks - 8) + 2 * h) + 16; k1 = k0 + 20; }
    if (wave < 4) {
      const u16* p0 = wo + (size_t)k0 * D + n; const u16* p1 = wo + (size_t)k1 * D + n;
#pragma unroll
      for (int e = 0; e < 4; e++) {
        asm volatile("global_load_ushort %0, %1, off offset:%2" : "=v"(wr_[e]) : "v"(p0), "i"(e * 1280));
        asm volatile("global_load_ushort %0, %1, off offset:%2" : "=v"(wr_[4 + e]) : "v"(p1), "i"(e * 1280));
      }
    }
  }
  auto rows4 = [&](int row, cf b, cf t0, cf t1, cf t2, cf t3) {
    unsigned h[4], l[4];
    batch4(h, l, b, t0, t1, t2, t3);
#pragma unroll
    for (int k = 0; k < 4; k++) { *(unsigned*)(wp + (row + k) * RS) = h[k]; *(unsigned*)(wp + PL + (row + k) * RS) = l[k]; }
  };
  rows4(0, one, one, Q1, Q2, Q3); rows4(4, P1, one, Q1, Q2, Q3); rows4(8, P2, one, Q1, Q2, Q3); rows4(12, P3, one, Q1, Q2, Q3);
  __builtin_amdgcn_wave_barrier();
  s16x8 bh[4], bl[4];
#pragma unroll
  for (int k = 0; k < 4; k++) { bh[k] = *(const s16x8*)(rp + k * 64); bl[k] = *(const s16x8*)(rp + PL + k * 64); }
  __builtin_amdgcn_wave_barrier();
  STAMP(0, 3);
  f32x4 res[3];
  auto mm = [&](int t) {
    __builtin_amdgcn_wave_barrier();
    f32x4 a = {0.f, 0.f, 0.f, 0.f}, a2 = {0.f, 0.f, 0.f, 0.f};
#pragma unroll
    for (int k = 0; k < 4; k++) {
      s16x8 ah = *(const s16x8*)(rp + k * 64), al = *(const s16x8*)(rp + PL + k * 64);
      a = __builtin_amdgcn_mfma_f32_16x16x32_bf16(ah, bh[k], a, 0, 0, 0);
      a2 = __builtin_amdgcn_mfma_f32_16x16x32_bf16(ah, bl[k], a2, 0, 0, 0);
      a2 = __builtin_amdgcn_mfma_f32_16x16x32_bf16(al, bh[k], a2, 0, 0, 0);
    }
    __builtin_amdgcn_wave_barrier();
    res[t] = a + a2;
  };
  rows4(0, C, one, S1, S2, S3); rows4(4, C, S4, S5, S6, S7); rows4(8, G1, one, S1, S2, S3); rows4(12, G1, S4, S5, S6, S7);
  mm(0);
  if (wave < 4) {
    asm volatile("s_waitcnt vmcnt(0)" : "+v"(wr_[0]), "+v"(wr_[1]), "+v"(wr_[2]), "+v"(wr_[3]), "+v"(wr_[4]), "+v"(wr_[5]), "+v"(wr_[6]), "+v"(wr_[7]) :: "memory");
    u32x4 wv = {wr_[0] | (wr_[1] << 16), wr_[2] | (wr_[3] << 16), wr_[4] | (wr_[5] << 16), wr_[6] | (wr_[7] << 16)};
    if (tid < 216 && blk * 216 + tid < 51200) st16(ws + WS_WOF + (size_t)wid * 16, wv);
  }
  STAMP(0, 4);
  rows4(0, G2, one, S2, S4, S6); rows4(4, G3, one, S2, S4, S6);
  {
    unsigned h[4], l[4];
    batch4(h, l, CT1, one, R1, R2, R3);
#pragma unroll
    for (int k = 0; k < 4; k++) { *(unsigned*)(wp + (8 + 2 * k) * RS) = h[k]; *(unsigned*)(wp + PL + (8 + 2 * k) * RS) = l[k]; }
    batch4(h, l, CS4, one, R1, R2, R3);
#pragma unroll
    for (int k = 0; k < 4; k++) { *(unsigned*)(wp + (9 + 2 * k) * RS) = h[k]; *(unsigned*)(wp + PL + (9 + 2 * k) * RS) = l[k]; }
  }
  mm(1);
  rows4(0, CT2, one, R1, R2, R3); rows4(4, CT3, one, R1, R2, R3); rows4(8, CT4, one, one, one, one);
  mm(2);
#pragma unroll
  for (int t = 0; t < 3; t++) *(f32x4*)(tb + t * 1024 + lane * 16) = res[t];
  __syncthreads();
  {
    const int d0 = (blk * 8) / 3;
#pragma unroll
    for (int it = 0; it < 2; it++) {
      const int e = tid + 512 * it;
      const int seg = e / 192, ent = e - 192 * seg;
      const int ds = d0 + seg;
      const int wlo = max(0, 3 * ds - 8 * blk), whi = min(8, 3 * ds + 3 - 8 * blk);
      if (seg < 4 && wlo < 8) {
        f32x4 a = *(const f32x4*)(smem + wlo * (2 * PL) + ent * 16);
        for (int w = wlo + 1; w < whi; w++) a += *(const f32x4*)(smem + w * (2 * PL) + ent * 16);
        st16(ws + WS_KIMG + (size_t)ds * 6144 + (3 * ds < 8 * blk ? 3072 : 0) + ent * 16, __builtin_bit_cast(u32x4, a));
      }
    }
  }
  asm volatile("s_waitcnt vmcnt(0)" :: "v"(pfv) : "memory");
}

__global__ __launch_bounds__(512, 4) void k1(const u16* __restrict__ x, const u16* __restrict__ logA, const u16* __restrict__ Aim,
                                          const u16* __restrict__ Cr, const u16* __restrict__ Ci, const u16* __restrict__ logdt,
                                          const u16* __restrict__ wqkv, const u16* __restrict__ gw, const u16* __restrict__ wo,
                                          char* __restrict__ ws) {
  __shared__ __attribute__((aligned(16))) char smem[K1_LDS];
  int b = blockIdx.x;
  STAMP(0, 0);
  if (b < NB_GEMM) {
    const int xcd = b & 7, idx = b >> 3;
    const int rowblk = (xcd >> 1) * 4 + (idx & 3), cb = (xcd & 1) * 8 + (idx >> 2);
    if (cb < 5) gemm_role<true>(smem, x, wqkv, ws, rowblk, cb);
    else        gemm_role<false>(smem, x, wqkv, ws, rowblk, cb);
    STAMP(0, 1);
    return;
  }
  b -= NB_GEMM;
  if (b < NB_KG) { kgen_role(smem, logA, Aim, Cr, Ci, logdt, x, wqkv, wo, ws, b); STAMP(0, 1); return; }
  b -= NB_KG;
  gate_role(x, gw, (float*)(ws + WS_GATE), b);
  STAMP(0, 1);
}

constexpr int K2_CS = 4160;
constexpr int K2_KOFF = 8 * K2_CS;
constexpr int K2_TAB = K2_KOFF + 2 * 4608;
constexpr int K2_LDS = K2_TAB + 3072;
constexpr int NB_KV = 32, NB_WO = 0;
__global__ __launch_bounds__(256, 3) void k2(const u16* __restrict__ Dskip, char* __restrict__ ws) {
  __shared__ __attribute__((aligned(16))) char smem[K2_LDS];
  const int tid = threadIdx.x, lane = tid & 63;
  const int wave = __builtin_amdgcn_readfirstlane(tid >> 6);
  STAMP(1, 0);
  if (blockIdx.x < NB_KV) {
    const int hd = blockIdx.x;
    const int h = lane >> 5;
    float* fin = (float*)smem;
    if (tid < 105) {
      const f32x4* src = (const f32x4*)(ws + WS_PART) + (size_t)hd * 16 * 105 + tid;
      f32x4 v[16];
#pragma unroll
      for (int i = 0; i < 16; i++) v[i] = src[i * 105];
#pragma unroll
      for (int st = 1; st < 16; st <<= 1)
#pragma unroll
        for (int i = 0; i < 16; i += 2 * st) v[i] += v[i + st];
      *(f32x4*)(fin + tid * 4) = (f32x4){rb(v[0][0]), rb(v[0][1]), rb(v[0][2]), rb(v[0][3])};
    }
    __syncthreads();
    if (tid < 96) {
      const int m = lane & 31;
      int col = m < 16 ? m : ((m >> 2) == ((hd & 2) ? 5 : 4) ? 16 + (m & 3) : 20);
      if (tid < 64) {
        u32x4 o;
#pragma unroll
        for (int e = 0; e < 4; e++) o[e] = pack2(fin[col * 20 + 8 * h + 2 * e], fin[col * 20 + 8 * h + 2 * e + 1]);
        *(u32x4*)(ws + WS_KVT + (size_t)hd * 1280 + lane * 16) = o;
      } else {
        u32x2 o;
#pragma unroll
        for (int e = 0; e < 2; e++) o[e] = pack2(fin[col * 20 + 16 + 2 * e], fin[col * 20 + 17 + 2 * e]);
        *(u32x2*)(ws + WS_KVT + (size_t)hd * 1280 + 1024 + m * 8) = o;
      }
    }
    STAMP(1, 1);
    return;
  }
  const int d = blockIdx.x - (NB_KV + NB_WO);
  const u16* uT = (const u16*)(ws + WS_UT) + (size_t)d * UT_STRIDE;
  {
    const f32x4* ksrc = (const f32x4*)(ws + WS_KIMG + (size_t)d * 6144) + (tid < 192 ? tid : 191);
    const u32x4* usrc = (const u32x4*)uT;
    f32x4 p0 = ksrc[0];
    if ((d & 7) == 2 || (d & 7) == 5) p0 += ksrc[192];
    u32x4 c0 = usrc[tid + 1], c1 = usrc[tid + 2];
    u32x4 e0, e1;
    if (tid < 2) { e0 = usrc[tid + 257]; e1 = usrc[tid + 258]; }
    if (tid < 64) *(u32x4*)(smem + K2_KOFF + (tid >> 5) * 4608 + (tid & 31) * 16) = (u32x4){0u, 0u, 0u, 0u};
    if (tid < 192) *(f32x4*)(smem + K2_TAB + tid * 16) = p0;
    auto put_copies = [&](u32x4 a, u32x4 b, int xch) {
      unsigned w[8] = {a[0], a[1], a[2], a[3], b[0], b[1], b[2], b[3]};
      unsigned o[7];
#pragma unroll
      for (int k = 0; k < 7; k++) o[k] = __builtin_amdgcn_alignbyte(w[k + 1], w[k], 2);
      char* p = smem + xch * 16;
#pragma unroll
      for (int q = 0; q < 4; q++) {
        *(u32x4*)(p + (2 * q) * K2_CS) = (u32x4){o[q], o[q + 1], o[q + 2], o[q + 3]};
        *(u32x4*)(p + (2 * q + 1) * K2_CS) = (u32x4){w[q + 1], w[q + 2], w[q + 3], w[q + 4]};
      }
    };
    put_copies(c0, c1, tid);
    if (tid < 2) put_copies(e0, e1, tid + 256);
  }
  __syncthreads();
  {
    const float* tab = (const float*)(smem + K2_TAB);
    float kv[8];
#pragma unroll
    for (int e = 0; e < 8; e++) {
      const int j = tid * 8 + 7 - e;
      const int p = (int)rb((float)j);
      const int h = p >> 7, c = (p >> 3) & 15;
      int sh = 31 - __builtin_clz(h | 1); sh = sh > 3 ? 3 : sh;
      const int row = 8 * sh + (((h << 3) | (p & 7)) >> sh);
      kv[e] = tab[((row >> 2) * 16 + c) * 4 + (row & 3)];
    }
    u32x4 hi, lo;
#pragma unroll
    for (int e = 0; e < 4; e++) {
      hi[e] = pack2(kv[2 * e], kv[2 * e + 1]);
      lo[e] = pack2(ssub(kv[2 * e], lo_f(hi[e])), ssub(kv[2 * e + 1], hi_f(hi[e])));
    }
    *(u32x4*)(smem + K2_KOFF + 512 + tid * 16) = hi;
    *(u32x4*)(smem + K2_KOFF + 4608 + 512 + tid * 16) = lo;
  }
  __syncthreads();
  STAMP(1, 2);
  f32x4 acc[8];
#pragma unroll
  for (int t = 0; t < 8; t++) acc[t] = (f32x4){0.f, 0.f, 0.f, 0.f};
  {
    const int i = lane & 15, kg = lane >> 4, e = kg >> 1, bb = kg & 1;
    const char* ap = smem + (i & 7) * K2_CS + (2 * e - bb + 2 + (i >> 3)) * 16;
    const char* bp = smem + K2_KOFF + (16 + i - e) * 32 + bb * 16;
#pragma unroll
    for (int it = 0; it < 2; it++) {
      const int d0 = 2 * (wave + 4 * it);
      s16x8 af[8], bh[8], bl[8];
#pragma unroll
      for (int j = 0; j < 8; j++) af[j] = *(const s16x8*)(ap + (32 * j + 2 * d0) * 16);
#pragma unroll
      for (int m = 0; m < 8; m++) {
        bh[m] = *(const s16x8*)(bp + (16 * m - d0) * 32);
        bl[m] = *(const s16x8*)(bp + (16 * m - d0) * 32 + 4608);
      }
#pragma unroll
      for (int m = 0; m < 8; m++) {
#pragma unroll
        for (int j = 0; j + m < 8; j++) acc[j + m] = __builtin_amdgcn_mfma_f32_16x16x32_bf16(af[j], bh[m], acc[j + m], 0, 0, 0);
#pragma unroll
        for (int j = 0; j + m < 8; j++) acc[j + m] = __builtin_amdgcn_mfma_f32_16x16x32_bf16(af[j], bl[m], acc[j + m], 0, 0, 0);
      }
    }
  }
  STAMP(1, 3);
  u32x2 uu[2];
#pragma unroll
  for (int t = 0; t < 2; t++) uu[t] = *(const u32x2*)(uT + UT_PAD + 256 * (2 * wave + t) + 4 * lane);
  const float dsk = b2f(Dskip[d]);
  __syncthreads();
  float* red = (float*)smem;
#pragma unroll
  for (int t = 0; t < 8; t++) *(f32x4*)(red + ((wave * 8 + t) * 64 + (lane & 15) * 4 + (lane >> 4)) * 4) = acc[t];
  __syncthreads();
  u16* aT = (u16*)(ws + WS_AT) + (size_t)d * L;
#pragma unroll
  for (int t = 0; t < 2; t++) {
    const int tt = 2 * wave + t;
    f32x4 s = *(const f32x4*)(red + ((0 * 8 + tt) * 64 + lane) * 4);
    f32x4 s1 = *(const f32x4*)(red + ((1 * 8 + tt) * 64 + lane) * 4);
    f32x4 s2 = *(const f32x4*)(red + ((2 * 8 + tt) * 64 + lane) * 4);
    f32x4 s3 = *(const f32x4*)(red + ((3 * 8 + tt) * 64 + lane) * 4);
    s = (s + s1) + (s2 + s3);
    u32x2 ov;
#pragma unroll
    for (int e = 0; e < 2; e++) {
      unsigned y = pack2(s[2 * e], s[2 * e + 1]);
      unsigned k = pack2(lo_f(uu[t][e]) * dsk, hi_f(uu[t][e]) * dsk);
      ov[e] = pack2(lo_f(y) + lo_f(k), hi_f(y) + hi_f(k));
    }
    *(u32x2*)(aT + 256 * tt + 4 * lane) = ov;
  }
  STAMP(1, 1);
}

constexpr int K3_WO = 0;
constexpr int K3_KV = 102400;
constexpr int K3_AT = K3_KV + 40960;
constexpr int K3_LDS = K3_AT + 80 * 144;
__global__ __launch_bounds__(512, 2) void k3(char* __restrict__ ws, u16* __restrict__ out) {
  __shared__ __attribute__((aligned(16))) char smem[K3_LDS];
  const int tid = threadIdx.x, lane = tid & 63;
  const int wave = __builtin_amdgcn_readfirstlane(tid >> 6);
  STAMP(2, 0);
  const int xcd = blockIdx.x & 7, idx = blockIdx.x >> 3;
  const int rowblk = xcd * 4 + (idx >> 3), cb = idx & 7;
  const int l0 = rowblk * 64, c0 = cb * 80;
  const int kq = wave & 3, t = wave >> 2;
  const int r = lane & 31, h = lane >> 5;
#pragma unroll
  for (int i = 0; i < 5; i++) glds16(ws + WS_KVT + (size_t)(wave * 5 + i) * 1024 + lane * 16, smem + K3_KV + (wave * 5 + i) * 1024);
  u32x4 pf0[8]; u32x2 pf1[8];
  {
    const char* p0 = ws + WS_PQ + ((size_t)((rowblk * 32 + kq * 8) * 2 + t) * 64 + lane) * 16;
    const char* p1 = ws + WS_PQ + (size_t)L * 16 * 32 * 2 + ((size_t)((rowblk * 32 + kq * 8) * 2 + t) * 32 + r) * 8;
#pragma unroll
    for (int hh = 0; hh < 8; hh++) {
      asm volatile("global_load_dwordx4 %0, %1, off offset:%2" : "=v"(pf0[hh]) : "v"(p0 + (hh >> 1) * 4096), "i"((hh & 1) * 2048));
      asm volatile("global_load_dwordx2 %0, %1, off offset:%2" : "=v"(pf1[hh]) : "v"(p1), "i"(hh * 512));
    }
  }
  {
    const char* src = ws + WS_WOF + (size_t)cb * 102400 + lane * 16;
#pragma unroll
    for (int i = 0; i < 13; i++) { int p = i * 8 + wave; if (p < 100) glds16(src + p * 1024, smem + K3_WO + p * 1024); }
  }
  u32x4 av[2];
  {
    const char* aT = ws + WS_AT;
#pragma unroll
    for (int i = 0; i < 2; i++) {
      int id = tid + 512 * i; if (id > 639) id = 639;
      const char* p = aT + ((size_t)(c0 + (id >> 3)) * L + l0 + 8 * (id & 7)) * 2;
      asm volatile("global_load_dwordx4 %0, %1, off" : "=v"(av[i]) : "v"(p));
    }
  }
  float gt[2];
  {
    const float* gate = (const float*)(ws + WS_GATE) + l0;
#pragma unroll
    for (int i = 0; i < 2; i++) {
      int id = tid + 512 * i; if (id > 639) id = 639;
      asm volatile("global_load_dword %0, %1, off" : "=v"(gt[i]) : "v"(gate + id / 10));
    }
  }
  if (wave < 4) VMCNT(17); else VMCNT(16);
  asm volatile("" : "+v"(pf0[0]), "+v"(pf0[1]), "+v"(pf0[2]), "+v"(pf0[3]), "+v"(pf0[4]), "+v"(pf0[5]), "+v"(pf0[6]), "+v"(pf0[7]));
  asm volatile("" : "+v"(pf1[0]), "+v"(pf1[1]), "+v"(pf1[2]), "+v"(pf1[3]), "+v"(pf1[4]), "+v"(pf1[5]), "+v"(pf1[6]), "+v"(pf1[7]));
  RAW_BARRIER();
  STAMP(2, 2);
  u32x4 ob[10];
  {
    unsigned rem[8][2];
#pragma unroll
    for (int hh = 0; hh < 8; hh++) {
      const char* kp = smem + K3_KV + (kq * 8 + hh) * 1280;
      s16x8 a0 = *(const s16x8*)(kp + lane * 16);
      u32x2 a1h = *(const u32x2*)(kp + 1024 + r * 8);
      u32x4 a1u = {a1h[0], a1h[1], 0u, 0u};
      u32x4 b1u = {h ? 0u : pf1[hh][0], h ? 0u : pf1[hh][1], 0u, 0u};
      f32x16 c;
#pragma unroll
      for (int i = 0; i < 16; i++) c[i] = 0.f;
      c = __builtin_amdgcn_mfma_f32_32x32x16_bf16(a0, __builtin_bit_cast(s16x8, pf0[hh]), c, 0, 0, 0);
      c = __builtin_amdgcn_mfma_f32_32x32x16_bf16(__builtin_bit_cast(s16x8, a1u), __builtin_bit_cast(s16x8, b1u), c, 0, 0, 0);
      float den = rb(rb(c[12]) + 1e-6f);
      float inv = __builtin_amdgcn_rcpf(den);
      unsigned o[6];
#pragma unroll
      for (int q = 0; q < 6; q++) {
        unsigned n0 = pack2(c[2 * q], c[2 * q + 1]);
        o[q] = pack2(lo_f(n0) * inv, hi_f(n0) * inv);
      }
      ob[hh] = (u32x4){o[0], o[1], o[2], o[3]};
      rem[hh][0] = o[4]; rem[hh][1] = o[5];
    }
#pragma unroll
    for (int G = 0; G < 2; G++)
      ob[8 + G] = (u32x4){h ? rem[4 * G + 2][0] : rem[4 * G][0], h ? rem[4 * G + 2][1] : rem[4 * G][1],
                          h ? rem[4 * G + 3][0] : rem[4 * G + 1][0], h ? rem[4 * G + 3][1] : rem[4 * G + 1][1]};
  }
  STAMP(2, 3);
  VMCNT(0);
  asm volatile("" : "+v"(av[0]), "+v"(av[1]), "+v"(gt[0]), "+v"(gt[1]));
#pragma unroll
  for (int i = 0; i < 2; i++) { int id = tid + 512 * i; if (id < 640) *(u32x4*)(smem + K3_AT + (id >> 3) * 144 + (id & 7) * 16) = av[i]; }
  RAW_BARRIER();
  STAMP(2, 4);
  f32x16 acc[3];
#pragma unroll
  for (int c = 0; c < 3; c++)
#pragma unroll
    for (int i = 0; i < 16; i++) acc[c][i] = 0.f;
  {
    const char* wp = smem + K3_WO + kq * 25600;
    const int o2 = 2048 + (h * 16 + (lane & 15)) * 16;
#pragma unroll
    for (int ks = 0; ks < 10; ks++) {
      s16x8 w0 = *(const s16x8*)(wp + ks * 2560 + lane * 16);
      s16x8 w1 = *(const s16x8*)(wp + ks * 2560 + 1024 + lane * 16);
      s16x8 w2 = *(const s16x8*)(wp + ks * 2560 + o2);
      s16x8 b = __builtin_bit_cast(s16x8, ob[ks]);
      acc[0] = __builtin_amdgcn_mfma_f32_32x32x16_bf16(w0, b, acc[0], 0, 0, 0);
      acc[1] = __builtin_amdgcn_mfma_f32_32x32x16_bf16(w1, b, acc[1], 0, 0, 0);
      acc[2] = __builtin_amdgcn_mfma_f32_32x32x16_bf16(w2, b, acc[2], 0, 0, 0);
    }
  }
  STAMP(2, 5);
  RAW_BARRIER();
  {
    float* pr = (float*)smem + (size_t)(kq * 64 + 32 * t + r) * 84 + 4 * h;
#pragma unroll
    for (int c = 0; c < 3; c++)
#pragma unroll
      for (int q = 0; q < 4; q++)
        if (c < 2 || q < 2) *(f32x4*)(pr + 32 * c + 8 * q) = (f32x4){acc[c][4 * q], acc[c][4 * q + 1], acc[c][4 * q + 2], acc[c][4 * q + 3]};
  }
  RAW_BARRIER();
  {
#pragma unroll
    for (int i2 = 0; i2 < 2; i2++) {
      const int i = tid + 512 * i2;
      if (i < 640) {
        const int rr = i / 10, oc = i - 10 * rr;
        const float g = gt[i2]; const float g1 = rb(1.f - g);
        f32x4 s0 = {0.f, 0.f, 0.f, 0.f}, s1 = {0.f, 0.f, 0.f, 0.f};
#pragma unroll
        for (int w = 0; w < 4; w++) {
          const float* pr = (const float*)smem + (size_t)(w * 64 + rr) * 84 + oc * 8;
          s0 += *(const f32x4*)pr; s1 += *(const f32x4*)(pr + 4);
        }
        u32x4 ov;
#pragma unroll
        for (int e = 0; e < 4; e++) {
          unsigned bb = e < 2 ? pack2(s0[2 * e], s0[2 * e + 1]) : pack2(s1[2 * e - 4], s1[2 * e - 3]);
          float a0 = b2f(*(const u16*)(smem + K3_AT + (oc * 8 + 2 * e) * 144 + rr * 2));
          float a1 = b2f(*(const u16*)(smem + K3_AT + (oc * 8 + 2 * e + 1) * 144 + rr * 2));
          unsigned ga = pack2(g * a0, g * a1), gb = pack2(g1 * lo_f(bb), g1 * hi_f(bb));
          ov[e] = pack2(lo_f(ga) + lo_f(gb), hi_f(ga) + hi_f(gb));
        }
        *(u32x4*)(out + (size_t)(l0 + rr) * D + c0 + oc * 8) = ov;
      }
    }
  }
  STAMP(2, 1);
}

extern "C" void kernel_launch(void* const* d_in, const int* in_sizes, int n_in, void* d_out, int out_size, void* d_ws, size_t ws_size,
                              hipStream_t stream) {
  const u16* u = (const u16*)d_in[0]; const u16* logA = (const u16*)d_in[1]; const u16* Aim = (const u16*)d_in[2];
  const u16* Cr = (const u16*)d_in[3]; const u16* Ci = (const u16*)d_in[4]; const u16* logdt = (const u16*)d_in[5];
  const u16* Dsk = (const u16*)d_in[6]; const u16* wqkv = (const u16*)d_in[8]; const u16* wo = (const u16*)d_in[9];
  const u16* gw = (const u16*)d_in[10];
  char* ws = (char*)d_ws;
  k1<<<NB_GEMM + NB_GATE + NB_KG, 512, 0, stream>>>(u, logA, Aim, Cr, Ci, logdt, wqkv, gw, wo, ws);
  k2<<<NB_KV + NB_WO + D, 256, 0, stream>>>(Dsk, ws);
  k3<<<256, 512, 0, stream>>>(ws, (u16*)d_out);
}
```

```cpp
#include <hip/hip_runtime.h>
#include <hip/hip_bf16.h>
#include <stdint.h>

typedef unsigned short u16;
typedef __attribute__((ext_vector_type(8))) short s16x8;
typedef __attribute__((ext_vector_type(4))) short s16x4;
typedef __attribute__((ext_vector_type(4))) float f32x4;
typedef __attribute__((ext_vector_type(16))) float f32x16;
typedef __attribute__((ext_vector_type(4))) unsigned u32x4;
typedef __attribute__((ext_vector_type(2))) unsigned u32x2;
typedef __attribute__((address_space(3))) s16x4* lds_s16x4_p;
typedef const __attribute__((address_space(1))) void* gptr_t;
typedef __attribute__((address_space(3))) void* lptr_t;

constexpr int L = 2048, D = 640, NS = 192, NH = 32;
constexpr int UT_PAD = 32, UT_STRIDE = L + UT_PAD;
constexpr size_t WS_UT   = 0;
constexpr size_t WS_KIMG = WS_UT + (size_t)D * UT_STRIDE * 2;
constexpr size_t WS_PQ   = WS_KIMG + (size_t)D * 6144;
constexpr size_t WS_PART = WS_PQ + (size_t)L * D * 2;
constexpr size_t WS_GATE = WS_PART + (size_t)32 * 16 * 1680;
constexpr size_t WS_WOF  = WS_GATE + (size_t)L * 4;
constexpr size_t WS_KVT  = WS_WOF + (size_t)D * D * 2;
constexpr size_t WS_AT   = WS_KVT + (size_t)32 * 1280;
constexpr size_t WS_CNT  = WS_AT + (size_t)D * L * 2;
constexpr size_t WS_END  = WS_CNT + 4096;
#define STAMP(kid, slot) do {} while (0)

__device__ __forceinline__ float b2f(u16 v) { return __uint_as_float(((unsigned)v) << 16); }
typedef __bf16 bf16x2_t __attribute__((ext_vector_type(2)));
typedef float f32x2 __attribute__((ext_vector_type(2)));
__device__ __forceinline__ unsigned pack2(float a, float b) { f32x2 v = {a, b}; bf16x2_t r = __builtin_convertvector(v, bf16x2_t); return __builtin_bit_cast(unsigned, r); }
__device__ __forceinline__ float lo_f(unsigned u) { return __uint_as_float(u << 16); }
__device__ __forceinline__ float hi_f(unsigned u) { return __uint_as_float(u & 0xffff0000u); }
__device__ __forceinline__ float rb(float f) { return lo_f(pack2(f, f)); }
__device__ __forceinline__ unsigned phi2(float a, float b) {
  unsigned t = pack2(a, b); float t0 = lo_f(t), t1 = hi_f(t);
  float e0 = __builtin_amdgcn_fmed3f(__builtin_amdgcn_exp2f(t0 * 1.44269504f), 0.f, 1.f);
  float e1 = __builtin_amdgcn_fmed3f(__builtin_amdgcn_exp2f(t1 * 1.44269504f), 0.f, 1.f);
  unsigned r = pack2(__builtin_fmaxf(t0, 0.f) + e0, __builtin_fmaxf(t1, 0.f) + e1);
  return pack2(lo_f(r) + 1e-6f, hi_f(r) + 1e-6f);
}
#define VMCNT(n) asm volatile("s_waitcnt vmcnt(" #n ")" ::: "memory")
#define RAW_BARRIER() do { asm volatile("s_waitcnt lgkmcnt(0)" ::: "memory"); __builtin_amdgcn_s_barrier(); asm volatile("" ::: "memory"); } while (0)
__device__ __forceinline__ void glds16(const void* g, void* l) { __builtin_amdgcn_global_load_lds((gptr_t)g, (lptr_t)l, 16, 0, 0); }

#define OPQ_S(v) asm volatile("" : "+s"(v))
typedef f32x2 cf;
__device__ __forceinline__ float smul(float a, float b) { float o; asm("v_mul_f32 %0, %1, %2" : "=v"(o) : "v"(a), "v"(b)); return o; }
__device__ __forceinline__ float sfma(float a, float b, float c) { float o; asm("v_fma_f32 %0, %1, %2, %3" : "=v"(o) : "v"(a), "v"(b), "v"(c)); return o; }
__device__ __forceinline__ float sfnma(float a, float b, float c) { float o; asm("v_fma_f32 %0, -%1, %2, %3" : "=v"(o) : "v"(a), "v"(b), "v"(c)); return o; }
__device__ __forceinline__ float ssub(float a, float b) { float o; asm("v_sub_f32 %0, %1, %2" : "=v"(o) : "v"(a), "v"(b)); return o; }
__device__ __forceinline__ cf cmul(cf a, cf b) {
  cf o;
  asm("v_pk_mul_f32 %0, %1, %2 op_sel:[0,0] op_sel_hi:[0,1]\n\t"
      "v_pk_fma_f32 %0, %1, %2, %0 op_sel:[1,1,0] op_sel_hi:[1,0,1] neg_lo:[1,0,0]" : "=&v"(o) : "v"(a), "v"(b));
  return o;
}

constexpr int K1_LDS = 81920;
constexpr int NB_GEMM = 256, NB_GATE = 16, NB_KG = 240;
#define WT 1
__device__ __forceinline__ void st16(void* p, u32x4 v) {
  asm volatile("global_store_dwordx4 %0, %1, off sc1\n\ts_nop 2" :: "v"(p), "v"(v) : "memory");
}

#define BAR_ONLY() do { asm volatile("" ::: "memory"); __builtin_amdgcn_s_barrier(); asm volatile("" ::: "memory"); } while (0)
#define SB() __builtin_amdgcn_sched_barrier(0)
template <bool ISQ>
__device__ __forceinline__ void gemm_role(char* smem, const u16* __restrict__ x, const u16* __restrict__ W, char* __restrict__ ws,
                                          int rowblk, int cb) {
  const int tid = threadIdx.x, lane = tid & 63;
  const int wave = __builtin_amdgcn_readfirstlane(tid >> 6);
  const int row0 = rowblk * 128, col0 = cb * 128;
  const int kvj = cb - 5;
  char* sB = smem; char* sA = smem + 32768;
  const int wr = wave & 1, wc = (wave >> 1) & 1, hf = wave >> 2;
  f32x4 ep[2][4];
  if (wave >= 4) {
    const int lw = wave - 4;
    const char* pA = (const char*)x + (size_t)(row0 + 32 * lw + (lane >> 3)) * 1280 + (((lane & 7) ^ (lane >> 3)) << 4);
    const int fB = (lane >> 4) | ((lw & 1) << 2);
    const int c16 = (((lane & 15) >> 1) ^ fB) * 2 + (lane & 1);
    int bcol = col0 + 8 * c16;
    if (!ISQ) bcol = min(640 + 640 * (c16 >> 3) + ((60 * kvj) & ~7) + 8 * (c16 & 7), 1912);
    const char* pB = (const char*)W + (size_t)(8 * lw + (lane >> 4)) * 3840 + bcol * 2;
    u32x2 tr[8];
    int tro;
    {
      const int fr = lane & 15, g = lane >> 4, p4 = lane & 3, r = 8 * g + (fr >> 2);
      tro = 32768 + r * 128 + (((2 * lw + (p4 >> 1)) ^ (r & 7)) << 4) + (p4 & 1) * 8;
    }
    auto issA = [&](int slot_off, int h) {
      glds16(pA + (16 * h) * 1280, sA + slot_off + (4 * lw + 2 * h) * 1024);
      glds16(pA + (16 * h + 8) * 1280, sA + slot_off + (4 * lw + 2 * h + 1) * 1024);
    };
    auto issB = [&](int slot_off) {
      glds16(pB, sB + slot_off + (2 * lw) * 1024);
      glds16(pB + 4 * 3840, sB + slot_off + (2 * lw + 1) * 1024);
      pB += (size_t)32 * 3840;
    };
    issA(0, 0); issB(0); issA(0, 1); pA += 128;
    issB(8192); issA(16384, 0); issB(16384); issA(16384, 1); pA += 128;
    issB(24576); issA(32768, 0);
    VMCNT(12);
    BAR_ONLY();
    int ao = 32768, bo = 0;
#pragma unroll 1
    for (int j = 0; j < 10; j++) {
      if (j < 8) VMCNT(10); else if (j == 8) VMCNT(8); else VMCNT(0);
      BAR_ONLY();
      if (j < 8) { issB(bo); bo = (bo + 8192) & 32767; issA(ao, 1); pA += 128; }
      ao = ao == 32768 ? 0 : ao + 16384;
      if (j == cb) {
        const int a0 = (int)(size_t)(lptr_t)smem + ao;
        const int a = a0 + tro, a2 = a0 + (tro ^ 64) + 512;
        asm volatile("ds_read_b64_tr_b16 %0, %8\n\tds_read_b64_tr_b16 %1, %9\n\tds_read_b64_tr_b16 %2, %8 offset:4096\n\tds_read_b64_tr_b16 %3, %9 offset:4096\n\t"
                     "ds_read_b64_tr_b16 %4, %8 offset:8192\n\tds_read_b64_tr_b16 %5, %9 offset:8192\n\tds_read_b64_tr_b16 %6, %8 offset:12288\n\tds_read_b64_tr_b16 %7, %9 offset:12288\n\t"
                     "s_waitcnt lgkmcnt(0)"
                     : "=&v"(tr[0]), "=&v"(tr[1]), "=&v"(tr[2]), "=&v"(tr[3]), "=&v"(tr[4]), "=&v"(tr[5]), "=&v"(tr[6]), "=&v"(tr[7])
                     : "v"(a), "v"(a2) : "memory");
      }
      if (j < 8) VMCNT(8); else VMCNT(2);
      BAR_ONLY();
      if (j < 8) { issB(bo); bo = (bo + 8192) & 32767; }
      if (j < 7) issA(ao, 0);
    }
    if (cb < 10) {
      u16* dst = (u16*)(ws + WS_UT) + (size_t)(64 * cb + 16 * lw + (lane & 15)) * UT_STRIDE + 8 * (lane >> 4);
#pragma unroll
      for (int i = 0; i < 4; i++) st16(dst + UT_PAD + row0 + 32 * i, (u32x4){tr[2 * i][0], tr[2 * i][1], tr[2 * i + 1][0], tr[2 * i + 1][1]});
      if (rowblk == 0) st16(dst, (u32x4){0u, 0u, 0u, 0u});
    }
    BAR_ONLY();
    char* xch = smem + (wave & 3) * 8192 + lane * 16;
#pragma unroll
    for (int i = 0; i < 8; i++) ep[i >> 2][i & 3] = *(const f32x4*)(xch + i * 1024);
  } else {
    const int g = lane >> 4, fr = lane & 15;
    f32x4 acc[4][4];
#pragma unroll
    for (int m = 0; m < 4; m++)
#pragma unroll
      for (int c = 0; c < 4; c++) acc[m][c] = (f32x4){0.f, 0.f, 0.f, 0.f};
    const int q4 = fr >> 2, p4 = lane & 3;
    int aoff[2];
#pragma unroll
    for (int ks = 0; ks < 2; ks++) aoff[ks] = (wr * 64 + fr) * 128 + (((4 * ks + g) ^ (fr & 7)) << 4);
    int boff[4];
    {
      const int f = q4 | ((g & 1) << 2);
#pragma unroll
      for (int c = 0; c < 4; c++) boff[c] = (8 * g + q4) * 256 + (((4 * wc + c) ^ f) << 5) + p4 * 8;
    }
    s16x8 af[2][4]; s16x4 bl[4], bh[4];
    auto rdA = [&](int buf, int m, const char* pa) { af[buf][m] = *(const s16x8*)(pa + m * 2048); };
    auto rdBl = [&](int c, const char* pb) { bl[c] = __builtin_amdgcn_ds_read_tr16_b64_v4i16((lds_s16x4_p)(pb + boff[c])); };
    auto rdBh = [&](int c, const char* pb) { bh[c] = __builtin_amdgcn_ds_read_tr16_b64_v4i16((lds_s16x4_p)(pb + boff[c] + 1024)); };
    BAR_ONLY();
    STAMP(0, 2);
#pragma unroll
    for (int c = 0; c < 4; c++) { rdA(0, c, sA + aoff[0]); rdBl(c, sB); rdBh(c, sB); }
    int ao0 = 0, ao1 = 16384;
    auto grp = [&](int buf, int c, int m) {
      const s16x8 bf = __builtin_shufflevector(bl[c], bh[c], 0, 1, 2, 3, 4, 5, 6, 7);
      if (ISQ) acc[m][c] = __builtin_amdgcn_mfma_f32_16x16x32_bf16(bf, af[buf][m], acc[m][c], 0, 0, 0);
      else     acc[m][c] = __builtin_amdgcn_mfma_f32_16x16x32_bf16(af[buf][m], bf, acc[m][c], 0, 0, 0);
    };
#pragma unroll
    for (int c = 0; c < 2; c++)
#pragma unroll
      for (int m = 0; m < 4; m++) grp(0, c, m);
#pragma unroll 1
    for (int j2 = 0; j2 < 5; j2++) {
#pragma unroll
      for (int st = 0; st < 4; st++) {
        const int hs = st & 1;
        SB();
        RAW_BARRIER();
        SB();
        const char* pa = sA + (hs == 0 ? ao0 + aoff[1] : ao1 + aoff[0]);
        const char* pb = sB + ((st + 1) & 3) * 8192;
        grp(hs, 2, 0); SB(); rdA(hs ^ 1, 0, pa); rdA(hs ^ 1, 1, pa); SB();
        grp(hs, 2, 1); SB(); rdA(hs ^ 1, 2, pa); rdA(hs ^ 1, 3, pa); SB();
        grp(hs, 2, 2); SB(); rdBl(0, pb); rdBh(0, pb); SB();
        grp(hs, 2, 3); SB(); rdBl(1, pb); rdBh(1, pb); SB();
        grp(hs, 3, 0); SB(); rdBl(2, pb); rdBh(2, pb); SB();
        grp(hs, 3, 1); grp(hs, 3, 2); grp(hs, 3, 3); SB();
        rdBl(3, pb); rdBh(3, pb); SB();
        if (st < 3 || j2 < 4) {
#pragma unroll
          for (int c = 0; c < 2; c++)
#pragma unroll
            for (int m = 0; m < 4; m++) grp(hs ^ 1, c, m);
        }
        if (hs == 1) { ao0 = ao1; ao1 = ao1 == 32768 ? 0 : ao1 + 16384; }
      }
    }
    STAMP(0, 3);
    int l3 = threadIdx.x; asm volatile("" : "+v"(l3));
    char* xch = smem + (l3 >> 6) * 8192 + (l3 & 63) * 16;
#pragma unroll
    for (int i = 0; i < 8; i++) *(f32x4*)(xch + i * 1024) = acc[2 + (i >> 2)][i & 3];
#pragma unroll
    for (int i = 0; i < 8; i++) ep[i >> 2][i & 3] = acc[i >> 2][i & 3];
    RAW_BARRIER();
  }
  int l2 = threadIdx.x; asm volatile("" : "+v"(l2));
  const int g = (l2 >> 4) & 3, fr = l2 & 15;
  if (ISQ) {
    char* pq0 = ws + WS_PQ; char* pq1 = ws + WS_PQ + (size_t)L * 16 * 32 * 2;
#pragma unroll
    for (int c = 0; c < 4; c++) {
      const int n = col0 + 64 * wc + 16 * c + 4 * g;
      const int hd = n / 20, d0 = n - 20 * hd;
#pragma unroll
      for (int m = 0; m < 2; m++) {
        const int rb64 = rowblk * 2 + wr, r = 16 * m + fr;
        u32x2 o;
        o[0] = phi2(ep[m][c][0], ep[m][c][1]);
        o[1] = phi2(ep[m][c][2], ep[m][c][3]);
        const int fi = (rb64 * 32 + hd) * 2 + hf;
        char* dst = d0 < 16 ? pq0 + ((size_t)fi * 64 + (d0 >> 3) * 32 + r) * 16 + (d0 & 7) * 2 : pq1 + ((size_t)fi * 32 + r) * 8;
        *(u32x2*)dst = o;
      }
    }
  } else {
    const int o = (60 * kvj) & 7, zc = o ? 0 : 60;
    const int lane2 = l2 & 63;
    char* fb = smem + 32768;
#pragma unroll
    for (int c = 0; c < 4; c++) {
      u32x4 a;
      if (wc == 0) {
        a[0] = phi2(ep[0][c][0], ep[0][c][1]); a[1] = phi2(ep[0][c][2], ep[0][c][3]);
        a[2] = phi2(ep[1][c][0], ep[1][c][1]); a[3] = phi2(ep[1][c][2], ep[1][c][3]);
      } else {
        a[0] = pack2(ep[0][c][0], ep[0][c][1]); a[1] = pack2(ep[0][c][2], ep[0][c][3]);
        a[2] = pack2(ep[1][c][0], ep[1][c][1]); a[3] = pack2(ep[1][c][2], ep[1][c][3]);
        if (16 * c + fr == zc) a = (u32x4){0x3F803F80u, 0x3F803F80u, 0x3F803F80u, 0x3F803F80u};
      }
      *(u32x4*)(fb + ((((wc * 4 + wr * 2 + hf) * 4 + c) * 64 + lane2) << 4)) = a;
    }
    RAW_BARRIER();
    const int np = wave < 4 ? 1 : 2;
    for (int i = 0; i < np; i++) {
      const int p = wave < 4 ? wave : wave * 2 - 4 + i;
      int ta, tb;
      if (p < 10) { ta = (int)((0x3322211100ull >> (4 * p)) & 15); tb = (int)((0x3232121010ull >> (4 * p)) & 15); }
      else { ta = (o ? 2 : 0) + (p - 10); tb = o ? 0 : 3; }
      const char* pk = fb + ((ta * 64 + lane2) << 4);
      const char* pv = fb + 16384 + ((tb * 64 + lane2) << 4);
      f32x4 dd = {0.f, 0.f, 0.f, 0.f};
#pragma unroll
      for (int rg = 0; rg < 4; rg++)
        dd = __builtin_amdgcn_mfma_f32_16x16x32_bf16(*(const s16x8*)(pk + rg * 4096), *(const s16x8*)(pv + rg * 4096), dd, 0, 0, 0);
      const int kc0 = 16 * ta + 4 * g - o;
      const int hk = kc0 / 20, dk0 = kc0 - 20 * hk;
      const int vcol = 16 * tb + fr;
      const int dv = vcol == zc ? 20 : vcol - o - 20 * hk;
      if (kc0 >= 0 && kc0 < (kvj == 10 ? 40 : 60) && dv >= 0 && (dv < 20 || vcol == zc))
        *(f32x4*)(ws + WS_PART + (size_t)(((3 * kvj + hk) * 16 + rowblk) * 21 + dv) * 80 + dk0 * 4) = dd;
    }
  }
}

__device__ __forceinline__ void gate_load(unsigned* v, const u16* __restrict__ x, const u16* __restrict__ gw, int row, int lane) {
  const u16* px = x + (size_t)row * D + 2 * lane; const u16* pg = gw + 2 * lane;
#pragma unroll
  for (int i = 0; i < 5; i++) {
    asm volatile("global_load_dword %0, %1, off offset:%2" : "=v"(v[i]) : "v"(px), "i"(256 * i));
    asm volatile("global_load_dword %0, %1, off offset:%2" : "=v"(v[5 + i]) : "v"(pg), "i"(256 * i));
  }
}
__device__ __forceinline__ void gate_finish(unsigned* v, float* __restrict__ gate, int row, int lane) {
  asm volatile("s_waitcnt vmcnt(0)" : "+v"(v[0]), "+v"(v[1]), "+v"(v[2]), "+v"(v[3]), "+v"(v[4]), "+v"(v[5]), "+v"(v[6]), "+v"(v[7]), "+v"(v[8]), "+v"(v[9]) :: "memory");
  float s = 0.f;
#pragma unroll
  for (int i = 0; i < 5; i++) {
    unsigned p = pack2(lo_f(v[i]) * lo_f(v[5 + i]), hi_f(v[i]) * hi_f(v[5 + i]));
    s += lo_f(p) + hi_f(p);
  }
#pragma unroll
  for (int o = 32; o > 0; o >>= 1) s += __shfl_xor(s, o);
  if (lane == 0) { float a = rb(s); gate[row] = rb(1.f / (1.f + __expf(-a))); }
}

__device__ __forceinline__ cf zpow(float th, float tl, float rh, float rl, float p) {
  float e = __builtin_amdgcn_exp2f(th * p); e = fmaf(e, (tl * p) * 0.69314718f, e);
  float t = rh * p; float f = (t - __builtin_rintf(t)) + rl * p;
  cf o; o[0] = e * __builtin_amdgcn_cosf(f); o[1] = e * __builtin_amdgcn_sinf(f); return o;
}
#define PKM(o, a, b) "v_pk_mul_f32 " o ", " a ", " b " op_sel:[0,0] op_sel_hi:[0,1]\n\t"
#define PKF(o, a, b) "v_pk_fma_f32 " o ", " a ", " b ", " o " op_sel:[1,1,0] op_sel_hi:[1,0,1] neg_lo:[1,0,0]\n\t"
__device__ __forceinline__ void cmul4(cf& o0, cf& o1, cf& o2, cf& o3, cf a0, cf b0, cf a1, cf b1, cf a2, cf b2, cf a3, cf b3) {
  asm(PKM("%0", "%4", "%5") PKM("%1", "%6", "%7") PKM("%2", "%8", "%9") PKM("%3", "%10", "%11")
      PKF("%0", "%4", "%5") PKF("%1", "%6", "%7") PKF("%2", "%8", "%9") PKF("%3", "%10", "%11")
      : "=&v"(o0), "=&v"(o1), "=&v"(o2), "=&v"(o3)
      : "v"(a0), "v"(b0), "v"(a1), "v"(b1), "v"(a2), "v"(b2), "v"(a3), "v"(b3));
}
__device__ __forceinline__ void batch4(unsigned* h, cf b, cf t0, cf t1, cf t2, cf t3) {
  cf w0, w1, w2, w3;
  asm(PKM("%0", "%4", "%5") PKM("%1", "%4", "%6") PKM("%2", "%4", "%7") PKM("%3", "%4", "%8")
      PKF("%0", "%4", "%5") PKF("%1", "%4", "%6") PKF("%2", "%4", "%7") PKF("%3", "%4", "%8")
      : "=&v"(w0), "=&v"(w1), "=&v"(w2), "=&v"(w3) : "v"(b), "v"(t0), "v"(t1), "v"(t2), "v"(t3));
  h[0] = pack2(w0[0], w0[1]); h[1] = pack2(w1[0], w1[1]); h[2] = pack2(w2[0], w2[1]); h[3] = pack2(w3[0], w3[1]);
}
__device__ __forceinline__ void batch3(unsigned* h, cf b, cf t1, cf t2, cf t3) {
  cf w1, w2, w3;
  asm(PKM("%0", "%3", "%4") PKM("%1", "%3", "%5") PKM("%2", "%3", "%6")
      PKF("%0", "%3", "%4") PKF("%1", "%3", "%5") PKF("%2", "%3", "%6")
      : "=&v"(w1), "=&v"(w2), "=&v"(w3) : "v"(b), "v"(t1), "v"(t2), "v"(t3));
  h[0] = pack2(b[0], b[1]); h[1] = pack2(w1[0], w1[1]); h[2] = pack2(w2[0], w2[1]); h[3] = pack2(w3[0], w3[1]);
}
constexpr int KG_RS = 288, KG_PL = 16 * KG_RS;
__device__ __forceinline__ void kgen_role(char* smem, const u16* __restrict__ logA, const u16* __restrict__ Aim,
                                          const u16* __restrict__ Cr, const u16* __restrict__ Ci, const u16* __restrict__ logdt,
                                          const u16* __restrict__ x, const u16* __restrict__ W, const u16* __restrict__ wo,
                                          const u16* __restrict__ gw, char* __restrict__ ws, int blk) {
  const int tid = threadIdx.x, lane = tid & 63;
  const int wave = __builtin_amdgcn_readfirstlane(tid >> 6);
  constexpr int RS = KG_RS, PL = KG_PL;
  const int task = blk * 8 + wave;
  const int d = task / 3;
  const int pi = task * 64 + lane;
#define PF 1
  unsigned pfv = 0; const char* pp;
  {
    const int xcd = blk & 7, i = (blk >> 3) * 64 + lane;
    const int q = i & 15;
    const int ln = (xcd & 1) ? (q < 8 ? 12 + q : 14 + q) : (q < 13 ? q : 7 + q);
    if (i < 1280) pp = (const char*)W + (size_t)(80 * wave + (i >> 4)) * 3840 + ln * 128;
    else { const int id = wave * 640 + (i - 1280); pp = (const char*)x + (size_t)((xcd >> 1) * 512 + (id & 511)) * 1280 + (id >> 9) * 128; }
  }
  const int pfpt = wave == 0 ? 9 : (wave <= 2 ? 0 : (wave <= 5 ? 1 : 2));
#define PFLOAD(pt) do { __builtin_amdgcn_sched_barrier(0); if (pfpt == (pt)) asm volatile("global_load_dword %0, %1, off" : "+v"(pfv) : "v"(pp)); __builtin_amdgcn_sched_barrier(0); } while (0)
  PFLOAD(0);
  const float dt = rb(expf(b2f(logdt[d])));
  float ar;
  {
    const float la = b2f(logA[pi]);
    float h = la * 1.44269502f; float l = fmaf(la, 1.44269502f, -h) + la * 1.92596299e-8f;
    float e = __builtin_amdgcn_exp2f(h); e = fmaf(e, l * 0.69314718f, e);
    ar = -rb(e) * dt;
  }
  const float ai = b2f(Aim[pi]) * dt;
  cf C; C[0] = 2.f * (b2f(Cr[pi]) * dt); C[1] = 2.f * (b2f(Ci[pi]) * dt);
  const float th = ar * 1.44269502f, tl = fmaf(ar, 1.44269502f, -th) + ar * 1.92596299e-8f;
  const float rh = ai * 0.159154937f, rl = fmaf(ai, 0.159154937f, -rh) + ai * 6.42063833e-9f;
  char* tb = smem + wave * (2 * PL);
  char* wp = tb + lane * 4;
  const char* rp = tb + (lane & 15) * RS + (lane >> 4) * 16;
  STAMP(0, 2);
  PFLOAD(1);
  const cf one = {1.f, 0.f};
  cf S1 = zpow(th, tl, rh, rl, 1.f), Q1 = zpow(th, tl, rh, rl, 8.f), R1 = zpow(th, tl, rh, rl, 128.f);
  Q1[1] = -Q1[1];
  cf S2, Q2, R2, G1, S3, S4, Q3, P1, R3, T1, G2, G3, S5, S6, S7, P2, CT1, T2, P3, CS4, CT2, CT3, CT4, dm0;
  cmul4(S2, Q2, R2, G1, S1, S1, Q1, Q1, R1, R1, C, R1);
  cmul4(S3, S4, Q3, P1, S2, S1, S2, S2, Q2, Q1, Q2, Q2);
  cmul4(R3, T1, G2, P2, R2, R1, R2, R2, C, R2, P1, P1);
  cmul4(S5, S6, S7, P3, S4, S1, S4, S2, S4, S3, P2, P1);
  cmul4(G3, CT1, T2, dm0, C, R3, C, T1, T1, T1, C, R1);
  cmul4(CS4, CT2, CT3, dm0, CT1, S4, C, T2, CT1, T2, C, R1);
  CT4 = cmul(CT2, T2);
  PFLOAD(2);
  unsigned gv[10];
  gate_load(gv, x, gw, task, lane);
  const int wid = min(blk * 216 + (tid < 216 ? tid : 215), 51199);
  unsigned wr_[8];
  {
    const int slot = wid & 127, f = wid >> 7;
    const int s5 = f % 5, hb = 4 * ((f / 5) & 7), cb = f / 40;
    const int m = (slot >> 6) * 32 + (slot & 31), h = (slot >> 5) & 1;
    const int n = 64 * cb + m;
    int k0, k1;
    if (s5 < 4) { k0 = 20 * (hb + s5) + 4 * h; k1 = k0 + 8; }
    else { k0 = 20 * (hb + 2 * h) + 16; k1 = k0 + 20; }
    if (wave < 4) {
      const u16* p0 = wo + (size_t)k0 * D + n; const u16* p1 = wo + (size_t)k1 * D + n;
#pragma unroll
      for (int e = 0; e < 4; e++) {
        asm volatile("global_load_ushort %0, %1, off offset:%2" : "=v"(wr_[e]) : "v"(p0), "i"(e * 1280));
        asm volatile("global_load_ushort %0, %1, off offset:%2" : "=v"(wr_[4 + e]) : "v"(p1), "i"(e * 1280));
      }
    }
  }
  auto rows4 = [&](int pl, int row, int rs, cf b, cf t0, cf t1, cf t2, cf t3) {
    unsigned h[4];
    batch4(h, b, t0, t1, t2, t3);
#pragma unroll
    for (int k = 0; k < 4; k++) *(unsigned*)(wp + pl * PL + (row + k * rs) * RS) = h[k];
  };
  auto rows3 = [&](int pl, int row, int rs, cf b, cf t1, cf t2, cf t3) {
    unsigned h[4];
    batch3(h, b, t1, t2, t3);
#pragma unroll
    for (int k = 0; k < 4; k++) *(unsigned*)(wp + pl * PL + (row + k * rs) * RS) = h[k];
  };
  *(unsigned*)(wp) = 0x00003F80u;
  *(unsigned*)(wp + RS) = pack2(Q1[0], Q1[1]); *(unsigned*)(wp + 2 * RS) = pack2(Q2[0], Q2[1]); *(unsigned*)(wp + 3 * RS) = pack2(Q3[0], Q3[1]);
  rows3(0, 4, 1, P1, Q1, Q2, Q3); rows3(0, 8, 1, P2, Q1, Q2, Q3); rows3(0, 12, 1, P3, Q1, Q2, Q3);
  rows3(1, 0, 1, C, S1, S2, S3); rows4(1, 4, 1, C, S4, S5, S6, S7); rows3(1, 8, 1, G1, S1, S2, S3); rows4(1, 12, 1, G1, S4, S5, S6, S7);
  __builtin_amdgcn_wave_barrier();
  s16x8 bh[4];
  f32x4 res[3];
#pragma unroll
  for (int k = 0; k < 4; k++) bh[k] = *(const s16x8*)(rp + k * 64);
  auto mm = [&](int t, int pl) {
    f32x4 a = {0.f, 0.f, 0.f, 0.f};
#pragma unroll
    for (int k = 0; k < 4; k++) a = __builtin_amdgcn_mfma_f32_16x16x32_bf16(*(const s16x8*)(rp + pl * PL + k * 64), bh[k], a, 0, 0, 0);
    res[t] = a;
  };
  mm(0, 1);
  __builtin_amdgcn_wave_barrier();
  STAMP(0, 3);
  rows3(0, 0, 1, G2, S2, S4, S6); rows3(0, 4, 1, G3, S2, S4, S6); rows3(0, 8, 2, CT1, R1, R2, R3); rows3(0, 9, 2, CS4, R1, R2, R3);
  rows3(1, 0, 1, CT2, R1, R2, R3); rows3(1, 4, 1, CT3, R1, R2, R3);
  *(unsigned*)(wp + PL + 8 * RS) = pack2(CT4[0], CT4[1]);
  __builtin_amdgcn_wave_barrier();
  mm(1, 0); mm(2, 1);
  __builtin_amdgcn_wave_barrier();
  STAMP(0, 4);
  if (wave < 4) {
    asm volatile("s_waitcnt vmcnt(0)" : "+v"(wr_[0]), "+v"(wr_[1]), "+v"(wr_[2]), "+v"(wr_[3]), "+v"(wr_[4]), "+v"(wr_[5]), "+v"(wr_[6]), "+v"(wr_[7]) :: "memory");
    u32x4 wv = {wr_[0] | (wr_[1] << 16), wr_[2] | (wr_[3] << 16), wr_[4] | (wr_[5] << 16), wr_[6] | (wr_[7] << 16)};
    if (tid < 216 && blk * 216 + tid < 51200) st16(ws + WS_WOF + (size_t)wid * 16, wv);
  }
  gate_finish(gv, (float*)(ws + WS_GATE), task, lane);
#pragma unroll
  for (int t = 0; t < 3; t++) *(f32x4*)(tb + t * 1024 + lane * 16) = res[t];
  __syncthreads();
  {
    const int d0 = (blk * 8) / 3;
#pragma unroll
    for (int it = 0; it < 2; it++) {
      const int e = tid + 512 * it;
      const int seg = e / 192, ent = e - 192 * seg;
      const int ds = d0 + seg;
      const int wlo = max(0, 3 * ds - 8 * blk), whi = min(8, 3 * ds + 3 - 8 * blk);
      if (seg < 4 && wlo < 8) {
        f32x4 a = *(const f32x4*)(smem + wlo * (2 * PL) + ent * 16);
        for (int w = wlo + 1; w < whi; w++) a += *(const f32x4*)(smem + w * (2 * PL) + ent * 16);
        st16(ws + WS_KIMG + (size_t)ds * 6144 + (3 * ds < 8 * blk ? 3072 : 0) + ent * 16, __builtin_bit_cast(u32x4, a));
      }
    }
  }
  asm volatile("s_waitcnt vmcnt(0)" :: "v"(pfv) : "memory");
}

__global__ __launch_bounds__(512, 4) void k1(const u16* __restrict__ x, const u16* __restrict__ logA, const u16* __restrict__ Aim,
                                          const u16* __restrict__ Cr, const u16* __restrict__ Ci, const u16* __restrict__ logdt,
                                          const u16* __restrict__ wqkv, const u16* __restrict__ gw, const u16* __restrict__ wo,
                                          char* __restrict__ ws) {
  __shared__ __attribute__((aligned(16))) char smem[K1_LDS];
  int b = blockIdx.x;
  STAMP(0, 0);
  b = b < NB_KG ? b + NB_GEMM : (b < NB_KG + NB_GEMM ? b - NB_KG : b);
  if (b < NB_GEMM) {
    const int xcd = b & 7, idx = b >> 3;
    const int rowblk = (xcd >> 1) * 4 + (idx & 3), cb = (xcd & 1) * 8 + (idx >> 2);
    if (cb < 5) gemm_role<true>(smem, x, wqkv, ws, rowblk, cb);
    else        gemm_role<false>(smem, x, wqkv, ws, rowblk, cb);
    STAMP(0, 1);
    return;
  }
  b -= NB_GEMM;
  if (b < NB_KG) { kgen_role(smem, logA, Aim, Cr, Ci, logdt, x, wqkv, wo, gw, ws, b); STAMP(0, 1); return; }
  b -= NB_KG;
  if (b == 0) ((unsigned*)(ws + WS_CNT))[threadIdx.x] = 0u, ((unsigned*)(ws + WS_CNT))[threadIdx.x + 512] = 0u;
  {
    unsigned gv[10];
    const int row = NB_KG * 8 + b * 8 + (threadIdx.x >> 6);
    gate_load(gv, x, gw, row, threadIdx.x & 63);
    gate_finish(gv, (float*)(ws + WS_GATE), row, threadIdx.x & 63);
  }
  STAMP(0, 1);
}

constexpr int K2_CS = 4160;
constexpr int K2_KOFF = 4 * K2_CS;
constexpr int K2_TAB = K2_KOFF + 4608;
constexpr int K2_LDS = 40960;
constexpr int NB_KV = 32, NB_PROJ = 320;
typedef __attribute__((address_space(1))) unsigned gu32;
__device__ __forceinline__ const char* uni(const char* p) {
  size_t v = (size_t)p;
  unsigned lo = __builtin_amdgcn_readfirstlane((unsigned)v), hi = __builtin_amdgcn_readfirstlane((unsigned)(v >> 32));
  size_t o = ((size_t)hi << 32) | lo;
  asm volatile("s_nop 4" : "+s"(o));
  return (const char*)o;
}
__device__ __forceinline__ void wait_count(char* p, unsigned target) {
  gu32* c = (gu32*)(size_t)p;
  for (int n = 0; n < (1 << 22); n++) {
    if (__hip_atomic_load(c, __ATOMIC_RELAXED, __HIP_MEMORY_SCOPE_AGENT) >= target) break;
    __builtin_amdgcn_s_sleep(4);
  }
}
#define GLD4(dst, voff, sbase, pol) asm volatile("global_load_dwordx4 %0, %1, %2" pol : "=v"(dst) : "v"(voff), "s"(sbase))
#define GLD2(dst, voff, sbase, pol) asm volatile("global_load_dwordx2 %0, %1, %2" pol : "=v"(dst) : "v"(voff), "s"(sbase))
#define VMW(n, i) asm volatile("s_waitcnt vmcnt(" #n ")" : "+v"(pf0[i]), "+v"(pf1[i]), "+v"(ka[i]), "+v"(kb[i]) :: "memory")
__device__ __forceinline__ void proj_role(char* smem, char* __restrict__ ws, u16* __restrict__ out, int pb) {
  const int tid = threadIdx.x, lane = tid & 63;
  const int wave = __builtin_amdgcn_readfirstlane(tid >> 6);
  const int t = wave & 1, kh = wave >> 1;
  const int r = lane & 31, h = lane >> 5;
  const int xcd = pb & 7, idx = pb >> 3;
  const int rowblk = xcd * 4 + idx / 10, cb = idx % 10;
  const int l0 = rowblk * 64, c0 = cb * 64;
  const int lds0 = (int)(size_t)(lptr_t)smem;
  const char* wsrc = uni(ws + WS_WOF + (size_t)(cb * 2 + kh) * 40960 + t * 5120);
  const int l16 = lane * 16, r8 = r * 8;
  auto dma = [&](int G) {
    const char* sp = wsrc + G * 10240;
    const int lb = __builtin_amdgcn_readfirstlane(lds0 + (G & 1) * 20480 + kh * 10240 + t * 5120);
    asm volatile("s_mov_b32 m0, %2\n\ts_nop 0\n\tglobal_load_lds_dwordx4 %0, %1\n\tglobal_load_lds_dwordx4 %0, %1 offset:1024\n\t"
                 "global_load_lds_dwordx4 %0, %1 offset:2048\n\tglobal_load_lds_dwordx4 %0, %1 offset:3072"
                 :: "v"(l16), "s"(sp), "s"(lb) : "memory", "m0");
    asm volatile("s_mov_b32 m0, %2\n\ts_nop 0\n\tglobal_load_lds_dwordx4 %0, %1" :: "v"(l16), "s"(sp + 4096), "s"(lb + 4096) : "memory", "m0");
  };
  const char* p0 = uni(ws + WS_PQ + (size_t)((rowblk * 32 + kh * 16) * 2 + t) * 1024);
  const char* p1 = uni(ws + WS_PQ + (size_t)L * 16 * 32 * 2 + (size_t)((rowblk * 32 + kh * 16) * 2 + t) * 256);
  const char* kp = uni(ws + WS_KVT + (size_t)kh * 16 * 1280);
#define PW 2
  constexpr int W = PW;
  constexpr int WA[3][16] = {{2, 4, 4, 4, 4, 4, 9, 9, 4, 4, 9, 9, 4, 4, 4, 0}, {4, 6, 8, 8, 8, 8, 13, 13, 13, 8, 13, 13, 13, 8, 4, 0},
                             {6, 8, 10, 12, 12, 12, 17, 17, 17, 17, 17, 17, 17, 13, 4, 0}};
  constexpr int WD[3][4] = {{0, 0, 16, 16}, {0, 0, 16, 12}, {0, 0, 16, 8}};
  u32x4 pf0[W], ka[W]; u32x2 pf1[W], kb[W];
  dma(0); dma(1);
#pragma unroll
  for (int i = 0; i < W; i++) { GLD4(pf0[i], l16, p0 + i * 2048, ""); GLD2(pf1[i], r8, p1 + i * 512, ""); }
#define PWARM 1
  unsigned wm = 0;
  if (wave != 0) {
    const int l128 = lane * 128;
    const int o0 = (lane >> 3) * 2048 + (lane & 7) * 128;
    const int o1 = lane < 32 ? 16384 + o0 : 0;
    const int o2 = (lane >> 1) * 512 + (lane & 1) * 128;
    const int o3 = lane < 40 ? l128 : 0;
    asm volatile("global_load_dword %0, %1, %2" : "+v"(wm) : "v"(o0), "s"(p0 + 8192));
    asm volatile("global_load_dword %0, %1, %2" : "+v"(wm) : "v"(o1), "s"(p0 + 8192));
    asm volatile("global_load_dword %0, %1, %2" : "+v"(wm) : "v"(lane < 24 ? o2 : 0), "s"(p1 + 2048));
    asm volatile("global_load_dword %0, %1, %2" : "+v"(wm) : "v"(o3), "s"(wsrc + 20480));
    asm volatile("global_load_dword %0, %1, %2" : "+v"(wm) : "v"(o3), "s"(wsrc + 30720));
  }
  if (tid == 0) wait_count(ws + WS_CNT + xcd * 128, NB_KV);
  BAR_ONLY();
  STAMP(1, 2);
#pragma unroll
  for (int i = 0; i < W; i++) { GLD4(ka[i], l16, kp + i * 1280, " sc1"); GLD2(kb[i], r8, kp + i * 1280 + 1024, " sc1"); }
  f32x16 acc[2], c[2];
#pragma unroll
  for (int q = 0; q < 2; q++)
#pragma unroll
    for (int i = 0; i < 16; i++) acc[q][i] = 0.f;
  u32x4 rm = {0u, 0u, 0u, 0u};
  const char* wl = smem + kh * 10240 + l16;
  auto stageA = [&](int i) {
    const int j = i % W;
    asm volatile("s_waitcnt vmcnt(%4)" : "+v"(pf0[j]), "+v"(pf1[j]), "+v"(ka[j]), "+v"(kb[j]) : "n"(WA[W - 2][i]) : "memory");
    if (i == 0) asm volatile("" : "+v"(wm));
    u32x2 b1u = {h ? 0u : pf1[j][0], h ? 0u : pf1[j][1]};
    f32x16 z;
#pragma unroll
    for (int q = 0; q < 16; q++) z[q] = 0.f;
    z = __builtin_amdgcn_mfma_f32_32x32x16_bf16(__builtin_bit_cast(s16x8, ka[j]), __builtin_bit_cast(s16x8, pf0[j]), z, 0, 0, 0);
    c[i & 1] = __builtin_amdgcn_mfma_f32_32x32x8bf16_1k(__builtin_bit_cast(s16x4, kb[j]), __builtin_bit_cast(s16x4, b1u), z, 0, 0, 0);
    asm volatile("" : "+v"(c[i & 1]));
    if (i + W < 16) {
      GLD4(pf0[j], l16, p0 + (i + W) * 2048, ""); GLD2(pf1[j], r8, p1 + (i + W) * 512, "");
      GLD4(ka[j], l16, kp + (i + W) * 1280, " sc1"); GLD2(kb[j], r8, kp + (i + W) * 1280 + 1024, " sc1");
    }
  };
  stageA(0);
#pragma unroll
  for (int i = 0; i < 16; i++) {
    const int j = i & 3, G = i >> 2;
    if (i + 1 < 16) stageA(i + 1);
    if (j == 0) {
      if (G >= 2) asm volatile("s_waitcnt vmcnt(%0)" :: "n"(WD[W - 2][G]) : "memory");
      RAW_BARRIER();
      if (G == 1 || G == 2) dma(G + 1);
    }
    const char* wp = wl + (G & 1) * 20480;
    s16x8 w0 = *(const s16x8*)(wp + j * 2048), w1 = *(const s16x8*)(wp + j * 2048 + 1024);
    const f32x16 cc = c[i & 1];
    const float den = rb(rb(cc[12]) + 1e-6f);
    const float inv = __builtin_amdgcn_rcpf(den);
    unsigned o[6];
#pragma unroll
    for (int q = 0; q < 6; q++) {
      unsigned n0 = pack2(cc[2 * q], cc[2 * q + 1]);
      o[q] = pack2(lo_f(n0) * inv, hi_f(n0) * inv);
    }
    asm volatile("" : "+v"(o[4]), "+v"(o[5]));
    const bool keep = (j >> 1) == h;
    rm[2 * (j & 1)] = keep ? o[4] : rm[2 * (j & 1)]; rm[2 * (j & 1) + 1] = keep ? o[5] : rm[2 * (j & 1) + 1];
    u32x4 ob = {o[0], o[1], o[2], o[3]};
    acc[0] = __builtin_amdgcn_mfma_f32_32x32x16_bf16(w0, __builtin_bit_cast(s16x8, ob), acc[0], 0, 0, 0);
    acc[1] = __builtin_amdgcn_mfma_f32_32x32x16_bf16(w1, __builtin_bit_cast(s16x8, ob), acc[1], 0, 0, 0);
    if (j == 3) {
      s16x8 w2 = *(const s16x8*)(wp + 8192), w3 = *(const s16x8*)(wp + 9216);
      acc[0] = __builtin_amdgcn_mfma_f32_32x32x16_bf16(w2, __builtin_bit_cast(s16x8, rm), acc[0], 0, 0, 0);
      acc[1] = __builtin_amdgcn_mfma_f32_32x32x16_bf16(w3, __builtin_bit_cast(s16x8, rm), acc[1], 0, 0, 0);
    }
  }
  const float* gate = (const float*)(ws + WS_GATE) + l0;
  const float g_r = gate[32 * t + r];
  const float g_f[2] = {gate[tid >> 3], gate[(tid >> 3) + 32]};
  RAW_BARRIER();
  STAMP(1, 3);
  {
    char* xw = smem + ((t * 2 + (1 - kh)) * 4) * 1024 + l16;
#pragma unroll
    for (int q = 0; q < 4; q++) {
      f32x4 v = kh ? (f32x4){acc[0][4 * q], acc[0][4 * q + 1], acc[0][4 * q + 2], acc[0][4 * q + 3]}
                   : (f32x4){acc[1][4 * q], acc[1][4 * q + 1], acc[1][4 * q + 2], acc[1][4 * q + 3]};
      *(f32x4*)(xw + q * 1024) = v;
    }
  }
  RAW_BARRIER();
  {
    const char* xr = smem + ((t * 2 + kh) * 4) * 1024 + l16;
    const float g1 = rb(1.f - g_r);
    char* gbp = smem + 16384 + (32 * t + r) * 144 + (32 * kh + 4 * h) * 2;
#pragma unroll
    for (int q = 0; q < 4; q++) {
      f32x4 v = *(const f32x4*)(xr + q * 1024);
      f32x4 m = kh ? (f32x4){acc[1][4 * q], acc[1][4 * q + 1], acc[1][4 * q + 2], acc[1][4 * q + 3]}
                   : (f32x4){acc[0][4 * q], acc[0][4 * q + 1], acc[0][4 * q + 2], acc[0][4 * q + 3]};
      v += m;
      unsigned b0 = pack2(v[0], v[1]), b1 = pack2(v[2], v[3]);
      *(u32x2*)(gbp + q * 16) = (u32x2){pack2(g1 * lo_f(b0), g1 * hi_f(b0)), pack2(g1 * lo_f(b1), g1 * hi_f(b1))};
    }
  }
  STAMP(1, 4);
  if (tid == 0) wait_count(ws + WS_CNT + (8 + cb) * 128, 64);
  RAW_BARRIER();
  STAMP(1, 5);
  {
    u32x4 av[2];
    const char* ap = ws + WS_AT + ((size_t)(c0 + (tid >> 3)) * L + l0 + 8 * (tid & 7)) * 2;
    asm volatile("global_load_dwordx4 %0, %2, off sc1\n\tglobal_load_dwordx4 %1, %3, off sc1\n\ts_waitcnt vmcnt(0)"
                 : "=&v"(av[0]), "=&v"(av[1]) : "v"(ap), "v"(ap + (size_t)32 * L * 2) : "memory");
#pragma unroll
    for (int i = 0; i < 2; i++) *(u32x4*)(smem + 25600 + ((tid >> 3) + 32 * i) * 144 + (tid & 7) * 16) = av[i];
  }
  RAW_BARRIER();
#pragma unroll
  for (int i = 0; i < 2; i++) {
    const int rr = (tid >> 3) + 32 * i, oc = tid & 7;
    const float g = g_f[i];
    u32x4 gb = *(const u32x4*)(smem + 16384 + rr * 144 + oc * 16);
    u32x4 ov;
#pragma unroll
    for (int e = 0; e < 4; e++) {
      float a0 = b2f(*(const u16*)(smem + 25600 + (oc * 8 + 2 * e) * 144 + rr * 2));
      float a1 = b2f(*(const u16*)(smem + 25600 + (oc * 8 + 2 * e + 1) * 144 + rr * 2));
      unsigned ga = pack2(g * a0, g * a1);
      ov[e] = pack2(lo_f(ga) + lo_f(gb[e]), hi_f(ga) + hi_f(gb[e]));
    }
    *(u32x4*)(out + (size_t)(l0 + rr) * D + c0 + oc * 8) = ov;
  }
}

__global__ __launch_bounds__(256, 4) void k2(const u16* __restrict__ Dskip, char* __restrict__ ws, u16* __restrict__ out) {
  __shared__ __attribute__((aligned(16))) char smem[K2_LDS];
  const int tid = threadIdx.x, lane = tid & 63;
  const int wave = __builtin_amdgcn_readfirstlane(tid >> 6);
  STAMP(1, 0);
#define PPRIO 0
  if (blockIdx.x >= NB_KV + D) { __builtin_amdgcn_s_setprio(PPRIO); proj_role(smem, ws, out, blockIdx.x - (NB_KV + D)); STAMP(1, 1); return; }
  if (blockIdx.x < NB_KV) {
    __builtin_amdgcn_s_setprio(3);
    const int hd = blockIdx.x;
    const int h = lane >> 5;
    float* fin = (float*)smem;
    if (tid < 105) {
      const f32x4* src = (const f32x4*)(ws + WS_PART) + (size_t)hd * 16 * 105 + tid;
      f32x4 v[16];
#pragma unroll
      for (int i = 0; i < 16; i++) v[i] = src[i * 105];
#pragma unroll
      for (int st = 1; st < 16; st <<= 1)
#pragma unroll
        for (int i = 0; i < 16; i += 2 * st) v[i] += v[i + st];
      *(f32x4*)(fin + tid * 4) = (f32x4){rb(v[0][0]), rb(v[0][1]), rb(v[0][2]), rb(v[0][3])};
    }
    __syncthreads();
    if (wave == 0) {
      const int m = lane & 31;
      int col = m < 16 ? m : ((m >> 2) == ((hd & 2) ? 5 : 4) ? 16 + (m & 3) : 20);
      u32x4 o;
#pragma unroll
      for (int e = 0; e < 4; e++) o[e] = pack2(fin[col * 20 + 8 * h + 2 * e], fin[col * 20 + 8 * h + 2 * e + 1]);
      st16(ws + WS_KVT + (size_t)hd * 1280 + lane * 16, o);
      const int m0 = 2 * (lane & 15), m1 = m0 + 1;
      int ca = m0 < 16 ? m0 : ((m0 >> 2) == ((hd & 2) ? 5 : 4) ? 16 + (m0 & 3) : 20);
      int cb = m1 < 16 ? m1 : ((m1 >> 2) == ((hd & 2) ? 5 : 4) ? 16 + (m1 & 3) : 20);
      u32x4 o2 = {pack2(fin[ca * 20 + 16], fin[ca * 20 + 17]), pack2(fin[ca * 20 + 18], fin[ca * 20 + 19]),
                  pack2(fin[cb * 20 + 16], fin[cb * 20 + 17]), pack2(fin[cb * 20 + 18], fin[cb * 20 + 19])};
      if (lane < 16) st16(ws + WS_KVT + (size_t)hd * 1280 + 1024 + lane * 16, o2);
      VMCNT(0);
      if (lane < 8) __hip_atomic_fetch_add((gu32*)(size_t)(ws + WS_CNT + lane * 128), 1u, __ATOMIC_RELAXED, __HIP_MEMORY_SCOPE_AGENT);
    }
    STAMP(1, 1);
    return;
  }
  const int d = blockIdx.x - NB_KV;
  const u16* uT = (const u16*)(ws + WS_UT) + (size_t)d * UT_STRIDE;
  {
    const f32x4* ksrc = (const f32x4*)(ws + WS_KIMG + (size_t)d * 6144) + (tid < 192 ? tid : 191);
    const u32x4* usrc = (const u32x4*)uT;
    f32x4 p0 = ksrc[0];
    if ((d & 7) == 2 || (d & 7) == 5) p0 += ksrc[192];
    u32x4 c0 = usrc[tid + 1], c1 = usrc[tid + 2];
    u32x4 e0, e1;
    if (tid < 3) { e0 = usrc[min(tid + 257, 259)]; e1 = usrc[min(tid + 258, 259)]; }
    if (tid < 32) *(u32x4*)(smem + K2_KOFF + tid * 16) = (u32x4){0u, 0u, 0u, 0u};
    if (tid < 192) *(f32x4*)(smem + K2_TAB + tid * 16) = p0;
    auto put_copies = [&](u32x4 a, u32x4 b, int xch) {
      unsigned w[6] = {a[0], a[1], a[2], a[3], b[0], b[1]};
      unsigned o[5];
#pragma unroll
      for (int k = 0; k < 5; k++) o[k] = __builtin_amdgcn_alignbyte(w[k + 1], w[k], 2);
      char* p = smem + xch * 16;
#pragma unroll
      for (int q = 0; q < 2; q++) {
        *(u32x4*)(p + (2 * q) * K2_CS) = (u32x4){o[q], o[q + 1], o[q + 2], o[q + 3]};
        *(u32x4*)(p + (2 * q + 1) * K2_CS) = (u32x4){w[q + 1], w[q + 2], w[q + 3], w[q + 4]};
      }
    };
    put_copies(c0, c1, tid);
    if (tid < 3) put_copies(e0, e1, tid + 256);
  }
  __syncthreads();
  {
    const float* tab = (const float*)(smem + K2_TAB);
    float kv[8];
#pragma unroll
    for (int e = 0; e < 8; e++) {
      const int j = tid * 8 + 7 - e;
      const int p = (int)rb((float)j);
      const int h = p >> 7, c = (p >> 3) & 15;
      int sh = 31 - __builtin_clz(h | 1); sh = sh > 3 ? 3 : sh;
      const int row = 8 * sh + (((h << 3) | (p & 7)) >> sh);
      kv[e] = tab[((row >> 2) * 16 + c) * 4 + (row & 3)];
    }
    u32x4 hi;
#pragma unroll
    for (int e = 0; e < 4; e++) hi[e] = pack2(kv[2 * e], kv[2 * e + 1]);
    *(u32x4*)(smem + K2_KOFF + 512 + tid * 16) = hi;
  }
  __syncthreads();
  STAMP(1, 2);
  f32x4 acc[8];
#pragma unroll
  for (int t = 0; t < 8; t++) acc[t] = (f32x4){0.f, 0.f, 0.f, 0.f};
  {
    const int i = lane & 15, kg = lane >> 4, e = kg >> 1, bb = kg & 1;
    const char* ap = smem + (i & 3) * K2_CS + (2 * e - bb + 2) * 16 + (i >> 2) * 8;
    const char* bp = smem + K2_KOFF + (16 + i - e) * 32 + bb * 16;
#pragma unroll 1
    for (int it = 0; it < 2; it++) {
      const int d0 = 2 * (wave + 4 * it);
      s16x8 af[8];
#pragma unroll
      for (int j = 0; j < 8; j++) {
        u32x2 a0 = *(const volatile u32x2*)(ap + (32 * j + 2 * d0) * 16);
        u32x2 a1 = *(const volatile u32x2*)(ap + (32 * j + 2 * d0) * 16 + 8);
        af[j] = __builtin_bit_cast(s16x8, (u32x4){a0[0], a0[1], a1[0], a1[1]});
      }
      s16x8 bh[8];
#pragma unroll
      for (int m = 0; m < 8; m++) bh[m] = *(const s16x8*)(bp + (16 * m - d0) * 32);
#pragma unroll
      for (int m = 0; m < 8; m++)
#pragma unroll
        for (int j = 0; j + m < 8; j++) acc[j + m] = __builtin_amdgcn_mfma_f32_16x16x32_bf16(af[j], bh[m], acc[j + m], 0, 0, 0);
    }
  }
  STAMP(1, 3);
  const u32x4 uu = *(const u32x4*)(uT + UT_PAD + 8 * tid);
  const float dsk = b2f(Dskip[d]);
  __syncthreads();
  float* red = (float*)smem;
#pragma unroll
  for (int t = 0; t < 8; t++) *(f32x4*)(red + ((wave * 8 + t) * 64 + (lane & 15) * 4 + (lane >> 4)) * 4) = acc[t];
  __syncthreads();
  {
    u32x4 ov;
#pragma unroll
    for (int hf = 0; hf < 2; hf++) {
      const float* rp = red + (tid * 2 + hf) * 4;
      f32x4 s = *(const f32x4*)rp, s1 = *(const f32x4*)(rp + 2048), s2 = *(const f32x4*)(rp + 4096), s3 = *(const f32x4*)(rp + 6144);
      s = (s + s1) + (s2 + s3);
#pragma unroll
      for (int e = 0; e < 2; e++) {
        unsigned y = pack2(s[2 * e], s[2 * e + 1]);
        unsigned k = pack2(lo_f(uu[2 * hf + e]) * dsk, hi_f(uu[2 * hf + e]) * dsk);
        ov[2 * hf + e] = pack2(lo_f(y) + lo_f(k), hi_f(y) + hi_f(k));
      }
    }
    st16(ws + WS_AT + ((size_t)d * L + 8 * tid) * 2, ov);
  }
  VMCNT(0);
  BAR_ONLY();
  if (tid == 0) __hip_atomic_fetch_add((gu32*)(size_t)(ws + WS_CNT + (8 + (d >> 6)) * 128), 1u, __ATOMIC_RELAXED, __HIP_MEMORY_SCOPE_AGENT);
  STAMP(1, 1);
}

extern "C" void kernel_launch(void* const* d_in, const int* in_sizes, int n_in, void* d_out, int out_size, void* d_ws, size_t ws_size,
                              hipStream_t stream) {
  const u16* u = (const u16*)d_in[0]; const u16* logA = (const u16*)d_in[1]; const u16* Aim = (const u16*)d_in[2];
  const u16* Cr = (const u16*)d_in[3]; const u16* Ci = (const u16*)d_in[4]; const u16* logdt = (const u16*)d_in[5];
  const u16* Dsk = (const u16*)d_in[6]; const u16* wqkv = (const u16*)d_in[8]; const u16* wo = (const u16*)d_in[9];
  const u16* gw = (const u16*)d_in[10];
  char* ws = (char*)d_ws;
  k1<<<NB_GEMM + NB_GATE + NB_KG, 512, 0, stream>>>(u, logA, Aim, Cr, Ci, logdt, wqkv, gw, wo, ws);
  k2<<<NB_KV + D + NB_PROJ, 256, 0, stream>>>(Dsk, ws, (u16*)d_out);
}
```

```cpp
#include <hip/hip_runtime.h>
#include <hip/hip_bf16.h>
#include <stdint.h>

typedef unsigned short u16;
typedef __attribute__((ext_vector_type(8))) short s16x8;
typedef __attribute__((ext_vector_type(4))) short s16x4;
typedef __attribute__((ext_vector_type(4))) float f32x4;
typedef __attribute__((ext_vector_type(16))) float f32x16;
typedef __attribute__((ext_vector_type(4))) unsigned u32x4;
typedef __attribute__((ext_vector_type(2))) unsigned u32x2;
typedef __attribute__((address_space(3))) s16x4* lds_s16x4_p;
typedef const __attribute__((address_space(1))) void* gptr_t;
typedef __attribute__((address_space(3))) void* lptr_t;

constexpr int L = 2048, D = 640, NS = 192, NH = 32;
constexpr int UT_PAD = 32, UT_STRIDE = L + UT_PAD;
constexpr size_t WS_UT   = 0;
constexpr size_t WS_KIMG = WS_UT + (size_t)D * UT_STRIDE * 2;
constexpr size_t WS_PQ   = WS_KIMG + (size_t)D * 6144;
constexpr size_t WS_PART = WS_PQ + (size_t)L * D * 2;
constexpr size_t WS_GATE = WS_PART + (size_t)32 * 16 * 1680;
constexpr size_t WS_WOF  = WS_GATE + (size_t)L * 4;
constexpr size_t WS_KVT  = WS_WOF + (size_t)D * D * 2;
constexpr size_t WS_AT   = WS_KVT + (size_t)32 * 1280;
constexpr size_t WS_CNT  = WS_AT + (size_t)D * L * 2;
constexpr size_t WS_END  = WS_CNT + 4096;
#define STAMP(kid, slot) do {} while (0)

__device__ __forceinline__ float b2f(u16 v) { return __uint_as_float(((unsigned)v) << 16); }
typedef __bf16 bf16x2_t __attribute__((ext_vector_type(2)));
typedef float f32x2 __attribute__((ext_vector_type(2)));
__device__ __forceinline__ unsigned pack2(float a, float b) { f32x2 v = {a, b}; bf16x2_t r = __builtin_convertvector(v, bf16x2_t); return __builtin_bit_cast(unsigned, r); }
__device__ __forceinline__ float lo_f(unsigned u) { return __uint_as_float(u << 16); }
__device__ __forceinline__ float hi_f(unsigned u) { return __uint_as_float(u & 0xffff0000u); }
__device__ __forceinline__ float rb(float f) { return lo_f(pack2(f, f)); }
__device__ __forceinline__ unsigned phi2(float a, float b) {
  unsigned t = pack2(a, b); float t0 = lo_f(t), t1 = hi_f(t);
  float e0 = __builtin_amdgcn_fmed3f(__builtin_amdgcn_exp2f(t0 * 1.44269504f), 0.f, 1.f);
  float e1 = __builtin_amdgcn_fmed3f(__builtin_amdgcn_exp2f(t1 * 1.44269504f), 0.f, 1.f);
  unsigned r = pack2(__builtin_fmaxf(t0, 0.f) + e0, __builtin_fmaxf(t1, 0.f) + e1);
  return pack2(lo_f(r) + 1e-6f, hi_f(r) + 1e-6f);
}
#define VMCNT(n) asm volatile("s_waitcnt vmcnt(" #n ")" ::: "memory")
#define RAW_BARRIER() do { asm volatile("s_waitcnt lgkmcnt(0)" ::: "memory"); __builtin_amdgcn_s_barrier(); asm volatile("" ::: "memory"); } while (0)
__device__ __forceinline__ void glds16(const void* g, void* l) { __builtin_amdgcn_global_load_lds((gptr_t)g, (lptr_t)l, 16, 0, 0); }

#define OPQ_S(v) asm volatile("" : "+s"(v))
typedef f32x2 cf;
__device__ __forceinline__ float smul(float a, float b) { float o; asm("v_mul_f32 %0, %1, %2" : "=v"(o) : "v"(a), "v"(b)); return o; }
__device__ __forceinline__ float sfma(float a, float b, float c) { float o; asm("v_fma_f32 %0, %1, %2, %3" : "=v"(o) : "v"(a), "v"(b), "v"(c)); return o; }
__device__ __forceinline__ float sfnma(float a, float b, float c) { float o; asm("v_fma_f32 %0, -%1, %2, %3" : "=v"(o) : "v"(a), "v"(b), "v"(c)); return o; }
__device__ __forceinline__ float ssub(float a, float b) { float o; asm("v_sub_f32 %0, %1, %2" : "=v"(o) : "v"(a), "v"(b)); return o; }
__device__ __forceinline__ cf cmul(cf a, cf b) {
  cf o;
  asm("v_pk_mul_f32 %0, %1, %2 op_sel:[0,0] op_sel_hi:[0,1]\n\t"
      "v_pk_fma_f32 %0, %1, %2, %0 op_sel:[1,1,0] op_sel_hi:[1,0,1] neg_lo:[1,0,0]" : "=&v"(o) : "v"(a), "v"(b));
  return o;
}

#define CPF 3
constexpr int K1_LDS = 81920;
constexpr int NB_GEMM = 256, NB_GATE = 16, NB_KG = 240;
#define WT 1
__device__ __forceinline__ void st16(void* p, u32x4 v) {
  asm volatile("global_store_dwordx4 %0, %1, off sc1\n\ts_nop 2" :: "v"(p), "v"(v) : "memory");
}

#define BAR_ONLY() do { asm volatile("" ::: "memory"); __builtin_amdgcn_s_barrier(); asm volatile("" ::: "memory"); } while (0)
#define SB() __builtin_amdgcn_sched_barrier(0)
template <bool ISQ>
__device__ __forceinline__ void gemm_role(char* smem, const u16* __restrict__ x, const u16* __restrict__ W, char* __restrict__ ws,
                                          int rowblk, int cb) {
  const int tid = threadIdx.x, lane = tid & 63;
  const int wave = __builtin_amdgcn_readfirstlane(tid >> 6);
  const int row0 = rowblk * 128, col0 = cb * 128;
  const int kvj = cb - 5;
  char* sB = smem; char* sA = smem + 32768;
  const int wr = wave & 1, wc = (wave >> 1) & 1, hf = wave >> 2;
  f32x4 ep[2][4];
  if (wave >= 4) {
    const int lw = wave - 4;
    const char* pA = (const char*)x + (size_t)(row0 + 32 * lw + (lane >> 3)) * 1280 + (((lane & 7) ^ (lane >> 3)) << 4);
    const int fB = (lane >> 4) | ((lw & 1) << 2);
    const int c16 = (((lane & 15) >> 1) ^ fB) * 2 + (lane & 1);
    int bcol = col0 + 8 * c16;
    if (!ISQ) bcol = min(640 + 640 * (c16 >> 3) + ((60 * kvj) & ~7) + 8 * (c16 & 7), 1912);
    const char* pB = (const char*)W + (size_t)(8 * lw + (lane >> 4)) * 3840 + bcol * 2;
    u32x2 tr[8];
    int tro;
    {
      const int fr = lane & 15, g = lane >> 4, p4 = lane & 3, r = 8 * g + (fr >> 2);
      tro = 32768 + r * 128 + (((2 * lw + (p4 >> 1)) ^ (r & 7)) << 4) + (p4 & 1) * 8;
    }
    auto issA = [&](int slot_off, int h) {
      glds16(pA + (16 * h) * 1280, sA + slot_off + (4 * lw + 2 * h) * 1024);
      glds16(pA + (16 * h + 8) * 1280, sA + slot_off + (4 * lw + 2 * h + 1) * 1024);
    };
    auto issB = [&](int slot_off) {
      glds16(pB, sB + slot_off + (2 * lw) * 1024);
      glds16(pB + 4 * 3840, sB + slot_off + (2 * lw + 1) * 1024);
      pB += (size_t)32 * 3840;
    };
    issA(0, 0); issB(0); issA(0, 1); pA += 128;
    issB(8192); issA(16384, 0); issB(16384); issA(16384, 1); pA += 128;
    issB(24576); issA(32768, 0);
    VMCNT(12);
    BAR_ONLY();
    int ao = 32768, bo = 0;
#pragma unroll 1
    for (int j = 0; j < 10; j++) {
      if (j < 8) VMCNT(10); else if (j == 8) VMCNT(8); else VMCNT(0);
      BAR_ONLY();
      if (j < 8) { issB(bo); bo = (bo + 8192) & 32767; issA(ao, 1); pA += 128; }
      ao = ao == 32768 ? 0 : ao + 16384;
      if (j == cb) {
        const int a0 = (int)(size_t)(lptr_t)smem + ao;
        const int a = a0 + tro, a2 = a0 + (tro ^ 64) + 512;
        asm volatile("ds_read_b64_tr_b16 %0, %8\n\tds_read_b64_tr_b16 %1, %9\n\tds_read_b64_tr_b16 %2, %8 offset:4096\n\tds_read_b64_tr_b16 %3, %9 offset:4096\n\t"
                     "ds_read_b64_tr_b16 %4, %8 offset:8192\n\tds_read_b64_tr_b16 %5, %9 offset:8192\n\tds_read_b64_tr_b16 %6, %8 offset:12288\n\tds_read_b64_tr_b16 %7, %9 offset:12288\n\t"
                     "s_waitcnt lgkmcnt(0)"
                     : "=&v"(tr[0]), "=&v"(tr[1]), "=&v"(tr[2]), "=&v"(tr[3]), "=&v"(tr[4]), "=&v"(tr[5]), "=&v"(tr[6]), "=&v"(tr[7])
                     : "v"(a), "v"(a2) : "memory");
      }
      if (j < 8) VMCNT(8); else VMCNT(2);
      BAR_ONLY();
      if (j < 8) { issB(bo); bo = (bo + 8192) & 32767; }
      if (j < 7) issA(ao, 0);
    }
    if (cb < 10) {
      u16* dst = (u16*)(ws + WS_UT) + (size_t)(64 * cb + 16 * lw + (lane & 15)) * UT_STRIDE + 8 * (lane >> 4);
#pragma unroll
      for (int i = 0; i < 4; i++) st16(dst + UT_PAD + row0 + 32 * i, (u32x4){tr[2 * i][0], tr[2 * i][1], tr[2 * i + 1][0], tr[2 * i + 1][1]});
      if (rowblk == 0) st16(dst, (u32x4){0u, 0u, 0u, 0u});
    }
    BAR_ONLY();
    char* xch = smem + (wave & 3) * 8192 + lane * 16;
#pragma unroll
    for (int i = 0; i < 8; i++) ep[i >> 2][i & 3] = *(const f32x4*)(xch + i * 1024);
  } else {
    const int g = lane >> 4, fr = lane & 15;
    f32x4 acc[4][4];
#pragma unroll
    for (int m = 0; m < 4; m++)
#pragma unroll
      for (int c = 0; c < 4; c++) acc[m][c] = (f32x4){0.f, 0.f, 0.f, 0.f};
    const int q4 = fr >> 2, p4 = lane & 3;
    int aoff[2];
#pragma unroll
    for (int ks = 0; ks < 2; ks++) aoff[ks] = (wr * 64 + fr) * 128 + (((4 * ks + g) ^ (fr & 7)) << 4);
    int boff[4];
    {
      const int f = q4 | ((g & 1) << 2);
#pragma unroll
      for (int c = 0; c < 4; c++) boff[c] = (8 * g + q4) * 256 + (((4 * wc + c) ^ f) << 5) + p4 * 8;
    }
    s16x8 af[2][4]; s16x4 bl[4], bh[4];
    auto rdA = [&](int buf, int m, const char* pa) { af[buf][m] = *(const s16x8*)(pa + m * 2048); };
    auto rdBl = [&](int c, const char* pb) { bl[c] = __builtin_amdgcn_ds_read_tr16_b64_v4i16((lds_s16x4_p)(pb + boff[c])); };
    auto rdBh = [&](int c, const char* pb) { bh[c] = __builtin_amdgcn_ds_read_tr16_b64_v4i16((lds_s16x4_p)(pb + boff[c] + 1024)); };
    BAR_ONLY();
    STAMP(0, 2);
#pragma unroll
    for (int c = 0; c < 4; c++) { rdA(0, c, sA + aoff[0]); rdBl(c, sB); rdBh(c, sB); }
    int ao0 = 0, ao1 = 16384;
    auto grp = [&](int buf, int c, int m) {
      const s16x8 bf = __builtin_shufflevector(bl[c], bh[c], 0, 1, 2, 3, 4, 5, 6, 7);
      if (ISQ) acc[m][c] = __builtin_amdgcn_mfma_f32_16x16x32_bf16(bf, af[buf][m], acc[m][c], 0, 0, 0);
      else     acc[m][c] = __builtin_amdgcn_mfma_f32_16x16x32_bf16(af[buf][m], bf, acc[m][c], 0, 0, 0);
    };
#pragma unroll
    for (int c = 0; c < 2; c++)
#pragma unroll
      for (int m = 0; m < 4; m++) grp(0, c, m);
#pragma unroll 1
    for (int j2 = 0; j2 < 5; j2++) {
#pragma unroll
      for (int st = 0; st < 4; st++) {
        const int hs = st & 1;
        SB();
        RAW_BARRIER();
        SB();
        const char* pa = sA + (hs == 0 ? ao0 + aoff[1] : ao1 + aoff[0]);
        const char* pb = sB + ((st + 1) & 3) * 8192;
        grp(hs, 2, 0); SB(); rdA(hs ^ 1, 0, pa); rdA(hs ^ 1, 1, pa); SB();
        grp(hs, 2, 1); SB(); rdA(hs ^ 1, 2, pa); rdA(hs ^ 1, 3, pa); SB();
        grp(hs, 2, 2); SB(); rdBl(0, pb); rdBh(0, pb); SB();
        grp(hs, 2, 3); SB(); rdBl(1, pb); rdBh(1, pb); SB();
        grp(hs, 3, 0); SB(); rdBl(2, pb); rdBh(2, pb); SB();
        grp(hs, 3, 1); grp(hs, 3, 2); grp(hs, 3, 3); SB();
        rdBl(3, pb); rdBh(3, pb); SB();
        if (st < 3 || j2 < 4) {
#pragma unroll
          for (int c = 0; c < 2; c++)
#pragma unroll
            for (int m = 0; m < 4; m++) grp(hs ^ 1, c, m);
        }
        if (hs == 1) { ao0 = ao1; ao1 = ao1 == 32768 ? 0 : ao1 + 16384; }
      }
    }
    STAMP(0, 3);
    int l3 = threadIdx.x; asm volatile("" : "+v"(l3));
    char* xch = smem + (l3 >> 6) * 8192 + (l3 & 63) * 16;
#pragma unroll
    for (int i = 0; i < 8; i++) *(f32x4*)(xch + i * 1024) = acc[2 + (i >> 2)][i & 3];
#pragma unroll
    for (int i = 0; i < 8; i++) ep[i >> 2][i & 3] = acc[i >> 2][i & 3];
    RAW_BARRIER();
  }
  int l2 = threadIdx.x; asm volatile("" : "+v"(l2));
  const int g = (l2 >> 4) & 3, fr = l2 & 15;
  if (ISQ) {
    char* pq0 = ws + WS_PQ; char* pq1 = ws + WS_PQ + (size_t)L * 16 * 32 * 2;
#pragma unroll
    for (int c = 0; c < 4; c++) {
      const int n = col0 + 64 * wc + 16 * c + 4 * g;
      const int hd = n / 20, d0 = n - 20 * hd;
#pragma unroll
      for (int m = 0; m < 2; m++) {
        const int rb64 = rowblk * 2 + wr, r = 16 * m + fr;
        u32x2 o;
        o[0] = phi2(ep[m][c][0], ep[m][c][1]);
        o[1] = phi2(ep[m][c][2], ep[m][c][3]);
        const int fi = (rb64 * 32 + hd) * 2 + hf;
        char* dst = d0 < 16 ? pq0 + ((size_t)fi * 64 + (d0 >> 3) * 32 + r) * 16 + (d0 & 7) * 2 : pq1 + ((size_t)fi * 32 + r) * 8;
        *(u32x2*)dst = o;
      }
    }
  } else {
    const int o = (60 * kvj) & 7, zc = o ? 0 : 60;
    const int lane2 = l2 & 63;
    char* fb = smem + 32768;
#pragma unroll
    for (int c = 0; c < 4; c++) {
      u32x4 a;
      if (wc == 0) {
        a[0] = phi2(ep[0][c][0], ep[0][c][1]); a[1] = phi2(ep[0][c][2], ep[0][c][3]);
        a[2] = phi2(ep[1][c][0], ep[1][c][1]); a[3] = phi2(ep[1][c][2], ep[1][c][3]);
      } else {
        a[0] = pack2(ep[0][c][0], ep[0][c][1]); a[1] = pack2(ep[0][c][2], ep[0][c][3]);
        a[2] = pack2(ep[1][c][0], ep[1][c][1]); a[3] = pack2(ep[1][c][2], ep[1][c][3]);
        if (16 * c + fr == zc) a = (u32x4){0x3F803F80u, 0x3F803F80u, 0x3F803F80u, 0x3F803F80u};
      }
      *(u32x4*)(fb + ((((wc * 4 + wr * 2 + hf) * 4 + c) * 64 + lane2) << 4)) = a;
    }
    RAW_BARRIER();
    const int np = wave < 4 ? 1 : 2;
    for (int i = 0; i < np; i++) {
      const int p = wave < 4 ? wave : wave * 2 - 4 + i;
      int ta, tb;
      if (p < 10) { ta = (int)((0x3322211100ull >> (4 * p)) & 15); tb = (int)((0x3232121010ull >> (4 * p)) & 15); }
      else { ta = (o ? 2 : 0) + (p - 10); tb = o ? 0 : 3; }
      const char* pk = fb + ((ta * 64 + lane2) << 4);
      const char* pv = fb + 16384 + ((tb * 64 + lane2) << 4);
      f32x4 dd = {0.f, 0.f, 0.f, 0.f};
#pragma unroll
      for (int rg = 0; rg < 4; rg++)
        dd = __builtin_amdgcn_mfma_f32_16x16x32_bf16(*(const s16x8*)(pk + rg * 4096), *(const s16x8*)(pv + rg * 4096), dd, 0, 0, 0);
      const int kc0 = 16 * ta + 4 * g - o;
      const int hk = kc0 / 20, dk0 = kc0 - 20 * hk;
      const int vcol = 16 * tb + fr;
      const int dv = vcol == zc ? 20 : vcol - o - 20 * hk;
      if (kc0 >= 0 && kc0 < (kvj == 10 ? 40 : 60) && dv >= 0 && (dv < 20 || vcol == zc))
        *(f32x4*)(ws + WS_PART + (size_t)(((3 * kvj + hk) * 16 + rowblk) * 21 + dv) * 80 + dk0 * 4) = dd;
    }
  }
}

__device__ __forceinline__ void gate_load(unsigned* v, const u16* __restrict__ x, const u16* __restrict__ gw, int row, int lane) {
  const u16* px = x + (size_t)row * D + 2 * lane; const u16* pg = gw + 2 * lane;
#pragma unroll
  for (int i = 0; i < 5; i++) {
    asm volatile("global_load_dword %0, %1, off offset:%2" : "=v"(v[i]) : "v"(px), "i"(256 * i));
    asm volatile("global_load_dword %0, %1, off offset:%2" : "=v"(v[5 + i]) : "v"(pg), "i"(256 * i));
  }
}
__device__ __forceinline__ void gate_finish(unsigned* v, float* __restrict__ gate, int row, int lane) {
  asm volatile("s_waitcnt vmcnt(0)" : "+v"(v[0]), "+v"(v[1]), "+v"(v[2]), "+v"(v[3]), "+v"(v[4]), "+v"(v[5]), "+v"(v[6]), "+v"(v[7]), "+v"(v[8]), "+v"(v[9]) :: "memory");
  float s = 0.f;
#pragma unroll
  for (int i = 0; i < 5; i++) {
    unsigned p = pack2(lo_f(v[i]) * lo_f(v[5 + i]), hi_f(v[i]) * hi_f(v[5 + i]));
    s += lo_f(p) + hi_f(p);
  }
#pragma unroll
  for (int o = 32; o > 0; o >>= 1) s += __shfl_xor(s, o);
  if (lane == 0) { float a = rb(s); gate[row] = rb(1.f / (1.f + __expf(-a))); }
}

__device__ __forceinline__ cf zpow(float th, float tl, float rh, float rl, float p) {
  float e = __builtin_amdgcn_exp2f(th * p); e = fmaf(e, (tl * p) * 0.69314718f, e);
  float t = rh * p; float f = (t - __builtin_rintf(t)) + rl * p;
  cf o; o[0] = e * __builtin_amdgcn_cosf(f); o[1] = e * __builtin_amdgcn_sinf(f); return o;
}
#define PKM(o, a, b) "v_pk_mul_f32 " o ", " a ", " b " op_sel:[0,0] op_sel_hi:[0,1]\n\t"
#define PKF(o, a, b) "v_pk_fma_f32 " o ", " a ", " b ", " o " op_sel:[1,1,0] op_sel_hi:[1,0,1] neg_lo:[1,0,0]\n\t"
__device__ __forceinline__ void cmul4(cf& o0, cf& o1, cf& o2, cf& o3, cf a0, cf b0, cf a1, cf b1, cf a2, cf b2, cf a3, cf b3) {
  asm(PKM("%0", "%4", "%5") PKM("%1", "%6", "%7") PKM("%2", "%8", "%9") PKM("%3", "%10", "%11")
      PKF("%0", "%4", "%5") PKF("%1", "%6", "%7") PKF("%2", "%8", "%9") PKF("%3", "%10", "%11")
      : "=&v"(o0), "=&v"(o1), "=&v"(o2), "=&v"(o3)
      : "v"(a0), "v"(b0), "v"(a1), "v"(b1), "v"(a2), "v"(b2), "v"(a3), "v"(b3));
}
__device__ __forceinline__ void batch4(unsigned* h, cf b, cf t0, cf t1, cf t2, cf t3) {
  cf w0, w1, w2, w3;
  asm(PKM("%0", "%4", "%5") PKM("%1", "%4", "%6") PKM("%2", "%4", "%7") PKM("%3", "%4", "%8")
      PKF("%0", "%4", "%5") PKF("%1", "%4", "%6") PKF("%2", "%4", "%7") PKF("%3", "%4", "%8")
      : "=&v"(w0), "=&v"(w1), "=&v"(w2), "=&v"(w3) : "v"(b), "v"(t0), "v"(t1), "v"(t2), "v"(t3));
  h[0] = pack2(w0[0], w0[1]); h[1] = pack2(w1[0], w1[1]); h[2] = pack2(w2[0], w2[1]); h[3] = pack2(w3[0], w3[1]);
}
__device__ __forceinline__ void batch3(unsigned* h, cf b, cf t1, cf t2, cf t3) {
  cf w1, w2, w3;
  asm(PKM("%0", "%3", "%4") PKM("%1", "%3", "%5") PKM("%2", "%3", "%6")
      PKF("%0", "%3", "%4") PKF("%1", "%3", "%5") PKF("%2", "%3", "%6")
      : "=&v"(w1), "=&v"(w2), "=&v"(w3) : "v"(b), "v"(t1), "v"(t2), "v"(t3));
  h[0] = pack2(b[0], b[1]); h[1] = pack2(w1[0], w1[1]); h[2] = pack2(w2[0], w2[1]); h[3] = pack2(w3[0], w3[1]);
}
constexpr int KG_RS = 288, KG_PL = 16 * KG_RS;
__device__ __forceinline__ void kgen_role(char* smem, const u16* __restrict__ logA, const u16* __restrict__ Aim,
                                          const u16* __restrict__ Cr, const u16* __restrict__ Ci, const u16* __restrict__ logdt,
                                          const u16* __restrict__ x, const u16* __restrict__ W, const u16* __restrict__ wo,
                                          const u16* __restrict__ gw, char* __restrict__ ws, int blk, unsigned pfv) {
  const int tid = threadIdx.x, lane = tid & 63;
  const int wave = __builtin_amdgcn_readfirstlane(tid >> 6);
  constexpr int RS = KG_RS, PL = KG_PL;
  const int task = blk * 8 + wave;
  const int d = task / 3;
  const int pi = task * 64 + lane;
#define PF 1
#define PFM 1
#define PFS0 3
#define PFS1 10
  const char* pp;
  {
    const int xcd = blk & 7, i = (blk >> 3) * 64 + lane;
    const int q = i & 15;
    const int ln = (xcd & 1) ? (q < 8 ? 12 + q : 14 + q) : (q < 13 ? q : 7 + q);
    if (i < 1280) pp = (const char*)W + (size_t)(80 * wave + (i >> 4)) * 3840 + ln * 128;
    else { const int id = wave * 640 + (i - 1280); pp = (const char*)x + (size_t)((xcd >> 1) * 512 + (id & 511)) * 1280 + (id >> 9) * 128; }
  }
  const int pfpt = wave == 0 ? 9 : (wave <= 2 ? 0 : (wave <= 5 ? 1 : 2));
#define PFLOAD(pt) do { __builtin_amdgcn_sched_barrier(0); if (pfpt == (pt)) asm volatile("global_load_dword %0, %1, off" : "+v"(pfv) : "v"(pp)); __builtin_amdgcn_sched_barrier(0); } while (0)
  PFLOAD(0);
  const float dt = rb(expf(b2f(logdt[d])));
  float ar;
  {
    const float la = b2f(logA[pi]);
    float h = la * 1.44269502f; float l = fmaf(la, 1.44269502f, -h) + la * 1.92596299e-8f;
    float e = __builtin_amdgcn_exp2f(h); e = fmaf(e, l * 0.69314718f, e);
    ar = -rb(e) * dt;
  }
  const float ai = b2f(Aim[pi]) * dt;
  cf C; C[0] = 2.f * (b2f(Cr[pi]) * dt); C[1] = 2.f * (b2f(Ci[pi]) * dt);
  const float th = ar * 1.44269502f, tl = fmaf(ar, 1.44269502f, -th) + ar * 1.92596299e-8f;
  const float rh = ai * 0.159154937f, rl = fmaf(ai, 0.159154937f, -rh) + ai * 6.42063833e-9f;
  char* tb = smem + wave * (2 * PL);
  char* wp = tb + lane * 4;
  const char* rp = tb + (lane & 15) * RS + (lane >> 4) * 16;
  STAMP(0, 2);
  PFLOAD(1);
  const cf one = {1.f, 0.f};
  cf S1 = zpow(th, tl, rh, rl, 1.f), Q1 = zpow(th, tl, rh, rl, 8.f), R1 = zpow(th, tl, rh, rl, 128.f);
  Q1[1] = -Q1[1];
  cf S2, Q2, R2, G1, S3, S4, Q3, P1, R3, T1, G2, G3, S5, S6, S7, P2, CT1, T2, P3, CS4, CT2, CT3, CT4, dm0;
  cmul4(S2, Q2, R2, G1, S1, S1, Q1, Q1, R1, R1, C, R1);
  cmul4(S3, S4, Q3, P1, S2, S1, S2, S2, Q2, Q1, Q2, Q2);
  cmul4(R3, T1, G2, P2, R2, R1, R2, R2, C, R2, P1, P1);
  cmul4(S5, S6, S7, P3, S4, S1, S4, S2, S4, S3, P2, P1);
  cmul4(G3, CT1, T2, dm0, C, R3, C, T1, T1, T1, C, R1);
  cmul4(CS4, CT2, CT3, dm0, CT1, S4, C, T2, CT1, T2, C, R1);
  CT4 = cmul(CT2, T2);
  PFLOAD(2);
  unsigned gv[10];
  gate_load(gv, x, gw, task, lane);
  const int wid = min(blk * 216 + (tid < 216 ? tid : 215), 51199);
  unsigned wr_[8];
  {
    const int slot = wid & 127, f = wid >> 7;
    const int s5 = f % 5, hb = 4 * ((f / 5) & 7), cb = f / 40;
    const int m = (slot >> 6) * 32 + (slot & 31), h = (slot >> 5) & 1;
    const int n = 64 * cb + m;
    int k0, k1;
    if (s5 < 4) { k0 = 20 * (hb + s5) + 4 * h; k1 = k0 + 8; }
    else { k0 = 20 * (hb + 2 * h) + 16; k1 = k0 + 20; }
    if (wave < 4) {
      const u16* p0 = wo + (size_t)k0 * D + n; const u16* p1 = wo + (size_t)k1 * D + n;
#pragma unroll
      for (int e = 0; e < 4; e++) {
        asm volatile("global_load_ushort %0, %1, off offset:%2" : "=v"(wr_[e]) : "v"(p0), "i"(e * 1280));
        asm volatile("global_load_ushort %0, %1, off offset:%2" : "=v"(wr_[4 + e]) : "v"(p1), "i"(e * 1280));
      }
    }
  }
  auto rows4 = [&](int pl, int row, int rs, cf b, cf t0, cf t1, cf t2, cf t3) {
    unsigned h[4];
    batch4(h, b, t0, t1, t2, t3);
#pragma unroll
    for (int k = 0; k < 4; k++) *(unsigned*)(wp + pl * PL + (row + k * rs) * RS) = h[k];
  };
  auto rows3 = [&](int pl, int row, int rs, cf b, cf t1, cf t2, cf t3) {
    unsigned h[4];
    batch3(h, b, t1, t2, t3);
#pragma unroll
    for (int k = 0; k < 4; k++) *(unsigned*)(wp + pl * PL + (row + k * rs) * RS) = h[k];
  };
  *(unsigned*)(wp) = 0x00003F80u;
  *(unsigned*)(wp + RS) = pack2(Q1[0], Q1[1]); *(unsigned*)(wp + 2 * RS) = pack2(Q2[0], Q2[1]); *(unsigned*)(wp + 3 * RS) = pack2(Q3[0], Q3[1]);
  rows3(0, 4, 1, P1, Q1, Q2, Q3); rows3(0, 8, 1, P2, Q1, Q2, Q3); rows3(0, 12, 1, P3, Q1, Q2, Q3);
  rows3(1, 0, 1, C, S1, S2, S3); rows4(1, 4, 1, C, S4, S5, S6, S7); rows3(1, 8, 1, G1, S1, S2, S3); rows4(1, 12, 1, G1, S4, S5, S6, S7);
  __builtin_amdgcn_wave_barrier();
  s16x8 bh[4];
  f32x4 res[3];
#pragma unroll
  for (int k = 0; k < 4; k++) bh[k] = *(const s16x8*)(rp + k * 64);
  auto mm = [&](int t, int pl) {
    f32x4 a = {0.f, 0.f, 0.f, 0.f};
#pragma unroll
    for (int k = 0; k < 4; k++) a = __builtin_amdgcn_mfma_f32_16x16x32_bf16(*(const s16x8*)(rp + pl * PL + k * 64), bh[k], a, 0, 0, 0);
    res[t] = a;
  };
  mm(0, 1);
  __builtin_amdgcn_wave_barrier();
  STAMP(0, 3);
  rows3(0, 0, 1, G2, S2, S4, S6); rows3(0, 4, 1, G3, S2, S4, S6); rows3(0, 8, 2, CT1, R1, R2, R3); rows3(0, 9, 2, CS4, R1, R2, R3);
  rows3(1, 0, 1, CT2, R1, R2, R3); rows3(1, 4, 1, CT3, R1, R2, R3);
  *(unsigned*)(wp + PL + 8 * RS) = pack2(CT4[0], CT4[1]);
  __builtin_amdgcn_wave_barrier();
  mm(1, 0); mm(2, 1);
  __builtin_amdgcn_wave_barrier();
  STAMP(0, 4);
  if (wave < 4) {
    asm volatile("s_waitcnt vmcnt(0)" : "+v"(wr_[0]), "+v"(wr_[1]), "+v"(wr_[2]), "+v"(wr_[3]), "+v"(wr_[4]), "+v"(wr_[5]), "+v"(wr_[6]), "+v"(wr_[7]) :: "memory");
    u32x4 wv = {wr_[0] | (wr_[1] << 16), wr_[2] | (wr_[3] << 16), wr_[4] | (wr_[5] << 16), wr_[6] | (wr_[7] << 16)};
    if (tid < 216 && blk * 216 + tid < 51200) st16(ws + WS_WOF + (size_t)wid * 16, wv);
  }
  gate_finish(gv, (float*)(ws + WS_GATE), task, lane);
#pragma unroll
  for (int t = 0; t < 3; t++) *(f32x4*)(tb + t * 1024 + lane * 16) = res[t];
  __syncthreads();
  {
    const int d0 = (blk * 8) / 3;
#pragma unroll
    for (int it = 0; it < 2; it++) {
      const int e = tid + 512 * it;
      const int seg = e / 192, ent = e - 192 * seg;
      const int ds = d0 + seg;
      const int wlo = max(0, 3 * ds - 8 * blk), whi = min(8, 3 * ds + 3 - 8 * blk);
      if (seg < 4 && wlo < 8) {
        f32x4 a = *(const f32x4*)(smem + wlo * (2 * PL) + ent * 16);
        for (int w = wlo + 1; w < whi; w++) a += *(const f32x4*)(smem + w * (2 * PL) + ent * 16);
        st16(ws + WS_KIMG + (size_t)ds * 6144 + (3 * ds < 8 * blk ? 3072 : 0) + ent * 16, __builtin_bit_cast(u32x4, a));
      }
    }
  }
  asm volatile("s_waitcnt vmcnt(0)" :: "v"(pfv) : "memory");
}

__global__ __launch_bounds__(512, 4) void k1(const u16* __restrict__ x, const u16* __restrict__ logA, const u16* __restrict__ Aim,
                                          const u16* __restrict__ Cr, const u16* __restrict__ Ci, const u16* __restrict__ logdt,
                                          const u16* __restrict__ wqkv, const u16* __restrict__ gw, const u16* __restrict__ wo,
                                          char* __restrict__ ws) {
  __shared__ __attribute__((aligned(16))) char smem[K1_LDS];
  int b = blockIdx.x;
  unsigned cpf = 0;
  if (b < 32 && threadIdx.x >= 192) {
    const char* pc; int len;
    asm volatile("s_getpc_b64 %0\n\ts_mov_b32 %1, code_tail_mark-_Z2k1PKtS0_S0_S0_S0_S0_S0_S0_S0_Pc-128" : "=s"(pc), "=s"(len));
    const int off = min((int)(threadIdx.x - 192) * 128, len);
    asm volatile("global_load_dword %0, %1, %2" : "+v"(cpf) : "v"(off), "s"(pc));
  }
  STAMP(0, 0);
  b = b < NB_KG ? b + NB_GEMM : (b < NB_KG + NB_GEMM ? b - NB_KG : b);
  if (b < NB_GEMM) {
    const int xcd = b & 7, idx = b >> 3;
    const int rowblk = (xcd >> 1) * 4 + (idx & 3), cb = (xcd & 1) * 8 + (idx >> 2);
    if (cb < 5) gemm_role<true>(smem, x, wqkv, ws, rowblk, cb);
    else        gemm_role<false>(smem, x, wqkv, ws, rowblk, cb);
    STAMP(0, 1);
    return;
  }
  b -= NB_GEMM;
  if (b < NB_KG) { kgen_role(smem, logA, Aim, Cr, Ci, logdt, x, wqkv, wo, gw, ws, b, cpf); STAMP(0, 1); return; }
  b -= NB_KG;
  if (b == 0) ((unsigned*)(ws + WS_CNT))[threadIdx.x] = 0u, ((unsigned*)(ws + WS_CNT))[threadIdx.x + 512] = 0u;
  {
    unsigned gv[10];
    const int row = NB_KG * 8 + b * 8 + (threadIdx.x >> 6);
    gate_load(gv, x, gw, row, threadIdx.x & 63);
    gate_finish(gv, (float*)(ws + WS_GATE), row, threadIdx.x & 63);
  }
  STAMP(0, 1);
}

constexpr int K2_CS = 4160;
constexpr int K2_KOFF = 4 * K2_CS;
constexpr int K2_TAB = K2_KOFF + 4608;
constexpr int K2_LDS = 40960;
constexpr int NB_KV = 32, NB_PROJ = 320;
typedef __attribute__((address_space(1))) unsigned gu32;
__device__ __forceinline__ const char* uni(const char* p) {
  size_t v = (size_t)p;
  unsigned lo = __builtin_amdgcn_readfirstlane((unsigned)v), hi = __builtin_amdgcn_readfirstlane((unsigned)(v >> 32));
  size_t o = ((size_t)hi << 32) | lo;
  asm volatile("s_nop 4" : "+s"(o));
  return (const char*)o;
}
__device__ __forceinline__ void wait_count(char* p, unsigned target) {
  gu32* c = (gu32*)(size_t)p;
  for (int n = 0; n < (1 << 22); n++) {
    if (__hip_atomic_load(c, __ATOMIC_RELAXED, __HIP_MEMORY_SCOPE_AGENT) >= target) break;
    __builtin_amdgcn_s_sleep(4);
  }
}
#define GLD4(dst, voff, sbase, pol) asm volatile("global_load_dwordx4 %0, %1, %2" pol : "=v"(dst) : "v"(voff), "s"(sbase))
#define GLD2(dst, voff, sbase, pol) asm volatile("global_load_dwordx2 %0, %1, %2" pol : "=v"(dst) : "v"(voff), "s"(sbase))
#define VMW(n, i) asm volatile("s_waitcnt vmcnt(" #n ")" : "+v"(pf0[i]), "+v"(pf1[i]), "+v"(ka[i]), "+v"(kb[i]) :: "memory")
__device__ __forceinline__ void proj_role(char* smem, char* __restrict__ ws, u16* __restrict__ out, int pb) {
  const int tid = threadIdx.x, lane = tid & 63;
  const int wave = __builtin_amdgcn_readfirstlane(tid >> 6);
  const int t = wave & 1, kh = wave >> 1;
  const int r = lane & 31, h = lane >> 5;
  const int xcd = pb & 7, idx = pb >> 3;
  const int rowblk = xcd * 4 + idx / 10, cb = idx % 10;
  const int l0 = rowblk * 64, c0 = cb * 64;
  const int lds0 = (int)(size_t)(lptr_t)smem;
  const char* wsrc = uni(ws + WS_WOF + (size_t)(cb * 2 + kh) * 40960 + t * 5120);
  const int l16 = lane * 16, r8 = r * 8;
  auto dma = [&](int G) {
    const char* sp = wsrc + G * 10240;
    const int lb = __builtin_amdgcn_readfirstlane(lds0 + (G & 1) * 20480 + kh * 10240 + t * 5120);
    asm volatile("s_mov_b32 m0, %2\n\ts_nop 0\n\tglobal_load_lds_dwordx4 %0, %1\n\tglobal_load_lds_dwordx4 %0, %1 offset:1024\n\t"
                 "global_load_lds_dwordx4 %0, %1 offset:2048\n\tglobal_load_lds_dwordx4 %0, %1 offset:3072"
                 :: "v"(l16), "s"(sp), "s"(lb) : "memory", "m0");
    asm volatile("s_mov_b32 m0, %2\n\ts_nop 0\n\tglobal_load_lds_dwordx4 %0, %1" :: "v"(l16), "s"(sp + 4096), "s"(lb + 4096) : "memory", "m0");
  };
  const char* p0 = uni(ws + WS_PQ + (size_t)((rowblk * 32 + kh * 16) * 2 + t) * 1024);
  const char* p1 = uni(ws + WS_PQ + (size_t)L * 16 * 32 * 2 + (size_t)((rowblk * 32 + kh * 16) * 2 + t) * 256);
  const char* kp = uni(ws + WS_KVT + (size_t)kh * 16 * 1280);
#define PW 2
  constexpr int W = PW;
  constexpr int WA[3][16] = {{2, 4, 4, 4, 4, 4, 9, 9, 4, 4, 9, 9, 4, 4, 4, 0}, {4, 6, 8, 8, 8, 8, 13, 13, 13, 8, 13, 13, 13, 8, 4, 0},
                             {6, 8, 10, 12, 12, 12, 17, 17, 17, 17, 17, 17, 17, 13, 4, 0}};
  constexpr int WD[3][4] = {{0, 0, 16, 16}, {0, 0, 16, 12}, {0, 0, 16, 8}};
  u32x4 pf0[W], ka[W]; u32x2 pf1[W], kb[W];
  dma(0); dma(1);
#pragma unroll
  for (int i = 0; i < W; i++) { GLD4(pf0[i], l16, p0 + i * 2048, ""); GLD2(pf1[i], r8, p1 + i * 512, ""); }
#define PWARM 1
  unsigned wm = 0;
  if (wave != 0) {
    const int l128 = lane * 128;
    const int o0 = (lane >> 3) * 2048 + (lane & 7) * 128;
    const int o1 = lane < 32 ? 16384 + o0 : 0;
    const int o2 = (lane >> 1) * 512 + (lane & 1) * 128;
    const int o3 = lane < 40 ? l128 : 0;
    asm volatile("global_load_dword %0, %1, %2" : "+v"(wm) : "v"(o0), "s"(p0 + 8192));
    asm volatile("global_load_dword %0, %1, %2" : "+v"(wm) : "v"(o1), "s"(p0 + 8192));
    asm volatile("global_load_dword %0, %1, %2" : "+v"(wm) : "v"(lane < 24 ? o2 : 0), "s"(p1 + 2048));
    asm volatile("global_load_dword %0, %1, %2" : "+v"(wm) : "v"(o3), "s"(wsrc + 20480));
    asm volatile("global_load_dword %0, %1, %2" : "+v"(wm) : "v"(o3), "s"(wsrc + 30720));
  }
  if (tid == 0) wait_count(ws + WS_CNT + xcd * 128, NB_KV);
  BAR_ONLY();
  STAMP(1, 2);
#pragma unroll
  for (int i = 0; i < W; i++) { GLD4(ka[i], l16, kp + i * 1280, " sc1"); GLD2(kb[i], r8, kp + i * 1280 + 1024, " sc1"); }
  f32x16 acc[2], c[2];
#pragma unroll
  for (int q = 0; q < 2; q++)
#pragma unroll
    for (int i = 0; i < 16; i++) acc[q][i] = 0.f;
  u32x4 rm = {0u, 0u, 0u, 0u};
  const char* wl = smem + kh * 10240 + l16;
  auto stageA = [&](int i) {
    const int j = i % W;
    asm volatile("s_waitcnt vmcnt(%4)" : "+v"(pf0[j]), "+v"(pf1[j]), "+v"(ka[j]), "+v"(kb[j]) : "n"(WA[W - 2][i]) : "memory");
    if (i == 0) asm volatile("" : "+v"(wm));
    u32x2 b1u = {h ? 0u : pf1[j][0], h ? 0u : pf1[j][1]};
    f32x16 z;
#pragma unroll
    for (int q = 0; q < 16; q++) z[q] = 0.f;
    z = __builtin_amdgcn_mfma_f32_32x32x16_bf16(__builtin_bit_cast(s16x8, ka[j]), __builtin_bit_cast(s16x8, pf0[j]), z, 0, 0, 0);
    c[i & 1] = __builtin_amdgcn_mfma_f32_32x32x8bf16_1k(__builtin_bit_cast(s16x4, kb[j]), __builtin_bit_cast(s16x4, b1u), z, 0, 0, 0);
    asm volatile("" : "+v"(c[i & 1]));
    if (i + W < 16) {
      GLD4(pf0[j], l16, p0 + (i + W) * 2048, ""); GLD2(pf1[j], r8, p1 + (i + W) * 512, "");
      GLD4(ka[j], l16, kp + (i + W) * 1280, " sc1"); GLD2(kb[j], r8, kp + (i + W) * 1280 + 1024, " sc1");
    }
  };
  stageA(0);
#pragma unroll
  for (int i = 0; i < 16; i++) {
    const int j = i & 3, G = i >> 2;
    if (i + 1 < 16) stageA(i + 1);
    if (j == 0) {
      if (G >= 2) asm volatile("s_waitcnt vmcnt(%0)" :: "n"(WD[W - 2][G]) : "memory");
      RAW_BARRIER();
      if (G == 1 || G == 2) dma(G + 1);
    }
    const char* wp = wl + (G & 1) * 20480;
    s16x8 w0 = *(const s16x8*)(wp + j * 2048), w1 = *(const s16x8*)(wp + j * 2048 + 1024);
    const f32x16 cc = c[i & 1];
    const float den = rb(rb(cc[12]) + 1e-6f);
    const float inv = __builtin_amdgcn_rcpf(den);
    unsigned o[6];
#pragma unroll
    for (int q = 0; q < 6; q++) {
      unsigned n0 = pack2(cc[2 * q], cc[2 * q + 1]);
      o[q] = pack2(lo_f(n0) * inv, hi_f(n0) * inv);
    }
    asm volatile("" : "+v"(o[4]), "+v"(o[5]));
    const bool keep = (j >> 1) == h;
    rm[2 * (j & 1)] = keep ? o[4] : rm[2 * (j & 1)]; rm[2 * (j & 1) + 1] = keep ? o[5] : rm[2 * (j & 1) + 1];
    u32x4 ob = {o[0], o[1], o[2], o[3]};
    acc[0] = __builtin_amdgcn_mfma_f32_32x32x16_bf16(w0, __builtin_bit_cast(s16x8, ob), acc[0], 0, 0, 0);
    acc[1] = __builtin_amdgcn_mfma_f32_32x32x16_bf16(w1, __builtin_bit_cast(s16x8, ob), acc[1], 0, 0, 0);
    if (j == 3) {
      s16x8 w2 = *(const s16x8*)(wp + 8192), w3 = *(const s16x8*)(wp + 9216);
      acc[0] = __builtin_amdgcn_mfma_f32_32x32x16_bf16(w2, __builtin_bit_cast(s16x8, rm), acc[0], 0, 0, 0);
      acc[1] = __builtin_amdgcn_mfma_f32_32x32x16_bf16(w3, __builtin_bit_cast(s16x8, rm), acc[1], 0, 0, 0);
    }
  }
  const float* gate = (const float*)(ws + WS_GATE) + l0;
  const float g_r = gate[32 * t + r];
  const float g_f[2] = {gate[tid >> 3], gate[(tid >> 3) + 32]};
  RAW_BARRIER();
  STAMP(1, 3);
  {
    char* xw = smem + ((t * 2 + (1 - kh)) * 4) * 1024 + l16;
#pragma unroll
    for (int q = 0; q < 4; q++) {
      f32x4 v = kh ? (f32x4){acc[0][4 * q], acc[0][4 * q + 1], acc[0][4 * q + 2], acc[0][4 * q + 3]}
                   : (f32x4){acc[1][4 * q], acc[1][4 * q + 1], acc[1][4 * q + 2], acc[1][4 * q + 3]};
      *(f32x4*)(xw + q * 1024) = v;
    }
  }
  RAW_BARRIER();
  {
    const char* xr = smem + ((t * 2 + kh) * 4) * 1024 + l16;
    const float g1 = rb(1.f - g_r);
    char* gbp = smem + 16384 + (32 * t + r) * 144 + (32 * kh + 4 * h) * 2;
#pragma unroll
    for (int q = 0; q < 4; q++) {
      f32x4 v = *(const f32x4*)(xr + q * 1024);
      f32x4 m = kh ? (f32x4){acc[1][4 * q], acc[1][4 * q + 1], acc[1][4 * q + 2], acc[1][4 * q + 3]}
                   : (f32x4){acc[0][4 * q], acc[0][4 * q + 1], acc[0][4 * q + 2], acc[0][4 * q + 3]};
      v += m;
      unsigned b0 = pack2(v[0], v[1]), b1 = pack2(v[2], v[3]);
      *(u32x2*)(gbp + q * 16) = (u32x2){pack2(g1 * lo_f(b0), g1 * hi_f(b0)), pack2(g1 * lo_f(b1), g1 * hi_f(b1))};
    }
  }
  STAMP(1, 4);
  if (tid == 0) wait_count(ws + WS_CNT + (8 + cb) * 128, 64);
  RAW_BARRIER();
  STAMP(1, 5);
  {
    u32x4 av[2];
    const char* ap = ws + WS_AT + ((size_t)(c0 + (tid >> 3)) * L + l0 + 8 * (tid & 7)) * 2;
    asm volatile("global_load_dwordx4 %0, %2, off sc1\n\tglobal_load_dwordx4 %1, %3, off sc1\n\ts_waitcnt vmcnt(0)"
                 : "=&v"(av[0]), "=&v"(av[1]) : "v"(ap), "v"(ap + (size_t)32 * L * 2) : "memory");
#pragma unroll
    for (int i = 0; i < 2; i++) *(u32x4*)(smem + 25600 + ((tid >> 3) + 32 * i) * 144 + (tid & 7) * 16) = av[i];
  }
  RAW_BARRIER();
#pragma unroll
  for (int i = 0; i < 2; i++) {
    const int rr = (tid >> 3) + 32 * i, oc = tid & 7;
    const float g = g_f[i];
    u32x4 gb = *(const u32x4*)(smem + 16384 + rr * 144 + oc * 16);
    u32x4 ov;
#pragma unroll
    for (int e = 0; e < 4; e++) {
      float a0 = b2f(*(const u16*)(smem + 25600 + (oc * 8 + 2 * e) * 144 + rr * 2));
      float a1 = b2f(*(const u16*)(smem + 25600 + (oc * 8 + 2 * e + 1) * 144 + rr * 2));
      unsigned ga = pack2(g * a0, g * a1);
      ov[e] = pack2(lo_f(ga) + lo_f(gb[e]), hi_f(ga) + hi_f(gb[e]));
    }
    *(u32x4*)(out + (size_t)(l0 + rr) * D + c0 + oc * 8) = ov;
  }
}

__global__ __launch_bounds__(256, 4) void k2(const u16* __restrict__ Dskip, char* __restrict__ ws, u16* __restrict__ out) {
  __shared__ __attribute__((aligned(16))) char smem[K2_LDS];
  const int tid = threadIdx.x, lane = tid & 63;
  const int wave = __builtin_amdgcn_readfirstlane(tid >> 6);
  unsigned cpf = 0;
  if (blockIdx.x < NB_KV && tid >= 128) {
    const char* pc; int len;
    asm volatile("s_getpc_b64 %0\n\ts_mov_b32 %1, code_tail_mark-_Z2k2PKtPcPt-128" : "=s"(pc), "=s"(len));
    const int off = min((tid - 128) * 128, len);
    asm volatile("global_load_dword %0, %1, %2" : "+v"(cpf) : "v"(off), "s"(pc));
  }
  STAMP(1, 0);
#define PPRIO 0
  if (blockIdx.x >= NB_KV + D) { __builtin_amdgcn_s_setprio(PPRIO); proj_role(smem, ws, out, blockIdx.x - (NB_KV + D)); STAMP(1, 1); return; }
  if (blockIdx.x < NB_KV) {
    __builtin_amdgcn_s_setprio(3);
    const int hd = blockIdx.x;
    const int h = lane >> 5;
    float* fin = (float*)smem;
    if (tid < 105) {
      const f32x4* src = (const f32x4*)(ws + WS_PART) + (size_t)hd * 16 * 105 + tid;
      f32x4 v[16];
#pragma unroll
      for (int i = 0; i < 16; i++) v[i] = src[i * 105];
#pragma unroll
      for (int st = 1; st < 16; st <<= 1)
#pragma unroll
        for (int i = 0; i < 16; i += 2 * st) v[i] += v[i + st];
      *(f32x4*)(fin + tid * 4) = (f32x4){rb(v[0][0]), rb(v[0][1]), rb(v[0][2]), rb(v[0][3])};
    }
    __syncthreads();
    if (wave == 0) {
      const int m = lane & 31;
      int col = m < 16 ? m : ((m >> 2) == ((hd & 2) ? 5 : 4) ? 16 + (m & 3) : 20);
      u32x4 o;
#pragma unroll
      for (int e = 0; e < 4; e++) o[e] = pack2(fin[col * 20 + 8 * h + 2 * e], fin[col * 20 + 8 * h + 2 * e + 1]);
      st16(ws + WS_KVT + (size_t)hd * 1280 + lane * 16, o);
      const int m0 = 2 * (lane & 15), m1 = m0 + 1;
      int ca = m0 < 16 ? m0 : ((m0 >> 2) == ((hd & 2) ? 5 : 4) ? 16 + (m0 & 3) : 20);
      int cb = m1 < 16 ? m1 : ((m1 >> 2) == ((hd & 2) ? 5 : 4) ? 16 + (m1 & 3) : 20);
      u32x4 o2 = {pack2(fin[ca * 20 + 16], fin[ca * 20 + 17]), pack2(fin[ca * 20 + 18], fin[ca * 20 + 19]),
                  pack2(fin[cb * 20 + 16], fin[cb * 20 + 17]), pack2(fin[cb * 20 + 18], fin[cb * 20 + 19])};
      if (lane < 16) st16(ws + WS_KVT + (size_t)hd * 1280 + 1024 + lane * 16, o2);
      VMCNT(0);
      if (lane < 8) __hip_atomic_fetch_add((gu32*)(size_t)(ws + WS_CNT + lane * 128), 1u, __ATOMIC_RELAXED, __HIP_MEMORY_SCOPE_AGENT);
    }
    asm volatile("code_tail_mark:\n\ts_waitcnt vmcnt(0)" :: "v"(cpf) : "memory");
    STAMP(1, 1);
    return;
  }
  const int d = blockIdx.x - NB_KV;
  const u16* uT = (const u16*)(ws + WS_UT) + (size_t)d * UT_STRIDE;
  {
    const f32x4* ksrc = (const f32x4*)(ws + WS_KIMG + (size_t)d * 6144) + (tid < 192 ? tid : 191);
    const u32x4* usrc = (const u32x4*)uT;
    f32x4 p0 = ksrc[0];
    if ((d & 7) == 2 || (d & 7) == 5) p0 += ksrc[192];
    u32x4 c0 = usrc[tid + 1], c1 = usrc[tid + 2];
    u32x4 e0, e1;
    if (tid < 3) { e0 = usrc[min(tid + 257, 259)]; e1 = usrc[min(tid + 258, 259)]; }
    if (tid < 32) *(u32x4*)(smem + K2_KOFF + tid * 16) = (u32x4){0u, 0u, 0u, 0u};
    if (tid < 192) *(f32x4*)(smem + K2_TAB + tid * 16) = p0;
    auto put_copies = [&](u32x4 a, u32x4 b, int xch) {
      unsigned w[6] = {a[0], a[1], a[2], a[3], b[0], b[1]};
      unsigned o[5];
#pragma unroll
      for (int k = 0; k < 5; k++) o[k] = __builtin_amdgcn_alignbyte(w[k + 1], w[k], 2);
      char* p = smem + xch * 16;
#pragma unroll
      for (int q = 0; q < 2; q++) {
        *(u32x4*)(p + (2 * q) * K2_CS) = (u32x4){o[q], o[q + 1], o[q + 2], o[q + 3]};
        *(u32x4*)(p + (2 * q + 1) * K2_CS) = (u32x4){w[q + 1], w[q + 2], w[q + 3], w[q + 4]};
      }
    };
    put_copies(c0, c1, tid);
    if (tid < 3) put_copies(e0, e1, tid + 256);
  }
  __syncthreads();
  {
    const float* tab = (const float*)(smem + K2_TAB);
    float kv[8];
#pragma unroll
    for (int e = 0; e < 8; e++) {
      const int j = tid * 8 + 7 - e;
      const int p = (int)rb((float)j);
      const int h = p >> 7, c = (p >> 3) & 15;
      int sh = 31 - __builtin_clz(h | 1); sh = sh > 3 ? 3 : sh;
      const int row = 8 * sh + (((h << 3) | (p & 7)) >> sh);
      kv[e] = tab[((row >> 2) * 16 + c) * 4 + (row & 3)];
    }
    u32x4 hi;
#pragma unroll
    for (int e = 0; e < 4; e++) hi[e] = pack2(kv[2 * e], kv[2 * e + 1]);
    *(u32x4*)(smem + K2_KOFF + 512 + tid * 16) = hi;
  }
  __syncthreads();
  STAMP(1, 2);
  f32x4 acc[8];
#pragma unroll
  for (int t = 0; t < 8; t++) acc[t] = (f32x4){0.f, 0.f, 0.f, 0.f};
  {
    const int i = lane & 15, kg = lane >> 4, e = kg >> 1, bb = kg & 1;
    const char* ap = smem + (i & 3) * K2_CS + (2 * e - bb + 2) * 16 + (i >> 2) * 8;
    const char* bp = smem + K2_KOFF + (16 + i - e) * 32 + bb * 16;
#pragma unroll 1
    for (int it = 0; it < 2; it++) {
      const int d0 = 2 * (wave + 4 * it);
      s16x8 af[8];
#pragma unroll
      for (int j = 0; j < 8; j++) {
        u32x2 a0 = *(const volatile u32x2*)(ap + (32 * j + 2 * d0) * 16);
        u32x2 a1 = *(const volatile u32x2*)(ap + (32 * j + 2 * d0) * 16 + 8);
        af[j] = __builtin_bit_cast(s16x8, (u32x4){a0[0], a0[1], a1[0], a1[1]});
      }
      s16x8 bh[8];
#pragma unroll
      for (int m = 0; m < 8; m++) bh[m] = *(const s16x8*)(bp + (16 * m - d0) * 32);
#pragma unroll
      for (int m = 0; m < 8; m++)
#pragma unroll
        for (int j = 0; j + m < 8; j++) acc[j + m] = __builtin_amdgcn_mfma_f32_16x16x32_bf16(af[j], bh[m], acc[j + m], 0, 0, 0);
    }
  }
  STAMP(1, 3);
  const u32x4 uu = *(const u32x4*)(uT + UT_PAD + 8 * tid);
  const float dsk = b2f(Dskip[d]);
  __syncthreads();
  float* red = (float*)smem;
#pragma unroll
  for (int t = 0; t < 8; t++) *(f32x4*)(red + ((wave * 8 + t) * 64 + (lane & 15) * 4 + (lane >> 4)) * 4) = acc[t];
  __syncthreads();
  {
    u32x4 ov;
#pragma unroll
    for (int hf = 0; hf < 2; hf++) {
      const float* rp = red + (tid * 2 + hf) * 4;
      f32x4 s = *(const f32x4*)rp, s1 = *(const f32x4*)(rp + 2048), s2 = *(const f32x4*)(rp + 4096), s3 = *(const f32x4*)(rp + 6144);
      s = (s + s1) + (s2 + s3);
#pragma unroll
      for (int e = 0; e < 2; e++) {
        unsigned y = pack2(s[2 * e], s[2 * e + 1]);
        unsigned k = pack2(lo_f(uu[2 * hf + e]) * dsk, hi_f(uu[2 * hf + e]) * dsk);
        ov[2 * hf + e] = pack2(lo_f(y) + lo_f(k), hi_f(y) + hi_f(k));
      }
    }
    st16(ws + WS_AT + ((size_t)d * L + 8 * tid) * 2, ov);
  }
  VMCNT(0);
  BAR_ONLY();
  if (tid == 0) __hip_atomic_fetch_add((gu32*)(size_t)(ws + WS_CNT + (8 + (d >> 6)) * 128), 1u, __ATOMIC_RELAXED, __HIP_MEMORY_SCOPE_AGENT);
  STAMP(1, 1);
}

extern "C" void kernel_launch(void* const* d_in, const int* in_sizes, int n_in, void* d_out, int out_size, void* d_ws, size_t ws_size,
                              hipStream_t stream) {
  const u16* u = (const u16*)d_in[0]; const u16* logA = (const u16*)d_in[1]; const u16* Aim = (const u16*)d_in[2];
  const u16* Cr = (const u16*)d_in[3]; const u16* Ci = (const u16*)d_in[4]; const u16* logdt = (const u16*)d_in[5];
  const u16* Dsk = (const u16*)d_in[6]; const u16* wqkv = (const u16*)d_in[8]; const u16* wo = (const u16*)d_in[9];
  const u16* gw = (const u16*)d_in[10];
  char* ws = (char*)d_ws;
  k1<<<NB_GEMM + NB_GATE + NB_KG, 512, 0, stream>>>(u, logA, Aim, Cr, Ci, logdt, wqkv, gw, wo, ws);
  k2<<<NB_KV + D + NB_PROJ, 256, 0, stream>>>(Dsk, ws, (u16*)d_out);
}
```

```cpp
#include <hip/hip_runtime.h>
#include <hip/hip_bf16.h>
#include <stdint.h>

typedef unsigned short u16;
typedef __attribute__((ext_vector_type(8))) short s16x8;
typedef __attribute__((ext_vector_type(4))) short s16x4;
typedef __attribute__((ext_vector_type(4))) float f32x4;
typedef __attribute__((ext_vector_type(16))) float f32x16;
typedef __attribute__((ext_vector_type(4))) unsigned u32x4;
typedef __attribute__((ext_vector_type(2))) unsigned u32x2;
typedef __attribute__((address_space(3))) s16x4* lds_s16x4_p;
typedef const __attribute__((address_space(1))) void* gptr_t;
typedef __attribute__((address_space(3))) void* lptr_t;

constexpr int L = 2048, D = 640, NS = 192, NH = 32;
constexpr int UT_PAD = 32, UT_STRIDE = L + UT_PAD;
constexpr size_t WS_UT   = 0;
constexpr size_t WS_KIMG = WS_UT + (size_t)D * UT_STRIDE * 2;
constexpr size_t WS_PQ   = WS_KIMG + (size_t)D * 6144;
constexpr size_t WS_PART = WS_PQ + (size_t)L * D * 2;
constexpr size_t WS_GATE = WS_PART + (size_t)32 * 16 * 1680;
constexpr size_t WS_WOF  = WS_GATE + (size_t)L * 4;
constexpr size_t WS_KVT  = WS_WOF + (size_t)D * D * 2;
constexpr size_t WS_AT   = WS_KVT + (size_t)32 * 1280;
constexpr size_t WS_CNT  = WS_AT + (size_t)D * L * 2;
constexpr size_t WS_END  = WS_CNT + 4096;
#define STAMP(kid, slot) do {} while (0)

__device__ __forceinline__ float b2f(u16 v) { return __uint_as_float(((unsigned)v) << 16); }
typedef __bf16 bf16x2_t __attribute__((ext_vector_type(2)));
typedef float f32x2 __attribute__((ext_vector_type(2)));
__device__ __forceinline__ unsigned pack2(float a, float b) { f32x2 v = {a, b}; bf16x2_t r = __builtin_convertvector(v, bf16x2_t); return __builtin_bit_cast(unsigned, r); }
__device__ __forceinline__ float lo_f(unsigned u) { return __uint_as_float(u << 16); }
__device__ __forceinline__ float hi_f(unsigned u) { return __uint_as_float(u & 0xffff0000u); }
__device__ __forceinline__ float rb(float f) { return lo_f(pack2(f, f)); }
__device__ __forceinline__ unsigned phi2(float a, float b) {
  unsigned t = pack2(a, b); float t0 = lo_f(t), t1 = hi_f(t);
  float e0 = __builtin_amdgcn_fmed3f(__builtin_amdgcn_exp2f(t0 * 1.44269504f), 0.f, 1.f);
  float e1 = __builtin_amdgcn_fmed3f(__builtin_amdgcn_exp2f(t1 * 1.44269504f), 0.f, 1.f);
  unsigned r = pack2(__builtin_fmaxf(t0, 0.f) + e0, __builtin_fmaxf(t1, 0.f) + e1);
  return pack2(lo_f(r) + 1e-6f, hi_f(r) + 1e-6f);
}
#define VMCNT(n) asm volatile("s_waitcnt vmcnt(" #n ")" ::: "memory")
#define RAW_BARRIER() do { asm volatile("s_waitcnt lgkmcnt(0)" ::: "memory"); __builtin_amdgcn_s_barrier(); asm volatile("" ::: "memory"); } while (0)
__device__ __forceinline__ void glds16(const void* g, void* l) { __builtin_amdgcn_global_load_lds((gptr_t)g, (lptr_t)l, 16, 0, 0); }

#define OPQ_S(v) asm volatile("" : "+s"(v))
typedef f32x2 cf;
__device__ __forceinline__ float smul(float a, float b) { float o; asm("v_mul_f32 %0, %1, %2" : "=v"(o) : "v"(a), "v"(b)); return o; }
__device__ __forceinline__ float sfma(float a, float b, float c) { float o; asm("v_fma_f32 %0, %1, %2, %3" : "=v"(o) : "v"(a), "v"(b), "v"(c)); return o; }
__device__ __forceinline__ float sfnma(float a, float b, float c) { float o; asm("v_fma_f32 %0, -%1, %2, %3" : "=v"(o) : "v"(a), "v"(b), "v"(c)); return o; }
__device__ __forceinline__ float ssub(float a, float b) { float o; asm("v_sub_f32 %0, %1, %2" : "=v"(o) : "v"(a), "v"(b)); return o; }
__device__ __forceinline__ cf cmul(cf a, cf b) {
  cf o;
  asm("v_pk_mul_f32 %0, %1, %2 op_sel:[0,0] op_sel_hi:[0,1]\n\t"
      "v_pk_fma_f32 %0, %1, %2, %0 op_sel:[1,1,0] op_sel_hi:[1,0,1] neg_lo:[1,0,0]" : "=&v"(o) : "v"(a), "v"(b));
  return o;
}

#define CPF 3
constexpr int K1_LDS = 81920;
constexpr int NB_GEMM = 256, NB_GATE = 16, NB_KG = 240;
#define WT 1
__device__ __forceinline__ void st16(void* p, u32x4 v) {
  asm volatile("global_store_dwordx4 %0, %1, off sc1\n\ts_nop 2" :: "v"(p), "v"(v) : "memory");
}

#define BAR_ONLY() do { asm volatile("" ::: "memory"); __builtin_amdgcn_s_barrier(); asm volatile("" ::: "memory"); } while (0)
#define SB() __builtin_amdgcn_sched_barrier(0)
template <bool ISQ>
__device__ __forceinline__ void gemm_role(char* smem, const u16* __restrict__ x, const u16* __restrict__ W, char* __restrict__ ws,
                                          int rowblk, int cb) {
  const int tid = threadIdx.x, lane = tid & 63;
  const int wave = __builtin_amdgcn_readfirstlane(tid >> 6);
  const int row0 = rowblk * 128, col0 = cb * 128;
  const int kvj = cb - 5;
  char* sB = smem; char* sA = smem + 32768;
  const int wr = wave & 1, wc = (wave >> 1) & 1, hf = wave >> 2;
  f32x4 ep[2][4];
  if (wave >= 4) {
    const int lw = wave - 4;
    const char* pA = (const char*)x + (size_t)(row0 + 32 * lw + (lane >> 3)) * 1280 + (((lane & 7) ^ (lane >> 3)) << 4);
    const int fB = (lane >> 4) | ((lw & 1) << 2);
    const int c16 = (((lane & 15) >> 1) ^ fB) * 2 + (lane & 1);
    int bcol = col0 + 8 * c16;
    if (!ISQ) bcol = min(640 + 640 * (c16 >> 3) + ((60 * kvj) & ~7) + 8 * (c16 & 7), 1912);
    const char* pB = (const char*)W + (size_t)(8 * lw + (lane >> 4)) * 3840 + bcol * 2;
    u32x2 tr[8];
    int tro;
    {
      const int fr = lane & 15, g = lane >> 4, p4 = lane & 3, r = 8 * g + (fr >> 2);
      tro = 32768 + r * 128 + (((2 * lw + (p4 >> 1)) ^ (r & 7)) << 4) + (p4 & 1) * 8;
    }
    auto issA = [&](int slot_off, int h) {
      glds16(pA + (16 * h) * 1280, sA + slot_off + (4 * lw + 2 * h) * 1024);
      glds16(pA + (16 * h + 8) * 1280, sA + slot_off + (4 * lw + 2 * h + 1) * 1024);
    };
    auto issB = [&](int slot_off) {
      glds16(pB, sB + slot_off + (2 * lw) * 1024);
      glds16(pB + 4 * 3840, sB + slot_off + (2 * lw + 1) * 1024);
      pB += (size_t)32 * 3840;
    };
    issA(0, 0); issB(0); issA(0, 1); pA += 128;
    issB(8192); issA(16384, 0); issB(16384); issA(16384, 1); pA += 128;
    issB(24576); issA(32768, 0);
    VMCNT(12);
    BAR_ONLY();
    int ao = 32768, bo = 0;
#pragma unroll 1
    for (int j = 0; j < 10; j++) {
      if (j < 8) VMCNT(10); else if (j == 8) VMCNT(8); else VMCNT(0);
      BAR_ONLY();
      if (j < 8) { issB(bo); bo = (bo + 8192) & 32767; issA(ao, 1); pA += 128; }
      ao = ao == 32768 ? 0 : ao + 16384;
      if (j == cb) {
        const int a0 = (int)(size_t)(lptr_t)smem + ao;
        const int a = a0 + tro, a2 = a0 + (tro ^ 64) + 512;
        asm volatile("ds_read_b64_tr_b16 %0, %8\n\tds_read_b64_tr_b16 %1, %9\n\tds_read_b64_tr_b16 %2, %8 offset:4096\n\tds_read_b64_tr_b16 %3, %9 offset:4096\n\t"
                     "ds_read_b64_tr_b16 %4, %8 offset:8192\n\tds_read_b64_tr_b16 %5, %9 offset:8192\n\tds_read_b64_tr_b16 %6, %8 offset:12288\n\tds_read_b64_tr_b16 %7, %9 offset:12288\n\t"
                     "s_waitcnt lgkmcnt(0)"
                     : "=&v"(tr[0]), "=&v"(tr[1]), "=&v"(tr[2]), "=&v"(tr[3]), "=&v"(tr[4]), "=&v"(tr[5]), "=&v"(tr[6]), "=&v"(tr[7])
                     : "v"(a), "v"(a2) : "memory");
      }
      if (j < 8) VMCNT(8); else VMCNT(2);
      BAR_ONLY();
      if (j < 8) { issB(bo); bo = (bo + 8192) & 32767; }
      if (j < 7) issA(ao, 0);
    }
    if (cb < 10) {
      u16* dst = (u16*)(ws + WS_UT) + (size_t)(64 * cb + 16 * lw + (lane & 15)) * UT_STRIDE + 8 * (lane >> 4);
#pragma unroll
      for (int i = 0; i < 4; i++) st16(dst + UT_PAD + row0 + 32 * i, (u32x4){tr[2 * i][0], tr[2 * i][1], tr[2 * i + 1][0], tr[2 * i + 1][1]});
      if (rowblk == 0) st16(dst, (u32x4){0u, 0u, 0u, 0u});
    }
    BAR_ONLY();
    char* xch = smem + (wave & 3) * 8192 + lane * 16;
#pragma unroll
    for (int i = 0; i < 8; i++) ep[i >> 2][i & 3] = *(const f32x4*)(xch + i * 1024);
  } else {
    const int g = lane >> 4, fr = lane & 15;
    f32x4 acc[4][4];
#pragma unroll
    for (int m = 0; m < 4; m++)
#pragma unroll
      for (int c = 0; c < 4; c++) acc[m][c] = (f32x4){0.f, 0.f, 0.f, 0.f};
    const int q4 = fr >> 2, p4 = lane & 3;
    int aoff[2];
#pragma unroll
    for (int ks = 0; ks < 2; ks++) aoff[ks] = (wr * 64 + fr) * 128 + (((4 * ks + g) ^ (fr & 7)) << 4);
    int boff[4];
    {
      const int f = q4 | ((g & 1) << 2);
#pragma unroll
      for (int c = 0; c < 4; c++) boff[c] = (8 * g + q4) * 256 + (((4 * wc + c) ^ f) << 5) + p4 * 8;
    }
    s16x8 af[2][4]; s16x4 bl[4], bh[4];
    auto rdA = [&](int buf, int m, const char* pa) { af[buf][m] = *(const s16x8*)(pa + m * 2048); };
    auto rdBl = [&](int c, const char* pb) { bl[c] = __builtin_amdgcn_ds_read_tr16_b64_v4i16((lds_s16x4_p)(pb + boff[c])); };
    auto rdBh = [&](int c, const char* pb) { bh[c] = __builtin_amdgcn_ds_read_tr16_b64_v4i16((lds_s16x4_p)(pb + boff[c] + 1024)); };
    BAR_ONLY();
    STAMP(0, 2);
#pragma unroll
    for (int c = 0; c < 4; c++) { rdA(0, c, sA + aoff[0]); rdBl(c, sB); rdBh(c, sB); }
    int ao0 = 0, ao1 = 16384;
    auto grp = [&](int buf, int c, int m) {
      const s16x8 bf = __builtin_shufflevector(bl[c], bh[c], 0, 1, 2, 3, 4, 5, 6, 7);
      if (ISQ) acc[m][c] = __builtin_amdgcn_mfma_f32_16x16x32_bf16(bf, af[buf][m], acc[m][c], 0, 0, 0);
      else     acc[m][c] = __builtin_amdgcn_mfma_f32_16x16x32_bf16(af[buf][m], bf, acc[m][c], 0, 0, 0);
    };
#pragma unroll
    for (int c = 0; c < 2; c++)
#pragma unroll
      for (int m = 0; m < 4; m++) grp(0, c, m);
#pragma unroll 1
    for (int j2 = 0; j2 < 5; j2++) {
#pragma unroll
      for (int st = 0; st < 4; st++) {
        const int hs = st & 1;
        SB();
        RAW_BARRIER();
        SB();
        const char* pa = sA + (hs == 0 ? ao0 + aoff[1] : ao1 + aoff[0]);
        const char* pb = sB + ((st + 1) & 3) * 8192;
        grp(hs, 2, 0); SB(); rdA(hs ^ 1, 0, pa); rdA(hs ^ 1, 1, pa); SB();
        grp(hs, 2, 1); SB(); rdA(hs ^ 1, 2, pa); rdA(hs ^ 1, 3, pa); SB();
        grp(hs, 2, 2); SB(); rdBl(0, pb); rdBh(0, pb); SB();
        grp(hs, 2, 3); SB(); rdBl(1, pb); rdBh(1, pb); SB();
        grp(hs, 3, 0); SB(); rdBl(2, pb); rdBh(2, pb); SB();
        grp(hs, 3, 1); grp(hs, 3, 2); grp(hs, 3, 3); SB();
        rdBl(3, pb); rdBh(3, pb); SB();
        if (st < 3 || j2 < 4) {
#pragma unroll
          for (int c = 0; c < 2; c++)
#pragma unroll
            for (int m = 0; m < 4; m++) grp(hs ^ 1, c, m);
        }
        if (hs == 1) { ao0 = ao1; ao1 = ao1 == 32768 ? 0 : ao1 + 16384; }
      }
    }
    STAMP(0, 3);
    int l3 = threadIdx.x; asm volatile("" : "+v"(l3));
    char* xch = smem + (l3 >> 6) * 8192 + (l3 & 63) * 16;
#pragma unroll
    for (int i = 0; i < 8; i++) *(f32x4*)(xch + i * 1024) = acc[2 + (i >> 2)][i & 3];
#pragma unroll
    for (int i = 0; i < 8; i++) ep[i >> 2][i & 3] = acc[i >> 2][i & 3];
    RAW_BARRIER();
  }
  int l2 = threadIdx.x; asm volatile("" : "+v"(l2));
  const int g = (l2 >> 4) & 3, fr = l2 & 15;
  if (ISQ) {
    char* pq0 = ws + WS_PQ; char* pq1 = ws + WS_PQ + (size_t)L * 16 * 32 * 2;
#pragma unroll
    for (int c = 0; c < 4; c++) {
      const int n = col0 + 64 * wc + 16 * c + 4 * g;
      const int hd = n / 20, d0 = n - 20 * hd;
#pragma unroll
      for (int m = 0; m < 2; m++) {
        const int rb64 = rowblk * 2 + wr, r = 16 * m + fr;
        u32x2 o;
        o[0] = phi2(ep[m][c][0], ep[m][c][1]);
        o[1] = phi2(ep[m][c][2], ep[m][c][3]);
        const int fi = (rb64 * 32 + hd) * 2 + hf;
        char* dst = d0 < 16 ? pq0 + ((size_t)fi * 64 + (d0 >> 3) * 32 + r) * 16 + (d0 & 7) * 2 : pq1 + ((size_t)fi * 32 + r) * 8;
        *(u32x2*)dst = o;
      }
    }
  } else {
    const int o = (60 * kvj) & 7, zc = o ? 0 : 60;
    const int lane2 = l2 & 63;
    char* fb = smem + 32768;
#pragma unroll
    for (int c = 0; c < 4; c++) {
      u32x4 a;
      if (wc == 0) {
        a[0] = phi2(ep[0][c][0], ep[0][c][1]); a[1] = phi2(ep[0][c][2], ep[0][c][3]);
        a[2] = phi2(ep[1][c][0], ep[1][c][1]); a[3] = phi2(ep[1][c][2], ep[1][c][3]);
      } else {
        a[0] = pack2(ep[0][c][0], ep[0][c][1]); a[1] = pack2(ep[0][c][2], ep[0][c][3]);
        a[2] = pack2(ep[1][c][0], ep[1][c][1]); a[3] = pack2(ep[1][c][2], ep[1][c][3]);
        if (16 * c + fr == zc) a = (u32x4){0x3F803F80u, 0x3F803F80u, 0x3F803F80u, 0x3F803F80u};
      }
      *(u32x4*)(fb + ((((wc * 4 + wr * 2 + hf) * 4 + c) * 64 + lane2) << 4)) = a;
    }
    RAW_BARRIER();
    const int np = wave < 4 ? 1 : 2;
    for (int i = 0; i < np; i++) {
      const int p = wave < 4 ? wave : wave * 2 - 4 + i;
      int ta, tb;
      if (p < 10) { ta = (int)((0x3322211100ull >> (4 * p)) & 15); tb = (int)((0x3232121010ull >> (4 * p)) & 15); }
      else { ta = (o ? 2 : 0) + (p - 10); tb = o ? 0 : 3; }
      const char* pk = fb + ((ta * 64 + lane2) << 4);
      const char* pv = fb + 16384 + ((tb * 64 + lane2) << 4);
      f32x4 dd = {0.f, 0.f, 0.f, 0.f};
#pragma unroll
      for (int rg = 0; rg < 4; rg++)
        dd = __builtin_amdgcn_mfma_f32_16x16x32_bf16(*(const s16x8*)(pk + rg * 4096), *(const s16x8*)(pv + rg * 4096), dd, 0, 0, 0);
      const int kc0 = 16 * ta + 4 * g - o;
      const int hk = kc0 / 20, dk0 = kc0 - 20 * hk;
      const int vcol = 16 * tb + fr;
      const int dv = vcol == zc ? 20 : vcol - o - 20 * hk;
      if (kc0 >= 0 && kc0 < (kvj == 10 ? 40 : 60) && dv >= 0 && (dv < 20 || vcol == zc))
        *(f32x4*)(ws + WS_PART + (size_t)(((3 * kvj + hk) * 16 + rowblk) * 21 + dv) * 80 + dk0 * 4) = dd;
    }
  }
}

__device__ __forceinline__ void gate_load(unsigned* v, const u16* __restrict__ x, const u16* __restrict__ gw, int row, int lane) {
  const u16* px = x + (size_t)row * D + 2 * lane; const u16* pg = gw + 2 * lane;
#pragma unroll
  for (int i = 0; i < 5; i++) {
    asm volatile("global_load_dword %0, %1, off offset:%2" : "=v"(v[i]) : "v"(px), "i"(256 * i));
    asm volatile("global_load_dword %0, %1, off offset:%2" : "=v"(v[5 + i]) : "v"(pg), "i"(256 * i));
  }
}
__device__ __forceinline__ void gate_finish(unsigned* v, float* __restrict__ gate, int row, int lane) {
  asm volatile("s_waitcnt vmcnt(0)" : "+v"(v[0]), "+v"(v[1]), "+v"(v[2]), "+v"(v[3]), "+v"(v[4]), "+v"(v[5]), "+v"(v[6]), "+v"(v[7]), "+v"(v[8]), "+v"(v[9]) :: "memory");
  float s = 0.f;
#pragma unroll
  for (int i = 0; i < 5; i++) {
    unsigned p = pack2(lo_f(v[i]) * lo_f(v[5 + i]), hi_f(v[i]) * hi_f(v[5 + i]));
    s += lo_f(p) + hi_f(p);
  }
#pragma unroll
  for (int o = 32; o > 0; o >>= 1) s += __shfl_xor(s, o);
  if (lane == 0) { float a = rb(s); gate[row] = rb(1.f / (1.f + __expf(-a))); }
}

__device__ __forceinline__ cf zpow(float th, float tl, float rh, float rl, float p) {
  float e = __builtin_amdgcn_exp2f(th * p); e = fmaf(e, (tl * p) * 0.69314718f, e);
  float t = rh * p; float f = (t - __builtin_rintf(t)) + rl * p;
  cf o; o[0] = e * __builtin_amdgcn_cosf(f); o[1] = e * __builtin_amdgcn_sinf(f); return o;
}
#define PKM(o, a, b) "v_pk_mul_f32 " o ", " a ", " b " op_sel:[0,0] op_sel_hi:[0,1]\n\t"
#define PKF(o, a, b) "v_pk_fma_f32 " o ", " a ", " b ", " o " op_sel:[1,1,0] op_sel_hi:[1,0,1] neg_lo:[1,0,0]\n\t"
__device__ __forceinline__ void cmul4(cf& o0, cf& o1, cf& o2, cf& o3, cf a0, cf b0, cf a1, cf b1, cf a2, cf b2, cf a3, cf b3) {
  asm(PKM("%0", "%4", "%5") PKM("%1", "%6", "%7") PKM("%2", "%8", "%9") PKM("%3", "%10", "%11")
      PKF("%0", "%4", "%5") PKF("%1", "%6", "%7") PKF("%2", "%8", "%9") PKF("%3", "%10", "%11")
      : "=&v"(o0), "=&v"(o1), "=&v"(o2), "=&v"(o3)
      : "v"(a0), "v"(b0), "v"(a1), "v"(b1), "v"(a2), "v"(b2), "v"(a3), "v"(b3));
}
__device__ __forceinline__ void batch4(unsigned* h, cf b, cf t0, cf t1, cf t2, cf t3) {
  cf w0, w1, w2, w3;
  asm(PKM("%0", "%4", "%5") PKM("%1", "%4", "%6") PKM("%2", "%4", "%7") PKM("%3", "%4", "%8")
      PKF("%0", "%4", "%5") PKF("%1", "%4", "%6") PKF("%2", "%4", "%7") PKF("%3", "%4", "%8")
      : "=&v"(w0), "=&v"(w1), "=&v"(w2), "=&v"(w3) : "v"(b), "v"(t0), "v"(t1), "v"(t2), "v"(t3));
  h[0] = pack2(w0[0], w0[1]); h[1] = pack2(w1[0], w1[1]); h[2] = pack2(w2[0], w2[1]); h[3] = pack2(w3[0], w3[1]);
}
__device__ __forceinline__ void batch3(unsigned* h, cf b, cf t1, cf t2, cf t3) {
  cf w1, w2, w3;
  asm(PKM("%0", "%3", "%4") PKM("%1", "%3", "%5") PKM("%2", "%3", "%6")
      PKF("%0", "%3", "%4") PKF("%1", "%3", "%5") PKF("%2", "%3", "%6")
      : "=&v"(w1), "=&v"(w2), "=&v"(w3) : "v"(b), "v"(t1), "v"(t2), "v"(t3));
  h[0] = pack2(b[0], b[1]); h[1] = pack2(w1[0], w1[1]); h[2] = pack2(w2[0], w2[1]); h[3] = pack2(w3[0], w3[1]);
}
constexpr int KG_RS = 288, KG_PL = 16 * KG_RS;
__device__ __forceinline__ void kgen_role(char* smem, const u16* __restrict__ logA, const u16* __restrict__ Aim,
                                          const u16* __restrict__ Cr, const u16* __restrict__ Ci, const u16* __restrict__ logdt,
                                          const u16* __restrict__ x, const u16* __restrict__ W, const u16* __restrict__ wo,
                                          const u16* __restrict__ gw, char* __restrict__ ws, int blk, unsigned pfv) {
  const int tid = threadIdx.x, lane = tid & 63;
  const int wave = __builtin_amdgcn_readfirstlane(tid >> 6);
  constexpr int RS = KG_RS, PL = KG_PL;
  const int task = blk * 8 + wave;
  const int d = task / 3;
  const int pi = task * 64 + lane;
#define PF 1
#define PFM 1
#define PFS0 3
#define PFS1 10
  const char* pp;
  {
    const int xcd = blk & 7, i = (blk >> 3) * 64 + lane;
    const int q = i & 15;
    const int ln = (xcd & 1) ? (q < 8 ? 12 + q : 14 + q) : (q < 13 ? q : 7 + q);
    if (i < 1280) pp = (const char*)W + (size_t)(80 * wave + (i >> 4)) * 3840 + ln * 128;
    else { const int id = wave * 640 + (i - 1280); pp = (const char*)x + (size_t)((xcd >> 1) * 512 + (id & 511)) * 1280 + (id >> 9) * 128; }
  }
  const int pfpt = wave == 0 ? 9 : (wave <= 2 ? 0 : (wave <= 5 ? 1 : 2));
#define PFLOAD(pt) do { __builtin_amdgcn_sched_barrier(0); if (pfpt == (pt)) asm volatile("global_load_dword %0, %1, off" : "+v"(pfv) : "v"(pp)); __builtin_amdgcn_sched_barrier(0); } while (0)
  PFLOAD(0);
  const float dt = rb(expf(b2f(logdt[d])));
  float ar;
  {
    const float la = b2f(logA[pi]);
    float h = la * 1.44269502f; float l = fmaf(la, 1.44269502f, -h) + la * 1.92596299e-8f;
    float e = __builtin_amdgcn_exp2f(h); e = fmaf(e, l * 0.69314718f, e);
    ar = -rb(e) * dt;
  }
  const float ai = b2f(Aim[pi]) * dt;
  cf C; C[0] = 2.f * (b2f(Cr[pi]) * dt); C[1] = 2.f * (b2f(Ci[pi]) * dt);
  const float th = ar * 1.44269502f, tl = fmaf(ar, 1.44269502f, -th) + ar * 1.92596299e-8f;
  const float rh = ai * 0.159154937f, rl = fmaf(ai, 0.159154937f, -rh) + ai * 6.42063833e-9f;
  char* tb = smem + wave * (2 * PL);
  char* wp = tb + lane * 4;
  const char* rp = tb + (lane & 15) * RS + (lane >> 4) * 16;
  STAMP(0, 2);
  PFLOAD(1);
  const cf one = {1.f, 0.f};
  cf S1 = zpow(th, tl, rh, rl, 1.f), Q1 = zpow(th, tl, rh, rl, 8.f), R1 = zpow(th, tl, rh, rl, 128.f);
  Q1[1] = -Q1[1];
  cf S2, Q2, R2, G1, S3, S4, Q3, P1, R3, T1, G2, G3, S5, S6, S7, P2, CT1, T2, P3, CS4, CT2, CT3, CT4, dm0;
  cmul4(S2, Q2, R2, G1, S1, S1, Q1, Q1, R1, R1, C, R1);
  cmul4(S3, S4, Q3, P1, S2, S1, S2, S2, Q2, Q1, Q2, Q2);
  cmul4(R3, T1, G2, P2, R2, R1, R2, R2, C, R2, P1, P1);
  cmul4(S5, S6, S7, P3, S4, S1, S4, S2, S4, S3, P2, P1);
  cmul4(G3, CT1, T2, dm0, C, R3, C, T1, T1, T1, C, R1);
  cmul4(CS4, CT2, CT3, dm0, CT1, S4, C, T2, CT1, T2, C, R1);
  CT4 = cmul(CT2, T2);
  PFLOAD(2);
  unsigned gv[10];
  gate_load(gv, x, gw, task, lane);
  const int wid = min(blk * 216 + (tid < 216 ? tid : 215), 51199);
  unsigned wr_[8];
  {
    const int slot = wid % 160, f = wid / 160;
    const int s5 = (f >> 2) % 5, hb = 8 * (f & 3) + 4 * (((f >> 2) / 5) & 1), cb = f / 40;
    const int m = slot < 128 ? (slot >> 6) * 32 + (slot & 31) : 64 + (slot & 15), h = slot < 128 ? (slot >> 5) & 1 : (slot >> 4) & 1;
    const int n = 80 * cb + m;
    int k0, k1;
    if (s5 < 4) { k0 = 20 * (hb + s5) + 4 * h; k1 = k0 + 8; }
    else { k0 = 20 * (hb + 2 * h) + 16; k1 = k0 + 20; }
    if (wave < 4) {
      const u16* p0 = wo + (size_t)k0 * D + n; const u16* p1 = wo + (size_t)k1 * D + n;
#pragma unroll
      for (int e = 0; e < 4; e++) {
        asm volatile("global_load_ushort %0, %1, off offset:%2" : "=v"(wr_[e]) : "v"(p0), "i"(e * 1280));
        asm volatile("global_load_ushort %0, %1, off offset:%2" : "=v"(wr_[4 + e]) : "v"(p1), "i"(e * 1280));
      }
    }
  }
  auto rows4 = [&](int pl, int row, int rs, cf b, cf t0, cf t1, cf t2, cf t3) {
    unsigned h[4];
    batch4(h, b, t0, t1, t2, t3);
#pragma unroll
    for (int k = 0; k < 4; k++) *(unsigned*)(wp + pl * PL + (row + k * rs) * RS) = h[k];
  };
  auto rows3 = [&](int pl, int row, int rs, cf b, cf t1, cf t2, cf t3) {
    unsigned h[4];
    batch3(h, b, t1, t2, t3);
#pragma unroll
    for (int k = 0; k < 4; k++) *(unsigned*)(wp + pl * PL + (row + k * rs) * RS) = h[k];
  };
  *(unsigned*)(wp) = 0x00003F80u;
  *(unsigned*)(wp + RS) = pack2(Q1[0], Q1[1]); *(unsigned*)(wp + 2 * RS) = pack2(Q2[0], Q2[1]); *(unsigned*)(wp + 3 * RS) = pack2(Q3[0], Q3[1]);
  rows3(0, 4, 1, P1, Q1, Q2, Q3); rows3(0, 8, 1, P2, Q1, Q2, Q3); rows3(0, 12, 1, P3, Q1, Q2, Q3);
  rows3(1, 0, 1, C, S1, S2, S3); rows4(1, 4, 1, C, S4, S5, S6, S7); rows3(1, 8, 1, G1, S1, S2, S3); rows4(1, 12, 1, G1, S4, S5, S6, S7);
  __builtin_amdgcn_wave_barrier();
  s16x8 bh[4];
  f32x4 res[3];
#pragma unroll
  for (int k = 0; k < 4; k++) bh[k] = *(const s16x8*)(rp + k * 64);
  auto mm = [&](int t, int pl) {
    f32x4 a = {0.f, 0.f, 0.f, 0.f};
#pragma unroll
    for (int k = 0; k < 4; k++) a = __builtin_amdgcn_mfma_f32_16x16x32_bf16(*(const s16x8*)(rp + pl * PL + k * 64), bh[k], a, 0, 0, 0);
    res[t] = a;
  };
  mm(0, 1);
  __builtin_amdgcn_wave_barrier();
  STAMP(0, 3);
  rows3(0, 0, 1, G2, S2, S4, S6); rows3(0, 4, 1, G3, S2, S4, S6); rows3(0, 8, 2, CT1, R1, R2, R3); rows3(0, 9, 2, CS4, R1, R2, R3);
  rows3(1, 0, 1, CT2, R1, R2, R3); rows3(1, 4, 1, CT3, R1, R2, R3);
  *(unsigned*)(wp + PL + 8 * RS) = pack2(CT4[0], CT4[1]);
  __builtin_amdgcn_wave_barrier();
  mm(1, 0); mm(2, 1);
  __builtin_amdgcn_wave_barrier();
  STAMP(0, 4);
  if (wave < 4) {
    asm volatile("s_waitcnt vmcnt(0)" : "+v"(wr_[0]), "+v"(wr_[1]), "+v"(wr_[2]), "+v"(wr_[3]), "+v"(wr_[4]), "+v"(wr_[5]), "+v"(wr_[6]), "+v"(wr_[7]) :: "memory");
    u32x4 wv = {wr_[0] | (wr_[1] << 16), wr_[2] | (wr_[3] << 16), wr_[4] | (wr_[5] << 16), wr_[6] | (wr_[7] << 16)};
    if (tid < 216 && blk * 216 + tid < 51200) st16(ws + WS_WOF + (size_t)wid * 16, wv);
  }
  gate_finish(gv, (float*)(ws + WS_GATE), task, lane);
#pragma unroll
  for (int t = 0; t < 3; t++) *(f32x4*)(tb + t * 1024 + lane * 16) = res[t];
  __syncthreads();
  {
    const int d0 = (blk * 8) / 3;
#pragma unroll
    for (int it = 0; it < 2; it++) {
      const int e = tid + 512 * it;
      const int seg = e / 192, ent = e - 192 * seg;
      const int ds = d0 + seg;
      const int wlo = max(0, 3 * ds - 8 * blk), whi = min(8, 3 * ds + 3 - 8 * blk);
      if (seg < 4 && wlo < 8) {
        f32x4 a = *(const f32x4*)(smem + wlo * (2 * PL) + ent * 16);
        for (int w = wlo + 1; w < whi; w++) a += *(const f32x4*)(smem + w * (2 * PL) + ent * 16);
        st16(ws + WS_KIMG + (size_t)ds * 6144 + (3 * ds < 8 * blk ? 3072 : 0) + ent * 16, __builtin_bit_cast(u32x4, a));
      }
    }
  }
  asm volatile("s_waitcnt vmcnt(0)" :: "v"(pfv) : "memory");
}

__global__ __launch_bounds__(512, 4) void k1(const u16* __restrict__ x, const u16* __restrict__ logA, const u16* __restrict__ Aim,
                                          const u16* __restrict__ Cr, const u16* __restrict__ Ci, const u16* __restrict__ logdt,
                                          const u16* __restrict__ wqkv, const u16* __restrict__ gw, const u16* __restrict__ wo,
                                          char* __restrict__ ws) {
  __shared__ __attribute__((aligned(16))) char smem[K1_LDS];
  int b = blockIdx.x;
  unsigned cpf = 0;
  if (b < 32 && threadIdx.x >= 192) {
    const char* pc; int len;
    asm volatile("s_getpc_b64 %0\n\ts_mov_b32 %1, code_tail_mark-_Z2k1PKtS0_S0_S0_S0_S0_S0_S0_S0_Pc-128" : "=s"(pc), "=s"(len));
    const int off = min((int)(threadIdx.x - 192) * 128, len);
    asm volatile("global_load_dword %0, %1, %2" : "+v"(cpf) : "v"(off), "s"(pc));
  }
  STAMP(0, 0);
  b = b < NB_KG ? b + NB_GEMM : (b < NB_KG + NB_GEMM ? b - NB_KG : b);
  if (b < NB_GEMM) {
    const int xcd = b & 7, idx = b >> 3;
    const int rowblk = (xcd >> 1) * 4 + (idx & 3), cb = (xcd & 1) * 8 + (idx >> 2);
    if (cb < 5) gemm_role<true>(smem, x, wqkv, ws, rowblk, cb);
    else        gemm_role<false>(smem, x, wqkv, ws, rowblk, cb);
    STAMP(0, 1);
    return;
  }
  b -= NB_GEMM;
  if (b < NB_KG) { kgen_role(smem, logA, Aim, Cr, Ci, logdt, x, wqkv, wo, gw, ws, b, cpf); STAMP(0, 1); return; }
  b -= NB_KG;
  if (b == 0) ((unsigned*)(ws + WS_CNT))[threadIdx.x] = 0u, ((unsigned*)(ws + WS_CNT))[threadIdx.x + 512] = 0u;
  {
    unsigned gv[10];
    const int row = NB_KG * 8 + b * 8 + (threadIdx.x >> 6);
    gate_load(gv, x, gw, row, threadIdx.x & 63);
    gate_finish(gv, (float*)(ws + WS_GATE), row, threadIdx.x & 63);
  }
  STAMP(0, 1);
}

constexpr int K2_LDS = 163840;
constexpr int K2_CS = 4160;
constexpr int K2_KOFF = 4 * K2_CS;
constexpr int K2_CH = K2_KOFF + 4608;
constexpr int K2_CONV = K2_LDS - 3 * K2_CH;
constexpr int K2_TAB = 81920;
constexpr int K2_FIN = K2_TAB + 9216;
constexpr int K2_STG = 114688;
constexpr int NB_KV = 32;
typedef __attribute__((address_space(1))) unsigned gu32;
__device__ __forceinline__ const char* uni(const char* p) {
  size_t v = (size_t)p;
  unsigned lo = __builtin_amdgcn_readfirstlane((unsigned)v), hi = __builtin_amdgcn_readfirstlane((unsigned)(v >> 32));
  size_t o = ((size_t)hi << 32) | lo;
  asm volatile("s_nop 4" : "+s"(o));
  return (const char*)o;
}
__device__ __forceinline__ void wait_count(char* p, unsigned target) {
  gu32* c = (gu32*)(size_t)p;
  for (int n = 0; n < (1 << 22); n++) {
    if (__hip_atomic_load(c, __ATOMIC_RELAXED, __HIP_MEMORY_SCOPE_AGENT) >= target) break;
    __builtin_amdgcn_s_sleep(2);
  }
}
#define GLD4(dst, voff, sbase, pol) asm volatile("global_load_dwordx4 %0, %1, %2" pol : "=v"(dst) : "v"(voff), "s"(sbase))
#define GLD2(dst, voff, sbase, pol) asm volatile("global_load_dwordx2 %0, %1, %2" pol : "=v"(dst) : "v"(voff), "s"(sbase))
#define GLD1(dst, voff, sbase, pol) asm volatile("global_load_dword %0, %1, %2" pol : "=v"(dst) : "v"(voff), "s"(sbase))
template <int G>
__device__ __forceinline__ void conv_unit(f32x4* acc, int ap, int bp) {
  constexpr int NT = 8 - G;
  u32x2 a0[NT], a1[NT]; s16x8 bh[NT];
#pragma unroll
  for (int j = 0; j < NT; j++)
    asm volatile("ds_read_b64 %0, %3 offset:%5\n\tds_read_b64 %1, %3 offset:%6\n\tds_read_b128 %2, %4 offset:%5"
                 : "=&v"(a0[j]), "=&v"(a1[j]), "=&v"(bh[j]) : "v"(ap), "v"(bp), "n"(512 * j), "n"(512 * j + 8) : "memory");
#pragma unroll
  for (int x = 0; x < NT; x++) {
    asm volatile("s_waitcnt lgkmcnt(%3)" : "+v"(a0[x]), "+v"(a1[x]), "+v"(bh[x]) : "n"(3 * (NT - 1 - x) > 15 ? 15 : 3 * (NT - 1 - x)) : "memory");
#pragma unroll
    for (int y = 0; y <= x; y++)
#pragma unroll
      for (int sw = 0; sw < 2; sw++) {
        if (sw && y == x) continue;
        const int j = sw ? y : x, m = sw ? x : y, T = j + m;
        const int ti = G == 0 ? (T == 0 ? 0 : T == 3 ? 1 : T == 4 ? 2 : T == 7 ? 3 : -1) : (T == 1 ? 0 : T == 2 ? 1 : T == 5 ? 2 : T == 6 ? 3 : -1);
        if (ti >= 0) acc[ti] = __builtin_amdgcn_mfma_f32_16x16x32_bf16(__builtin_bit_cast(s16x8, (u32x4){a0[j][0], a0[j][1], a1[j][0], a1[j][1]}), bh[m], acc[ti], 0, 0, 0);
      }
    SB();
  }
}

__global__ __launch_bounds__(512, 2) void k2(const u16* __restrict__ Dskip, char* __restrict__ ws, u16* __restrict__ out) {
  __shared__ __attribute__((aligned(16))) char smem[K2_LDS];
  const int tid = threadIdx.x, lane = tid & 63;
  const int wave = __builtin_amdgcn_readfirstlane(tid >> 6);
  const int b = blockIdx.x;
  unsigned cpf = 0;
  if (b < 32 && tid < 192) {
    const char* pc; int len;
    asm volatile("s_getpc_b64 %0\n\ts_mov_b32 %1, code_tail_mark-_Z2k2PKtPcPt-128" : "=s"(pc), "=s"(len));
    const int off = min(tid * 128, len);
    asm volatile("global_load_dword %0, %1, %2" : "+v"(cpf) : "v"(off), "s"(pc));
  }
  STAMP(1, 0);
  const int lds0 = (int)(size_t)(lptr_t)smem;
  const int l16 = lane * 16;
  const int odd = b & 1;
  const int chA = 5 * (b >> 1) + 3 * odd, chH = 5 * (b >> 1) + 2;
  const int xcd = b & 7, idx = b >> 3;
  const int rowblk = xcd * 4 + (idx >> 3), cb = idx & 7;
  const int l0 = rowblk * 64, c0 = cb * 80;
  const int t = wave & 1, kh = wave >> 1;
  const int r = lane & 31, h = lane >> 5;
  const bool kvw = b < NB_KV && wave >= 6;
  f32x4 kvp[16];
  if (kvw) {
    const char* src = ws + WS_PART + (size_t)b * 16 * 1680;
    const int o = min(tid - 384, 104) * 16;
#pragma unroll
    for (int i = 0; i < 16; i++) GLD4(kvp[i], o, src + i * 1680, "");
  }
  u32x4 in0[3], in1[3];
  int dsto[3];
#pragma unroll
  for (int rr = 0; rr < 3; rr++) {
    const int id = wave + 8 * rr;
    int o0, o1;
    if (id < 13) {
      const int i = min(64 * id + lane, 776);
      const int ch = i / 259, xx = i - 259 * ch;
      const int base = (int)WS_UT + (ch == 2 ? chH : chA + ch) * (UT_STRIDE * 2);
      o0 = base + min(xx + 1, 259) * 16; o1 = base + min(xx + 2, 259) * 16;
      dsto[rr] = K2_CONV + ch * K2_CH + xx * 16;
    } else {
      const int k = min(id - 13, 8), ch = k / 3;
      o0 = (int)WS_KIMG + (ch == 2 ? chH : chA + ch) * 6144 + (k - 3 * ch) * 1024 + l16; o1 = o0 + 3072;
      dsto[rr] = K2_TAB + k * 1024 + l16;
    }
    GLD4(in0[rr], o0, ws, ""); GLD4(in1[rr], o1, ws, "");
  }
  const int jg = (wave >> 1) & 1;
  const int fo = (lane & 15) * 16 + (lane >> 4) * 4;
  int ft[3];
  ft[0] = (wave & 1) ? 4 + jg : jg;
  ft[1] = (wave & 1) ? 7 - jg : 3 - jg;
  { const int w3 = wave & 3; ft[2] = (w3 & 2) * 2 + ((w3 & 1) ? 3 - odd : odd); }
  u32x2 us[3];
  {
    const int oa = (int)WS_UT + (chA + (wave >> 2)) * (UT_STRIDE * 2) + (UT_PAD + fo) * 2;
    const int oh = (int)WS_UT + chH * (UT_STRIDE * 2) + (UT_PAD + fo) * 2;
    GLD2(us[0], oa + ft[0] * 512, ws, ""); GLD2(us[1], oa + ft[1] * 512, ws, ""); GLD2(us[2], oh + ft[2] * 512, ws, "");
  }
  float g_r;
  GLD1(g_r, (int)WS_GATE + (l0 + 32 * t + r) * 4, ws, "");
  unsigned dsA, dsH;
  asm volatile("global_load_ushort %0, %1, %2" : "=v"(dsA) : "v"((chA + (wave >> 2)) * 2), "s"(Dskip));
  asm volatile("global_load_ushort %0, %1, %2" : "=v"(dsH) : "v"(chH * 2), "s"(Dskip));
  const char* wsrc = ws + WS_WOF + (size_t)cb * 102400;
  auto dma1 = [&](int p) {
    asm volatile("s_mov_b32 m0, %2\n\ts_nop 0\n\tglobal_load_lds_dwordx4 %0, %1" :: "v"(l16), "s"(wsrc + p * 1024), "s"(lds0 + p * 1024) : "memory", "m0");
  };
  auto dma4 = [&](int p) {
    asm volatile("s_mov_b32 m0, %2\n\ts_nop 0\n\tglobal_load_lds_dwordx4 %0, %1\n\tglobal_load_lds_dwordx4 %0, %1 offset:1024\n\t"
                 "global_load_lds_dwordx4 %0, %1 offset:2048\n\tglobal_load_lds_dwordx4 %0, %1 offset:3072"
                 :: "v"(l16), "s"(wsrc + p * 1024), "s"(lds0 + p * 1024) : "memory", "m0");
  };
  dma4(10 * wave); dma4(10 * wave + 4); dma1(10 * wave + 8); dma1(10 * wave + 9);
  u32x4 pf0[8]; u32x2 pf1[8];
  {
    const char* p0 = ws + WS_PQ + (size_t)((rowblk * 32 + kh * 8) * 2 + t) * 1024;
    const char* p1 = ws + WS_PQ + (size_t)L * 16 * 32 * 2 + (size_t)((rowblk * 32 + kh * 8) * 2 + t) * 256;
    const int r8 = r * 8;
#pragma unroll
    for (int i = 0; i < 8; i++) { GLD4(pf0[i], l16, p0 + i * 2048, ""); GLD2(pf1[i], r8, p1 + i * 512, ""); }
  }
  int tix[8];
#pragma unroll
  for (int e = 0; e < 8; e++) {
    const int j = (tid & 255) * 8 + 7 - e;
    const int p = (int)rb((float)j);
    const int hh = p >> 7, c = (p >> 3) & 15;
    int sh = 31 - __builtin_clz(hh | 1); sh = sh > 3 ? 3 : sh;
    const int row = 8 * sh + (((hh << 3) | (p & 7)) >> sh);
    tix[e] = K2_TAB + (tid >> 8) * 3072 + (((row >> 2) * 16 + c) * 4 + (row & 3)) * 4;
  }
  if (tid < 96) *(u32x4*)(smem + K2_CONV + (tid >> 5) * K2_CH + K2_KOFF + (tid & 31) * 16) = (u32x4){0u, 0u, 0u, 0u};
  asm volatile("s_waitcnt vmcnt(32)" : "+v"(in0[0]), "+v"(in1[0]), "+v"(in0[1]), "+v"(in1[1]), "+v"(in0[2]), "+v"(in1[2]), "+v"(cpf) :: "memory");
  STAMP(1, 2);
#pragma unroll
  for (int rr = 0; rr < 3; rr++) {
    const int id = wave + 8 * rr;
    char* p = smem + dsto[rr];
    if (id < 13) {
      const u32x4 a = in0[rr], bb = in1[rr];
      unsigned w[6] = {a[0], a[1], a[2], a[3], bb[0], bb[1]};
      unsigned o[5];
#pragma unroll
      for (int k = 0; k < 5; k++) o[k] = __builtin_amdgcn_alignbyte(w[k + 1], w[k], 2);
#pragma unroll
      for (int q = 0; q < 2; q++) {
        *(u32x4*)(p + (2 * q) * K2_CS) = (u32x4){o[q], o[q + 1], o[q + 2], o[q + 3]};
        *(u32x4*)(p + (2 * q + 1) * K2_CS) = (u32x4){w[q + 1], w[q + 2], w[q + 3], w[q + 4]};
      }
    } else if (id < 22) {
      const int ch = (id - 13) / 3, d = ch == 2 ? chH : chA + ch;
      f32x4 v = __builtin_bit_cast(f32x4, in0[rr]);
      if ((d & 7) == 2 || (d & 7) == 5) v += __builtin_bit_cast(f32x4, in1[rr]);
      *(f32x4*)p = v;
    }
  }
  if (kvw) {
    asm volatile("" : "+v"(kvp[0]), "+v"(kvp[1]), "+v"(kvp[2]), "+v"(kvp[3]), "+v"(kvp[4]), "+v"(kvp[5]), "+v"(kvp[6]), "+v"(kvp[7]));
    asm volatile("" : "+v"(kvp[8]), "+v"(kvp[9]), "+v"(kvp[10]), "+v"(kvp[11]), "+v"(kvp[12]), "+v"(kvp[13]), "+v"(kvp[14]), "+v"(kvp[15]));
#pragma unroll
    for (int st = 1; st < 16; st <<= 1)
#pragma unroll
      for (int i = 0; i < 16; i += 2 * st) kvp[i] += kvp[i + st];
    if (tid - 384 < 105) *(f32x4*)(smem + K2_FIN + (tid - 384) * 16) = (f32x4){rb(kvp[0][0]), rb(kvp[0][1]), rb(kvp[0][2]), rb(kvp[0][3])};
  }
  RAW_BARRIER();
  if (kvw && wave == 7) {
    const float* fin = (const float*)(smem + K2_FIN);
    const int m = lane & 31;
    int col = m < 16 ? m : ((m >> 2) == ((b & 2) ? 5 : 4) ? 16 + (m & 3) : 20);
    u32x4 o;
#pragma unroll
    for (int e = 0; e < 4; e++) o[e] = pack2(fin[col * 20 + 8 * h + 2 * e], fin[col * 20 + 8 * h + 2 * e + 1]);
    st16(ws + WS_KVT + (size_t)b * 1280 + lane * 16, o);
    const int m0 = 2 * (lane & 15), m1 = m0 + 1;
    int ca = m0 < 16 ? m0 : ((m0 >> 2) == ((b & 2) ? 5 : 4) ? 16 + (m0 & 3) : 20);
    int cc = m1 < 16 ? m1 : ((m1 >> 2) == ((b & 2) ? 5 : 4) ? 16 + (m1 & 3) : 20);
    u32x4 o2 = {pack2(fin[ca * 20 + 16], fin[ca * 20 + 17]), pack2(fin[ca * 20 + 18], fin[ca * 20 + 19]),
                pack2(fin[cc * 20 + 16], fin[cc * 20 + 17]), pack2(fin[cc * 20 + 18], fin[cc * 20 + 19])};
    if (lane < 16) st16(ws + WS_KVT + (size_t)b * 1280 + 1024 + lane * 16, o2);
  }
  {
    u32x4 hi;
#pragma unroll
    for (int e = 0; e < 4; e++) hi[e] = pack2(*(const float*)(smem + tix[2 * e]), *(const float*)(smem + tix[2 * e + 1]));
    *(u32x4*)(smem + K2_CONV + (tid >> 8) * K2_CH + K2_KOFF + 512 + (tid & 255) * 16) = hi;
    if (wave < 4) {
#pragma unroll
      for (int e = 0; e < 4; e++) hi[e] = pack2(*(const float*)(smem + tix[2 * e] + 6144), *(const float*)(smem + tix[2 * e + 1] + 6144));
      *(u32x4*)(smem + K2_CONV + 2 * K2_CH + K2_KOFF + 512 + tid * 16) = hi;
    }
  }
  if (kvw && wave == 7) {
    VMCNT(0);
    if (lane < 8) __hip_atomic_fetch_add((gu32*)(size_t)(ws + WS_CNT + lane * 128), 1u, __ATOMIC_RELAXED, __HIP_MEMORY_SCOPE_AGENT);
  }
  RAW_BARRIER();
  dma1(80 + wave); dma1(88 + wave); dma1(96);
  STAMP(1, 3);
  f32x4 ca[4], chh[4];
#pragma unroll
  for (int i = 0; i < 4; i++) { ca[i] = (f32x4){0.f, 0.f, 0.f, 0.f}; chh[i] = (f32x4){0.f, 0.f, 0.f, 0.f}; }
  {
    const int i = lane & 15, kg = lane >> 4, e = kg >> 1, bb = kg & 1;
    const int ap = lds0 + K2_CONV + (i & 3) * K2_CS + (2 * e - bb + 2) * 16 + (i >> 2) * 8;
    const int bp = lds0 + K2_CONV + K2_KOFF + (16 + i - e) * 32 + bb * 16;
    const int apj = ap + (wave >> 2) * K2_CH + (wave & 1) * 256;
    const int bpj = bp + (wave >> 2) * K2_CH - (wave & 1) * 256;
    if (jg == 0) {
#pragma unroll 1
      for (int it = 0; it < 4; it++) conv_unit<0>(ca, apj + it * 64, bpj - it * 64);
    } else {
#pragma unroll 1
      for (int it = 0; it < 4; it++) conv_unit<1>(ca, apj + it * 64, bpj - it * 64);
    }
    if (odd == 0) conv_unit<0>(chh, ap + 2 * K2_CH + wave * 64, bp + 2 * K2_CH - wave * 64);
    else          conv_unit<1>(chh, ap + 2 * K2_CH + wave * 64, bp + 2 * K2_CH - wave * 64);
  }
  STAMP(1, 4);
  if (tid == 0) wait_count(ws + WS_CNT + xcd * 128, NB_KV);
  RAW_BARRIER();
  u32x4 ka[8]; u32x2 kb[8];
  {
    const char* kp = ws + WS_KVT + (size_t)kh * 8 * 1280;
    const int r8 = r * 8;
#pragma unroll
    for (int i = 0; i < 8; i++) { GLD4(ka[i], l16, kp + i * 1280, " sc1"); GLD2(kb[i], r8, kp + i * 1280 + 1024, " sc1"); }
  }
  dma1(97 + (wave < 2 ? wave : 2));
  {
    if (tid == 0) *(unsigned*)(smem + K2_STG - 16) = 0u;
    char* sp = smem + K2_STG + l16;
    *(f32x4*)(sp + (wave * 2) * 1024) = (wave & 1) ? ca[0] : ca[2];
    *(f32x4*)(sp + (wave * 2 + 1) * 1024) = (wave & 1) ? ca[1] : ca[3];
#pragma unroll
    for (int i = 0; i < 4; i++) *(f32x4*)(sp + 16384 + (i * 8 + wave) * 1024) = chh[i];
  }
  RAW_BARRIER();
  {
    const char* sp = smem + K2_STG + l16;
    f32x4 y[3];
    y[0] = ((wave & 1) ? ca[2] : ca[0]) + *(const f32x4*)(sp + ((wave ^ 1) * 2) * 1024);
    y[1] = ((wave & 1) ? ca[3] : ca[1]) + *(const f32x4*)(sp + ((wave ^ 1) * 2 + 1) * 1024);
    asm volatile("s_waitcnt vmcnt(17)" : "+v"(us[0]), "+v"(us[1]), "+v"(us[2]), "+v"(g_r), "+v"(dsA), "+v"(dsH) :: "memory");
    const float dskA = lo_f(dsA), dskH = lo_f(dsH);
    auto fin = [&](f32x4 s, u32x2 uu, float dsk, int ch, int tile) {
      u32x2 ov;
#pragma unroll
      for (int e = 0; e < 2; e++) {
        unsigned yy = pack2(s[2 * e], s[2 * e + 1]);
        unsigned k = pack2(lo_f(uu[e]) * dsk, hi_f(uu[e]) * dsk);
        ov[e] = pack2(lo_f(yy) + lo_f(k), hi_f(yy) + hi_f(k));
      }
      const int o = (int)WS_AT + (ch * L + tile * 256 + fo) * 2;
      asm volatile("global_store_dwordx2 %0, %1, %2 sc1\n\ts_nop 1" :: "v"(o), "v"(ov), "s"(ws) : "memory");
    };
    fin(y[0], us[0], dskA, chA + (wave >> 2), ft[0]);
    fin(y[1], us[1], dskA, chA + (wave >> 2), ft[1]);
    if (wave < 4) {
      const char* hp = sp + 16384 + wave * 8192;
      f32x4 v[8];
#pragma unroll
      for (int i = 0; i < 8; i++) v[i] = *(const f32x4*)(hp + i * 1024);
      y[2] = ((v[0] + v[1]) + (v[2] + v[3])) + ((v[4] + v[5]) + (v[6] + v[7]));
      fin(y[2], us[2], dskH, chH, ft[2]);
      VMCNT(3);
    } else {
      VMCNT(2);
    }
  }
#pragma unroll
  for (int i = 0; i < 8; i++) asm volatile("" : "+v"(pf0[i]), "+v"(pf1[i]), "+v"(ka[i]), "+v"(kb[i]));
  RAW_BARRIER();
  STAMP(1, 5);
  f32x16 acc[3], c[2];
#pragma unroll
  for (int q = 0; q < 3; q++)
#pragma unroll
    for (int i = 0; i < 16; i++) acc[q][i] = 0.f;
  u32x4 rm = {0u, 0u, 0u, 0u};
  const char* wl = smem + kh * 2560 + l16;
  const char* wl2 = smem + kh * 2560 + 2048 + (h * 16 + (lane & 15)) * 16;
  auto stageA = [&](int i) {
    u32x2 b1u = {h ? 0u : pf1[i][0], h ? 0u : pf1[i][1]};
    f32x16 z;
#pragma unroll
    for (int q = 0; q < 16; q++) z[q] = 0.f;
    z = __builtin_amdgcn_mfma_f32_32x32x16_bf16(__builtin_bit_cast(s16x8, ka[i]), __builtin_bit_cast(s16x8, pf0[i]), z, 0, 0, 0);
    c[i & 1] = __builtin_amdgcn_mfma_f32_32x32x8bf16_1k(__builtin_bit_cast(s16x4, kb[i]), __builtin_bit_cast(s16x4, b1u), z, 0, 0, 0);
    asm volatile("" : "+v"(c[i & 1]));
  };
  unsigned pollv = 0;
  u32x4 av[2];
  stageA(0);
#pragma unroll
  for (int i = 0; i < 8; i++) {
    const int j = i & 3, G = i >> 2;
    const int wo_ = G * 51200 + j * 10240;
    s16x8 w0 = *(const s16x8*)(wl + wo_), w1 = *(const s16x8*)(wl + wo_ + 1024), w2 = *(const s16x8*)(wl2 + wo_);
    if (i + 1 < 8) stageA(i + 1);
    const f32x16 cc = c[i & 1];
    const float den = rb(rb(cc[12]) + 1e-6f);
    const float inv = __builtin_amdgcn_rcpf(den);
    unsigned o[6];
#pragma unroll
    for (int q = 0; q < 6; q++) o[q] = pack2(cc[2 * q] * inv, cc[2 * q + 1] * inv);
    asm volatile("" : "+v"(o[4]), "+v"(o[5]));
    const bool keep = (j >> 1) == h;
    rm[2 * (j & 1)] = keep ? o[4] : rm[2 * (j & 1)]; rm[2 * (j & 1) + 1] = keep ? o[5] : rm[2 * (j & 1) + 1];
    u32x4 ob = {o[0], o[1], o[2], o[3]};
    acc[0] = __builtin_amdgcn_mfma_f32_32x32x16_bf16(w0, __builtin_bit_cast(s16x8, ob), acc[0], 0, 0, 0);
    acc[1] = __builtin_amdgcn_mfma_f32_32x32x16_bf16(w1, __builtin_bit_cast(s16x8, ob), acc[1], 0, 0, 0);
    acc[2] = __builtin_amdgcn_mfma_f32_32x32x16_bf16(w2, __builtin_bit_cast(s16x8, ob), acc[2], 0, 0, 0);
    if (j == 3) {
      const int wr_ = G * 51200 + 40960;
      s16x8 w3 = *(const s16x8*)(wl + wr_), w4 = *(const s16x8*)(wl + wr_ + 1024), w5 = *(const s16x8*)(wl2 + wr_);
      acc[0] = __builtin_amdgcn_mfma_f32_32x32x16_bf16(w3, __builtin_bit_cast(s16x8, rm), acc[0], 0, 0, 0);
      acc[1] = __builtin_amdgcn_mfma_f32_32x32x16_bf16(w4, __builtin_bit_cast(s16x8, rm), acc[1], 0, 0, 0);
      acc[2] = __builtin_amdgcn_mfma_f32_32x32x16_bf16(w5, __builtin_bit_cast(s16x8, rm), acc[2], 0, 0, 0);
    }
    if (i == 1) {
      VMCNT(0);
      unsigned old = 0;
      if (lane == 0) old = __hip_atomic_fetch_add((unsigned*)(smem + K2_STG - 16), 1u, __ATOMIC_RELAXED, __HIP_MEMORY_SCOPE_WORKGROUP);
      if (lane == 0 && old == 7u) __hip_atomic_fetch_add((gu32*)(size_t)(ws + WS_CNT + (8 + (b >> 5)) * 128), 1u, __ATOMIC_RELAXED, __HIP_MEMORY_SCOPE_AGENT);
    }
    if (i == 3) { RAW_BARRIER(); }
#define POLL_I 4
    if (i == POLL_I) GLD1(pollv, (int)WS_CNT + (8 + cb) * 128, ws, " sc1");
    if (i == POLL_I + 2) {
      asm volatile("s_waitcnt vmcnt(0)" : "+v"(pollv) :: "memory");
      if (__builtin_amdgcn_readfirstlane(pollv) < 32u) wait_count(ws + WS_CNT + (8 + cb) * 128, 32);
      asm volatile("" ::: "memory");
      const int o = (int)WS_AT + ((c0 + (tid >> 3)) * L + l0 + 8 * (tid & 7)) * 2;
      GLD4(av[0], o, ws, " sc1"); GLD4(av[1], o + (tid < 128 ? 64 * L * 2 : 0), ws, " sc1");
    }
  }
  STAMP(1, 6);
  {
    char* sp = smem + 102400 + t * 30720 + l16;
#pragma unroll
    for (int g = 0; g < 10; g++) {
      const int own = g & 3;
      f32x4 v = {acc[g >> 2][4 * (g & 3)], acc[g >> 2][4 * (g & 3) + 1], acc[g >> 2][4 * (g & 3) + 2], acc[g >> 2][4 * (g & 3) + 3]};
      if (kh != own) *(f32x4*)(sp + (g * 3 + ((kh - own - 1) & 3)) * 1024) = v;
    }
    asm volatile("s_waitcnt vmcnt(0)" : "+v"(av[0]), "+v"(av[1]) :: "memory");
    *(u32x4*)(smem + (tid >> 3) * 192 + (tid & 7) * 16) = av[0];
    if (tid < 128) *(u32x4*)(smem + (64 + (tid >> 3)) * 192 + (tid & 7) * 16) = av[1];
  }
  RAW_BARRIER();
  STAMP(1, 7);
  {
    const char* sp = smem + 102400 + t * 30720 + l16;
    const float g1 = rb(1.f - g_r);
    const char* atp = smem + (8 * kh + 4 * h + ((lane & 15) >> 2)) * 192 + (32 * t + (lane & 16) + 4 * (lane & 3)) * 2;
    u16* op = out + (size_t)(l0 + 32 * t + r) * D + c0 + 8 * kh + 4 * h;
#pragma unroll
    for (int tl = 0; tl < 3; tl++) {
      if (tl == 2 && kh >= 2) break;
      f32x4 m;
      if (kh == 0)      m = (f32x4){acc[tl][0], acc[tl][1], acc[tl][2], acc[tl][3]};
      else if (kh == 1) m = (f32x4){acc[tl][4], acc[tl][5], acc[tl][6], acc[tl][7]};
      else if (kh == 2) m = (f32x4){acc[tl][8], acc[tl][9], acc[tl][10], acc[tl][11]};
      else              m = (f32x4){acc[tl][12], acc[tl][13], acc[tl][14], acc[tl][15]};
      const char* pp = sp + (tl * 4 + kh) * 3072;
      f32x4 p0 = *(const f32x4*)pp, p1 = *(const f32x4*)(pp + 1024), p2 = *(const f32x4*)(pp + 2048);
      f32x4 v = (m + p1) + (p0 + p2);
      s16x4 at = __builtin_amdgcn_ds_read_tr16_b64_v4i16((lds_s16x4_p)(atp + tl * 32 * 192));
      u32x2 au = __builtin_bit_cast(u32x2, at);
      u32x2 ov;
#pragma unroll
      for (int e = 0; e < 2; e++) {
        unsigned bq = pack2(v[2 * e], v[2 * e + 1]);
        unsigned gb = pack2(g1 * lo_f(bq), g1 * hi_f(bq));
        unsigned ga = pack2(g_r * lo_f(au[e]), g_r * hi_f(au[e]));
        ov[e] = pack2(lo_f(ga) + lo_f(gb), hi_f(ga) + hi_f(gb));
      }
      *(u32x2*)(op + 32 * tl) = ov;
    }
  }
  asm volatile("code_tail_mark:" ::: "memory");
  STAMP(1, 1);
}

extern "C" void kernel_launch(void* const* d_in, const int* in_sizes, int n_in, void* d_out, int out_size, void* d_ws, size_t ws_size,
                              hipStream_t stream) {
  const u16* u = (const u16*)d_in[0]; const u16* logA = (const u16*)d_in[1]; const u16* Aim = (const u16*)d_in[2];
  const u16* Cr = (const u16*)d_in[3]; const u16* Ci = (const u16*)d_in[4]; const u16* logdt = (const u16*)d_in[5];
  const u16* Dsk = (const u16*)d_in[6]; const u16* wqkv = (const u16*)d_in[8]; const u16* wo = (const u16*)d_in[9];
  const u16* gw = (const u16*)d_in[10];
  char* ws = (char*)d_ws;
  k1<<<NB_GEMM + NB_GATE + NB_KG, 512, 0, stream>>>(u, logA, Aim, Cr, Ci, logdt, wqkv, gw, wo, ws);
  k2<<<256, 512, 0, stream>>>(Dsk, ws, (u16*)d_out);
}
```

```cpp
#include <hip/hip_runtime.h>
#include <hip/hip_bf16.h>
#include <stdint.h>

typedef unsigned short u16;
typedef __attribute__((ext_vector_type(8))) short s16x8;
typedef __attribute__((ext_vector_type(4))) short s16x4;
typedef __attribute__((ext_vector_type(4))) float f32x4;
typedef __attribute__((ext_vector_type(16))) float f32x16;
typedef __attribute__((ext_vector_type(4))) unsigned u32x4;
typedef __attribute__((ext_vector_type(2))) unsigned u32x2;
typedef __attribute__((address_space(3))) s16x4* lds_s16x4_p;
typedef const __attribute__((address_space(1))) void* gptr_t;
typedef __attribute__((address_space(3))) void* lptr_t;

constexpr int L = 2048, D = 640, NS = 192, NH = 32;
constexpr int UT_PAD = 32, UT_STRIDE = L + UT_PAD;
constexpr size_t WS_UT   = 0;
constexpr size_t WS_KIMG = WS_UT + (size_t)D * UT_STRIDE * 2;
constexpr size_t WS_PQ   = WS_KIMG + (size_t)D * 6144;
constexpr size_t WS_PART = WS_PQ + (size_t)L * D * 2;
constexpr size_t WS_GATE = WS_PART + (size_t)32 * 16 * 1680;
constexpr size_t WS_WOF  = WS_GATE + (size_t)L * 4;
constexpr size_t WS_KVT  = WS_WOF + (size_t)D * D * 2;
constexpr size_t WS_AT   = WS_KVT + (size_t)32 * 1280;
constexpr size_t WS_CNT  = WS_AT + (size_t)D * L * 2;
constexpr size_t WS_END  = WS_CNT + 4096;
#define STAMP(kid, slot) do {} while (0)

__device__ __forceinline__ float b2f(u16 v) { return __uint_as_float(((unsigned)v) << 16); }
typedef __bf16 bf16x2_t __attribute__((ext_vector_type(2)));
typedef float f32x2 __attribute__((ext_vector_type(2)));
__device__ __forceinline__ unsigned pack2(float a, float b) { f32x2 v = {a, b}; bf16x2_t r = __builtin_convertvector(v, bf16x2_t); return __builtin_bit_cast(unsigned, r); }
__device__ __forceinline__ float lo_f(unsigned u) { return __uint_as_float(u << 16); }
__device__ __forceinline__ float hi_f(unsigned u) { return __uint_as_float(u & 0xffff0000u); }
__device__ __forceinline__ float rb(float f) { return lo_f(pack2(f, f)); }
__device__ __forceinline__ unsigned phi2(float a, float b) {
  unsigned t = pack2(a, b); float t0 = lo_f(t), t1 = hi_f(t);
  float e0 = __builtin_amdgcn_fmed3f(__builtin_amdgcn_exp2f(t0 * 1.44269504f), 0.f, 1.f);
  float e1 = __builtin_amdgcn_fmed3f(__builtin_amdgcn_exp2f(t1 * 1.44269504f), 0.f, 1.f);
  unsigned r = pack2(__builtin_fmaxf(t0, 0.f) + e0, __builtin_fmaxf(t1, 0.f) + e1);
  return pack2(lo_f(r) + 1e-6f, hi_f(r) + 1e-6f);
}
#define VMCNT(n) asm volatile("s_waitcnt vmcnt(" #n ")" ::: "memory")
#define RAW_BARRIER() do { asm volatile("s_waitcnt lgkmcnt(0)" ::: "memory"); __builtin_amdgcn_s_barrier(); asm volatile("" ::: "memory"); } while (0)
__device__ __forceinline__ void glds16(const void* g, void* l) { __builtin_amdgcn_global_load_lds((gptr_t)g, (lptr_t)l, 16, 0, 0); }

#define OPQ_S(v) asm volatile("" : "+s"(v))
typedef f32x2 cf;
__device__ __forceinline__ float smul(float a, float b) { float o; asm("v_mul_f32 %0, %1, %2" : "=v"(o) : "v"(a), "v"(b)); return o; }
__device__ __forceinline__ float sfma(float a, float b, float c) { float o; asm("v_fma_f32 %0, %1, %2, %3" : "=v"(o) : "v"(a), "v"(b), "v"(c)); return o; }
__device__ __forceinline__ float sfnma(float a, float b, float c) { float o; asm("v_fma_f32 %0, -%1, %2, %3" : "=v"(o) : "v"(a), "v"(b), "v"(c)); return o; }
__device__ __forceinline__ float ssub(float a, float b) { float o; asm("v_sub_f32 %0, %1, %2" : "=v"(o) : "v"(a), "v"(b)); return o; }
__device__ __forceinline__ cf cmul(cf a, cf b) {
  cf o;
  asm("v_pk_mul_f32 %0, %1, %2 op_sel:[0,0] op_sel_hi:[0,1]\n\t"
      "v_pk_fma_f32 %0, %1, %2, %0 op_sel:[1,1,0] op_sel_hi:[1,0,1] neg_lo:[1,0,0]" : "=&v"(o) : "v"(a), "v"(b));
  return o;
}

#define CPF 3
constexpr int K1_LDS = 81920;
constexpr int NB_GEMM = 256, NB_GATE = 16, NB_KG = 240;
#define WT 1
__device__ __forceinline__ void st16(void* p, u32x4 v) {
  asm volatile("global_store_dwordx4 %0, %1, off sc1\n\ts_nop 2" :: "v"(p), "v"(v) : "memory");
}

#define BAR_ONLY() do { asm volatile("" ::: "memory"); __builtin_amdgcn_s_barrier(); asm volatile("" ::: "memory"); } while (0)
#define SB() __builtin_amdgcn_sched_barrier(0)
template <bool ISQ>
__device__ __forceinline__ void gemm_role(char* smem, const u16* __restrict__ x, const u16* __restrict__ W, char* __restrict__ ws,
                                          int rowblk, int cb) {
  const int tid = threadIdx.x, lane = tid & 63;
  const int wave = __builtin_amdgcn_readfirstlane(tid >> 6);
  const int row0 = rowblk * 128, col0 = cb * 128;
  const int kvj = cb - 5;
  char* sB = smem; char* sA = smem + 32768;
  const int wr = wave & 1, wc = (wave >> 1) & 1, hf = wave >> 2;
  f32x4 ep[2][4];
  if (wave >= 4) {
    const int lw = wave - 4;
    const char* pA = (const char*)x + (size_t)(row0 + 32 * lw + (lane >> 3)) * 1280 + (((lane & 7) ^ (lane >> 3)) << 4);
    const int fB = (lane >> 4) | ((lw & 1) << 2);
    const int c16 = (((lane & 15) >> 1) ^ fB) * 2 + (lane & 1);
    int bcol = col0 + 8 * c16;
    if (!ISQ) bcol = min(640 + 640 * (c16 >> 3) + ((60 * kvj) & ~7) + 8 * (c16 & 7), 1912);
    const char* pB = (const char*)W + (size_t)(8 * lw + (lane >> 4)) * 3840 + bcol * 2;
    u32x2 tr[8];
    int tro;
    {
      const int fr = lane & 15, g = lane >> 4, p4 = lane & 3, r = 8 * g + (fr >> 2);
      tro = 32768 + r * 128 + (((2 * lw + (p4 >> 1)) ^ (r & 7)) << 4) + (p4 & 1) * 8;
    }
    auto issA = [&](int slot_off, int h) {
      glds16(pA + (16 * h) * 1280, sA + slot_off + (4 * lw + 2 * h) * 1024);
      glds16(pA + (16 * h + 8) * 1280, sA + slot_off + (4 * lw + 2 * h + 1) * 1024);
    };
    auto issB = [&](int slot_off) {
      glds16(pB, sB + slot_off + (2 * lw) * 1024);
      glds16(pB + 4 * 3840, sB + slot_off + (2 * lw + 1) * 1024);
      pB += (size_t)32 * 3840;
    };
    issA(0, 0); issB(0); issA(0, 1); pA += 128;
    issB(8192); issA(16384, 0); issB(16384); issA(16384, 1); pA += 128;
    issB(24576); issA(32768, 0);
    VMCNT(12);
    BAR_ONLY();
    int ao = 32768, bo = 0;
#pragma unroll 1
    for (int j = 0; j < 10; j++) {
      if (j < 8) VMCNT(10); else if (j == 8) VMCNT(8); else VMCNT(0);
      BAR_ONLY();
      if (j < 8) { issB(bo); bo = (bo + 8192) & 32767; issA(ao, 1); pA += 128; }
      ao = ao == 32768 ? 0 : ao + 16384;
      if (j == cb) {
        const int a0 = (int)(size_t)(lptr_t)smem + ao;
        const int a = a0 + tro, a2 = a0 + (tro ^ 64) + 512;
        asm volatile("ds_read_b64_tr_b16 %0, %8\n\tds_read_b64_tr_b16 %1, %9\n\tds_read_b64_tr_b16 %2, %8 offset:4096\n\tds_read_b64_tr_b16 %3, %9 offset:4096\n\t"
                     "ds_read_b64_tr_b16 %4, %8 offset:8192\n\tds_read_b64_tr_b16 %5, %9 offset:8192\n\tds_read_b64_tr_b16 %6, %8 offset:12288\n\tds_read_b64_tr_b16 %7, %9 offset:12288\n\t"
                     "s_waitcnt lgkmcnt(0)"
                     : "=&v"(tr[0]), "=&v"(tr[1]), "=&v"(tr[2]), "=&v"(tr[3]), "=&v"(tr[4]), "=&v"(tr[5]), "=&v"(tr[6]), "=&v"(tr[7])
                     : "v"(a), "v"(a2) : "memory");
      }
      if (j < 8) VMCNT(8); else VMCNT(2);
      BAR_ONLY();
      if (j < 8) { issB(bo); bo = (bo + 8192) & 32767; }
      if (j < 7) issA(ao, 0);
    }
    if (cb < 10) {
      u16* dst = (u16*)(ws + WS_UT) + (size_t)(64 * cb + 16 * lw + (lane & 15)) * UT_STRIDE + 8 * (lane >> 4);
#pragma unroll
      for (int i = 0; i < 4; i++) st16(dst + UT_PAD + row0 + 32 * i, (u32x4){tr[2 * i][0], tr[2 * i][1], tr[2 * i + 1][0], tr[2 * i + 1][1]});
      if (rowblk == 0) st16(dst, (u32x4){0u, 0u, 0u, 0u});
    }
    BAR_ONLY();
    char* xch = smem + (wave & 3) * 8192 + lane * 16;
#pragma unroll
    for (int i = 0; i < 8; i++) ep[i >> 2][i & 3] = *(const f32x4*)(xch + i * 1024);
  } else {
    const int g = lane >> 4, fr = lane & 15;
    f32x4 acc[4][4];
#pragma unroll
    for (int m = 0; m < 4; m++)
#pragma unroll
      for (int c = 0; c < 4; c++) acc[m][c] = (f32x4){0.f, 0.f, 0.f, 0.f};
    const int q4 = fr >> 2, p4 = lane & 3;
    int aoff[2];
#pragma unroll
    for (int ks = 0; ks < 2; ks++) aoff[ks] = (wr * 64 + fr) * 128 + (((4 * ks + g) ^ (fr & 7)) << 4);
    int boff[4];
    {
      const int f = q4 | ((g & 1) << 2);
#pragma unroll
      for (int c = 0; c < 4; c++) boff[c] = (8 * g + q4) * 256 + (((4 * wc + c) ^ f) << 5) + p4 * 8;
    }
    s16x8 af[2][4]; s16x4 bl[4], bh[4];
    auto rdA = [&](int buf, int m, const char* pa) { af[buf][m] = *(const s16x8*)(pa + m * 2048); };
    auto rdBl = [&](int c, const char* pb) { bl[c] = __builtin_amdgcn_ds_read_tr16_b64_v4i16((lds_s16x4_p)(pb + boff[c])); };
    auto rdBh = [&](int c, const char* pb) { bh[c] = __builtin_amdgcn_ds_read_tr16_b64_v4i16((lds_s16x4_p)(pb + boff[c] + 1024)); };
    BAR_ONLY();
    STAMP(0, 2);
#pragma unroll
    for (int c = 0; c < 4; c++) { rdA(0, c, sA + aoff[0]); rdBl(c, sB); rdBh(c, sB); }
    int ao0 = 0, ao1 = 16384;
    auto grp = [&](int buf, int c, int m) {
      const s16x8 bf = __builtin_shufflevector(bl[c], bh[c], 0, 1, 2, 3, 4, 5, 6, 7);
      if (ISQ) acc[m][c] = __builtin_amdgcn_mfma_f32_16x16x32_bf16(bf, af[buf][m], acc[m][c], 0, 0, 0);
      else     acc[m][c] = __builtin_amdgcn_mfma_f32_16x16x32_bf16(af[buf][m], bf, acc[m][c], 0, 0, 0);
    };
#pragma unroll
    for (int c = 0; c < 2; c++)
#pragma unroll
      for (int m = 0; m < 4; m++) grp(0, c, m);
#pragma unroll 1
    for (int j2 = 0; j2 < 5; j2++) {
#pragma unroll
      for (int st = 0; st < 4; st++) {
        const int hs = st & 1;
        SB();
        RAW_BARRIER();
        SB();
        const char* pa = sA + (hs == 0 ? ao0 + aoff[1] : ao1 + aoff[0]);
        const char* pb = sB + ((st + 1) & 3) * 8192;
        grp(hs, 2, 0); SB(); rdA(hs ^ 1, 0, pa); rdA(hs ^ 1, 1, pa); SB();
        grp(hs, 2, 1); SB(); rdA(hs ^ 1, 2, pa); rdA(hs ^ 1, 3, pa); SB();
        grp(hs, 2, 2); SB(); rdBl(0, pb); rdBh(0, pb); SB();
        grp(hs, 2, 3); SB(); rdBl(1, pb); rdBh(1, pb); SB();
        grp(hs, 3, 0); SB(); rdBl(2, pb); rdBh(2, pb); SB();
        grp(hs, 3, 1); grp(hs, 3, 2); grp(hs, 3, 3); SB();
        rdBl(3, pb); rdBh(3, pb); SB();
        if (st < 3 || j2 < 4) {
#pragma unroll
          for (int c = 0; c < 2; c++)
#pragma unroll
            for (int m = 0; m < 4; m++) grp(hs ^ 1, c, m);
        }
        if (hs == 1) { ao0 = ao1; ao1 = ao1 == 32768 ? 0 : ao1 + 16384; }
      }
    }
    STAMP(0, 3);
    int l3 = threadIdx.x; asm volatile("" : "+v"(l3));
    char* xch = smem + (l3 >> 6) * 8192 + (l3 & 63) * 16;
#pragma unroll
    for (int i = 0; i < 8; i++) *(f32x4*)(xch + i * 1024) = acc[2 + (i >> 2)][i & 3];
#pragma unroll
    for (int i = 0; i < 8; i++) ep[i >> 2][i & 3] = acc[i >> 2][i & 3];
    RAW_BARRIER();
  }
  int l2 = threadIdx.x; asm volatile("" : "+v"(l2));
  const int g = (l2 >> 4) & 3, fr = l2 & 15;
  if (ISQ) {
    char* pq0 = ws + WS_PQ; char* pq1 = ws + WS_PQ + (size_t)L * 16 * 32 * 2;
#pragma unroll
    for (int c = 0; c < 4; c++) {
      const int n = col0 + 64 * wc + 16 * c + 4 * g;
      const int hd = n / 20, d0 = n - 20 * hd;
#pragma unroll
      for (int m = 0; m < 2; m++) {
        const int rb64 = rowblk * 2 + wr, r = 16 * m + fr;
        u32x2 o;
        o[0] = phi2(ep[m][c][0], ep[m][c][1]);
        o[1] = phi2(ep[m][c][2], ep[m][c][3]);
        const int fi = (rb64 * 32 + hd) * 2 + hf;
        char* dst = d0 < 16 ? pq0 + ((size_t)fi * 64 + (d0 >> 3) * 32 + r) * 16 + (d0 & 7) * 2 : pq1 + ((size_t)fi * 32 + r) * 8;
        *(u32x2*)dst = o;
      }
    }
  } else {
    const int o = (60 * kvj) & 7, zc = o ? 0 : 60;
    const int lane2 = l2 & 63;
    char* fb = smem + 32768;
#pragma unroll
    for (int c = 0; c < 4; c++) {
      u32x4 a;
      if (wc == 0) {
        a[0] = phi2(ep[0][c][0], ep[0][c][1]); a[1] = phi2(ep[0][c][2], ep[0][c][3]);
        a[2] = phi2(ep[1][c][0], ep[1][c][1]); a[3] = phi2(ep[1][c][2], ep[1][c][3]);
      } else {
        a[0] = pack2(ep[0][c][0], ep[0][c][1]); a[1] = pack2(ep[0][c][2], ep[0][c][3]);
        a[2] = pack2(ep[1][c][0], ep[1][c][1]); a[3] = pack2(ep[1][c][2], ep[1][c][3]);
        if (16 * c + fr == zc) a = (u32x4){0x3F803F80u, 0x3F803F80u, 0x3F803F80u, 0x3F803F80u};
      }
      *(u32x4*)(fb + ((((wc * 4 + wr * 2 + hf) * 4 + c) * 64 + lane2) << 4)) = a;
    }
    RAW_BARRIER();
    const int np = wave < 4 ? 1 : 2;
    for (int i = 0; i < np; i++) {
      const int p = wave < 4 ? wave : wave * 2 - 4 + i;
      int ta, tb;
      if (p < 10) { ta = (int)((0x3322211100ull >> (4 * p)) & 15); tb = (int)((0x3232121010ull >> (4 * p)) & 15); }
      else { ta = (o ? 2 : 0) + (p - 10); tb = o ? 0 : 3; }
      const char* pk = fb + ((ta * 64 + lane2) << 4);
      const char* pv = fb + 16384 + ((tb * 64 + lane2) << 4);
      f32x4 dd = {0.f, 0.f, 0.f, 0.f};
#pragma unroll
      for (int rg = 0; rg < 4; rg++)
        dd = __builtin_amdgcn_mfma_f32_16x16x32_bf16(*(const s16x8*)(pk + rg * 4096), *(const s16x8*)(pv + rg * 4096), dd, 0, 0, 0);
      const int kc0 = 16 * ta + 4 * g - o;
      const int hk = kc0 / 20, dk0 = kc0 - 20 * hk;
      const int vcol = 16 * tb + fr;
      const int dv = vcol == zc ? 20 : vcol - o - 20 * hk;
      if (kc0 >= 0 && kc0 < (kvj == 10 ? 40 : 60) && dv >= 0 && (dv < 20 || vcol == zc))
        *(f32x4*)(ws + WS_PART + (size_t)(((3 * kvj + hk) * 16 + rowblk) * 21 + dv) * 80 + dk0 * 4) = dd;
    }
  }
}

__device__ __forceinline__ void gate_load(unsigned* v, const u16* __restrict__ x, const u16* __restrict__ gw, int row, int lane) {
  const u16* px = x + (size_t)row * D + 2 * lane; const u16* pg = gw + 2 * lane;
#pragma unroll
  for (int i = 0; i < 5; i++) {
    asm volatile("global_load_dword %0, %1, off offset:%2" : "=v"(v[i]) : "v"(px), "i"(256 * i));
    asm volatile("global_load_dword %0, %1, off offset:%2" : "=v"(v[5 + i]) : "v"(pg), "i"(256 * i));
  }
}
__device__ __forceinline__ void gate_finish(unsigned* v, float* __restrict__ gate, int row, int lane) {
  asm volatile("s_waitcnt vmcnt(0)" : "+v"(v[0]), "+v"(v[1]), "+v"(v[2]), "+v"(v[3]), "+v"(v[4]), "+v"(v[5]), "+v"(v[6]), "+v"(v[7]), "+v"(v[8]), "+v"(v[9]) :: "memory");
  float s = 0.f;
#pragma unroll
  for (int i = 0; i < 5; i++) {
    unsigned p = pack2(lo_f(v[i]) * lo_f(v[5 + i]), hi_f(v[i]) * hi_f(v[5 + i]));
    s += lo_f(p) + hi_f(p);
  }
#pragma unroll
  for (int o = 32; o > 0; o >>= 1) s += __shfl_xor(s, o);
  if (lane == 0) { float a = rb(s); gate[row] = rb(1.f / (1.f + __expf(-a))); }
}

__device__ __forceinline__ cf zpow(float th, float tl, float rh, float rl, float p) {
  float e = __builtin_amdgcn_exp2f(th * p); e = fmaf(e, (tl * p) * 0.69314718f, e);
  float t = rh * p; float f = (t - __builtin_rintf(t)) + rl * p;
  cf o; o[0] = e * __builtin_amdgcn_cosf(f); o[1] = e * __builtin_amdgcn_sinf(f); return o;
}
#define PKM(o, a, b) "v_pk_mul_f32 " o ", " a ", " b " op_sel:[0,0] op_sel_hi:[0,1]\n\t"
#define PKF(o, a, b) "v_pk_fma_f32 " o ", " a ", " b ", " o " op_sel:[1,1,0] op_sel_hi:[1,0,1] neg_lo:[1,0,0]\n\t"
__device__ __forceinline__ void cmul4(cf& o0, cf& o1, cf& o2, cf& o3, cf a0, cf b0, cf a1, cf b1, cf a2, cf b2, cf a3, cf b3) {
  asm(PKM("%0", "%4", "%5") PKM("%1", "%6", "%7") PKM("%2", "%8", "%9") PKM("%3", "%10", "%11")
      PKF("%0", "%4", "%5") PKF("%1", "%6", "%7") PKF("%2", "%8", "%9") PKF("%3", "%10", "%11")
      : "=&v"(o0), "=&v"(o1), "=&v"(o2), "=&v"(o3)
      : "v"(a0), "v"(b0), "v"(a1), "v"(b1), "v"(a2), "v"(b2), "v"(a3), "v"(b3));
}
__device__ __forceinline__ void batch4(unsigned* h, cf b, cf t0, cf t1, cf t2, cf t3) {
  cf w0, w1, w2, w3;
  asm(PKM("%0", "%4", "%5") PKM("%1", "%4", "%6") PKM("%2", "%4", "%7") PKM("%3", "%4", "%8")
      PKF("%0", "%4", "%5") PKF("%1", "%4", "%6") PKF("%2", "%4", "%7") PKF("%3", "%4", "%8")
      : "=&v"(w0), "=&v"(w1), "=&v"(w2), "=&v"(w3) : "v"(b), "v"(t0), "v"(t1), "v"(t2), "v"(t3));
  h[0] = pack2(w0[0], w0[1]); h[1] = pack2(w1[0], w1[1]); h[2] = pack2(w2[0], w2[1]); h[3] = pack2(w3[0], w3[1]);
}
__device__ __forceinline__ void batch3(unsigned* h, cf b, cf t1, cf t2, cf t3) {
  cf w1, w2, w3;
  asm(PKM("%0", "%3", "%4") PKM("%1", "%3", "%5") PKM("%2", "%3", "%6")
      PKF("%0", "%3", "%4") PKF("%1", "%3", "%5") PKF("%2", "%3", "%6")
      : "=&v"(w1), "=&v"(w2), "=&v"(w3) : "v"(b), "v"(t1), "v"(t2), "v"(t3));
  h[0] = pack2(b[0], b[1]); h[1] = pack2(w1[0], w1[1]); h[2] = pack2(w2[0], w2[1]); h[3] = pack2(w3[0], w3[1]);
}
constexpr int KG_RS = 288, KG_PL = 16 * KG_RS;
__device__ __forceinline__ void kgen_role(char* smem, const u16* __restrict__ logA, const u16* __restrict__ Aim,
                                          const u16* __restrict__ Cr, const u16* __restrict__ Ci, const u16* __restrict__ logdt,
                                          const u16* __restrict__ x, const u16* __restrict__ W, const u16* __restrict__ wo,
                                          const u16* __restrict__ gw, char* __restrict__ ws, int blk, unsigned pfv) {
  const int tid = threadIdx.x, lane = tid & 63;
  const int wave = __builtin_amdgcn_readfirstlane(tid >> 6);
  constexpr int RS = KG_RS, PL = KG_PL;
  const int task = blk * 8 + wave;
  const int d = task / 3;
  const int pi = task * 64 + lane;
#define PF 1
#define PFM 1
#define PFS0 3
#define PFS1 10
  const char* pp;
  {
    const int xcd = blk & 7, i = (blk >> 3) * 64 + lane;
    const int q = i & 15;
    const int ln = (xcd & 1) ? (q < 8 ? 12 + q : 14 + q) : (q < 13 ? q : 7 + q);
    if (i < 1280) pp = (const char*)W + (size_t)(80 * wave + (i >> 4)) * 3840 + ln * 128;
    else { const int id = wave * 640 + (i - 1280); pp = (const char*)x + (size_t)((xcd >> 1) * 512 + (id & 511)) * 1280 + (id >> 9) * 128; }
  }
  const int pfpt = wave == 0 ? 9 : (wave <= 2 ? 0 : (wave <= 5 ? 1 : 2));
#define PFLOAD(pt) do { __builtin_amdgcn_sched_barrier(0); if (pfpt == (pt)) asm volatile("global_load_dword %0, %1, off" : "+v"(pfv) : "v"(pp)); __builtin_amdgcn_sched_barrier(0); } while (0)
  PFLOAD(0);
  const float dt = rb(expf(b2f(logdt[d])));
  float ar;
  {
    const float la = b2f(logA[pi]);
    float h = la * 1.44269502f; float l = fmaf(la, 1.44269502f, -h) + la * 1.92596299e-8f;
    float e = __builtin_amdgcn_exp2f(h); e = fmaf(e, l * 0.69314718f, e);
    ar = -rb(e) * dt;
  }
  const float ai = b2f(Aim[pi]) * dt;
  cf C; C[0] = 2.f * (b2f(Cr[pi]) * dt); C[1] = 2.f * (b2f(Ci[pi]) * dt);
  const float th = ar * 1.44269502f, tl = fmaf(ar, 1.44269502f, -th) + ar * 1.92596299e-8f;
  const float rh = ai * 0.159154937f, rl = fmaf(ai, 0.159154937f, -rh) + ai * 6.42063833e-9f;
  char* tb = smem + wave * (2 * PL);
  char* wp = tb + lane * 4;
  const char* rp = tb + (lane & 15) * RS + (lane >> 4) * 16;
  STAMP(0, 2);
  PFLOAD(1);
  const cf one = {1.f, 0.f};
  cf S1 = zpow(th, tl, rh, rl, 1.f), Q1 = zpow(th, tl, rh, rl, 8.f), R1 = zpow(th, tl, rh, rl, 128.f);
  Q1[1] = -Q1[1];
  cf S2, Q2, R2, G1, S3, S4, Q3, P1, R3, T1, G2, G3, S5, S6, S7, P2, CT1, T2, P3, CS4, CT2, CT3, CT4, dm0;
  cmul4(S2, Q2, R2, G1, S1, S1, Q1, Q1, R1, R1, C, R1);
  cmul4(S3, S4, Q3, P1, S2, S1, S2, S2, Q2, Q1, Q2, Q2);
  cmul4(R3, T1, G2, P2, R2, R1, R2, R2, C, R2, P1, P1);
  cmul4(S5, S6, S7, P3, S4, S1, S4, S2, S4, S3, P2, P1);
  cmul4(G3, CT1, T2, dm0, C, R3, C, T1, T1, T1, C, R1);
  cmul4(CS4, CT2, CT3, dm0, CT1, S4, C, T2, CT1, T2, C, R1);
  CT4 = cmul(CT2, T2);
  PFLOAD(2);
  unsigned gv[10];
  gate_load(gv, x, gw, task, lane);
  const int wid = min(blk * 216 + (tid < 216 ? tid : 215), 51199);
  unsigned wr_[8];
  {
    const int slot = wid % 160, f = wid / 160;
    const int s5 = (f >> 2) % 5, hb = 8 * (f & 3) + 4 * (((f >> 2) / 5) & 1), cb = f / 40;
    const int m = slot < 128 ? (slot >> 6) * 32 + (slot & 31) : 64 + (slot & 15), h = slot < 128 ? (slot >> 5) & 1 : (slot >> 4) & 1;
    const int n = 80 * cb + m;
    int k0, k1;
    if (s5 < 4) { k0 = 20 * (hb + s5) + 4 * h; k1 = k0 + 8; }
    else { k0 = 20 * (hb + 2 * h) + 16; k1 = k0 + 20; }
    if (wave < 4) {
      const u16* p0 = wo + (size_t)k0 * D + n; const u16* p1 = wo + (size_t)k1 * D + n;
#pragma unroll
      for (int e = 0; e < 4; e++) {
        asm volatile("global_load_ushort %0, %1, off offset:%2" : "=v"(wr_[e]) : "v"(p0), "i"(e * 1280));
        asm volatile("global_load_ushort %0, %1, off offset:%2" : "=v"(wr_[4 + e]) : "v"(p1), "i"(e * 1280));
      }
    }
  }
  auto rows4 = [&](int pl, int row, int rs, cf b, cf t0, cf t1, cf t2, cf t3) {
    unsigned h[4];
    batch4(h, b, t0, t1, t2, t3);
#pragma unroll
    for (int k = 0; k < 4; k++) *(unsigned*)(wp + pl * PL + (row + k * rs) * RS) = h[k];
  };
  auto rows3 = [&](int pl, int row, int rs, cf b, cf t1, cf t2, cf t3) {
    unsigned h[4];
    batch3(h, b, t1, t2, t3);
#pragma unroll
    for (int k = 0; k < 4; k++) *(unsigned*)(wp + pl * PL + (row + k * rs) * RS) = h[k];
  };
  *(unsigned*)(wp) = 0x00003F80u;
  *(unsigned*)(wp + RS) = pack2(Q1[0], Q1[1]); *(unsigned*)(wp + 2 * RS) = pack2(Q2[0], Q2[1]); *(unsigned*)(wp + 3 * RS) = pack2(Q3[0], Q3[1]);
  rows3(0, 4, 1, P1, Q1, Q2, Q3); rows3(0, 8, 1, P2, Q1, Q2, Q3); rows3(0, 12, 1, P3, Q1, Q2, Q3);
  rows3(1, 0, 1, C, S1, S2, S3); rows4(1, 4, 1, C, S4, S5, S6, S7); rows3(1, 8, 1, G1, S1, S2, S3); rows4(1, 12, 1, G1, S4, S5, S6, S7);
  __builtin_amdgcn_wave_barrier();
  s16x8 bh[4];
  f32x4 res[3];
#pragma unroll
  for (int k = 0; k < 4; k++) bh[k] = *(const s16x8*)(rp + k * 64);
  auto mm = [&](int t, int pl) {
    f32x4 a = {0.f, 0.f, 0.f, 0.f};
#pragma unroll
    for (int k = 0; k < 4; k++) a = __builtin_amdgcn_mfma_f32_16x16x32_bf16(*(const s16x8*)(rp + pl * PL + k * 64), bh[k], a, 0, 0, 0);
    res[t] = a;
  };
  mm(0, 1);
  __builtin_amdgcn_wave_barrier();
  STAMP(0, 3);
  rows3(0, 0, 1, G2, S2, S4, S6); rows3(0, 4, 1, G3, S2, S4, S6); rows3(0, 8, 2, CT1, R1, R2, R3); rows3(0, 9, 2, CS4, R1, R2, R3);
  rows3(1, 0, 1, CT2, R1, R2, R3); rows3(1, 4, 1, CT3, R1, R2, R3);
  *(unsigned*)(wp + PL + 8 * RS) = pack2(CT4[0], CT4[1]);
  __builtin_amdgcn_wave_barrier();
  mm(1, 0); mm(2, 1);
  __builtin_amdgcn_wave_barrier();
  STAMP(0, 4);
  if (wave < 4) {
    asm volatile("s_waitcnt vmcnt(0)" : "+v"(wr_[0]), "+v"(wr_[1]), "+v"(wr_[2]), "+v"(wr_[3]), "+v"(wr_[4]), "+v"(wr_[5]), "+v"(wr_[6]), "+v"(wr_[7]) :: "memory");
    u32x4 wv = {wr_[0] | (wr_[1] << 16), wr_[2] | (wr_[3] << 16), wr_[4] | (wr_[5] << 16), wr_[6] | (wr_[7] << 16)};
    if (tid < 216 && blk * 216 + tid < 51200) st16(ws + WS_WOF + (size_t)wid * 16, wv);
  }
  gate_finish(gv, (float*)(ws + WS_GATE), task, lane);
#pragma unroll
  for (int t = 0; t < 3; t++) *(f32x4*)(tb + t * 1024 + lane * 16) = res[t];
  __syncthreads();
  {
    const int d0 = (blk * 8) / 3;
#pragma unroll
    for (int it = 0; it < 2; it++) {
      const int e = tid + 512 * it;
      const int seg = e / 192, ent = e - 192 * seg;
      const int ds = d0 + seg;
      const int wlo = max(0, 3 * ds - 8 * blk), whi = min(8, 3 * ds + 3 - 8 * blk);
      if (seg < 4 && wlo < 8) {
        f32x4 a = *(const f32x4*)(smem + wlo * (2 * PL) + ent * 16);
        for (int w = wlo + 1; w < whi; w++) a += *(const f32x4*)(smem + w * (2 * PL) + ent * 16);
        st16(ws + WS_KIMG + (size_t)ds * 6144 + (3 * ds < 8 * blk ? 3072 : 0) + ent * 16, __builtin_bit_cast(u32x4, a));
      }
    }
  }
  asm volatile("s_waitcnt vmcnt(0)" :: "v"(pfv) : "memory");
}

__global__ __launch_bounds__(512, 4) void k1(const u16* __restrict__ x, const u16* __restrict__ logA, const u16* __restrict__ Aim,
                                          const u16* __restrict__ Cr, const u16* __restrict__ Ci, const u16* __restrict__ logdt,
                                          const u16* __restrict__ wqkv, const u16* __restrict__ gw, const u16* __restrict__ wo,
                                          char* __restrict__ ws) {
  __shared__ __attribute__((aligned(16))) char smem[K1_LDS];
  int b = blockIdx.x;
  unsigned cpf = 0;
  if (b < 32 && threadIdx.x >= 192) {
    const char* pc; int len;
    asm volatile("s_getpc_b64 %0\n\ts_mov_b32 %1, code_tail_mark-_Z2k1PKtS0_S0_S0_S0_S0_S0_S0_S0_Pc-128" : "=s"(pc), "=s"(len));
    const int off = min((int)(threadIdx.x - 192) * 128, len);
    asm volatile("global_load_dword %0, %1, %2" : "+v"(cpf) : "v"(off), "s"(pc));
  }
  STAMP(0, 0);
  b = b < NB_KG ? b + NB_GEMM : (b < NB_KG + NB_GEMM ? b - NB_KG : b);
  if (b < NB_GEMM) {
    const int xcd = b & 7, idx = b >> 3;
    const int rowblk = (xcd >> 1) * 4 + (idx & 3), cb = (xcd & 1) * 8 + (idx >> 2);
    if (cb < 5) gemm_role<true>(smem, x, wqkv, ws, rowblk, cb);
    else        gemm_role<false>(smem, x, wqkv, ws, rowblk, cb);
    STAMP(0, 1);
    return;
  }
  b -= NB_GEMM;
  if (b < NB_KG) { kgen_role(smem, logA, Aim, Cr, Ci, logdt, x, wqkv, wo, gw, ws, b, cpf); STAMP(0, 1); return; }
  b -= NB_KG;
  if (b == 0) ((unsigned*)(ws + WS_CNT))[threadIdx.x] = 0u, ((unsigned*)(ws + WS_CNT))[threadIdx.x + 512] = 0u;
  {
    unsigned gv[10];
    const int row = NB_KG * 8 + b * 8 + (threadIdx.x >> 6);
    gate_load(gv, x, gw, row, threadIdx.x & 63);
    gate_finish(gv, (float*)(ws + WS_GATE), row, threadIdx.x & 63);
  }
  STAMP(0, 1);
}

constexpr int K2_LDS = 163840;
constexpr int K2_CS = 4160;
constexpr int K2_KOFF = 4 * K2_CS;
constexpr int K2_CH = K2_KOFF + 4608;
constexpr int K2_CONV = K2_LDS - 3 * K2_CH;
constexpr int K2_TAB = 81920;
constexpr int K2_FIN = K2_TAB + 9216;
constexpr int K2_STG = 114688;
constexpr int NB_KV = 32;
typedef __attribute__((address_space(1))) unsigned gu32;
__device__ __forceinline__ const char* uni(const char* p) {
  size_t v = (size_t)p;
  unsigned lo = __builtin_amdgcn_readfirstlane((unsigned)v), hi = __builtin_amdgcn_readfirstlane((unsigned)(v >> 32));
  size_t o = ((size_t)hi << 32) | lo;
  asm volatile("s_nop 4" : "+s"(o));
  return (const char*)o;
}
__device__ __forceinline__ void wait_count(char* p, unsigned target) {
  gu32* c = (gu32*)(size_t)p;
  for (int n = 0; n < (1 << 22); n++) {
    if (__hip_atomic_load(c, __ATOMIC_RELAXED, __HIP_MEMORY_SCOPE_AGENT) >= target) break;
    __builtin_amdgcn_s_sleep(8);
  }
}
#define GLD4(dst, voff, sbase, pol) asm volatile("global_load_dwordx4 %0, %1, %2" pol : "=v"(dst) : "v"(voff), "s"(sbase))
#define GLD2(dst, voff, sbase, pol) asm volatile("global_load_dwordx2 %0, %1, %2" pol : "=v"(dst) : "v"(voff), "s"(sbase))
#define GLD1(dst, voff, sbase, pol) asm volatile("global_load_dword %0, %1, %2" pol : "=v"(dst) : "v"(voff), "s"(sbase))
template <int G>
__device__ __forceinline__ void conv_unit(f32x4* acc, int ap, int bp) {
  constexpr int NT = 8 - G;
  u32x2 a0[NT], a1[NT]; s16x8 bh[NT];
#pragma unroll
  for (int j = 0; j < NT; j++)
    asm volatile("ds_read_b64 %0, %3 offset:%5\n\tds_read_b64 %1, %3 offset:%6\n\tds_read_b128 %2, %4 offset:%5"
                 : "=&v"(a0[j]), "=&v"(a1[j]), "=&v"(bh[j]) : "v"(ap), "v"(bp), "n"(512 * j), "n"(512 * j + 8) : "memory");
#pragma unroll
  for (int x = 0; x < NT; x++) {
    asm volatile("s_waitcnt lgkmcnt(%3)" : "+v"(a0[x]), "+v"(a1[x]), "+v"(bh[x]) : "n"(3 * (NT - 1 - x) > 15 ? 15 : 3 * (NT - 1 - x)) : "memory");
#pragma unroll
    for (int y = 0; y <= x; y++)
#pragma unroll
      for (int sw = 0; sw < 2; sw++) {
        if (sw && y == x) continue;
        const int j = sw ? y : x, m = sw ? x : y, T = j + m;
        const int ti = G == 0 ? (T == 0 ? 0 : T == 3 ? 1 : T == 4 ? 2 : T == 7 ? 3 : -1) : (T == 1 ? 0 : T == 2 ? 1 : T == 5 ? 2 : T == 6 ? 3 : -1);
        if (ti >= 0) acc[ti] = __builtin_amdgcn_mfma_f32_16x16x32_bf16(__builtin_bit_cast(s16x8, (u32x4){a0[j][0], a0[j][1], a1[j][0], a1[j][1]}), bh[m], acc[ti], 0, 0, 0);
      }
    SB();
  }
}

__global__ __launch_bounds__(512, 2) void k2(const u16* __restrict__ Dskip, char* __restrict__ ws, u16* __restrict__ out) {
  __shared__ __attribute__((aligned(16))) char smem[K2_LDS];
  const int tid = threadIdx.x, lane = tid & 63;
  const int wave = __builtin_amdgcn_readfirstlane(tid >> 6);
  const int b = blockIdx.x;
  unsigned cpf = 0;
  if (b < 32 && tid < 192) {
    const char* pc; int len;
    asm volatile("s_getpc_b64 %0\n\ts_mov_b32 %1, code_tail_mark-_Z2k2PKtPcPt-128" : "=s"(pc), "=s"(len));
    const int off = min(tid * 128, len);
    asm volatile("global_load_dword %0, %1, %2" : "+v"(cpf) : "v"(off), "s"(pc));
  }
  STAMP(1, 0);
  const int lds0 = (int)(size_t)(lptr_t)smem;
  const int l16 = lane * 16;
  const int odd = b & 1;
  const int chA = 5 * (b >> 1) + 3 * odd, chH = 5 * (b >> 1) + 2;
  const int xcd = b & 7, idx = b >> 3;
  const int rowblk = xcd * 4 + (idx >> 3), cb = idx & 7;
  const int l0 = rowblk * 64, c0 = cb * 80;
  const int t = wave & 1, kh = wave >> 1;
  const int r = lane & 31, h = lane >> 5;
  const bool kvw = b < NB_KV && wave >= 6;
  f32x4 kvp[16];
  if (kvw) {
    const char* src = ws + WS_PART + (size_t)b * 16 * 1680;
    const int o = min(tid - 384, 104) * 16;
#pragma unroll
    for (int i = 0; i < 16; i++) GLD4(kvp[i], o, src + i * 1680, "");
  }
  u32x4 in0[3], in1[3];
  int dsto[3];
#pragma unroll
  for (int rr = 0; rr < 3; rr++) {
    const int id = wave + 8 * rr;
    int o0, o1;
    if (id < 13) {
      const int i = min(64 * id + lane, 776);
      const int ch = i / 259, xx = i - 259 * ch;
      const int base = (int)WS_UT + (ch == 2 ? chH : chA + ch) * (UT_STRIDE * 2);
      o0 = base + min(xx + 1, 259) * 16; o1 = base + min(xx + 2, 259) * 16;
      dsto[rr] = K2_CONV + ch * K2_CH + xx * 16;
    } else {
      const int k = min(id - 13, 8), ch = k / 3;
      o0 = (int)WS_KIMG + (ch == 2 ? chH : chA + ch) * 6144 + (k - 3 * ch) * 1024 + l16; o1 = o0 + 3072;
      dsto[rr] = K2_TAB + k * 1024 + l16;
    }
    GLD4(in0[rr], o0, ws, ""); GLD4(in1[rr], o1, ws, "");
  }
  const int jg = (wave >> 1) & 1;
  const int fo = (lane & 15) * 16 + (lane >> 4) * 4;
  int ft[3];
  ft[0] = (wave & 1) ? 4 + jg : jg;
  ft[1] = (wave & 1) ? 7 - jg : 3 - jg;
  { const int w3 = wave & 3; ft[2] = (w3 & 2) * 2 + ((w3 & 1) ? 3 - odd : odd); }
  u32x2 us[3];
  {
    const int oa = (int)WS_UT + (chA + (wave >> 2)) * (UT_STRIDE * 2) + (UT_PAD + fo) * 2;
    const int oh = (int)WS_UT + chH * (UT_STRIDE * 2) + (UT_PAD + fo) * 2;
    GLD2(us[0], oa + ft[0] * 512, ws, ""); GLD2(us[1], oa + ft[1] * 512, ws, ""); GLD2(us[2], oh + ft[2] * 512, ws, "");
  }
  float g_r;
  GLD1(g_r, (int)WS_GATE + (l0 + 32 * t + r) * 4, ws, "");
  unsigned dsA, dsH;
  asm volatile("global_load_ushort %0, %1, %2" : "=v"(dsA) : "v"((chA + (wave >> 2)) * 2), "s"(Dskip));
  asm volatile("global_load_ushort %0, %1, %2" : "=v"(dsH) : "v"(chH * 2), "s"(Dskip));
  const char* wsrc = ws + WS_WOF + (size_t)cb * 102400;
  auto dma1 = [&](int p) {
    asm volatile("s_mov_b32 m0, %2\n\ts_nop 0\n\tglobal_load_lds_dwordx4 %0, %1" :: "v"(l16), "s"(wsrc + p * 1024), "s"(lds0 + p * 1024) : "memory", "m0");
  };
  auto dma4 = [&](int p) {
    asm volatile("s_mov_b32 m0, %2\n\ts_nop 0\n\tglobal_load_lds_dwordx4 %0, %1\n\tglobal_load_lds_dwordx4 %0, %1 offset:1024\n\t"
                 "global_load_lds_dwordx4 %0, %1 offset:2048\n\tglobal_load_lds_dwordx4 %0, %1 offset:3072"
                 :: "v"(l16), "s"(wsrc + p * 1024), "s"(lds0 + p * 1024) : "memory", "m0");
  };
  u32x4 pf0[8]; u32x2 pf1[8];
  auto bulk = [&]() {
    dma4(10 * wave); dma4(10 * wave + 4); dma1(10 * wave + 8); dma1(10 * wave + 9);
    const char* p0 = ws + WS_PQ + (size_t)((rowblk * 32 + kh * 8) * 2 + t) * 1024;
    const char* p1 = ws + WS_PQ + (size_t)L * 16 * 32 * 2 + (size_t)((rowblk * 32 + kh * 8) * 2 + t) * 256;
    const int r8 = r * 8;
#pragma unroll
    for (int i = 0; i < 8; i++) { GLD4(pf0[i], l16, p0 + i * 2048, ""); GLD2(pf1[i], r8, p1 + i * 512, ""); }
  };
  int tix[8];
#pragma unroll
  for (int e = 0; e < 8; e++) {
    const int j = (tid & 255) * 8 + 7 - e;
    const int p = (int)rb((float)j);
    const int hh = p >> 7, c = (p >> 3) & 15;
    int sh = 31 - __builtin_clz(hh | 1); sh = sh > 3 ? 3 : sh;
    const int row = 8 * sh + (((hh << 3) | (p & 7)) >> sh);
    tix[e] = K2_TAB + (tid >> 8) * 3072 + (((row >> 2) * 16 + c) * 4 + (row & 3)) * 4;
  }
  if (tid < 96) *(u32x4*)(smem + K2_CONV + (tid >> 5) * K2_CH + K2_KOFF + (tid & 31) * 16) = (u32x4){0u, 0u, 0u, 0u};
  asm volatile("s_waitcnt vmcnt(6)" : "+v"(in0[0]), "+v"(in1[0]), "+v"(in0[1]), "+v"(in1[1]), "+v"(in0[2]), "+v"(in1[2]), "+v"(cpf) :: "memory");
  STAMP(1, 2);
#pragma unroll
  for (int rr = 0; rr < 3; rr++) {
    const int id = wave + 8 * rr;
    char* p = smem + dsto[rr];
    if (id < 13) {
      const u32x4 a = in0[rr], bb = in1[rr];
      unsigned w[6] = {a[0], a[1], a[2], a[3], bb[0], bb[1]};
      unsigned o[5];
#pragma unroll
      for (int k = 0; k < 5; k++) o[k] = __builtin_amdgcn_alignbyte(w[k + 1], w[k], 2);
#pragma unroll
      for (int q = 0; q < 2; q++) {
        *(u32x4*)(p + (2 * q) * K2_CS) = (u32x4){o[q], o[q + 1], o[q + 2], o[q + 3]};
        *(u32x4*)(p + (2 * q + 1) * K2_CS) = (u32x4){w[q + 1], w[q + 2], w[q + 3], w[q + 4]};
      }
    } else if (id < 22) {
      const int ch = (id - 13) / 3, d = ch == 2 ? chH : chA + ch;
      f32x4 v = __builtin_bit_cast(f32x4, in0[rr]);
      if ((d & 7) == 2 || (d & 7) == 5) v += __builtin_bit_cast(f32x4, in1[rr]);
      *(f32x4*)p = v;
    }
  }
  if (kvw) {
    asm volatile("" : "+v"(kvp[0]), "+v"(kvp[1]), "+v"(kvp[2]), "+v"(kvp[3]), "+v"(kvp[4]), "+v"(kvp[5]), "+v"(kvp[6]), "+v"(kvp[7]));
    asm volatile("" : "+v"(kvp[8]), "+v"(kvp[9]), "+v"(kvp[10]), "+v"(kvp[11]), "+v"(kvp[12]), "+v"(kvp[13]), "+v"(kvp[14]), "+v"(kvp[15]));
#pragma unroll
    for (int st = 1; st < 16; st <<= 1)
#pragma unroll
      for (int i = 0; i < 16; i += 2 * st) kvp[i] += kvp[i + st];
    if (tid - 384 < 105) *(f32x4*)(smem + K2_FIN + (tid - 384) * 16) = (f32x4){rb(kvp[0][0]), rb(kvp[0][1]), rb(kvp[0][2]), rb(kvp[0][3])};
  }
  if (!(kvw && wave == 7)) bulk();
  RAW_BARRIER();
  if (kvw && wave == 7) {
    const float* fin = (const float*)(smem + K2_FIN);
    const int m = lane & 31;
    int col = m < 16 ? m : ((m >> 2) == ((b & 2) ? 5 : 4) ? 16 + (m & 3) : 20);
    u32x4 o;
#pragma unroll
    for (int e = 0; e < 4; e++) o[e] = pack2(fin[col * 20 + 8 * h + 2 * e], fin[col * 20 + 8 * h + 2 * e + 1]);
    st16(ws + WS_KVT + (size_t)b * 1280 + lane * 16, o);
    const int m0 = 2 * (lane & 15), m1 = m0 + 1;
    int ca = m0 < 16 ? m0 : ((m0 >> 2) == ((b & 2) ? 5 : 4) ? 16 + (m0 & 3) : 20);
    int cc = m1 < 16 ? m1 : ((m1 >> 2) == ((b & 2) ? 5 : 4) ? 16 + (m1 & 3) : 20);
    u32x4 o2 = {pack2(fin[ca * 20 + 16], fin[ca * 20 + 17]), pack2(fin[ca * 20 + 18], fin[ca * 20 + 19]),
                pack2(fin[cc * 20 + 16], fin[cc * 20 + 17]), pack2(fin[cc * 20 + 18], fin[cc * 20 + 19])};
    if (lane < 16) st16(ws + WS_KVT + (size_t)b * 1280 + 1024 + lane * 16, o2);
  }
  {
    u32x4 hi;
#pragma unroll
    for (int e = 0; e < 4; e++) hi[e] = pack2(*(const float*)(smem + tix[2 * e]), *(const float*)(smem + tix[2 * e + 1]));
    *(u32x4*)(smem + K2_CONV + (tid >> 8) * K2_CH + K2_KOFF + 512 + (tid & 255) * 16) = hi;
    if (wave < 4) {
#pragma unroll
      for (int e = 0; e < 4; e++) hi[e] = pack2(*(const float*)(smem + tix[2 * e] + 6144), *(const float*)(smem + tix[2 * e + 1] + 6144));
      *(u32x4*)(smem + K2_CONV + 2 * K2_CH + K2_KOFF + 512 + tid * 16) = hi;
    }
  }
  if (kvw && wave == 7) {
    VMCNT(0);
    if (lane < 8) __hip_atomic_fetch_add((gu32*)(size_t)(ws + WS_CNT + lane * 128), 1u, __ATOMIC_RELAXED, __HIP_MEMORY_SCOPE_AGENT);
    bulk();
  }
  RAW_BARRIER();
  dma1(80 + wave); dma1(88 + wave); dma1(96);
  STAMP(1, 3);
  f32x4 ca[4], chh[4];
  unsigned pollv = 0;
#pragma unroll
  for (int i = 0; i < 4; i++) { ca[i] = (f32x4){0.f, 0.f, 0.f, 0.f}; chh[i] = (f32x4){0.f, 0.f, 0.f, 0.f}; }
  {
    const int i = lane & 15, kg = lane >> 4, e = kg >> 1, bb = kg & 1;
    const int ap = lds0 + K2_CONV + (i & 3) * K2_CS + (2 * e - bb + 2) * 16 + (i >> 2) * 8;
    const int bp = lds0 + K2_CONV + K2_KOFF + (16 + i - e) * 32 + bb * 16;
    const int apj = ap + (wave >> 2) * K2_CH + (wave & 1) * 256;
    const int bpj = bp + (wave >> 2) * K2_CH - (wave & 1) * 256;
    if (jg == 0) {
#pragma unroll 1
      for (int it = 0; it < 4; it++) conv_unit<0>(ca, apj + it * 64, bpj - it * 64);
    } else {
#pragma unroll 1
      for (int it = 0; it < 4; it++) conv_unit<1>(ca, apj + it * 64, bpj - it * 64);
    }
    GLD1(pollv, (int)WS_CNT + xcd * 128, ws, " sc1");
    if (odd == 0) conv_unit<0>(chh, ap + 2 * K2_CH + wave * 64, bp + 2 * K2_CH - wave * 64);
    else          conv_unit<1>(chh, ap + 2 * K2_CH + wave * 64, bp + 2 * K2_CH - wave * 64);
  }
  STAMP(1, 4);
  asm volatile("s_waitcnt vmcnt(0)" : "+v"(pollv) :: "memory");
  if (__builtin_amdgcn_readfirstlane(pollv) < (unsigned)NB_KV) wait_count(ws + WS_CNT + xcd * 128, NB_KV);
  asm volatile("" ::: "memory");
  u32x4 ka[8]; u32x2 kb[8];
  {
    const char* kp = ws + WS_KVT + (size_t)kh * 8 * 1280;
    const int r8 = r * 8;
#pragma unroll
    for (int i = 0; i < 8; i++) { GLD4(ka[i], l16, kp + i * 1280, " sc1"); GLD2(kb[i], r8, kp + i * 1280 + 1024, " sc1"); }
  }
  RAW_BARRIER();
  dma1(97 + (wave < 2 ? wave : 2));
  {
    if (tid == 0) *(unsigned*)(smem + K2_STG - 16) = 0u;
    char* sp = smem + K2_STG + l16;
    *(f32x4*)(sp + (wave * 2) * 1024) = (wave & 1) ? ca[0] : ca[2];
    *(f32x4*)(sp + (wave * 2 + 1) * 1024) = (wave & 1) ? ca[1] : ca[3];
#pragma unroll
    for (int i = 0; i < 4; i++) *(f32x4*)(sp + 16384 + (i * 8 + wave) * 1024) = chh[i];
  }
  RAW_BARRIER();
  {
    const char* sp = smem + K2_STG + l16;
    f32x4 y[3];
    y[0] = ((wave & 1) ? ca[2] : ca[0]) + *(const f32x4*)(sp + ((wave ^ 1) * 2) * 1024);
    y[1] = ((wave & 1) ? ca[3] : ca[1]) + *(const f32x4*)(sp + ((wave ^ 1) * 2 + 1) * 1024);
    asm volatile("s_waitcnt vmcnt(17)" : "+v"(us[0]), "+v"(us[1]), "+v"(us[2]), "+v"(g_r), "+v"(dsA), "+v"(dsH) :: "memory");
    const float dskA = lo_f(dsA), dskH = lo_f(dsH);
    auto fin = [&](f32x4 s, u32x2 uu, float dsk, int ch, int tile) {
      u32x2 ov;
#pragma unroll
      for (int e = 0; e < 2; e++) {
        unsigned yy = pack2(s[2 * e], s[2 * e + 1]);
        unsigned k = pack2(lo_f(uu[e]) * dsk, hi_f(uu[e]) * dsk);
        ov[e] = pack2(lo_f(yy) + lo_f(k), hi_f(yy) + hi_f(k));
      }
      const int o = (int)WS_AT + (ch * L + tile * 256 + fo) * 2;
      asm volatile("global_store_dwordx2 %0, %1, %2 sc1\n\ts_nop 1" :: "v"(o), "v"(ov), "s"(ws) : "memory");
    };
    fin(y[0], us[0], dskA, chA + (wave >> 2), ft[0]);
    fin(y[1], us[1], dskA, chA + (wave >> 2), ft[1]);
    if (wave < 4) {
      const char* hp = sp + 16384 + wave * 8192;
      f32x4 v[8];
#pragma unroll
      for (int i = 0; i < 8; i++) v[i] = *(const f32x4*)(hp + i * 1024);
      y[2] = ((v[0] + v[1]) + (v[2] + v[3])) + ((v[4] + v[5]) + (v[6] + v[7]));
      fin(y[2], us[2], dskH, chH, ft[2]);
      VMCNT(3);
    } else {
      VMCNT(2);
    }
  }
#pragma unroll
  for (int i = 0; i < 8; i++) asm volatile("" : "+v"(pf0[i]), "+v"(pf1[i]), "+v"(ka[i]), "+v"(kb[i]));
  RAW_BARRIER();
  STAMP(1, 5);
  f32x16 acc[3], c[2];
#pragma unroll
  for (int q = 0; q < 3; q++)
#pragma unroll
    for (int i = 0; i < 16; i++) acc[q][i] = 0.f;
  u32x4 rm = {0u, 0u, 0u, 0u};
  const char* wl = smem + kh * 2560 + l16;
  const char* wl2 = smem + kh * 2560 + 2048 + (h * 16 + (lane & 15)) * 16;
  auto stageA = [&](int i) {
    u32x2 b1u = {h ? 0u : pf1[i][0], h ? 0u : pf1[i][1]};
    f32x16 z;
#pragma unroll
    for (int q = 0; q < 16; q++) z[q] = 0.f;
    z = __builtin_amdgcn_mfma_f32_32x32x16_bf16(__builtin_bit_cast(s16x8, ka[i]), __builtin_bit_cast(s16x8, pf0[i]), z, 0, 0, 0);
    c[i & 1] = __builtin_amdgcn_mfma_f32_32x32x8bf16_1k(__builtin_bit_cast(s16x4, kb[i]), __builtin_bit_cast(s16x4, b1u), z, 0, 0, 0);
    asm volatile("" : "+v"(c[i & 1]));
  };
  u32x4 av[2];
  stageA(0);
#pragma unroll
  for (int i = 0; i < 8; i++) {
    const int j = i & 3, G = i >> 2;
    const int wo_ = G * 51200 + j * 10240;
    s16x8 w0 = *(const s16x8*)(wl + wo_), w1 = *(const s16x8*)(wl + wo_ + 1024), w2 = *(const s16x8*)(wl2 + wo_);
    if (i + 1 < 8) stageA(i + 1);
    const f32x16 cc = c[i & 1];
    const float den = rb(rb(cc[12]) + 1e-6f);
    const float inv = __builtin_amdgcn_rcpf(den);
    unsigned o[6];
#pragma unroll
    for (int q = 0; q < 6; q++) o[q] = pack2(cc[2 * q] * inv, cc[2 * q + 1] * inv);
    asm volatile("" : "+v"(o[4]), "+v"(o[5]));
    const bool keep = (j >> 1) == h;
    rm[2 * (j & 1)] = keep ? o[4] : rm[2 * (j & 1)]; rm[2 * (j & 1) + 1] = keep ? o[5] : rm[2 * (j & 1) + 1];
    u32x4 ob = {o[0], o[1], o[2], o[3]};
    acc[0] = __builtin_amdgcn_mfma_f32_32x32x16_bf16(w0, __builtin_bit_cast(s16x8, ob), acc[0], 0, 0, 0);
    acc[1] = __builtin_amdgcn_mfma_f32_32x32x16_bf16(w1, __builtin_bit_cast(s16x8, ob), acc[1], 0, 0, 0);
    acc[2] = __builtin_amdgcn_mfma_f32_32x32x16_bf16(w2, __builtin_bit_cast(s16x8, ob), acc[2], 0, 0, 0);
    if (j == 3) {
      const int wr_ = G * 51200 + 40960;
      s16x8 w3 = *(const s16x8*)(wl + wr_), w4 = *(const s16x8*)(wl + wr_ + 1024), w5 = *(const s16x8*)(wl2 + wr_);
      acc[0] = __builtin_amdgcn_mfma_f32_32x32x16_bf16(w3, __builtin_bit_cast(s16x8, rm), acc[0], 0, 0, 0);
      acc[1] = __builtin_amdgcn_mfma_f32_32x32x16_bf16(w4, __builtin_bit_cast(s16x8, rm), acc[1], 0, 0, 0);
      acc[2] = __builtin_amdgcn_mfma_f32_32x32x16_bf16(w5, __builtin_bit_cast(s16x8, rm), acc[2], 0, 0, 0);
    }
    if (i == 1) {
      VMCNT(0);
      unsigned old = 0;
      if (lane == 0) old = __hip_atomic_fetch_add((unsigned*)(smem + K2_STG - 16), 1u, __ATOMIC_RELAXED, __HIP_MEMORY_SCOPE_WORKGROUP);
      if (lane == 0 && old == 7u) __hip_atomic_fetch_add((gu32*)(size_t)(ws + WS_CNT + (8 + (b >> 5)) * 128), 1u, __ATOMIC_RELAXED, __HIP_MEMORY_SCOPE_AGENT);
    }
    if (i == 3) { RAW_BARRIER(); }
#define POLL_I 5
    if (i == POLL_I) GLD1(pollv, (int)WS_CNT + (8 + cb) * 128, ws, " sc1");
    if (i == POLL_I + 2) {
      asm volatile("s_waitcnt vmcnt(0)" : "+v"(pollv) :: "memory");
      if (__builtin_amdgcn_readfirstlane(pollv) < 32u) wait_count(ws + WS_CNT + (8 + cb) * 128, 32);
      asm volatile("" ::: "memory");
      const int o = (int)WS_AT + ((c0 + (tid >> 3)) * L + l0 + 8 * (tid & 7)) * 2;
      GLD4(av[0], o, ws, " sc1"); GLD4(av[1], o + (tid < 128 ? 64 * L * 2 : 0), ws, " sc1");
    }
  }
  STAMP(1, 6);
  {
    char* sp = smem + 102400 + t * 30720 + l16;
#pragma unroll
    for (int g = 0; g < 10; g++) {
      const int own = g & 3;
      f32x4 v = {acc[g >> 2][4 * (g & 3)], acc[g >> 2][4 * (g & 3) + 1], acc[g >> 2][4 * (g & 3) + 2], acc[g >> 2][4 * (g & 3) + 3]};
      if (kh != own) *(f32x4*)(sp + (g * 3 + ((kh - own - 1) & 3)) * 1024) = v;
    }
    asm volatile("s_waitcnt vmcnt(0)" : "+v"(av[0]), "+v"(av[1]) :: "memory");
    *(u32x4*)(smem + (tid >> 3) * 192 + (tid & 7) * 16) = av[0];
    if (tid < 128) *(u32x4*)(smem + (64 + (tid >> 3)) * 192 + (tid & 7) * 16) = av[1];
  }
  RAW_BARRIER();
  STAMP(1, 7);
  {
    const char* sp = smem + 102400 + t * 30720 + l16;
    const float g1 = rb(1.f - g_r);
    const char* atp = smem + (8 * kh + 4 * h + ((lane & 15) >> 2)) * 192 + (32 * t + (lane & 16) + 4 * (lane & 3)) * 2;
    u16* op = out + (size_t)(l0 + 32 * t + r) * D + c0 + 8 * kh + 4 * h;
#pragma unroll
    for (int tl = 0; tl < 3; tl++) {
      if (tl == 2 && kh >= 2) break;
      f32x4 m;
      if (kh == 0)      m = (f32x4){acc[tl][0], acc[tl][1], acc[tl][2], acc[tl][3]};
      else if (kh == 1) m = (f32x4){acc[tl][4], acc[tl][5], acc[tl][6], acc[tl][7]};
      else if (kh == 2) m = (f32x4){acc[tl][8], acc[tl][9], acc[tl][10], acc[tl][11]};
      else              m = (f32x4){acc[tl][12], acc[tl][13], acc[tl][14], acc[tl][15]};
      const char* pp = sp + (tl * 4 + kh) * 3072;
      f32x4 p0 = *(const f32x4*)pp, p1 = *(const f32x4*)(pp + 1024), p2 = *(const f32x4*)(pp + 2048);
      f32x4 v = (m + p1) + (p0 + p2);
      s16x4 at = __builtin_amdgcn_ds_read_tr16_b64_v4i16((lds_s16x4_p)(atp + tl * 32 * 192));
      u32x2 au = __builtin_bit_cast(u32x2, at);
      u32x2 ov;
#pragma unroll
      for (int e = 0; e < 2; e++) {
        unsigned bq = pack2(v[2 * e], v[2 * e + 1]);
        unsigned gb = pack2(g1 * lo_f(bq), g1 * hi_f(bq));
        unsigned ga = pack2(g_r * lo_f(au[e]), g_r * hi_f(au[e]));
        ov[e] = pack2(lo_f(ga) + lo_f(gb), hi_f(ga) + hi_f(gb));
      }
      *(u32x2*)(op + 32 * tl) = ov;
    }
  }
  asm volatile("code_tail_mark:" ::: "memory");
  STAMP(1, 1);
}

extern "C" void kernel_launch(void* const* d_in, const int* in_sizes, int n_in, void* d_out, int out_size, void* d_ws, size_t ws_size,
                              hipStream_t stream) {
  const u16* u = (const u16*)d_in[0]; const u16* logA = (const u16*)d_in[1]; const u16* Aim = (const u16*)d_in[2];
  const u16* Cr = (const u16*)d_in[3]; const u16* Ci = (const u16*)d_in[4]; const u16* logdt = (const u16*)d_in[5];
  const u16* Dsk = (const u16*)d_in[6]; const u16* wqkv = (const u16*)d_in[8]; const u16* wo = (const u16*)d_in[9];
  const u16* gw = (const u16*)d_in[10];
  char* ws = (char*)d_ws;
  k1<<<NB_GEMM + NB_GATE + NB_KG, 512, 0, stream>>>(u, logA, Aim, Cr, Ci, logdt, wqkv, gw, wo, ws);
  k2<<<256, 512, 0, stream>>>(Dsk, ws, (u16*)d_out);
}
```

```cpp
#include <hip/hip_runtime.h>
#include <hip/hip_bf16.h>
#include <stdint.h>

typedef unsigned short u16;
typedef __attribute__((ext_vector_type(8))) short s16x8;
typedef __attribute__((ext_vector_type(4))) short s16x4;
typedef __attribute__((ext_vector_type(4))) float f32x4;
typedef __attribute__((ext_vector_type(16))) float f32x16;
typedef __attribute__((ext_vector_type(4))) unsigned u32x4;
typedef __attribute__((ext_vector_type(2))) unsigned u32x2;
typedef __attribute__((address_space(3))) s16x4* lds_s16x4_p;
typedef const __attribute__((address_space(1))) void* gptr_t;
typedef __attribute__((address_space(3))) void* lptr_t;

constexpr int L = 2048, D = 640, NS = 192, NH = 32;
constexpr int UT_PAD = 32, UT_STRIDE = L + UT_PAD;
constexpr size_t WS_UT   = 0;
constexpr size_t WS_KIMG = WS_UT + (size_t)D * UT_STRIDE * 2;
constexpr size_t WS_PQ   = WS_KIMG + (size_t)D * 6144;
constexpr size_t WS_PART = WS_PQ + (size_t)L * D * 2;
constexpr size_t WS_GATE = WS_PART + (size_t)32 * 16 * 1680;
constexpr size_t WS_WOF  = WS_GATE + (size_t)L * 4;
constexpr size_t WS_KVT  = WS_WOF + (size_t)D * D * 2;
constexpr size_t WS_AT   = WS_KVT + (size_t)32 * 1280;
constexpr size_t WS_CNT  = WS_AT + (size_t)D * L * 2;
constexpr size_t WS_END  = WS_CNT + 4096;
#define TGET(v) do {} while (0)
#define STEPT(i) do {} while (0)
#define TPUT(kid, slot, thr, v) do {} while (0)
#define STAMP(kid, slot) do {} while (0)

__device__ __forceinline__ float b2f(u16 v) { return __uint_as_float(((unsigned)v) << 16); }
typedef __bf16 bf16x2_t __attribute__((ext_vector_type(2)));
typedef float f32x2 __attribute__((ext_vector_type(2)));
__device__ __forceinline__ unsigned pack2(float a, float b) { f32x2 v = {a, b}; bf16x2_t r = __builtin_convertvector(v, bf16x2_t); return __builtin_bit_cast(unsigned, r); }
__device__ __forceinline__ float lo_f(unsigned u) { return __uint_as_float(u << 16); }
__device__ __forceinline__ float hi_f(unsigned u) { return __uint_as_float(u & 0xffff0000u); }
__device__ __forceinline__ float rb(float f) { return lo_f(pack2(f, f)); }
__device__ __forceinline__ unsigned phi2(float a, float b) {
  unsigned t = pack2(a, b); float t0 = lo_f(t), t1 = hi_f(t);
  float e0 = __builtin_amdgcn_fmed3f(__builtin_amdgcn_exp2f(t0 * 1.44269504f), 0.f, 1.f);
  float e1 = __builtin_amdgcn_fmed3f(__builtin_amdgcn_exp2f(t1 * 1.44269504f), 0.f, 1.f);
  unsigned r = pack2(__builtin_fmaxf(t0, 0.f) + e0, __builtin_fmaxf(t1, 0.f) + e1);
  return pack2(lo_f(r) + 1e-6f, hi_f(r) + 1e-6f);
}
#define VMCNT(n) asm volatile("s_waitcnt vmcnt(" #n ")" ::: "memory")
#define RAW_BARRIER() do { asm volatile("s_waitcnt lgkmcnt(0)" ::: "memory"); __builtin_amdgcn_s_barrier(); asm volatile("" ::: "memory"); } while (0)
__device__ __forceinline__ void glds16(const void* g, void* l) { __builtin_amdgcn_global_load_lds((gptr_t)g, (lptr_t)l, 16, 0, 0); }

#define OPQ_S(v) asm volatile("" : "+s"(v))
typedef f32x2 cf;
__device__ __forceinline__ float smul(float a, float b) { float o; asm("v_mul_f32 %0, %1, %2" : "=v"(o) : "v"(a), "v"(b)); return o; }
__device__ __forceinline__ float sfma(float a, float b, float c) { float o; asm("v_fma_f32 %0, %1, %2, %3" : "=v"(o) : "v"(a), "v"(b), "v"(c)); return o; }
__device__ __forceinline__ float sfnma(float a, float b, float c) { float o; asm("v_fma_f32 %0, -%1, %2, %3" : "=v"(o) : "v"(a), "v"(b), "v"(c)); return o; }
__device__ __forceinline__ float ssub(float a, float b) { float o; asm("v_sub_f32 %0, %1, %2" : "=v"(o) : "v"(a), "v"(b)); return o; }
__device__ __forceinline__ cf cmul(cf a, cf b) {
  cf o;
  asm("v_pk_mul_f32 %0, %1, %2 op_sel:[0,0] op_sel_hi:[0,1]\n\t"
      "v_pk_fma_f32 %0, %1, %2, %0 op_sel:[1,1,0] op_sel_hi:[1,0,1] neg_lo:[1,0,0]" : "=&v"(o) : "v"(a), "v"(b));
  return o;
}

#define CPF 3
#define LPF 1
#define PFD 0
#define PFM_ 1
#define NP0 5
#define SPRIO 3
#define NI0 208
#define NB2 3
#define TB0 1
#define TBD 2
#define MIDX ((rowblk & 3) + 4 * (cb & 7))
#define MPS 3
#define PT0 30
#define PT1 110
#define PTD 20
constexpr int K1_LDS = 81920;
#define GLDS 1
constexpr int NB_GEMM = 256, NB_GATE = GLDS ? 0 : 16, NB_KG = 240;
#define WT 1
__device__ __forceinline__ void st16(void* p, u32x4 v) {
  asm volatile("global_store_dwordx4 %0, %1, off sc1\n\ts_nop 2" :: "v"(p), "v"(v) : "memory");
}

#define BAR_ONLY() do { asm volatile("" ::: "memory"); __builtin_amdgcn_s_barrier(); asm volatile("" ::: "memory"); } while (0)
#define SB() __builtin_amdgcn_sched_barrier(0)
template <bool ISQ>
__device__ __forceinline__ void mfma_part(char* smem, char* __restrict__ ws, int wave, f32x4 (&ep)[2][4]) {
  const int lane = threadIdx.x & 63;
  char* sB = smem; char* sA = smem + 32768;
  const int wr = wave & 1, wc = (wave >> 1) & 1;
  const int g = lane >> 4, fr = lane & 15;
  f32x4 acc[4][4];
#pragma unroll
  for (int m = 0; m < 4; m++)
#pragma unroll
    for (int c = 0; c < 4; c++) acc[m][c] = (f32x4){0.f, 0.f, 0.f, 0.f};
  const int q4 = fr >> 2, p4 = lane & 3;
  int aoff[2];
#pragma unroll
  for (int ks = 0; ks < 2; ks++) aoff[ks] = (wr * 64 + fr) * 128 + (((4 * ks + g) ^ (fr & 7)) << 4);
  int boff[4];
  {
    const int f = q4 | ((g & 1) << 2);
#pragma unroll
    for (int c = 0; c < 4; c++) boff[c] = (8 * g + q4) * 256 + (((4 * wc + c) ^ f) << 5) + p4 * 8;
  }
  s16x8 af[2][4]; s16x4 bl[4], bh[4];
  auto rdA = [&](int buf, int m, const char* pa) { af[buf][m] = *(const s16x8*)(pa + m * 2048); };
  auto rdBl = [&](int c, const char* pb) { bl[c] = __builtin_amdgcn_ds_read_tr16_b64_v4i16((lds_s16x4_p)(pb + boff[c])); };
  auto rdBh = [&](int c, const char* pb) { bh[c] = __builtin_amdgcn_ds_read_tr16_b64_v4i16((lds_s16x4_p)(pb + boff[c] + 1024)); };
  BAR_ONLY();
  STAMP(0, 2);
#pragma unroll
  for (int c = 0; c < 4; c++) rdA(0, c, sA + aoff[0]);
#pragma unroll
  for (int c = 0; c < 3; c++) { rdBl(c, sB); rdBh(c, sB); }
  int ao0 = 0, ao1 = 16384;
  auto grp = [&](int buf, int c, int m) {
    const s16x8 bf = __builtin_shufflevector(bl[c], bh[c], 0, 1, 2, 3, 4, 5, 6, 7);
    if (ISQ) acc[m][c] = __builtin_amdgcn_mfma_f32_16x16x32_bf16(bf, af[buf][m], acc[m][c], 0, 0, 0);
    else     acc[m][c] = __builtin_amdgcn_mfma_f32_16x16x32_bf16(af[buf][m], bf, acc[m][c], 0, 0, 0);
  };
#pragma unroll 1
  for (int j2 = 0; j2 < 5; j2++) {
#pragma unroll
    for (int st = 0; st < 4; st++) {
      const int hs = st & 1;
      SB();
      asm volatile("s_waitcnt lgkmcnt(2)" ::: "memory"); __builtin_amdgcn_s_barrier(); asm volatile("" ::: "memory");
      SB();
      const char* pa = sA + (hs == 0 ? ao0 + aoff[1] : ao1 + aoff[0]);
      const char* pc = sB + st * 8192;
      const char* pb = sB + ((st + 1) & 3) * 8192;
      grp(hs, 0, 0); SB(); rdBl(3, pc); rdBh(3, pc); SB();
      grp(hs, 0, 1); SB(); rdA(hs ^ 1, 0, pa); rdA(hs ^ 1, 1, pa); SB();
      grp(hs, 0, 2); SB(); rdA(hs ^ 1, 2, pa); rdA(hs ^ 1, 3, pa); SB();
      grp(hs, 0, 3); SB();
#pragma unroll
      for (int c = 1; c < 4; c++) {
        grp(hs, c, 0); SB(); rdBl(c - 1, pb); rdBh(c - 1, pb); SB();
        grp(hs, c, 1); grp(hs, c, 2); grp(hs, c, 3); SB();
      }
      if (hs == 1) { ao0 = ao1; ao1 = ao1 == 32768 ? 0 : ao1 + 16384; }
    }
  }
  STAMP(0, 3);
  int l3 = threadIdx.x; asm volatile("" : "+v"(l3));
  char* xch = smem + 32768 + (l3 >> 6) * 8192 + (l3 & 63) * 16;
#pragma unroll
  for (int i = 0; i < 8; i++) *(f32x4*)(xch + i * 1024) = acc[2 + (i >> 2)][i & 3];
#pragma unroll
  for (int i = 0; i < 8; i++) ep[i >> 2][i & 3] = acc[i >> 2][i & 3];
  RAW_BARRIER();
}
__device__ __forceinline__ void dma4x(const int (&vo)[4], const char* sbase, int ldsa) {
  asm volatile("s_mov_b32 m0, %5\n\ts_nop 0\n\tglobal_load_lds_dwordx4 %0, %4\n\tglobal_load_lds_dwordx4 %1, %4 offset:1024\n\t"
               "global_load_lds_dwordx4 %2, %4 offset:2048\n\tglobal_load_lds_dwordx4 %3, %4 offset:3072"
               :: "v"(vo[0]), "v"(vo[1]), "v"(vo[2]), "v"(vo[3]), "s"(sbase), "s"(ldsa) : "memory", "m0");
}
__device__ __forceinline__ void gemm_role(char* smem, const u16* __restrict__ x, const u16* __restrict__ W, const u16* __restrict__ gw, char* __restrict__ ws,
                                          int rowblk, int cb) {
  const int tid = threadIdx.x, lane = tid & 63;
  const int wave = __builtin_amdgcn_readfirstlane(tid >> 6);
  const bool ISQ = cb < 5;
  const int row0 = rowblk * 128, col0 = cb * 128;
  const int kvj = cb - 5;
  const int wr = wave & 1, wc = (wave >> 1) & 1, hf = wave >> 2;
  f32x4 ep[2][4];
  if (wave >= 4) {
    const int lw = wave & 1;
    const int l0 = (int)(size_t)(lptr_t)smem;
    unsigned pf0 = 0;
    const int pidx = (rowblk & 3) + 4 * (cb & 7);
    auto pfaddr = [&](int stg, int r) {
      const int q = r & 15;
      const int ln = (cb & 8) ? (q < 8 ? 12 + q : 14 + q) : (q < 13 ? q : 7 + q);
      return r < 16 ? (const char*)x + ((rowblk >> 2) * 512 + pidx * 16 + q) * 1280 + stg * 128
                    : (const char*)W + (64 * stg + pidx * 2 + (r >> 4) - 1) * 3840 + ln * 128;
    };
    {
      const int q = (wave - 4) * (NI0 / 4) + min(lane, NI0 / 4 - 1), stg = q / 48;
      asm volatile("global_load_dword %0, %1, off" : "+v"(pf0) : "v"(pfaddr(stg, q - 48 * stg)));
      SB();
    }
    __builtin_amdgcn_s_setprio(SPRIO);
    if (wave < 6) {
      int voA[4], voB[4];
#pragma unroll
      for (int i = 0; i < 4; i++) {
        voA[i] = (64 * lw + 8 * i + (lane >> 3)) * 1280 + (((lane & 7) ^ (lane >> 3)) << 4) - 1024 * i;
        const int fB = (lane >> 4) | ((i >> 1) << 2);
        const int c16 = (((lane & 15) >> 1) ^ fB) * 2 + (lane & 1);
        int bcol = col0 + 8 * c16;
        if (!ISQ) bcol = min(640 + 640 * (c16 >> 3) + ((60 * kvj) & ~7) + 8 * (c16 & 7), 1912);
        voB[i] = (16 * lw + 4 * i + (lane >> 4)) * 3840 + bcol * 2 - 1024 * i;
      }
      const char* pA = (const char*)x + row0 * 1280;
      const char* pB = (const char*)W;
      const int lA = l0 + 32768 + lw * 8192, lB = l0 + lw * 4096;
      auto issA = [&](int slot_off, int h) { dma4x(voA, pA + h * 32 * 1280, lA + slot_off + h * 4096); if (h) pA += 128; };
      auto issB = [&](int slot_off) { dma4x(voB, pB, lB + slot_off); pB += 32 * 3840; };
      TGET(tl0);
      asm volatile("s_waitcnt vmcnt(0)" :: "v"(pf0) : "memory");
      TGET(tl1);
      issA(0, 0); issB(0); issA(0, 1);
      issB(8192); issA(16384, 0);
      VMCNT(8);
      TGET(tl2);
      BAR_ONLY();
      issB(16384); issA(16384, 1);
      VMCNT(12); BAR_ONLY();
      issB(24576); issA(32768, 0); issA(32768, 1);
      VMCNT(12); BAR_ONLY();
      issB(0); issA(0, 0);
      int ao = 0, bo = 8192;
#pragma unroll 1
      for (int j = 1; j < 7; j++) {
        VMCNT(12); BAR_ONLY();
        issB(bo); bo = (bo + 8192) & 32767; issA(ao, 1);
        ao = ao == 32768 ? 0 : ao + 16384;
        VMCNT(12); BAR_ONLY();
        issB(bo); bo = (bo + 8192) & 32767; issA(ao, 0);
      }
      VMCNT(12); BAR_ONLY();
      issB(8192); issA(0, 1);
      VMCNT(12); BAR_ONLY();
      issB(16384);
      VMCNT(8); BAR_ONLY();
      issB(24576);
      VMCNT(4); BAR_ONLY();
      VMCNT(0); BAR_ONLY();
      BAR_ONLY();
      TPUT(0, 4, 256, tl0);
      TPUT(0, 5, 256, tl1); TPUT(0, 6, 256, tl2);
      BAR_ONLY();
    } else {
      u32x4 trv[2][4];
      constexpr int CNT2 = 480 - NI0, NL2 = (CNT2 + 2 * NB2 - 1) / (2 * NB2);
      const char* pp[NB2];
#pragma unroll
      for (int i = 0; i < NB2; i++) {
        const int it = NI0 * 32 + ((pidx & 3) * 8 + (pidx >> 2)) * CNT2 + min((2 * i + lw) * NL2 + min(lane, NL2 - 1), CNT2 - 1);
        const int stg = it / 1536, r = it - 1536 * stg, q = r & 15;
        const int ln = (cb & 8) ? (q < 8 ? 12 + q : 14 + q) : (q < 13 ? q : 7 + q);
        pp[i] = r < 512 ? (const char*)x + ((rowblk >> 2) * 512 + r) * 1280 + stg * 128 : (const char*)W + (64 * stg + (r >> 4) - 32) * 3840 + ln * 128;
      }
      const int tb2 = TB0 + TBD * (pidx & 3);
      int tro;
      {
        const int fr = lane & 15, g = lane >> 4, p4 = lane & 3, r = 8 * g + (fr >> 2);
        tro = l0 + 32768 + r * 128 + (((4 * lw + (p4 >> 1)) ^ (r & 7)) << 4) + (p4 & 1) * 8;
      }
      auto transp = [&](int ao) {
#pragma unroll
        for (int hh = 0; hh < 2; hh++) {
          u32x2 tr[8];
          const int a = (tro ^ (32 * hh)) + ao, a2 = (a ^ 64) + 512;
          asm volatile("ds_read_b64_tr_b16 %0, %8\n\tds_read_b64_tr_b16 %1, %9\n\tds_read_b64_tr_b16 %2, %8 offset:4096\n\tds_read_b64_tr_b16 %3, %9 offset:4096\n\t"
                       "ds_read_b64_tr_b16 %4, %8 offset:8192\n\tds_read_b64_tr_b16 %5, %9 offset:8192\n\tds_read_b64_tr_b16 %6, %8 offset:12288\n\tds_read_b64_tr_b16 %7, %9 offset:12288\n\t"
                       "s_waitcnt lgkmcnt(0)"
                       : "=&v"(tr[0]), "=&v"(tr[1]), "=&v"(tr[2]), "=&v"(tr[3]), "=&v"(tr[4]), "=&v"(tr[5]), "=&v"(tr[6]), "=&v"(tr[7])
                       : "v"(a), "v"(a2) : "memory");
#pragma unroll
          for (int i = 0; i < 4; i++) trv[hh][i] = (u32x4){tr[2 * i][0], tr[2 * i][1], tr[2 * i + 1][0], tr[2 * i + 1][1]};
        }
      };
      unsigned pf2 = 0;
      auto burst = [&](int hs) {
        static_assert(NB2 == 3, "");
        asm volatile("s_cmp_lg_u32 %4, %5\n\ts_cbranch_scc1 1f\n\tglobal_load_dword %0, %1, off\n\tglobal_load_dword %0, %2, off\n\tglobal_load_dword %0, %3, off\n1:"
                     : "+v"(pf2) : "v"(pp[0]), "v"(pp[1]), "v"(pp[2]), "s"(hs), "s"(tb2) : "scc");
      };
      asm volatile("s_waitcnt vmcnt(0)" :: "v"(pf0) : "memory");
      BAR_ONLY();
      int ao = 0;
#pragma unroll 1
      for (int j = 0; j < 10; j++) {
        BAR_ONLY();
        if (!lw) STEPT(2 * j);
        burst(2 * j);
        if (j == cb) transp(ao);
        BAR_ONLY();
        if (!lw) STEPT(2 * j + 1);
        burst(2 * j + 1);
        ao = ao == 32768 ? 0 : ao + 16384;
      }
      asm volatile("s_waitcnt vmcnt(0)" :: "v"(pf2) : "memory");
      if (cb < 10) {
        u16* dst = (u16*)(ws + WS_UT) + (size_t)(64 * cb + 32 * lw + (lane & 15)) * UT_STRIDE + 8 * (lane >> 4);
#pragma unroll
        for (int hh = 0; hh < 2; hh++) {
#pragma unroll
          for (int i = 0; i < 4; i++) st16(dst + hh * 16 * UT_STRIDE + UT_PAD + row0 + 32 * i, trv[hh][i]);
          if (rowblk == 0) st16(dst + hh * 16 * UT_STRIDE, (u32x4){0u, 0u, 0u, 0u});
        }
      }
      BAR_ONLY();
    }
    __builtin_amdgcn_s_setprio(0);
    char* xch = smem + 32768 + (wave & 3) * 8192 + lane * 16;
#pragma unroll
    for (int i = 0; i < 8; i++) ep[i >> 2][i & 3] = *(const f32x4*)(xch + i * 1024);
  } else if (ISQ) {
    mfma_part<true>(smem, ws, wave, ep);
  } else {
    mfma_part<false>(smem, ws, wave, ep);
  }
  int l2 = threadIdx.x; asm volatile("" : "+v"(l2));
  const int g = (l2 >> 4) & 3, fr = l2 & 15;
  if (ISQ) {
    char* pq0 = ws + WS_PQ; char* pq1 = ws + WS_PQ + (size_t)L * 16 * 32 * 2;
#pragma unroll
    for (int c = 0; c < 4; c++) {
      const int n = col0 + 64 * wc + 16 * c + 4 * g;
      const int hd = n / 20, d0 = n - 20 * hd;
#pragma unroll
      for (int m = 0; m < 2; m++) {
        const int rb64 = rowblk * 2 + wr, r = 16 * m + fr;
        u32x2 o;
        o[0] = phi2(ep[m][c][0], ep[m][c][1]);
        o[1] = phi2(ep[m][c][2], ep[m][c][3]);
        const int fi = (rb64 * 32 + hd) * 2 + hf;
        char* dst = d0 < 16 ? pq0 + ((size_t)fi * 64 + (d0 >> 3) * 32 + r) * 16 + (d0 & 7) * 2 : pq1 + ((size_t)fi * 32 + r) * 8;
        *(u32x2*)dst = o;
      }
    }
  } else {
    const int o = (60 * kvj) & 7, zc = o ? 0 : 60;
    const int lane2 = l2 & 63;
    char* fb = smem;
#pragma unroll
    for (int c = 0; c < 4; c++) {
      u32x4 a;
      if (wc == 0) {
        a[0] = phi2(ep[0][c][0], ep[0][c][1]); a[1] = phi2(ep[0][c][2], ep[0][c][3]);
        a[2] = phi2(ep[1][c][0], ep[1][c][1]); a[3] = phi2(ep[1][c][2], ep[1][c][3]);
      } else {
        a[0] = pack2(ep[0][c][0], ep[0][c][1]); a[1] = pack2(ep[0][c][2], ep[0][c][3]);
        a[2] = pack2(ep[1][c][0], ep[1][c][1]); a[3] = pack2(ep[1][c][2], ep[1][c][3]);
        if (16 * c + fr == zc) a = (u32x4){0x3F803F80u, 0x3F803F80u, 0x3F803F80u, 0x3F803F80u};
      }
      *(u32x4*)(fb + ((((wc * 4 + wr * 2 + hf) * 4 + c) * 64 + lane2) << 4)) = a;
    }
    RAW_BARRIER();
    const int np = wave < 4 ? 1 : 2;
    for (int i = 0; i < np; i++) {
      const int p = wave < 4 ? wave : wave * 2 - 4 + i;
      int ta, tb;
      if (p < 10) { ta = (int)((0x3322211100ull >> (4 * p)) & 15); tb = (int)((0x3232121010ull >> (4 * p)) & 15); }
      else { ta = (o ? 2 : 0) + (p - 10); tb = o ? 0 : 3; }
      const char* pk = fb + ((ta * 64 + lane2) << 4);
      const char* pv = fb + 16384 + ((tb * 64 + lane2) << 4);
      f32x4 dd = {0.f, 0.f, 0.f, 0.f};
#pragma unroll
      for (int rg = 0; rg < 4; rg++)
        dd = __builtin_amdgcn_mfma_f32_16x16x32_bf16(*(const s16x8*)(pk + rg * 4096), *(const s16x8*)(pv + rg * 4096), dd, 0, 0, 0);
      const int kc0 = 16 * ta + 4 * g - o;
      const int hk = kc0 / 20, dk0 = kc0 - 20 * hk;
      const int vcol = 16 * tb + fr;
      const int dv = vcol == zc ? 20 : vcol - o - 20 * hk;
      if (kc0 >= 0 && kc0 < (kvj == 10 ? 40 : 60) && dv >= 0 && (dv < 20 || vcol == zc))
        *(f32x4*)(ws + WS_PART + (size_t)(((3 * kvj + hk) * 16 + rowblk) * 21 + dv) * 80 + dk0 * 4) = dd;
    }
  }
}

__device__ __forceinline__ cf zpow(float th, float tl, float rh, float rl, float p) {
  float e = __builtin_amdgcn_exp2f(th * p); e = fmaf(e, (tl * p) * 0.69314718f, e);
  float t = rh * p; float f = (t - __builtin_rintf(t)) + rl * p;
  cf o; o[0] = e * __builtin_amdgcn_cosf(f); o[1] = e * __builtin_amdgcn_sinf(f); return o;
}
#define PKM(o, a, b) "v_pk_mul_f32 " o ", " a ", " b " op_sel:[0,0] op_sel_hi:[0,1]\n\t"
#define PKF(o, a, b) "v_pk_fma_f32 " o ", " a ", " b ", " o " op_sel:[1,1,0] op_sel_hi:[1,0,1] neg_lo:[1,0,0]\n\t"
__device__ __forceinline__ void cmul4(cf& o0, cf& o1, cf& o2, cf& o3, cf a0, cf b0, cf a1, cf b1, cf a2, cf b2, cf a3, cf b3) {
  asm(PKM("%0", "%4", "%5") PKM("%1", "%6", "%7") PKM("%2", "%8", "%9") PKM("%3", "%10", "%11")
      PKF("%0", "%4", "%5") PKF("%1", "%6", "%7") PKF("%2", "%8", "%9") PKF("%3", "%10", "%11")
      : "=&v"(o0), "=&v"(o1), "=&v"(o2), "=&v"(o3)
      : "v"(a0), "v"(b0), "v"(a1), "v"(b1), "v"(a2), "v"(b2), "v"(a3), "v"(b3));
}
__device__ __forceinline__ void batch4(unsigned* h, cf b, cf t0, cf t1, cf t2, cf t3) {
  cf w0, w1, w2, w3;
  asm(PKM("%0", "%4", "%5") PKM("%1", "%4", "%6") PKM("%2", "%4", "%7") PKM("%3", "%4", "%8")
      PKF("%0", "%4", "%5") PKF("%1", "%4", "%6") PKF("%2", "%4", "%7") PKF("%3", "%4", "%8")
      : "=&v"(w0), "=&v"(w1), "=&v"(w2), "=&v"(w3) : "v"(b), "v"(t0), "v"(t1), "v"(t2), "v"(t3));
  h[0] = pack2(w0[0], w0[1]); h[1] = pack2(w1[0], w1[1]); h[2] = pack2(w2[0], w2[1]); h[3] = pack2(w3[0], w3[1]);
}
__device__ __forceinline__ void batch3(unsigned* h, cf b, cf t1, cf t2, cf t3) {
  cf w1, w2, w3;
  asm(PKM("%0", "%3", "%4") PKM("%1", "%3", "%5") PKM("%2", "%3", "%6")
      PKF("%0", "%3", "%4") PKF("%1", "%3", "%5") PKF("%2", "%3", "%6")
      : "=&v"(w1), "=&v"(w2), "=&v"(w3) : "v"(b), "v"(t1), "v"(t2), "v"(t3));
  h[0] = pack2(b[0], b[1]); h[1] = pack2(w1[0], w1[1]); h[2] = pack2(w2[0], w2[1]); h[3] = pack2(w3[0], w3[1]);
}
constexpr int KG_RS = 288, KG_PL = 16 * KG_RS;
__device__ __forceinline__ void kgen_role(char* smem, const u16* __restrict__ logA, const u16* __restrict__ Aim,
                                          const u16* __restrict__ Cr, const u16* __restrict__ Ci, const u16* __restrict__ logdt,
                                          const u16* __restrict__ x, const u16* __restrict__ W, const u16* __restrict__ wo,
                                          const u16* __restrict__ gw, char* __restrict__ ws, int blk, unsigned pfv) {
  const int tid = threadIdx.x, lane = tid & 63;
  const int wave = __builtin_amdgcn_readfirstlane(tid >> 6);
  constexpr int RS = KG_RS, PL = KG_PL;
  const int task = blk * 8 + wave;
  const int d = task / 3;
  const int pi = task * 64 + lane;
  const bool wow = wave < 4 && blk < 200;
  int wid;
  unsigned wr_[8];
  {
    const int wt = blk * 4 + wave, oct = wt / 10, n = (wt - 10 * oct) * 64 + lane;
    const int h = oct & 1, kh = (oct >> 1) & 3, gs5 = oct >> 3, grp = gs5 >= 5, s5 = gs5 - 5 * grp, hb = 8 * kh + 4 * grp;
    const int cb = n / 80, m = n - 80 * cb;
    const int slot = m < 64 ? (m >> 5) * 64 + h * 32 + (m & 31) : 64 + h * 16 + m;
    wid = ((cb * 10 + gs5) * 4 + kh) * 160 + slot;
    int k0, k1;
    if (s5 < 4) { k0 = 20 * (hb + s5) + 4 * h; k1 = k0 + 8; }
    else { k0 = 20 * (hb + 2 * h) + 16; k1 = k0 + 20; }
    if (wow) {
      const u16* p0 = wo + (size_t)k0 * D + n; const u16* p1 = wo + (size_t)k1 * D + n;
#pragma unroll
      for (int e = 0; e < 4; e++) {
        asm volatile("global_load_ushort %0, %1, off offset:%2" : "=v"(wr_[e]) : "v"(p0), "i"(e * 1280));
        asm volatile("global_load_ushort %0, %1, off offset:%2" : "=v"(wr_[4 + e]) : "v"(p1), "i"(e * 1280));
      }
    }
  }
  const float dt = rb(expf(b2f(logdt[d])));
  float ar;
  {
    const float la = b2f(logA[pi]);
    float h = la * 1.44269502f; float l = fmaf(la, 1.44269502f, -h) + la * 1.92596299e-8f;
    float e = __builtin_amdgcn_exp2f(h); e = fmaf(e, l * 0.69314718f, e);
    ar = -rb(e) * dt;
  }
  const float ai = b2f(Aim[pi]) * dt;
  cf C; C[0] = 2.f * (b2f(Cr[pi]) * dt); C[1] = 2.f * (b2f(Ci[pi]) * dt);
  const float th = ar * 1.44269502f, tl = fmaf(ar, 1.44269502f, -th) + ar * 1.92596299e-8f;
  const float rh = ai * 0.159154937f, rl = fmaf(ai, 0.159154937f, -rh) + ai * 6.42063833e-9f;
  char* tb = smem + wave * (2 * PL);
  char* wp = tb + lane * 4;
  const char* rp = tb + (lane & 15) * RS + (lane >> 4) * 16;
  STAMP(0, 2);
  const cf one = {1.f, 0.f};
  cf S1 = zpow(th, tl, rh, rl, 1.f), Q1 = zpow(th, tl, rh, rl, 8.f), R1 = zpow(th, tl, rh, rl, 128.f);
  Q1[1] = -Q1[1];
  cf S2, Q2, R2, G1, S3, S4, Q3, P1, R3, T1, G2, G3, S5, S6, S7, P2, CT1, T2, P3, CS4, CT2, CT3, CT4, dm0;
  cmul4(S2, Q2, R2, G1, S1, S1, Q1, Q1, R1, R1, C, R1);
  cmul4(S3, S4, Q3, P1, S2, S1, S2, S2, Q2, Q1, Q2, Q2);
  cmul4(R3, T1, G2, P2, R2, R1, R2, R2, C, R2, P1, P1);
  cmul4(S5, S6, S7, P3, S4, S1, S4, S2, S4, S3, P2, P1);
  cmul4(G3, CT1, T2, dm0, C, R3, C, T1, T1, T1, C, R1);
  cmul4(CS4, CT2, CT3, dm0, CT1, S4, C, T2, CT1, T2, C, R1);
  CT4 = cmul(CT2, T2);
  auto rows4 = [&](int pl, int row, int rs, cf b, cf t0, cf t1, cf t2, cf t3) {
    unsigned h[4];
    batch4(h, b, t0, t1, t2, t3);
#pragma unroll
    for (int k = 0; k < 4; k++) *(unsigned*)(wp + pl * PL + (row + k * rs) * RS) = h[k];
  };
  auto rows3 = [&](int pl, int row, int rs, cf b, cf t1, cf t2, cf t3) {
    unsigned h[4];
    batch3(h, b, t1, t2, t3);
#pragma unroll
    for (int k = 0; k < 4; k++) *(unsigned*)(wp + pl * PL + (row + k * rs) * RS) = h[k];
  };
  *(unsigned*)(wp) = 0x00003F80u;
  *(unsigned*)(wp + RS) = pack2(Q1[0], Q1[1]); *(unsigned*)(wp + 2 * RS) = pack2(Q2[0], Q2[1]); *(unsigned*)(wp + 3 * RS) = pack2(Q3[0], Q3[1]);
  rows3(0, 4, 1, P1, Q1, Q2, Q3); rows3(0, 8, 1, P2, Q1, Q2, Q3); rows3(0, 12, 1, P3, Q1, Q2, Q3);
  rows3(1, 0, 1, C, S1, S2, S3); rows4(1, 4, 1, C, S4, S5, S6, S7); rows3(1, 8, 1, G1, S1, S2, S3); rows4(1, 12, 1, G1, S4, S5, S6, S7);
  __builtin_amdgcn_wave_barrier();
  s16x8 bh[4];
  f32x4 res[3];
#pragma unroll
  for (int k = 0; k < 4; k++) bh[k] = *(const s16x8*)(rp + k * 64);
  auto mm = [&](int t, int pl) {
    f32x4 a = {0.f, 0.f, 0.f, 0.f};
#pragma unroll
    for (int k = 0; k < 4; k++) a = __builtin_amdgcn_mfma_f32_16x16x32_bf16(*(const s16x8*)(rp + pl * PL + k * 64), bh[k], a, 0, 0, 0);
    res[t] = a;
  };
  mm(0, 1);
  __builtin_amdgcn_wave_barrier();
  STAMP(0, 3);
  rows3(0, 0, 1, G2, S2, S4, S6); rows3(0, 4, 1, G3, S2, S4, S6); rows3(0, 8, 2, CT1, R1, R2, R3); rows3(0, 9, 2, CS4, R1, R2, R3);
  rows3(1, 0, 1, CT2, R1, R2, R3); rows3(1, 4, 1, CT3, R1, R2, R3);
  *(unsigned*)(wp + PL + 8 * RS) = pack2(CT4[0], CT4[1]);
  __builtin_amdgcn_wave_barrier();
  mm(1, 0); mm(2, 1);
  __builtin_amdgcn_wave_barrier();
  STAMP(0, 4);
  if (wow) {
    asm volatile("s_waitcnt vmcnt(0)" : "+v"(wr_[0]), "+v"(wr_[1]), "+v"(wr_[2]), "+v"(wr_[3]), "+v"(wr_[4]), "+v"(wr_[5]), "+v"(wr_[6]), "+v"(wr_[7]) :: "memory");
    u32x4 wv = {wr_[0] | (wr_[1] << 16), wr_[2] | (wr_[3] << 16), wr_[4] | (wr_[5] << 16), wr_[6] | (wr_[7] << 16)};
    st16(ws + WS_WOF + (size_t)wid * 16, wv);
  }
  if (blk == 239) ((unsigned*)(ws + WS_CNT))[tid] = 0u, ((unsigned*)(ws + WS_CNT))[tid + 512] = 0u;
#pragma unroll
  for (int t = 0; t < 3; t++) *(f32x4*)(tb + t * 1024 + lane * 16) = res[t];
  __syncthreads();
  {
    const int d0 = (blk * 8) / 3;
#pragma unroll
    for (int it = 0; it < 2; it++) {
      const int e = tid + 512 * it;
      const int seg = e / 192, ent = e - 192 * seg;
      const int ds = d0 + seg;
      const int wlo = max(0, 3 * ds - 8 * blk), whi = min(8, 3 * ds + 3 - 8 * blk);
      if (seg < 4 && wlo < 8) {
        f32x4 a = *(const f32x4*)(smem + wlo * (2 * PL) + ent * 16);
        for (int w = wlo + 1; w < whi; w++) a += *(const f32x4*)(smem + w * (2 * PL) + ent * 16);
        st16(ws + WS_KIMG + (size_t)ds * 6144 + (3 * ds < 8 * blk ? 3072 : 0) + ent * 16, __builtin_bit_cast(u32x4, a));
      }
    }
  }
  asm volatile("s_waitcnt vmcnt(0)" :: "v"(pfv) : "memory");
  const int gt = wave * 30 + (blk >> 3);
  if (gt < 64) {
    const int row = (blk & 7) * 256 + gt * 4 + (lane >> 4);
    const u32x4* px = (const u32x4*)(x + (size_t)row * D) + (lane & 15); const u32x4* pg = (const u32x4*)gw + (lane & 15);
    u32x4 xv[5], gv[5];
#pragma unroll
    for (int i = 0; i < 5; i++) { xv[i] = px[16 * i]; gv[i] = pg[16 * i]; }
    float gs = 0.f;
#pragma unroll
    for (int i = 0; i < 5; i++)
#pragma unroll
      for (int e = 0; e < 4; e++) {
        unsigned p = pack2(lo_f(xv[i][e]) * lo_f(gv[i][e]), hi_f(xv[i][e]) * hi_f(gv[i][e]));
        gs = __builtin_amdgcn_fdot2_f32_bf16(__builtin_bit_cast(bf16x2_t, p), __builtin_bit_cast(bf16x2_t, 0x3F803F80u), gs, false);
      }
    gs += __shfl_xor(gs, 1); gs += __shfl_xor(gs, 2); gs += __shfl_xor(gs, 4); gs += __shfl_xor(gs, 8);
    if ((lane & 15) == 0) { float a = rb(gs); ((float*)(ws + WS_GATE))[row] = rb(1.f / (1.f + __expf(-a))); }
  }
}

__global__ __launch_bounds__(512, 4) void k1(const u16* __restrict__ x, const u16* __restrict__ logA, const u16* __restrict__ Aim,
                                          const u16* __restrict__ Cr, const u16* __restrict__ Ci, const u16* __restrict__ logdt,
                                          const u16* __restrict__ wqkv, const u16* __restrict__ gw, const u16* __restrict__ wo,
                                          char* __restrict__ ws) {
  __shared__ __attribute__((aligned(16))) char smem[K1_LDS];
  int b = blockIdx.x;
  unsigned cpf = 0;
  asm volatile("" :: "s"(x), "s"(logA), "s"(Aim), "s"(Cr), "s"(Ci), "s"(logdt), "s"(wqkv), "s"(gw), "s"(wo), "s"(ws));
  if (b < NB_KG && threadIdx.x >= 501) {
    const char* pc; int len;
    asm volatile("s_getpc_b64 %0\n\ts_mov_b32 %1, code_tail_mark-_Z2k1PKtS0_S0_S0_S0_S0_S0_S0_S0_Pc-128" : "=s"(pc), "=s"(len));
    const int off = min((int)((threadIdx.x - 501) * 30 + (b >> 3)) * 128, len);
    asm volatile("global_load_dword %0, %1, %2" : "+v"(cpf) : "v"(off), "s"(pc));
  }
  if (b < 8 && threadIdx.x >= 490 && threadIdx.x < 500) asm volatile("global_load_dword %0, %1, %2" : "+v"(cpf) : "v"((threadIdx.x - 490) * 128), "s"(gw));
  STAMP(0, 0);
  b = b < NB_KG ? b + NB_GEMM : (b < NB_KG + NB_GEMM ? b - NB_KG : b);
  if (b < NB_GEMM) {
    const int xcd = b & 7, idx = b >> 3;
    const int rowblk = (xcd >> 1) * 4 + (idx & 3), cb = (xcd & 1) * 8 + (idx >> 2);
    gemm_role(smem, x, wqkv, gw, ws, rowblk, cb);
    STAMP(0, 1);
    return;
  }
  b -= NB_GEMM;
  if (b < NB_KG) { kgen_role(smem, logA, Aim, Cr, Ci, logdt, x, wqkv, wo, gw, ws, b, cpf); STAMP(0, 1); return; }
}

constexpr int K2_LDS = 163840;
constexpr int K2_CS = 4160;
constexpr int K2_KOFF = 4 * K2_CS;
constexpr int K2_CH = K2_KOFF + 4608;
constexpr int K2_CONV = K2_LDS - 3 * K2_CH;
constexpr int K2_TAB = 81920;
constexpr int K2_FIN = K2_TAB + 9216;
constexpr int K2_STG = 114688;
constexpr int NB_KV = 32;
typedef __attribute__((address_space(1))) unsigned gu32;
__device__ __forceinline__ const char* uni(const char* p) {
  size_t v = (size_t)p;
  unsigned lo = __builtin_amdgcn_readfirstlane((unsigned)v), hi = __builtin_amdgcn_readfirstlane((unsigned)(v >> 32));
  size_t o = ((size_t)hi << 32) | lo;
  asm volatile("s_nop 4" : "+s"(o));
  return (const char*)o;
}
__device__ __forceinline__ void wait_count(char* p, unsigned target) {
  gu32* c = (gu32*)(size_t)p;
  for (int n = 0; n < (1 << 22); n++) {
    if (__hip_atomic_load(c, __ATOMIC_RELAXED, __HIP_MEMORY_SCOPE_AGENT) >= target) break;
    __builtin_amdgcn_s_sleep(8);
  }
}
#define GLD4(dst, voff, sbase, pol) asm volatile("global_load_dwordx4 %0, %1, %2" pol : "=v"(dst) : "v"(voff), "s"(sbase))
#define GLD2(dst, voff, sbase, pol) asm volatile("global_load_dwordx2 %0, %1, %2" pol : "=v"(dst) : "v"(voff), "s"(sbase))
#define GLD1(dst, voff, sbase, pol) asm volatile("global_load_dword %0, %1, %2" pol : "=v"(dst) : "v"(voff), "s"(sbase))
template <int G>
__device__ __forceinline__ void conv_unit(f32x4* acc, int ap, int bp) {
  constexpr int NT = 8 - G;
  u32x2 a0[NT], a1[NT]; s16x8 bh[NT];
#pragma unroll
  for (int j = 0; j < NT; j++)
    asm volatile("ds_read_b64 %0, %3 offset:%5\n\tds_read_b64 %1, %3 offset:%6\n\tds_read_b128 %2, %4 offset:%5"
                 : "=&v"(a0[j]), "=&v"(a1[j]), "=&v"(bh[j]) : "v"(ap), "v"(bp), "n"(512 * j), "n"(512 * j + 8) : "memory");
#pragma unroll
  for (int x = 0; x < NT; x++) {
    asm volatile("s_waitcnt lgkmcnt(%3)" : "+v"(a0[x]), "+v"(a1[x]), "+v"(bh[x]) : "n"(3 * (NT - 1 - x) > 15 ? 15 : 3 * (NT - 1 - x)) : "memory");
#pragma unroll
    for (int y = 0; y <= x; y++)
#pragma unroll
      for (int sw = 0; sw < 2; sw++) {
        if (sw && y == x) continue;
        const int j = sw ? y : x, m = sw ? x : y, T = j + m;
        const int ti = G == 0 ? (T == 0 ? 0 : T == 3 ? 1 : T == 4 ? 2 : T == 7 ? 3 : -1) : (T == 1 ? 0 : T == 2 ? 1 : T == 5 ? 2 : T == 6 ? 3 : -1);
        if (ti >= 0) acc[ti] = __builtin_amdgcn_mfma_f32_16x16x32_bf16(__builtin_bit_cast(s16x8, (u32x4){a0[j][0], a0[j][1], a1[j][0], a1[j][1]}), bh[m], acc[ti], 0, 0, 0);
      }
    SB();
  }
}

__global__ __launch_bounds__(512, 2) void k2(const u16* __restrict__ Dskip, char* __restrict__ ws, u16* __restrict__ out) {
  __shared__ __attribute__((aligned(16))) char smem[K2_LDS];
  const int tid = threadIdx.x, lane = tid & 63;
  const int wave = __builtin_amdgcn_readfirstlane(tid >> 6);
  const int b = blockIdx.x;
  unsigned cpf = 0;
  if (b < 32 && tid < 192) {
    const char* pc; int len;
    asm volatile("s_getpc_b64 %0\n\ts_mov_b32 %1, code_tail_mark-_Z2k2PKtPcPt-128" : "=s"(pc), "=s"(len));
    const int off = min(tid * 128, len);
    asm volatile("global_load_dword %0, %1, %2" : "+v"(cpf) : "v"(off), "s"(pc));
  }
  STAMP(1, 0);
  const int lds0 = (int)(size_t)(lptr_t)smem;
  const int l16 = lane * 16;
  const int odd = b & 1;
  const int chA = 5 * (b >> 1) + 3 * odd, chH = 5 * (b >> 1) + 2;
  const int xcd = b & 7, idx = b >> 3;
  const int rowblk = xcd * 4 + (idx >> 3), cb = idx & 7;
  const int l0 = rowblk * 64, c0 = cb * 80;
  const int t = wave & 1, kh = wave >> 1;
  const int r = lane & 31, h = lane >> 5;
  const bool kvw = b < NB_KV && wave >= 6;
  f32x4 kvp[16];
  if (kvw) {
    const char* src = ws + WS_PART + (size_t)b * 16 * 1680;
    const int o = min(tid - 384, 104) * 16;
#pragma unroll
    for (int i = 0; i < 16; i++) GLD4(kvp[i], o, src + i * 1680, "");
  }
  u32x4 in0[3], in1[3];
  int dsto[3];
#pragma unroll
  for (int rr = 0; rr < 3; rr++) {
    const int id = wave + 8 * rr;
    int o0, o1;
    if (id < 13) {
      const int i = min(64 * id + lane, 776);
      const int ch = i / 259, xx = i - 259 * ch;
      const int base = (int)WS_UT + (ch == 2 ? chH : chA + ch) * (UT_STRIDE * 2);
      o0 = base + min(xx + 1, 259) * 16; o1 = base + min(xx + 2, 259) * 16;
      dsto[rr] = K2_CONV + ch * K2_CH + xx * 16;
    } else {
      const int k = min(id - 13, 8), ch = k / 3;
      o0 = (int)WS_KIMG + (ch == 2 ? chH : chA + ch) * 6144 + (k - 3 * ch) * 1024 + l16; o1 = o0 + 3072;
      dsto[rr] = K2_TAB + k * 1024 + l16;
    }
    GLD4(in0[rr], o0, ws, ""); GLD4(in1[rr], o1, ws, "");
  }
  const int jg = (wave >> 1) & 1;
  const int fo = (lane & 15) * 16 + (lane >> 4) * 4;
  int ft[3];
  ft[0] = (wave & 1) ? 4 + jg : jg;
  ft[1] = (wave & 1) ? 7 - jg : 3 - jg;
  { const int w3 = wave & 3; ft[2] = (w3 & 2) * 2 + ((w3 & 1) ? 3 - odd : odd); }
  u32x2 us[3];
  {
    const int oa = (int)WS_UT + (chA + (wave >> 2)) * (UT_STRIDE * 2) + (UT_PAD + fo) * 2;
    const int oh = (int)WS_UT + chH * (UT_STRIDE * 2) + (UT_PAD + fo) * 2;
    GLD2(us[0], oa + ft[0] * 512, ws, ""); GLD2(us[1], oa + ft[1] * 512, ws, ""); GLD2(us[2], oh + ft[2] * 512, ws, "");
  }
  float g_r;
  GLD1(g_r, (int)WS_GATE + (l0 + 32 * t + r) * 4, ws, "");
  unsigned dsA, dsH;
  asm volatile("global_load_ushort %0, %1, %2" : "=v"(dsA) : "v"((chA + (wave >> 2)) * 2), "s"(Dskip));
  asm volatile("global_load_ushort %0, %1, %2" : "=v"(dsH) : "v"(chH * 2), "s"(Dskip));
  const char* wsrc = ws + WS_WOF + (size_t)cb * 102400;
  auto dma1 = [&](int p) {
    asm volatile("s_mov_b32 m0, %2\n\ts_nop 0\n\tglobal_load_lds_dwordx4 %0, %1" :: "v"(l16), "s"(wsrc + p * 1024), "s"(lds0 + p * 1024) : "memory", "m0");
  };
  auto dma4 = [&](int p) {
    asm volatile("s_mov_b32 m0, %2\n\ts_nop 0\n\tglobal_load_lds_dwordx4 %0, %1\n\tglobal_load_lds_dwordx4 %0, %1 offset:1024\n\t"
                 "global_load_lds_dwordx4 %0, %1 offset:2048\n\tglobal_load_lds_dwordx4 %0, %1 offset:3072"
                 :: "v"(l16), "s"(wsrc + p * 1024), "s"(lds0 + p * 1024) : "memory", "m0");
  };
  u32x4 pf0[8]; u32x2 pf1[8];
  auto bulk = [&]() {
    dma4(10 * wave); dma4(10 * wave + 4); dma1(10 * wave + 8); dma1(10 * wave + 9);
    const char* p0 = ws + WS_PQ + (size_t)((rowblk * 32 + kh * 8) * 2 + t) * 1024;
    const char* p1 = ws + WS_PQ + (size_t)L * 16 * 32 * 2 + (size_t)((rowblk * 32 + kh * 8) * 2 + t) * 256;
    const int r8 = r * 8;
#pragma unroll
    for (int i = 0; i < 8; i++) { GLD4(pf0[i], l16, p0 + i * 2048, ""); GLD2(pf1[i], r8, p1 + i * 512, ""); }
  };
  int tix[8];
#pragma unroll
  for (int e = 0; e < 8; e++) {
    const int j = (tid & 255) * 8 + 7 - e;
    const int p = (int)rb((float)j);
    const int hh = p >> 7, c = (p >> 3) & 15;
    int sh = 31 - __builtin_clz(hh | 1); sh = sh > 3 ? 3 : sh;
    const int row = 8 * sh + (((hh << 3) | (p & 7)) >> sh);
    tix[e] = K2_TAB + (tid >> 8) * 3072 + (((row >> 2) * 16 + c) * 4 + (row & 3)) * 4;
  }
  if (tid < 96) *(u32x4*)(smem + K2_CONV + (tid >> 5) * K2_CH + K2_KOFF + (tid & 31) * 16) = (u32x4){0u, 0u, 0u, 0u};
  asm volatile("s_waitcnt vmcnt(6)" : "+v"(in0[0]), "+v"(in1[0]), "+v"(in0[1]), "+v"(in1[1]), "+v"(in0[2]), "+v"(in1[2]), "+v"(cpf) :: "memory");
  STAMP(1, 2);
#pragma unroll
  for (int rr = 0; rr < 3; rr++) {
    const int id = wave + 8 * rr;
    char* p = smem + dsto[rr];
    if (id < 13) {
      const u32x4 a = in0[rr], bb = in1[rr];
      unsigned w[6] = {a[0], a[1], a[2], a[3], bb[0], bb[1]};
      unsigned o[5];
#pragma unroll
      for (int k = 0; k < 5; k++) o[k] = __builtin_amdgcn_alignbyte(w[k + 1], w[k], 2);
#pragma unroll
      for (int q = 0; q < 2; q++) {
        *(u32x4*)(p + (2 * q) * K2_CS) = (u32x4){o[q], o[q + 1], o[q + 2], o[q + 3]};
        *(u32x4*)(p + (2 * q + 1) * K2_CS) = (u32x4){w[q + 1], w[q + 2], w[q + 3], w[q + 4]};
      }
    } else if (id < 22) {
      const int ch = (id - 13) / 3, d = ch == 2 ? chH : chA + ch;
      f32x4 v = __builtin_bit_cast(f32x4, in0[rr]);
      if ((d & 7) == 2 || (d & 7) == 5) v += __builtin_bit_cast(f32x4, in1[rr]);
      *(f32x4*)p = v;
    }
  }
  if (kvw) {
    asm volatile("" : "+v"(kvp[0]), "+v"(kvp[1]), "+v"(kvp[2]), "+v"(kvp[3]), "+v"(kvp[4]), "+v"(kvp[5]), "+v"(kvp[6]), "+v"(kvp[7]));
    asm volatile("" : "+v"(kvp[8]), "+v"(kvp[9]), "+v"(kvp[10]), "+v"(kvp[11]), "+v"(kvp[12]), "+v"(kvp[13]), "+v"(kvp[14]), "+v"(kvp[15]));
#pragma unroll
    for (int st = 1; st < 16; st <<= 1)
#pragma unroll
      for (int i = 0; i < 16; i += 2 * st) kvp[i] += kvp[i + st];
    if (tid - 384 < 105) *(f32x4*)(smem + K2_FIN + (tid - 384) * 16) = (f32x4){rb(kvp[0][0]), rb(kvp[0][1]), rb(kvp[0][2]), rb(kvp[0][3])};
  }
  if (!(kvw && wave == 7)) bulk();
  RAW_BARRIER();
  if (kvw && wave == 7) {
    const float* fin = (const float*)(smem + K2_FIN);
    const int m = lane & 31;
    int col = m < 16 ? m : ((m >> 2) == ((b & 2) ? 5 : 4) ? 16 + (m & 3) : 20);
    u32x4 o;
#pragma unroll
    for (int e = 0; e < 4; e++) o[e] = pack2(fin[col * 20 + 8 * h + 2 * e], fin[col * 20 + 8 * h + 2 * e + 1]);
    st16(ws + WS_KVT + (size_t)b * 1280 + lane * 16, o);
    const int m0 = 2 * (lane & 15), m1 = m0 + 1;
    int ca = m0 < 16 ? m0 : ((m0 >> 2) == ((b & 2) ? 5 : 4) ? 16 + (m0 & 3) : 20);
    int cc = m1 < 16 ? m1 : ((m1 >> 2) == ((b & 2) ? 5 : 4) ? 16 + (m1 & 3) : 20);
    u32x4 o2 = {pack2(fin[ca * 20 + 16], fin[ca * 20 + 17]), pack2(fin[ca * 20 + 18], fin[ca * 20 + 19]),
                pack2(fin[cc * 20 + 16], fin[cc * 20 + 17]), pack2(fin[cc * 20 + 18], fin[cc * 20 + 19])};
    if (lane < 16) st16(ws + WS_KVT + (size_t)b * 1280 + 1024 + lane * 16, o2);
  }
  {
    u32x4 hi;
#pragma unroll
    for (int e = 0; e < 4; e++) hi[e] = pack2(*(const float*)(smem + tix[2 * e]), *(const float*)(smem + tix[2 * e + 1]));
    *(u32x4*)(smem + K2_CONV + (tid >> 8) * K2_CH + K2_KOFF + 512 + (tid & 255) * 16) = hi;
    if (wave < 4) {
#pragma unroll
      for (int e = 0; e < 4; e++) hi[e] = pack2(*(const float*)(smem + tix[2 * e] + 6144), *(const float*)(smem + tix[2 * e + 1] + 6144));
      *(u32x4*)(smem + K2_CONV + 2 * K2_CH + K2_KOFF + 512 + tid * 16) = hi;
    }
  }
  if (kvw && wave == 7) {
    VMCNT(0);
    if (lane < 8) __hip_atomic_fetch_add((gu32*)(size_t)(ws + WS_CNT + lane * 128), 1u, __ATOMIC_RELAXED, __HIP_MEMORY_SCOPE_AGENT);
    bulk();
  }
  RAW_BARRIER();
  dma1(80 + wave); dma1(88 + wave); dma1(96);
  STAMP(1, 3);
  f32x4 ca[4], chh[4];
  unsigned pollv = 0;
#pragma unroll
  for (int i = 0; i < 4; i++) { ca[i] = (f32x4){0.f, 0.f, 0.f, 0.f}; chh[i] = (f32x4){0.f, 0.f, 0.f, 0.f}; }
  {
    const int i = lane & 15, kg = lane >> 4, e = kg >> 1, bb = kg & 1;
    const int ap = lds0 + K2_CONV + (i & 3) * K2_CS + (2 * e - bb + 2) * 16 + (i >> 2) * 8;
    const int bp = lds0 + K2_CONV + K2_KOFF + (16 + i - e) * 32 + bb * 16;
    const int apj = ap + (wave >> 2) * K2_CH + (wave & 1) * 256;
    const int bpj = bp + (wave >> 2) * K2_CH - (wave & 1) * 256;
    if (jg == 0) {
#pragma unroll 1
      for (int it = 0; it < 4; it++) conv_unit<0>(ca, apj + it * 64, bpj - it * 64);
    } else {
#pragma unroll 1
      for (int it = 0; it < 4; it++) conv_unit<1>(ca, apj + it * 64, bpj - it * 64);
    }
    GLD1(pollv, (int)WS_CNT + xcd * 128, ws, " sc1");
    if (odd == 0) conv_unit<0>(chh, ap + 2 * K2_CH + wave * 64, bp + 2 * K2_CH - wave * 64);
    else          conv_unit<1>(chh, ap + 2 * K2_CH + wave * 64, bp + 2 * K2_CH - wave * 64);
  }
  STAMP(1, 4);
  asm volatile("s_waitcnt vmcnt(0)" : "+v"(pollv) :: "memory");
  if (__builtin_amdgcn_readfirstlane(pollv) < (unsigned)NB_KV) wait_count(ws + WS_CNT + xcd * 128, NB_KV);
  asm volatile("" ::: "memory");
  u32x4 ka[8]; u32x2 kb[8];
  {
    const char* kp = ws + WS_KVT + (size_t)kh * 8 * 1280;
    const int r8 = r * 8;
#pragma unroll
    for (int i = 0; i < 8; i++) { GLD4(ka[i], l16, kp + i * 1280, " sc1"); GLD2(kb[i], r8, kp + i * 1280 + 1024, " sc1"); }
  }
  RAW_BARRIER();
  dma1(97 + (wave < 2 ? wave : 2));
  {
    if (tid == 0) *(unsigned*)(smem + K2_STG - 16) = 0u;
    char* sp = smem + K2_STG + l16;
    *(f32x4*)(sp + (wave * 2) * 1024) = (wave & 1) ? ca[0] : ca[2];
    *(f32x4*)(sp + (wave * 2 + 1) * 1024) = (wave & 1) ? ca[1] : ca[3];
#pragma unroll
    for (int i = 0; i < 4; i++) *(f32x4*)(sp + 16384 + (i * 8 + wave) * 1024) = chh[i];
  }
  RAW_BARRIER();
  {
    const char* sp = smem + K2_STG + l16;
    f32x4 y[3];
    y[0] = ((wave & 1) ? ca[2] : ca[0]) + *(const f32x4*)(sp + ((wave ^ 1) * 2) * 1024);
    y[1] = ((wave & 1) ? ca[3] : ca[1]) + *(const f32x4*)(sp + ((wave ^ 1) * 2 + 1) * 1024);
    asm volatile("s_waitcnt vmcnt(17)" : "+v"(us[0]), "+v"(us[1]), "+v"(us[2]), "+v"(g_r), "+v"(dsA), "+v"(dsH) :: "memory");
    const float dskA = lo_f(dsA), dskH = lo_f(dsH);
    auto fin = [&](f32x4 s, u32x2 uu, float dsk, int ch, int tile) {
      u32x2 ov;
#pragma unroll
      for (int e = 0; e < 2; e++) {
        unsigned yy = pack2(s[2 * e], s[2 * e + 1]);
        unsigned k = pack2(lo_f(uu[e]) * dsk, hi_f(uu[e]) * dsk);
        ov[e] = pack2(lo_f(yy) + lo_f(k), hi_f(yy) + hi_f(k));
      }
      const int o = (int)WS_AT + (ch * L + tile * 256 + fo) * 2;
      asm volatile("global_store_dwordx2 %0, %1, %2 sc1\n\ts_nop 1" :: "v"(o), "v"(ov), "s"(ws) : "memory");
    };
    fin(y[0], us[0], dskA, chA + (wave >> 2), ft[0]);
    fin(y[1], us[1], dskA, chA + (wave >> 2), ft[1]);
    if (wave < 4) {
      const char* hp = sp + 16384 + wave * 8192;
      f32x4 v[8];
#pragma unroll
      for (int i = 0; i < 8; i++) v[i] = *(const f32x4*)(hp + i * 1024);
      y[2] = ((v[0] + v[1]) + (v[2] + v[3])) + ((v[4] + v[5]) + (v[6] + v[7]));
      fin(y[2], us[2], dskH, chH, ft[2]);
      VMCNT(3);
    } else {
      VMCNT(2);
    }
  }
#pragma unroll
  for (int i = 0; i < 8; i++) asm volatile("" : "+v"(pf0[i]), "+v"(pf1[i]), "+v"(ka[i]), "+v"(kb[i]));
  RAW_BARRIER();
  STAMP(1, 5);
  f32x16 acc[3], c[2];
#pragma unroll
  for (int q = 0; q < 3; q++)
#pragma unroll
    for (int i = 0; i < 16; i++) acc[q][i] = 0.f;
  u32x4 rm = {0u, 0u, 0u, 0u};
  const char* wl = smem + kh * 2560 + l16;
  const char* wl2 = smem + kh * 2560 + 2048 + (h * 16 + (lane & 15)) * 16;
  auto stageA = [&](int i) {
    u32x2 b1u = {h ? 0u : pf1[i][0], h ? 0u : pf1[i][1]};
    f32x16 z;
#pragma unroll
    for (int q = 0; q < 16; q++) z[q] = 0.f;
    z = __builtin_amdgcn_mfma_f32_32x32x16_bf16(__builtin_bit_cast(s16x8, ka[i]), __builtin_bit_cast(s16x8, pf0[i]), z, 0, 0, 0);
    c[i & 1] = __builtin_amdgcn_mfma_f32_32x32x8bf16_1k(__builtin_bit_cast(s16x4, kb[i]), __builtin_bit_cast(s16x4, b1u), z, 0, 0, 0);
    asm volatile("" : "+v"(c[i & 1]));
  };
  u32x4 av[2];
  stageA(0);
#pragma unroll
  for (int i = 0; i < 8; i++) {
    const int j = i & 3, G = i >> 2;
    const int wo_ = G * 51200 + j * 10240;
    s16x8 w0 = *(const s16x8*)(wl + wo_), w1 = *(const s16x8*)(wl + wo_ + 1024), w2 = *(const s16x8*)(wl2 + wo_);
    if (i + 1 < 8) stageA(i + 1);
    const f32x16 cc = c[i & 1];
    const float den = rb(rb(cc[12]) + 1e-6f);
    const float inv = __builtin_amdgcn_rcpf(den);
    unsigned o[6];
#pragma unroll
    for (int q = 0; q < 6; q++) o[q] = pack2(cc[2 * q] * inv, cc[2 * q + 1] * inv);
    asm volatile("" : "+v"(o[4]), "+v"(o[5]));
    const bool keep = (j >> 1) == h;
    rm[2 * (j & 1)] = keep ? o[4] : rm[2 * (j & 1)]; rm[2 * (j & 1) + 1] = keep ? o[5] : rm[2 * (j & 1) + 1];
    u32x4 ob = {o[0], o[1], o[2], o[3]};
    acc[0] = __builtin_amdgcn_mfma_f32_32x32x16_bf16(w0, __builtin_bit_cast(s16x8, ob), acc[0], 0, 0, 0);
    acc[1] = __builtin_amdgcn_mfma_f32_32x32x16_bf16(w1, __builtin_bit_cast(s16x8, ob), acc[1], 0, 0, 0);
    acc[2] = __builtin_amdgcn_mfma_f32_32x32x16_bf16(w2, __builtin_bit_cast(s16x8, ob), acc[2], 0, 0, 0);
    if (j == 3) {
      const int wr_ = G * 51200 + 40960;
      s16x8 w3 = *(const s16x8*)(wl + wr_), w4 = *(const s16x8*)(wl + wr_ + 1024), w5 = *(const s16x8*)(wl2 + wr_);
      acc[0] = __builtin_amdgcn_mfma_f32_32x32x16_bf16(w3, __builtin_bit_cast(s16x8, rm), acc[0], 0, 0, 0);
      acc[1] = __builtin_amdgcn_mfma_f32_32x32x16_bf16(w4, __builtin_bit_cast(s16x8, rm), acc[1], 0, 0, 0);
      acc[2] = __builtin_amdgcn_mfma_f32_32x32x16_bf16(w5, __builtin_bit_cast(s16x8, rm), acc[2], 0, 0, 0);
    }
    if (i == 1) {
      VMCNT(0);
      unsigned old = 0;
      if (lane == 0) old = __hip_atomic_fetch_add((unsigned*)(smem + K2_STG - 16), 1u, __ATOMIC_RELAXED, __HIP_MEMORY_SCOPE_WORKGROUP);
      if (lane == 0 && old == 7u) __hip_atomic_fetch_add((gu32*)(size_t)(ws + WS_CNT + (8 + (b >> 5)) * 128), 1u, __ATOMIC_RELAXED, __HIP_MEMORY_SCOPE_AGENT);
    }
    if (i == 3) { RAW_BARRIER(); }
#define POLL_I 5
    if (i == POLL_I) GLD1(pollv, (int)WS_CNT + (8 + cb) * 128, ws, " sc1");
    if (i == POLL_I + 2) {
      asm volatile("s_waitcnt vmcnt(0)" : "+v"(pollv) :: "memory");
      if (__builtin_amdgcn_readfirstlane(pollv) < 32u) wait_count(ws + WS_CNT + (8 + cb) * 128, 32);
      asm volatile("" ::: "memory");
      const int o = (int)WS_AT + ((c0 + (tid >> 3)) * L + l0 + 8 * (tid & 7)) * 2;
      GLD4(av[0], o, ws, " sc1"); GLD4(av[1], o + (tid < 128 ? 64 * L * 2 : 0), ws, " sc1");
    }
  }
  STAMP(1, 6);
  {
    char* sp = smem + 102400 + t * 30720 + l16;
#pragma unroll
    for (int g = 0; g < 10; g++) {
      const int own = g & 3;
      f32x4 v = {acc[g >> 2][4 * (g & 3)], acc[g >> 2][4 * (g & 3) + 1], acc[g >> 2][4 * (g & 3) + 2], acc[g >> 2][4 * (g & 3) + 3]};
      if (kh != own) *(f32x4*)(sp + (g * 3 + ((kh - own - 1) & 3)) * 1024) = v;
    }
    asm volatile("s_waitcnt vmcnt(0)" : "+v"(av[0]), "+v"(av[1]) :: "memory");
    *(u32x4*)(smem + (tid >> 3) * 192 + (tid & 7) * 16) = av[0];
    if (tid < 128) *(u32x4*)(smem + (64 + (tid >> 3)) * 192 + (tid & 7) * 16) = av[1];
  }
  RAW_BARRIER();
  STAMP(1, 7);
  {
    const char* sp = smem + 102400 + t * 30720 + l16;
    const float g1 = rb(1.f - g_r);
    const char* atp = smem + (8 * kh + 4 * h + ((lane & 15) >> 2)) * 192 + (32 * t + (lane & 16) + 4 * (lane & 3)) * 2;
    u16* op = out + (size_t)(l0 + 32 * t + r) * D + c0 + 8 * kh + 4 * h;
#pragma unroll
    for (int tl = 0; tl < 3; tl++) {
      if (tl == 2 && kh >= 2) break;
      f32x4 m;
      if (kh == 0)      m = (f32x4){acc[tl][0], acc[tl][1], acc[tl][2], acc[tl][3]};
      else if (kh == 1) m = (f32x4){acc[tl][4], acc[tl][5], acc[tl][6], acc[tl][7]};
      else if (kh == 2) m = (f32x4){acc[tl][8], acc[tl][9], acc[tl][10], acc[tl][11]};
      else              m = (f32x4){acc[tl][12], acc[tl][13], acc[tl][14], acc[tl][15]};
      const char* pp = sp + (tl * 4 + kh) * 3072;
      f32x4 p0 = *(const f32x4*)pp, p1 = *(const f32x4*)(pp + 1024), p2 = *(const f32x4*)(pp + 2048);
      f32x4 v = (m + p1) + (p0 + p2);
      s16x4 at = __builtin_amdgcn_ds_read_tr16_b64_v4i16((lds_s16x4_p)(atp + tl * 32 * 192));
      u32x2 au = __builtin_bit_cast(u32x2, at);
      u32x2 ov;
#pragma unroll
      for (int e = 0; e < 2; e++) {
        unsigned bq = pack2(v[2 * e], v[2 * e + 1]);
        unsigned gb = pack2(g1 * lo_f(bq), g1 * hi_f(bq));
        unsigned ga = pack2(g_r * lo_f(au[e]), g_r * hi_f(au[e]));
        ov[e] = pack2(lo_f(ga) + lo_f(gb), hi_f(ga) + hi_f(gb));
      }
      *(u32x2*)(op + 32 * tl) = ov;
    }
  }
  asm volatile("code_tail_mark:" ::: "memory");
  STAMP(1, 1);
}

extern "C" void kernel_launch(void* const* d_in, const int* in_sizes, int n_in, void* d_out, int out_size, void* d_ws, size_t ws_size,
                              hipStream_t stream) {
  const u16* u = (const u16*)d_in[0]; const u16* logA = (const u16*)d_in[1]; const u16* Aim = (const u16*)d_in[2];
  const u16* Cr = (const u16*)d_in[3]; const u16* Ci = (const u16*)d_in[4]; const u16* logdt = (const u16*)d_in[5];
  const u16* Dsk = (const u16*)d_in[6]; const u16* wqkv = (const u16*)d_in[8]; const u16* wo = (const u16*)d_in[9];
  const u16* gw = (const u16*)d_in[10];
  char* ws = (char*)d_ws;
  k1<<<NB_GEMM + NB_GATE + NB_KG, 512, 0, stream>>>(u, logA, Aim, Cr, Ci, logdt, wqkv, gw, wo, ws);
  k2<<<256, 512, 0, stream>>>(Dsk, ws, (u16*)d_out);
}
```

```cpp
#include <hip/hip_runtime.h>
#include <hip/hip_bf16.h>
#include <stdint.h>

typedef unsigned short u16;
typedef __attribute__((ext_vector_type(8))) short s16x8;
typedef __attribute__((ext_vector_type(4))) short s16x4;
typedef __attribute__((ext_vector_type(4))) float f32x4;
typedef __attribute__((ext_vector_type(16))) float f32x16;
typedef __attribute__((ext_vector_type(4))) unsigned u32x4;
typedef __attribute__((ext_vector_type(2))) unsigned u32x2;
typedef __attribute__((address_space(3))) s16x4* lds_s16x4_p;
typedef const __attribute__((address_space(1))) void* gptr_t;
typedef __attribute__((address_space(3))) void* lptr_t;

constexpr int L = 2048, D = 640, NS = 192, NH = 32;
constexpr int UT_PAD = 32, UT_STRIDE = L + UT_PAD;
constexpr size_t WS_UT   = 0;
constexpr size_t WS_KIMG = WS_UT + (size_t)D * UT_STRIDE * 2;
constexpr size_t WS_PQ   = WS_KIMG + (size_t)D * 4096;
constexpr size_t WS_PART = WS_PQ + (size_t)L * D * 2;
constexpr size_t WS_GATE = WS_PART + (size_t)32 * 16 * 1680;
constexpr size_t WS_WOF  = WS_GATE + (size_t)L * 4;
constexpr size_t WS_KVT  = WS_WOF + (size_t)D * D * 2;
constexpr size_t WS_AT   = WS_KVT + (size_t)32 * 1280;
constexpr size_t WS_CNT  = WS_AT + (size_t)D * L * 2;
constexpr size_t WS_END  = WS_CNT + 4096;
#define TGET(v) do {} while (0)
#define STEPT(i) do {} while (0)
#define TPUT(kid, slot, thr, v) do {} while (0)
#define STAMP(kid, slot) do {} while (0)

__device__ __forceinline__ float b2f(u16 v) { return __uint_as_float(((unsigned)v) << 16); }
typedef __bf16 bf16x2_t __attribute__((ext_vector_type(2)));
typedef float f32x2 __attribute__((ext_vector_type(2)));
__device__ __forceinline__ unsigned pack2(float a, float b) { f32x2 v = {a, b}; bf16x2_t r = __builtin_convertvector(v, bf16x2_t); return __builtin_bit_cast(unsigned, r); }
__device__ __forceinline__ float lo_f(unsigned u) { return __uint_as_float(u << 16); }
__device__ __forceinline__ float hi_f(unsigned u) { return __uint_as_float(u & 0xffff0000u); }
__device__ __forceinline__ float rb(float f) { return lo_f(pack2(f, f)); }
__device__ __forceinline__ unsigned phi2(float a, float b) {
  unsigned t = pack2(a, b); float t0 = lo_f(t), t1 = hi_f(t);
  float e0 = __builtin_amdgcn_fmed3f(__builtin_amdgcn_exp2f(t0 * 1.44269504f), 0.f, 1.f);
  float e1 = __builtin_amdgcn_fmed3f(__builtin_amdgcn_exp2f(t1 * 1.44269504f), 0.f, 1.f);
  unsigned r = pack2(__builtin_fmaxf(t0, 0.f) + e0, __builtin_fmaxf(t1, 0.f) + e1);
  return pack2(lo_f(r) + 1e-6f, hi_f(r) + 1e-6f);
}
#define VMCNT(n) asm volatile("s_waitcnt vmcnt(" #n ")" ::: "memory")
#define RAW_BARRIER() do { asm volatile("s_waitcnt lgkmcnt(0)" ::: "memory"); __builtin_amdgcn_s_barrier(); asm volatile("" ::: "memory"); } while (0)
__device__ __forceinline__ void glds16(const void* g, void* l) { __builtin_amdgcn_global_load_lds((gptr_t)g, (lptr_t)l, 16, 0, 0); }

#define OPQ_S(v) asm volatile("" : "+s"(v))
typedef f32x2 cf;
__device__ __forceinline__ float smul(float a, float b) { float o; asm("v_mul_f32 %0, %1, %2" : "=v"(o) : "v"(a), "v"(b)); return o; }
__device__ __forceinline__ float sfma(float a, float b, float c) { float o; asm("v_fma_f32 %0, %1, %2, %3" : "=v"(o) : "v"(a), "v"(b), "v"(c)); return o; }
__device__ __forceinline__ float sfnma(float a, float b, float c) { float o; asm("v_fma_f32 %0, -%1, %2, %3" : "=v"(o) : "v"(a), "v"(b), "v"(c)); return o; }
__device__ __forceinline__ float ssub(float a, float b) { float o; asm("v_sub_f32 %0, %1, %2" : "=v"(o) : "v"(a), "v"(b)); return o; }
__device__ __forceinline__ cf cmul(cf a, cf b) {
  cf o;
  asm("v_pk_mul_f32 %0, %1, %2 op_sel:[0,0] op_sel_hi:[0,1]\n\t"
      "v_pk_fma_f32 %0, %1, %2, %0 op_sel:[1,1,0] op_sel_hi:[1,0,1] neg_lo:[1,0,0]" : "=&v"(o) : "v"(a), "v"(b));
  return o;
}

#define CPF 3
#define LPF 1
#define PFD 0
#define PFM_ 1
#define NP0 5
#define SPRIO 3
#define NI0 208
#define NB2 3
#define TB0 1
#define TBD 2
#define MIDX ((rowblk & 3) + 4 * (cb & 7))
#define MPS 3
#define PT0 30
#define PT1 110
#define PTD 20
constexpr int K1_LDS = 81920;
#define GLDS 1
constexpr int NB_GEMM = 256, NB_GATE = GLDS ? 0 : 16, NB_KG = 240, NB_KC = 214;
#define NW0 3
#define K2PF 1
#define K2PFS "1"
#define KVP 1
#define PQA 1
#define PQB 5
#define STR2(x) #x
#define STR(x) STR2(x)
#define PQBS "2*(" STR(PQB) "-" STR(PQA) ")"
#define WTQ 0
#define WTP 0
#define WTO 0
#define WT 1
__device__ __forceinline__ void st16(void* p, u32x4 v) {
  asm volatile("global_store_dwordx4 %0, %1, off sc1\n\ts_nop 2" :: "v"(p), "v"(v) : "memory");
}

#define BAR_ONLY() do { asm volatile("" ::: "memory"); __builtin_amdgcn_s_barrier(); asm volatile("" ::: "memory"); } while (0)
#define SB() __builtin_amdgcn_sched_barrier(0)
template <bool ISQ>
__device__ __forceinline__ void mfma_part(char* smem, char* __restrict__ ws, int wave, f32x4 (&ep)[2][4]) {
  const int lane = threadIdx.x & 63;
  char* sB = smem; char* sA = smem + 32768;
  const int wr = wave & 1, wc = (wave >> 1) & 1;
  const int g = lane >> 4, fr = lane & 15;
  f32x4 acc[4][4];
#pragma unroll
  for (int m = 0; m < 4; m++)
#pragma unroll
    for (int c = 0; c < 4; c++) acc[m][c] = (f32x4){0.f, 0.f, 0.f, 0.f};
  const int q4 = fr >> 2, p4 = lane & 3;
  int aoff[2];
#pragma unroll
  for (int ks = 0; ks < 2; ks++) aoff[ks] = (wr * 64 + fr) * 128 + (((4 * ks + g) ^ (fr & 7)) << 4);
  int boff[4];
  {
    const int f = q4 | ((g & 1) << 2);
#pragma unroll
    for (int c = 0; c < 4; c++) boff[c] = (8 * g + q4) * 256 + (((4 * wc + c) ^ f) << 5) + p4 * 8;
  }
  s16x8 af[2][4]; s16x4 bl[4], bh[4];
  auto rdA = [&](int buf, int m, const char* pa) { af[buf][m] = *(const s16x8*)(pa + m * 2048); };
  auto rdBl = [&](int c, const char* pb) { bl[c] = __builtin_amdgcn_ds_read_tr16_b64_v4i16((lds_s16x4_p)(pb + boff[c])); };
  auto rdBh = [&](int c, const char* pb) { bh[c] = __builtin_amdgcn_ds_read_tr16_b64_v4i16((lds_s16x4_p)(pb + boff[c] + 1024)); };
  BAR_ONLY();
  STAMP(0, 2);
#pragma unroll
  for (int c = 0; c < 4; c++) rdA(0, c, sA + aoff[0]);
#pragma unroll
  for (int c = 0; c < 3; c++) { rdBl(c, sB); rdBh(c, sB); }
  int ao0 = 0, ao1 = 16384;
  auto grp = [&](int buf, int c, int m) {
    const s16x8 bf = __builtin_shufflevector(bl[c], bh[c], 0, 1, 2, 3, 4, 5, 6, 7);
    if (ISQ) acc[m][c] = __builtin_amdgcn_mfma_f32_16x16x32_bf16(bf, af[buf][m], acc[m][c], 0, 0, 0);
    else     acc[m][c] = __builtin_amdgcn_mfma_f32_16x16x32_bf16(af[buf][m], bf, acc[m][c], 0, 0, 0);
  };
#pragma unroll 1
  for (int j2 = 0; j2 < 5; j2++) {
#pragma unroll
    for (int st = 0; st < 4; st++) {
      const int hs = st & 1;
      SB();
      asm volatile("s_waitcnt lgkmcnt(2)" ::: "memory"); __builtin_amdgcn_s_barrier(); asm volatile("" ::: "memory");
      SB();
      const char* pa = sA + (hs == 0 ? ao0 + aoff[1] : ao1 + aoff[0]);
      const char* pc = sB + st * 8192;
      const char* pb = sB + ((st + 1) & 3) * 8192;
      grp(hs, 0, 0); SB(); rdBl(3, pc); rdBh(3, pc); SB();
      grp(hs, 0, 1); SB(); rdA(hs ^ 1, 0, pa); rdA(hs ^ 1, 1, pa); SB();
      grp(hs, 0, 2); SB(); rdA(hs ^ 1, 2, pa); rdA(hs ^ 1, 3, pa); SB();
      grp(hs, 0, 3); SB();
#pragma unroll
      for (int c = 1; c < 4; c++) {
        grp(hs, c, 0); SB(); rdBl(c - 1, pb); rdBh(c - 1, pb); SB();
        grp(hs, c, 1); grp(hs, c, 2); grp(hs, c, 3); SB();
      }
      if (hs == 1) { ao0 = ao1; ao1 = ao1 == 32768 ? 0 : ao1 + 16384; }
    }
  }
  STAMP(0, 3);
  int l3 = threadIdx.x; asm volatile("" : "+v"(l3));
  char* xch = smem + 32768 + (l3 >> 6) * 8192 + (l3 & 63) * 16;
#pragma unroll
  for (int i = 0; i < 8; i++) *(f32x4*)(xch + i * 1024) = acc[2 + (i >> 2)][i & 3];
#pragma unroll
  for (int i = 0; i < 8; i++) ep[i >> 2][i & 3] = acc[i >> 2][i & 3];
  RAW_BARRIER();
}
__device__ __forceinline__ void dma4x(const int (&vo)[4], const char* sbase, int ldsa) {
  asm volatile("s_mov_b32 m0, %5\n\ts_nop 0\n\tglobal_load_lds_dwordx4 %0, %4\n\tglobal_load_lds_dwordx4 %1, %4 offset:1024\n\t"
               "global_load_lds_dwordx4 %2, %4 offset:2048\n\tglobal_load_lds_dwordx4 %3, %4 offset:3072"
               :: "v"(vo[0]), "v"(vo[1]), "v"(vo[2]), "v"(vo[3]), "s"(sbase), "s"(ldsa) : "memory", "m0");
}
__device__ __forceinline__ void gemm_role(char* smem, const u16* __restrict__ x, const u16* __restrict__ W, const u16* __restrict__ gw, char* __restrict__ ws,
                                          int rowblk, int cb) {
  const int tid = threadIdx.x, lane = tid & 63;
  const int wave = __builtin_amdgcn_readfirstlane(tid >> 6);
  const bool ISQ = cb < 5;
  const int row0 = rowblk * 128, col0 = cb * 128;
  const int kvj = cb - 5;
  const int wr = wave & 1, wc = (wave >> 1) & 1, hf = wave >> 2;
  f32x4 ep[2][4];
  if (wave >= 4) {
    const int lw = wave & 1;
    const int l0 = (int)(size_t)(lptr_t)smem;
    unsigned pf0 = 0;
    const int pidx = (rowblk & 3) + 4 * (cb & 7);
    auto pfaddr = [&](int stg, int r) {
      const int q = r & 15;
      const int ln = (cb & 8) ? (q < 8 ? 12 + q : 14 + q) : (q < 13 ? q : 7 + q);
      return r < 16 ? (const char*)x + ((rowblk >> 2) * 512 + pidx * 16 + q) * 1280 + stg * 128
                    : (const char*)W + (64 * stg + pidx * 2 + (r >> 4) - 1) * 3840 + ln * 128;
    };
    {
      const int q = (wave - 4) * (NI0 / 4) + min(lane, NI0 / 4 - 1), stg = q / 48;
      asm volatile("global_load_dword %0, %1, off" : "+v"(pf0) : "v"(pfaddr(stg, q - 48 * stg)));
      SB();
    }
    __builtin_amdgcn_s_setprio(SPRIO);
    if (wave < 6) {
      int voA[4], voB[4];
#pragma unroll
      for (int i = 0; i < 4; i++) {
        voA[i] = (64 * lw + 8 * i + (lane >> 3)) * 1280 + (((lane & 7) ^ (lane >> 3)) << 4) - 1024 * i;
        const int fB = (lane >> 4) | ((i >> 1) << 2);
        const int c16 = (((lane & 15) >> 1) ^ fB) * 2 + (lane & 1);
        int bcol = col0 + 8 * c16;
        if (!ISQ) bcol = min(640 + 640 * (c16 >> 3) + ((60 * kvj) & ~7) + 8 * (c16 & 7), 1912);
        voB[i] = (16 * lw + 4 * i + (lane >> 4)) * 3840 + bcol * 2 - 1024 * i;
      }
      const char* pA = (const char*)x + row0 * 1280;
      const char* pB = (const char*)W;
      const int lA = l0 + 32768 + lw * 8192, lB = l0 + lw * 4096;
      auto issA = [&](int slot_off, int h) { dma4x(voA, pA + h * 32 * 1280, lA + slot_off + h * 4096); if (h) pA += 128; };
      auto issB = [&](int slot_off) { dma4x(voB, pB, lB + slot_off); pB += 32 * 3840; };
      TGET(tl0);
      asm volatile("s_waitcnt vmcnt(0)" :: "v"(pf0) : "memory");
      TGET(tl1);
      issA(0, 0); issB(0); issA(0, 1);
      issB(8192); issA(16384, 0);
      VMCNT(8);
      TGET(tl2);
      BAR_ONLY();
      issB(16384); issA(16384, 1);
      VMCNT(12); BAR_ONLY();
      issB(24576); issA(32768, 0); issA(32768, 1);
      VMCNT(12); BAR_ONLY();
      issB(0); issA(0, 0);
      int ao = 0, bo = 8192;
#pragma unroll 1
      for (int j = 1; j < 7; j++) {
        VMCNT(12); BAR_ONLY();
        issB(bo); bo = (bo + 8192) & 32767; issA(ao, 1);
        ao = ao == 32768 ? 0 : ao + 16384;
        VMCNT(12); BAR_ONLY();
        issB(bo); bo = (bo + 8192) & 32767; issA(ao, 0);
      }
      VMCNT(12); BAR_ONLY();
      issB(8192); issA(0, 1);
      VMCNT(12); BAR_ONLY();
      issB(16384);
      VMCNT(8); BAR_ONLY();
      issB(24576);
      VMCNT(4); BAR_ONLY();
      VMCNT(0); BAR_ONLY();
      BAR_ONLY();
      TPUT(0, 4, 256, tl0);
      TPUT(0, 5, 256, tl1); TPUT(0, 6, 256, tl2);
      BAR_ONLY();
    } else {
      u32x4 trv[2][4];
      constexpr int CNT2 = 480 - NI0, NL2 = (CNT2 + 2 * NB2 - 1) / (2 * NB2);
      const char* pp[NB2];
#pragma unroll
      for (int i = 0; i < NB2; i++) {
        const int it = NI0 * 32 + ((pidx & 3) * 8 + (pidx >> 2)) * CNT2 + min((2 * i + lw) * NL2 + min(lane, NL2 - 1), CNT2 - 1);
        const int stg = it / 1536, r = it - 1536 * stg, q = r & 15;
        const int ln = (cb & 8) ? (q < 8 ? 12 + q : 14 + q) : (q < 13 ? q : 7 + q);
        pp[i] = r < 512 ? (const char*)x + ((rowblk >> 2) * 512 + r) * 1280 + stg * 128 : (const char*)W + (64 * stg + (r >> 4) - 32) * 3840 + ln * 128;
      }
      const int tb2 = TB0 + TBD * (pidx & 3);
      int tro;
      {
        const int fr = lane & 15, g = lane >> 4, p4 = lane & 3, r = 8 * g + (fr >> 2);
        tro = l0 + 32768 + r * 128 + (((4 * lw + (p4 >> 1)) ^ (r & 7)) << 4) + (p4 & 1) * 8;
      }
      auto transp = [&](int ao) {
#pragma unroll
        for (int hh = 0; hh < 2; hh++) {
          u32x2 tr[8];
          const int a = (tro ^ (32 * hh)) + ao, a2 = (a ^ 64) + 512;
          asm volatile("ds_read_b64_tr_b16 %0, %8\n\tds_read_b64_tr_b16 %1, %9\n\tds_read_b64_tr_b16 %2, %8 offset:4096\n\tds_read_b64_tr_b16 %3, %9 offset:4096\n\t"
                       "ds_read_b64_tr_b16 %4, %8 offset:8192\n\tds_read_b64_tr_b16 %5, %9 offset:8192\n\tds_read_b64_tr_b16 %6, %8 offset:12288\n\tds_read_b64_tr_b16 %7, %9 offset:12288\n\t"
                       "s_waitcnt lgkmcnt(0)"
                       : "=&v"(tr[0]), "=&v"(tr[1]), "=&v"(tr[2]), "=&v"(tr[3]), "=&v"(tr[4]), "=&v"(tr[5]), "=&v"(tr[6]), "=&v"(tr[7])
                       : "v"(a), "v"(a2) : "memory");
#pragma unroll
          for (int i = 0; i < 4; i++) trv[hh][i] = (u32x4){tr[2 * i][0], tr[2 * i][1], tr[2 * i + 1][0], tr[2 * i + 1][1]};
        }
      };
      unsigned pf2 = 0;
      auto burst = [&](int hs) {
        static_assert(NB2 == 3, "");
        asm volatile("s_cmp_lg_u32 %4, %5\n\ts_cbranch_scc1 1f\n\tglobal_load_dword %0, %1, off\n\tglobal_load_dword %0, %2, off\n\tglobal_load_dword %0, %3, off\n1:"
                     : "+v"(pf2) : "v"(pp[0]), "v"(pp[1]), "v"(pp[2]), "s"(hs), "s"(tb2) : "scc");
      };
      asm volatile("s_waitcnt vmcnt(0)" :: "v"(pf0) : "memory");
      BAR_ONLY();
      int ao = 0;
#pragma unroll 1
      for (int j = 0; j < 10; j++) {
        BAR_ONLY();
        if (!lw) STEPT(2 * j);
        burst(2 * j);
        if (j == cb) transp(ao);
        BAR_ONLY();
        if (!lw) STEPT(2 * j + 1);
        burst(2 * j + 1);
        ao = ao == 32768 ? 0 : ao + 16384;
      }
      asm volatile("s_waitcnt vmcnt(0)" :: "v"(pf2) : "memory");
      if (cb < 10) {
        u16* dst = (u16*)(ws + WS_UT) + (size_t)(64 * cb + 32 * lw + (lane & 15)) * UT_STRIDE + 8 * (lane >> 4);
#pragma unroll
        for (int hh = 0; hh < 2; hh++) {
#pragma unroll
          for (int i = 0; i < 4; i++) st16(dst + hh * 16 * UT_STRIDE + UT_PAD + row0 + 32 * i, trv[hh][i]);
          if (rowblk == 0) st16(dst + hh * 16 * UT_STRIDE, (u32x4){0u, 0u, 0u, 0u});
        }
      }
      BAR_ONLY();
    }
    __builtin_amdgcn_s_setprio(0);
    char* xch = smem + 32768 + (wave & 3) * 8192 + lane * 16;
#pragma unroll
    for (int i = 0; i < 8; i++) ep[i >> 2][i & 3] = *(const f32x4*)(xch + i * 1024);
  } else if (ISQ) {
    mfma_part<true>(smem, ws, wave, ep);
  } else {
    mfma_part<false>(smem, ws, wave, ep);
  }
  int l2 = threadIdx.x; asm volatile("" : "+v"(l2));
  const int g = (l2 >> 4) & 3, fr = l2 & 15;
  if (ISQ) {
    char* pq0 = ws + WS_PQ; char* pq1 = ws + WS_PQ + (size_t)L * 16 * 32 * 2;
#pragma unroll
    for (int c = 0; c < 4; c++) {
      const int n = col0 + 64 * wc + 16 * c + 4 * g;
      const int hd = n / 20, d0 = n - 20 * hd;
#pragma unroll
      for (int m = 0; m < 2; m++) {
        const int rb64 = rowblk * 2 + wr, r = 16 * m + fr;
        u32x2 o;
        o[0] = phi2(ep[m][c][0], ep[m][c][1]);
        o[1] = phi2(ep[m][c][2], ep[m][c][3]);
        const int fi = (rb64 * 32 + hd) * 2 + hf;
        char* dst = d0 < 16 ? pq0 + ((size_t)fi * 64 + (d0 >> 3) * 32 + r) * 16 + (d0 & 7) * 2 : pq1 + ((size_t)fi * 32 + r) * 8;
        *(u32x2*)dst = o;
      }
    }
  } else {
    const int o = (60 * kvj) & 7, zc = o ? 0 : 60;
    const int lane2 = l2 & 63;
    char* fb = smem;
#pragma unroll
    for (int c = 0; c < 4; c++) {
      u32x4 a;
      if (wc == 0) {
        a[0] = phi2(ep[0][c][0], ep[0][c][1]); a[1] = phi2(ep[0][c][2], ep[0][c][3]);
        a[2] = phi2(ep[1][c][0], ep[1][c][1]); a[3] = phi2(ep[1][c][2], ep[1][c][3]);
      } else {
        a[0] = pack2(ep[0][c][0], ep[0][c][1]); a[1] = pack2(ep[0][c][2], ep[0][c][3]);
        a[2] = pack2(ep[1][c][0], ep[1][c][1]); a[3] = pack2(ep[1][c][2], ep[1][c][3]);
        if (16 * c + fr == zc) a = (u32x4){0x3F803F80u, 0x3F803F80u, 0x3F803F80u, 0x3F803F80u};
      }
      *(u32x4*)(fb + ((((wc * 4 + wr * 2 + hf) * 4 + c) * 64 + lane2) << 4)) = a;
    }
    RAW_BARRIER();
    const int np = wave < 4 ? 1 : 2;
    for (int i = 0; i < np; i++) {
      const int p = wave < 4 ? wave : wave * 2 - 4 + i;
      int ta, tb;
      if (p < 10) { ta = (int)((0x3322211100ull >> (4 * p)) & 15); tb = (int)((0x3232121010ull >> (4 * p)) & 15); }
      else { ta = (o ? 2 : 0) + (p - 10); tb = o ? 0 : 3; }
      const char* pk = fb + ((ta * 64 + lane2) << 4);
      const char* pv = fb + 16384 + ((tb * 64 + lane2) << 4);
      f32x4 dd = {0.f, 0.f, 0.f, 0.f};
#pragma unroll
      for (int rg = 0; rg < 4; rg++)
        dd = __builtin_amdgcn_mfma_f32_16x16x32_bf16(*(const s16x8*)(pk + rg * 4096), *(const s16x8*)(pv + rg * 4096), dd, 0, 0, 0);
      const int kc0 = 16 * ta + 4 * g - o;
      const int hk = kc0 / 20, dk0 = kc0 - 20 * hk;
      const int vcol = 16 * tb + fr;
      const int dv = vcol == zc ? 20 : vcol - o - 20 * hk;
      if (kc0 >= 0 && kc0 < (kvj == 10 ? 40 : 60) && dv >= 0 && (dv < 20 || vcol == zc))
        *(f32x4*)(ws + WS_PART + (size_t)(((3 * kvj + hk) * 16 + rowblk) * 21 + dv) * 80 + dk0 * 4) = dd;
    }
  }
}

__device__ __forceinline__ cf zpow(float th, float tl, float rh, float rl, float p) {
  float e = __builtin_amdgcn_exp2f(th * p); e = fmaf(e, (tl * p) * 0.69314718f, e);
  float t = rh * p; float f = (t - __builtin_rintf(t)) + rl * p;
  cf o; o[0] = e * __builtin_amdgcn_cosf(f); o[1] = e * __builtin_amdgcn_sinf(f); return o;
}
#define PKM(o, a, b) "v_pk_mul_f32 " o ", " a ", " b " op_sel:[0,0] op_sel_hi:[0,1]\n\t"
#define PKF(o, a, b) "v_pk_fma_f32 " o ", " a ", " b ", " o " op_sel:[1,1,0] op_sel_hi:[1,0,1] neg_lo:[1,0,0]\n\t"
__device__ __forceinline__ void cmul4(cf& o0, cf& o1, cf& o2, cf& o3, cf a0, cf b0, cf a1, cf b1, cf a2, cf b2, cf a3, cf b3) {
  asm(PKM("%0", "%4", "%5") PKM("%1", "%6", "%7") PKM("%2", "%8", "%9") PKM("%3", "%10", "%11")
      PKF("%0", "%4", "%5") PKF("%1", "%6", "%7") PKF("%2", "%8", "%9") PKF("%3", "%10", "%11")
      : "=&v"(o0), "=&v"(o1), "=&v"(o2), "=&v"(o3)
      : "v"(a0), "v"(b0), "v"(a1), "v"(b1), "v"(a2), "v"(b2), "v"(a3), "v"(b3));
}
__device__ __forceinline__ void batch4(unsigned* h, cf b, cf t0, cf t1, cf t2, cf t3) {
  cf w0, w1, w2, w3;
  asm(PKM("%0", "%4", "%5") PKM("%1", "%4", "%6") PKM("%2", "%4", "%7") PKM("%3", "%4", "%8")
      PKF("%0", "%4", "%5") PKF("%1", "%4", "%6") PKF("%2", "%4", "%7") PKF("%3", "%4", "%8")
      : "=&v"(w0), "=&v"(w1), "=&v"(w2), "=&v"(w3) : "v"(b), "v"(t0), "v"(t1), "v"(t2), "v"(t3));
  h[0] = pack2(w0[0], w0[1]); h[1] = pack2(w1[0], w1[1]); h[2] = pack2(w2[0], w2[1]); h[3] = pack2(w3[0], w3[1]);
}
__device__ __forceinline__ void batch3(unsigned* h, cf b, cf t1, cf t2, cf t3) {
  cf w1, w2, w3;
  asm(PKM("%0", "%3", "%4") PKM("%1", "%3", "%5") PKM("%2", "%3", "%6")
      PKF("%0", "%3", "%4") PKF("%1", "%3", "%5") PKF("%2", "%3", "%6")
      : "=&v"(w1), "=&v"(w2), "=&v"(w3) : "v"(b), "v"(t1), "v"(t2), "v"(t3));
  h[0] = pack2(b[0], b[1]); h[1] = pack2(w1[0], w1[1]); h[2] = pack2(w2[0], w2[1]); h[3] = pack2(w3[0], w3[1]);
}
constexpr int KG_RS = 288, KG_PL = 16 * KG_RS;
__device__ __forceinline__ void kgen_role(char* smem, const u16* __restrict__ logA, const u16* __restrict__ Aim,
                                          const u16* __restrict__ Cr, const u16* __restrict__ Ci, const u16* __restrict__ logdt,
                                          const u16* __restrict__ x, const u16* __restrict__ W, const u16* __restrict__ wo,
                                          const u16* __restrict__ gw, char* __restrict__ ws, int blk, unsigned pfv) {
  const int tid = threadIdx.x, lane = tid & 63;
  const int wave = __builtin_amdgcn_readfirstlane(tid >> 6);
  constexpr int RS = KG_RS, PL = KG_PL;
  const bool wow = wave < 4 && blk < 200;
  int wid;
  unsigned wr_[8];
  {
    const int wt = blk * 4 + wave, oct = wt / 10, n = (wt - 10 * oct) * 64 + lane;
    const int h = oct & 1, kh = (oct >> 1) & 3, gs5 = oct >> 3, grp = gs5 >= 5, s5 = gs5 - 5 * grp, hb = 8 * kh + 4 * grp;
    const int cb = n / 80, m = n - 80 * cb;
    const int slot = m < 64 ? (m >> 5) * 64 + h * 32 + (m & 31) : 64 + h * 16 + m;
    wid = ((cb * 10 + gs5) * 4 + kh) * 160 + slot;
    int k0, k1;
    if (s5 < 4) { k0 = 20 * (hb + s5) + 4 * h; k1 = k0 + 8; }
    else { k0 = 20 * (hb + 2 * h) + 16; k1 = k0 + 20; }
    if (wow) {
      const u16* p0 = wo + (size_t)k0 * D + n; const u16* p1 = wo + (size_t)k1 * D + n;
#pragma unroll
      for (int e = 0; e < 4; e++) {
        asm volatile("global_load_ushort %0, %1, off offset:%2" : "=v"(wr_[e]) : "v"(p0), "i"(e * 1280));
        asm volatile("global_load_ushort %0, %1, off offset:%2" : "=v"(wr_[4 + e]) : "v"(p1), "i"(e * 1280));
      }
    }
  }
  char* tb = smem + wave * (2 * PL);
  char* wp = tb + lane * 4;
  const char* rp = tb + (lane & 15) * RS + (lane >> 4) * 16;
  const int nrep = blk >= NB_KC ? 0 : (wave == 7 ? 2 : 1);
  unsigned pa[5], pb[5] = {0u, 0u, 0u, 0u, 0u};
  {
    const int task = min(blk * 9 + wave, 1919), pi = task * 64 + lane;
    pa[0] = logdt[task / 3]; pa[1] = logA[pi]; pa[2] = Aim[pi]; pa[3] = Cr[pi]; pa[4] = Ci[pi];
    if (wave == 7) {
      const int t2 = min(blk * 9 + 8, 1919), p2 = t2 * 64 + lane;
      pb[0] = logdt[t2 / 3]; pb[1] = logA[p2]; pb[2] = Aim[p2]; pb[3] = Cr[p2]; pb[4] = Ci[p2];
    }
    asm volatile("" : "+v"(pb[0]), "+v"(pb[1]), "+v"(pb[2]), "+v"(pb[3]), "+v"(pb[4]));
  }
#pragma unroll 1
  for (int rep = 0; rep < nrep; rep++) {
    const float dt = rb(expf(lo_f(pa[0])));
    float ar;
    {
      const float la = lo_f(pa[1]);
      float h = la * 1.44269502f; float l = fmaf(la, 1.44269502f, -h) + la * 1.92596299e-8f;
      float e = __builtin_amdgcn_exp2f(h); e = fmaf(e, l * 0.69314718f, e);
      ar = -rb(e) * dt;
    }
    const float ai = lo_f(pa[2]) * dt;
    cf C; C[0] = 2.f * (lo_f(pa[3]) * dt); C[1] = 2.f * (lo_f(pa[4]) * dt);
#pragma unroll
    for (int i = 0; i < 5; i++) pa[i] = pb[i];
    const float th = ar * 1.44269502f, tl = fmaf(ar, 1.44269502f, -th) + ar * 1.92596299e-8f;
    const float rh = ai * 0.159154937f, rl = fmaf(ai, 0.159154937f, -rh) + ai * 6.42063833e-9f;
    const cf one = {1.f, 0.f};
    cf S1 = zpow(th, tl, rh, rl, 1.f), Q1 = zpow(th, tl, rh, rl, 8.f), R1 = zpow(th, tl, rh, rl, 128.f);
    Q1[1] = -Q1[1];
    cf S2, Q2, R2, G1, S3, S4, Q3, P1, R3, T1, G2, G3, S5, S6, S7, P2, CT1, T2, P3, CS4, CT2, CT3, CT4, dm0;
    cmul4(S2, Q2, R2, G1, S1, S1, Q1, Q1, R1, R1, C, R1);
    cmul4(S3, S4, Q3, P1, S2, S1, S2, S2, Q2, Q1, Q2, Q2);
    cmul4(R3, T1, G2, P2, R2, R1, R2, R2, C, R2, P1, P1);
    cmul4(S5, S6, S7, P3, S4, S1, S4, S2, S4, S3, P2, P1);
    cmul4(G3, CT1, T2, dm0, C, R3, C, T1, T1, T1, C, R1);
    cmul4(CS4, CT2, CT3, dm0, CT1, S4, C, T2, CT1, T2, C, R1);
    CT4 = cmul(CT2, T2);
    auto rows4 = [&](int pl, int row, int rs, cf b, cf t0, cf t1, cf t2, cf t3) {
      unsigned h[4];
      batch4(h, b, t0, t1, t2, t3);
#pragma unroll
      for (int k = 0; k < 4; k++) *(unsigned*)(wp + pl * PL + (row + k * rs) * RS) = h[k];
    };
    auto rows3 = [&](int pl, int row, int rs, cf b, cf t1, cf t2, cf t3) {
      unsigned h[4];
      batch3(h, b, t1, t2, t3);
#pragma unroll
      for (int k = 0; k < 4; k++) *(unsigned*)(wp + pl * PL + (row + k * rs) * RS) = h[k];
    };
    *(unsigned*)(wp) = 0x00003F80u;
    *(unsigned*)(wp + RS) = pack2(Q1[0], Q1[1]); *(unsigned*)(wp + 2 * RS) = pack2(Q2[0], Q2[1]); *(unsigned*)(wp + 3 * RS) = pack2(Q3[0], Q3[1]);
    rows3(0, 4, 1, P1, Q1, Q2, Q3); rows3(0, 8, 1, P2, Q1, Q2, Q3); rows3(0, 12, 1, P3, Q1, Q2, Q3);
    rows3(1, 0, 1, C, S1, S2, S3); rows4(1, 4, 1, C, S4, S5, S6, S7); rows3(1, 8, 1, G1, S1, S2, S3); rows4(1, 12, 1, G1, S4, S5, S6, S7);
    __builtin_amdgcn_wave_barrier();
    s16x8 bh[4];
    f32x4 res[3];
#pragma unroll
    for (int k = 0; k < 4; k++) bh[k] = *(const s16x8*)(rp + k * 64);
    auto mm = [&](int t, int pl) {
      f32x4 a = {0.f, 0.f, 0.f, 0.f};
#pragma unroll
      for (int k = 0; k < 4; k++) a = __builtin_amdgcn_mfma_f32_16x16x32_bf16(*(const s16x8*)(rp + pl * PL + k * 64), bh[k], a, 0, 0, 0);
      res[t] = a;
    };
    mm(0, 1);
    __builtin_amdgcn_wave_barrier();
    rows3(0, 0, 1, G2, S2, S4, S6); rows3(0, 4, 1, G3, S2, S4, S6); rows3(0, 8, 2, CT1, R1, R2, R3); rows3(0, 9, 2, CS4, R1, R2, R3);
    rows3(1, 0, 1, CT2, R1, R2, R3); rows3(1, 4, 1, CT3, R1, R2, R3);
    *(unsigned*)(wp + PL + 8 * RS) = pack2(CT4[0], CT4[1]);
    __builtin_amdgcn_wave_barrier();
    mm(1, 0); mm(2, 1);
    __builtin_amdgcn_wave_barrier();
    char* rs = (wave == 7 && rep == 0) ? smem + 8 * (2 * PL) : tb;
#pragma unroll
    for (int t = 0; t < 3; t++) *(f32x4*)(rs + t * 1024 + lane * 16) = res[t];
    __builtin_amdgcn_wave_barrier();
  }
  STAMP(0, 4);
  if (wow) {
    asm volatile("s_waitcnt vmcnt(0)" : "+v"(wr_[0]), "+v"(wr_[1]), "+v"(wr_[2]), "+v"(wr_[3]), "+v"(wr_[4]), "+v"(wr_[5]), "+v"(wr_[6]), "+v"(wr_[7]) :: "memory");
    u32x4 wv = {wr_[0] | (wr_[1] << 16), wr_[2] | (wr_[3] << 16), wr_[4] | (wr_[5] << 16), wr_[6] | (wr_[7] << 16)};
    st16(ws + WS_WOF + (size_t)wid * 16, wv);
  }
  if (blk == 239) ((unsigned*)(ws + WS_CNT))[tid] = 0u, ((unsigned*)(ws + WS_CNT))[tid + 512] = 0u;
  if (blk < NB_KC) {
    int tix[8];
#pragma unroll
    for (int e = 0; e < 8; e++) {
      const int j = (tid & 255) * 8 + 7 - e;
      const int p = (int)rb((float)j);
      const int hh = p >> 7, c = (p >> 3) & 15;
      int sh = 31 - __builtin_clz(hh | 1); sh = sh > 3 ? 3 : sh;
      const int row = 8 * sh + (((hh << 3) | (p & 7)) >> sh);
      tix[e] = 4096 + (tid >> 8) * (2 * PL) + (((row >> 2) * 16 + c) * 4 + (row & 3)) * 4;
      asm volatile("" : "+v"(tix[e]));
    }
    __syncthreads();
#pragma unroll
    for (int it = 0; it < 2; it++) {
      const int e = tid + 512 * it;
      const int seg = e / 192, ent = e - 192 * seg;
      if (seg < 3) {
        const char* p0 = smem + 3 * seg * (2 * PL) + ent * 16;
        const f32x4 a = *(const f32x4*)p0 + *(const f32x4*)(p0 + (seg == 2 ? 2 : 1) * (2 * PL)) + *(const f32x4*)(p0 + (seg == 2 ? 1 : 2) * (2 * PL));
        *(f32x4*)(smem + 4096 + seg * (2 * PL) + ent * 16) = a;
      }
    }
    __syncthreads();
    const int d0 = 3 * blk + (tid >> 8);
    u32x4 hi;
#pragma unroll
    for (int e = 0; e < 4; e++) hi[e] = pack2(*(const float*)(smem + tix[2 * e]), *(const float*)(smem + tix[2 * e + 1]));
    if (d0 < D) st16(ws + WS_KIMG + (size_t)d0 * 4096 + (tid & 255) * 16, hi);
    if (wave < 4 && d0 + 2 < D) {
#pragma unroll
      for (int e = 0; e < 4; e++) hi[e] = pack2(*(const float*)(smem + tix[2 * e] + 4 * PL), *(const float*)(smem + tix[2 * e + 1] + 4 * PL));
      st16(ws + WS_KIMG + (size_t)(d0 + 2) * 4096 + tid * 16, hi);
    }
  }
  asm volatile("s_waitcnt vmcnt(0)" :: "v"(pfv) : "memory");
  const int gt = wave * 30 + (blk >> 3);
  if (gt < 64) {
    const int row = (blk & 7) * 256 + gt * 4 + (lane >> 4);
    const u32x4* px = (const u32x4*)(x + (size_t)row * D) + (lane & 15); const u32x4* pg = (const u32x4*)gw + (lane & 15);
    u32x4 xv[5], gv[5];
#pragma unroll
    for (int i = 0; i < 5; i++) { xv[i] = px[16 * i]; gv[i] = pg[16 * i]; }
    float gs = 0.f;
#pragma unroll
    for (int i = 0; i < 5; i++)
#pragma unroll
      for (int e = 0; e < 4; e++) {
        unsigned p = pack2(lo_f(xv[i][e]) * lo_f(gv[i][e]), hi_f(xv[i][e]) * hi_f(gv[i][e]));
        gs = __builtin_amdgcn_fdot2_f32_bf16(__builtin_bit_cast(bf16x2_t, p), __builtin_bit_cast(bf16x2_t, 0x3F803F80u), gs, false);
      }
    gs += __shfl_xor(gs, 1); gs += __shfl_xor(gs, 2); gs += __shfl_xor(gs, 4); gs += __shfl_xor(gs, 8);
    if ((lane & 15) == 0) { float a = rb(gs); ((float*)(ws + WS_GATE))[row] = rb(1.f / (1.f + __expf(-a))); }
  }
}

__global__ __launch_bounds__(512, 4) void k1(const u16* __restrict__ x, const u16* __restrict__ logA, const u16* __restrict__ Aim,
                                          const u16* __restrict__ Cr, const u16* __restrict__ Ci, const u16* __restrict__ logdt,
                                          const u16* __restrict__ wqkv, const u16* __restrict__ gw, const u16* __restrict__ wo,
                                          char* __restrict__ ws) {
  __shared__ __attribute__((aligned(16))) char smem[K1_LDS];
  int b = blockIdx.x;
  unsigned cpf = 0;
  asm volatile("" :: "s"(x), "s"(logA), "s"(Aim), "s"(Cr), "s"(Ci), "s"(logdt), "s"(wqkv), "s"(gw), "s"(wo), "s"(ws));
  if (b < NB_KG && threadIdx.x >= 501) {
    const char* pc; int len;
    asm volatile("s_getpc_b64 %0\n\ts_mov_b32 %1, code_tail_mark-_Z2k1PKtS0_S0_S0_S0_S0_S0_S0_S0_Pc-128" : "=s"(pc), "=s"(len));
    const int off = min((int)((threadIdx.x - 501) * 30 + (b >> 3)) * 128, len);
    asm volatile("global_load_dword %0, %1, %2" : "+v"(cpf) : "v"(off), "s"(pc));
  }
  if (b < 8 && threadIdx.x >= 490 && threadIdx.x < 500) asm volatile("global_load_dword %0, %1, %2" : "+v"(cpf) : "v"((threadIdx.x - 490) * 128), "s"(gw));
  STAMP(0, 0);
  b = b < NB_KG ? b + NB_GEMM : (b < NB_KG + NB_GEMM ? b - NB_KG : b);
  if (b < NB_GEMM) {
    const int xcd = b & 7, idx = b >> 3;
    const int rowblk = (xcd >> 1) * 4 + (idx & 3), cb = (xcd & 1) * 8 + (idx >> 2);
    gemm_role(smem, x, wqkv, gw, ws, rowblk, cb);
    STAMP(0, 1);
    return;
  }
  b -= NB_GEMM;
  if (b < NB_KG) { kgen_role(smem, logA, Aim, Cr, Ci, logdt, x, wqkv, wo, gw, ws, b, cpf); STAMP(0, 1); return; }
}

constexpr int K2_LDS = 163840;
constexpr int K2_CS = 4160;
constexpr int K2_KOFF = 4 * K2_CS;
constexpr int K2_CH = K2_KOFF + 4608;
constexpr int K2_CONV = K2_LDS - 3 * K2_CH;
constexpr int K2_TAB = 81920;
constexpr int K2_FIN = K2_TAB + 9216;
constexpr int K2_STG = 114688;
constexpr int NB_KV = 32;
typedef __attribute__((address_space(1))) unsigned gu32;
__device__ __forceinline__ const char* uni(const char* p) {
  size_t v = (size_t)p;
  unsigned lo = __builtin_amdgcn_readfirstlane((unsigned)v), hi = __builtin_amdgcn_readfirstlane((unsigned)(v >> 32));
  size_t o = ((size_t)hi << 32) | lo;
  asm volatile("s_nop 4" : "+s"(o));
  return (const char*)o;
}
__device__ __forceinline__ void wait_count(char* p, unsigned target) {
  gu32* c = (gu32*)(size_t)p;
  for (int n = 0; n < (1 << 22); n++) {
    if (__hip_atomic_load(c, __ATOMIC_RELAXED, __HIP_MEMORY_SCOPE_AGENT) >= target) break;
    __builtin_amdgcn_s_sleep(8);
  }
}
#define GLD4(dst, voff, sbase, pol) asm volatile("global_load_dwordx4 %0, %1, %2" pol : "=v"(dst) : "v"(voff), "s"(sbase))
#define GLD2(dst, voff, sbase, pol) asm volatile("global_load_dwordx2 %0, %1, %2" pol : "=v"(dst) : "v"(voff), "s"(sbase))
#define GLD1(dst, voff, sbase, pol) asm volatile("global_load_dword %0, %1, %2" pol : "=v"(dst) : "v"(voff), "s"(sbase))
template <int G>
__device__ __forceinline__ void conv_unit(f32x4* acc, int ap, int bp) {
  constexpr int NT = 8 - G;
  u32x2 a0[NT], a1[NT]; s16x8 bh[NT];
#pragma unroll
  for (int j = 0; j < NT; j++)
    asm volatile("ds_read_b64 %0, %3 offset:%5\n\tds_read_b64 %1, %3 offset:%6\n\tds_read_b128 %2, %4 offset:%5"
                 : "=&v"(a0[j]), "=&v"(a1[j]), "=&v"(bh[j]) : "v"(ap), "v"(bp), "n"(512 * j), "n"(512 * j + 8) : "memory");
#pragma unroll
  for (int x = 0; x < NT; x++) {
    asm volatile("s_waitcnt lgkmcnt(%3)" : "+v"(a0[x]), "+v"(a1[x]), "+v"(bh[x]) : "n"(3 * (NT - 1 - x) > 15 ? 15 : 3 * (NT - 1 - x)) : "memory");
#pragma unroll
    for (int y = 0; y <= x; y++)
#pragma unroll
      for (int sw = 0; sw < 2; sw++) {
        if (sw && y == x) continue;
        const int j = sw ? y : x, m = sw ? x : y, T = j + m;
        const int ti = G == 0 ? (T == 0 ? 0 : T == 3 ? 1 : T == 4 ? 2 : T == 7 ? 3 : -1) : (T == 1 ? 0 : T == 2 ? 1 : T == 5 ? 2 : T == 6 ? 3 : -1);
        if (ti >= 0) acc[ti] = __builtin_amdgcn_mfma_f32_16x16x32_bf16(__builtin_bit_cast(s16x8, (u32x4){a0[j][0], a0[j][1], a1[j][0], a1[j][1]}), bh[m], acc[ti], 0, 0, 0);
      }
    SB();
  }
}

__global__ __launch_bounds__(576) void k2(const u16* __restrict__ Dskip, char* __restrict__ ws, u16* __restrict__ out) {
  __shared__ __attribute__((aligned(16))) char smem[K2_LDS];
  const int tid = threadIdx.x, lane = tid & 63;
  const int wave = __builtin_amdgcn_readfirstlane(tid >> 6);
  const int b = blockIdx.x;
  unsigned cpf = 0;
  if (b < 32 && tid < 192) {
    const char* pc; int len;
    asm volatile("s_getpc_b64 %0\n\ts_mov_b32 %1, code_tail_mark-_Z2k2PKtPcPt-128" : "=s"(pc), "=s"(len));
    const int off = min(tid * 128, len);
    asm volatile("global_load_dword %0, %1, %2" : "+v"(cpf) : "v"(off), "s"(pc));
  }
  STAMP(1, 0);
  const int lds0 = (int)(size_t)(lptr_t)smem;
  const int l16 = lane * 16;
  const int xcd = b & 7, idx = b >> 3;
  const int odd = idx & 1;
  const int chA = 80 * xcd + 5 * (idx >> 1) + 3 * odd, chH = 80 * xcd + 5 * (idx >> 1) + 2;
  const int rowblk = xcd * 4 + (idx >> 3), cb = idx & 7;
  const int l0 = rowblk * 64, c0 = cb * 80;
  const int r = lane & 31, h = lane >> 5;
  if (wave == 8) {
    const char* wsrc = ws + WS_WOF + (size_t)cb * 102400;
    auto dma4 = [&](const char* src, int dst) {
      asm volatile("s_mov_b32 m0, %2\n\ts_nop 0\n\tglobal_load_lds_dwordx4 %0, %1\n\tglobal_load_lds_dwordx4 %0, %1 offset:1024\n\t"
                   "global_load_lds_dwordx4 %0, %1 offset:2048\n\tglobal_load_lds_dwordx4 %0, %1 offset:3072"
                   :: "v"(l16), "s"(src), "s"(lds0 + dst) : "memory", "m0");
    };
    auto kimg = [&]() {
      dma4(ws + WS_KIMG + chA * 4096, K2_CONV + K2_KOFF + 512);
      dma4(ws + WS_KIMG + chA * 4096 + 4096, K2_CONV + K2_CH + K2_KOFF + 512);
      dma4(ws + WS_KIMG + chH * 4096, K2_CONV + 2 * K2_CH + K2_KOFF + 512);
    };
    int p = 0;
    if (b < NB_KV) {
      f32x4 kvp[32];
      const char* src = ws + WS_PART + (size_t)b * 16 * 1680;
      const int o2 = min(lane + 64, 104) * 16;
#pragma unroll
      for (int i = 0; i < 16; i++) { GLD4(kvp[i], l16, src + i * 1680, ""); GLD4(kvp[16 + i], o2, src + i * 1680, ""); }
      kimg();
      VMCNT(0);
      BAR_ONLY();
#pragma unroll
      for (int i = 0; i < 32; i += 8)
        asm volatile("" : "+v"(kvp[i]), "+v"(kvp[i + 1]), "+v"(kvp[i + 2]), "+v"(kvp[i + 3]), "+v"(kvp[i + 4]), "+v"(kvp[i + 5]), "+v"(kvp[i + 6]), "+v"(kvp[i + 7]));
#pragma unroll
      for (int st = 1; st < 16; st <<= 1)
#pragma unroll
        for (int i = 0; i < 32; i += 2 * st) kvp[i] += kvp[i + st];
      *(f32x4*)(smem + l16) = kvp[0];
      *(f32x4*)(smem + o2) = kvp[16];
      __builtin_amdgcn_wave_barrier();
      const float* fin = (const float*)smem;
      const int m = lane & 31;
      int col = m < 16 ? m : ((m >> 2) == ((b & 2) ? 5 : 4) ? 16 + (m & 3) : 20);
      u32x4 o;
#pragma unroll
      for (int e = 0; e < 4; e++) o[e] = pack2(fin[col * 20 + 8 * h + 2 * e], fin[col * 20 + 8 * h + 2 * e + 1]);
      st16(ws + WS_KVT + (size_t)b * 1280 + lane * 16, o);
      const int m0 = 2 * (lane & 15), m1 = m0 + 1;
      int ca = m0 < 16 ? m0 : ((m0 >> 2) == ((b & 2) ? 5 : 4) ? 16 + (m0 & 3) : 20);
      int cc = m1 < 16 ? m1 : ((m1 >> 2) == ((b & 2) ? 5 : 4) ? 16 + (m1 & 3) : 20);
      u32x4 o3 = {pack2(fin[ca * 20 + 16], fin[ca * 20 + 17]), pack2(fin[ca * 20 + 18], fin[ca * 20 + 19]),
                  pack2(fin[cc * 20 + 16], fin[cc * 20 + 17]), pack2(fin[cc * 20 + 18], fin[cc * 20 + 19])};
      if (lane < 16) st16(ws + WS_KVT + (size_t)b * 1280 + 1024 + lane * 16, o3);
      VMCNT(0);
      if (lane < 8) __hip_atomic_fetch_add((gu32*)(size_t)(ws + WS_CNT + lane * 128), 1u, __ATOMIC_RELAXED, __HIP_MEMORY_SCOPE_AGENT);
    } else {
      kimg();
#pragma unroll
      for (int i = 0; i < NW0; i++) dma4(wsrc + i * 4096, i * 4096);
      p = NW0;
      asm volatile("s_waitcnt vmcnt(%0)" :: "n"(4 * NW0) : "memory");
      BAR_ONLY();
    }
#pragma unroll 1
    for (; p < 24; p++) dma4(wsrc + p * 4096, p * 4096);
    BAR_ONLY();
    dma4(wsrc + 98304, 98304);
    BAR_ONLY();
    VMCNT(0);
    BAR_ONLY();
    BAR_ONLY();
    {
      gu32* c = (gu32*)(size_t)(ws + WS_CNT + (8 + cb) * 128);
      for (int n = 0; n < (1 << 22); n++) {
        if (__hip_atomic_load(c, __ATOMIC_RELAXED, __HIP_MEMORY_SCOPE_AGENT) >= 32u) break;
        __builtin_amdgcn_s_sleep(1);
      }
      asm volatile("" ::: "memory");
      const int o = (lane >> 3) * (L * 2) + (((lane & 7) ^ ((lane >> 2) & 4)) << 4);
      const char* src = ws + WS_AT + (size_t)(c0 * L + l0) * 2;
#pragma unroll
      for (int i = 0; i < 10; i++)
        asm volatile("s_mov_b32 m0, %2\n\ts_nop 0\n\tglobal_load_lds_dwordx4 %0, %1 sc1" :: "v"(o), "s"(src + i * 8 * L * 2), "s"(lds0 + i * 1024) : "memory", "m0");
      VMCNT(0);
    }
    BAR_ONLY();
    return;
  }
  const int t = wave & 1, kh = wave >> 1;
  u32x4 in0[2];
  int dsto[2];
#pragma unroll
  for (int rr = 0; rr < 2; rr++) {
    const int f = (wave + 8 * rr) * 49 + min(lane, 49), i = min(f, 776);
    const int ch = i / 259, xx = i - 259 * ch;
    dsto[rr] = (lane < 49 && f < 777) ? K2_CONV + ch * K2_CH + xx * 16 : -1;
    GLD4(in0[rr], (int)WS_UT + (ch == 2 ? chH : chA + ch) * (UT_STRIDE * 2) + (xx + 1) * 16, ws, "");
  }
  const int jg = (wave >> 1) & 1;
  const int fo = (lane & 15) * 16 + (lane >> 4) * 4;
  int ft[3];
  ft[0] = (wave & 1) ? 4 + jg : jg;
  ft[1] = (wave & 1) ? 7 - jg : 3 - jg;
  { const int w3 = wave & 3; ft[2] = (w3 & 2) * 2 + ((w3 & 1) ? 3 - odd : odd); }
  u32x2 us[3];
  float g_r;
  GLD1(g_r, (int)WS_GATE + (l0 + 32 * t + r) * 4, ws, "");
  unsigned dsA, dsH;
  asm volatile("global_load_ushort %0, %1, %2" : "=v"(dsA) : "v"((chA + (wave >> 2)) * 2), "s"(Dskip));
  asm volatile("global_load_ushort %0, %1, %2" : "=v"(dsH) : "v"(chH * 2), "s"(Dskip));
  u32x4 pf0[8]; u32x2 pf1[8];
  const char* pq0p = ws + WS_PQ + (size_t)((rowblk * 32 + kh * 8) * 2 + t) * 1024;
  const char* pq1p = ws + WS_PQ + (size_t)L * 16 * 32 * 2 + (size_t)((rowblk * 32 + kh * 8) * 2 + t) * 256;
  const int r8 = r * 8;
#define PQL(i0, i1) _Pragma("unroll") for (int i = i0; i < i1; i++) { GLD4(pf0[i], l16, pq0p + i * 2048, ""); GLD2(pf1[i], r8, pq1p + i * 512, ""); }
  if (tid < 96) *(u32x4*)(smem + K2_CONV + (tid >> 5) * K2_CH + K2_KOFF + (tid & 31) * 16) = (u32x4){0u, 0u, 0u, 0u};
  asm volatile("s_waitcnt vmcnt(3)" : "+v"(in0[0]), "+v"(in0[1]), "+v"(cpf) :: "memory");
  STAMP(1, 2);
#pragma unroll
  for (int rr = 0; rr < 2; rr++) {
    const u32x4 a = in0[rr];
    unsigned w[6] = {a[0], a[1], a[2], a[3], (unsigned)__builtin_amdgcn_update_dpp(0, (int)a[0], 0x130, 0xf, 0xf, false),
                     (unsigned)__builtin_amdgcn_update_dpp(0, (int)a[1], 0x130, 0xf, 0xf, false)};
    unsigned o[5];
#pragma unroll
    for (int k = 0; k < 5; k++) o[k] = __builtin_amdgcn_alignbyte(w[k + 1], w[k], 2);
    if (dsto[rr] >= 0) {
      char* p = smem + dsto[rr];
#pragma unroll
      for (int q = 0; q < 2; q++) {
        *(u32x4*)(p + (2 * q) * K2_CS) = (u32x4){o[q], o[q + 1], o[q + 2], o[q + 3]};
        *(u32x4*)(p + (2 * q + 1) * K2_CS) = (u32x4){w[q + 1], w[q + 2], w[q + 3], w[q + 4]};
      }
    }
  }
  RAW_BARRIER();
  PQL(0, PQA);
  STAMP(1, 3);
  f32x4 ca[4], chh[4];
  unsigned pollv = 0;
#pragma unroll
  for (int i = 0; i < 4; i++) { ca[i] = (f32x4){0.f, 0.f, 0.f, 0.f}; chh[i] = (f32x4){0.f, 0.f, 0.f, 0.f}; }
  {
    const int i = lane & 15, kg = lane >> 4, e = kg >> 1, bb = kg & 1;
    const int ap = lds0 + K2_CONV + (i & 3) * K2_CS + (2 * e - bb + 2) * 16 + (i >> 2) * 8;
    const int bp = lds0 + K2_CONV + K2_KOFF + (16 + i - e) * 32 + bb * 16;
    const int apj = ap + (wave >> 2) * K2_CH + (wave & 1) * 256;
    const int bpj = bp + (wave >> 2) * K2_CH - (wave & 1) * 256;
    if (jg == 0) {
#pragma unroll 1
      for (int it = 0; it < 4; it++) conv_unit<0>(ca, apj + it * 64, bpj - it * 64);
    } else {
#pragma unroll 1
      for (int it = 0; it < 4; it++) conv_unit<1>(ca, apj + it * 64, bpj - it * 64);
    }
    GLD1(pollv, (int)WS_CNT + xcd * 128, ws, " sc1");
    PQL(PQA, PQB);
    if (odd == 0) conv_unit<0>(chh, ap + 2 * K2_CH + wave * 64, bp + 2 * K2_CH - wave * 64);
    else          conv_unit<1>(chh, ap + 2 * K2_CH + wave * 64, bp + 2 * K2_CH - wave * 64);
  }
  STAMP(1, 4);
  {
    const char* ua = smem + K2_CONV + 3 * K2_CS + (20 + fo) * 2;
    us[0] = *(const u32x2*)(ua + (wave >> 2) * K2_CH + ft[0] * 512); us[1] = *(const u32x2*)(ua + (wave >> 2) * K2_CH + ft[1] * 512);
    us[2] = *(const u32x2*)(ua + 2 * K2_CH + ft[2] * 512);
  }
  asm volatile("s_waitcnt vmcnt(" PQBS ")" : "+v"(pollv) :: "memory");
  if (__builtin_amdgcn_readfirstlane(pollv) < (unsigned)NB_KV) wait_count(ws + WS_CNT + xcd * 128, NB_KV);
  asm volatile("" ::: "memory");
  u32x4 ka[8]; u32x2 kb[8];
  const char* kp = ws + WS_KVT + (size_t)kh * 8 * 1280;
#pragma unroll
  for (int i = 0; i < 8; i++) { GLD4(ka[i], l16, kp + i * 1280, " sc1"); GLD2(kb[i], r8, kp + i * 1280 + 1024, " sc1"); }
  PQL(PQB, 8);
  RAW_BARRIER();
  {
    if (tid == 0) *(unsigned*)(smem + K2_STG - 16) = 0u;
    char* sp = smem + K2_STG + l16;
    *(f32x4*)(sp + (wave * 2) * 1024) = (wave & 1) ? ca[0] : ca[2];
    *(f32x4*)(sp + (wave * 2 + 1) * 1024) = (wave & 1) ? ca[1] : ca[3];
#pragma unroll
    for (int i = 0; i < 4; i++) *(f32x4*)(sp + 16384 + (i * 8 + wave) * 1024) = chh[i];
  }
  RAW_BARRIER();
  {
    const char* sp = smem + K2_STG + l16;
    f32x4 y[3];
    y[0] = ((wave & 1) ? ca[2] : ca[0]) + *(const f32x4*)(sp + ((wave ^ 1) * 2) * 1024);
    y[1] = ((wave & 1) ? ca[3] : ca[1]) + *(const f32x4*)(sp + ((wave ^ 1) * 2 + 1) * 1024);
    asm volatile("" : "+v"(g_r), "+v"(dsA), "+v"(dsH) :: "memory");
    const float dskA = lo_f(dsA), dskH = lo_f(dsH);
    auto fin = [&](f32x4 s, u32x2 uu, float dsk, int ch, int tile) {
      u32x2 ov;
#pragma unroll
      for (int e = 0; e < 2; e++) {
        unsigned yy = pack2(s[2 * e], s[2 * e + 1]);
        unsigned k = pack2(lo_f(uu[e]) * dsk, hi_f(uu[e]) * dsk);
        ov[e] = pack2(lo_f(yy) + lo_f(k), hi_f(yy) + hi_f(k));
      }
      const int o = (int)WS_AT + (ch * L + tile * 256 + fo) * 2;
      asm volatile("global_store_dwordx2 %0, %1, %2 sc1\n\ts_nop 1" :: "v"(o), "v"(ov), "s"(ws) : "memory");
    };
    fin(y[0], us[0], dskA, chA + (wave >> 2), ft[0]);
    fin(y[1], us[1], dskA, chA + (wave >> 2), ft[1]);
    if (wave < 4) {
      const char* hp = sp + 16384 + wave * 8192;
      f32x4 v[8];
#pragma unroll
      for (int i = 0; i < 8; i++) v[i] = *(const f32x4*)(hp + i * 1024);
      y[2] = ((v[0] + v[1]) + (v[2] + v[3])) + ((v[4] + v[5]) + (v[6] + v[7]));
      fin(y[2], us[2], dskH, chH, ft[2]);
      VMCNT(3);
    } else {
      VMCNT(2);
    }
  }
#pragma unroll
  for (int i = 0; i < 8; i++) asm volatile("" : "+v"(pf0[i]), "+v"(pf1[i]), "+v"(ka[i]), "+v"(kb[i]));
  RAW_BARRIER();
  STAMP(1, 5);
  f32x16 acc[3], c[2];
#pragma unroll
  for (int q = 0; q < 3; q++)
#pragma unroll
    for (int i = 0; i < 16; i++) acc[q][i] = 0.f;
  u32x4 rm = {0u, 0u, 0u, 0u};
  const char* wl = smem + kh * 2560 + l16;
  const char* wl2 = smem + kh * 2560 + 2048 + (h * 16 + (lane & 15)) * 16;
  auto stageA = [&](int i) {
    u32x2 b1u = {h ? 0u : pf1[i][0], h ? 0u : pf1[i][1]};
    f32x16 z;
#pragma unroll
    for (int q = 0; q < 16; q++) z[q] = 0.f;
    z = __builtin_amdgcn_mfma_f32_32x32x16_bf16(__builtin_bit_cast(s16x8, ka[i]), __builtin_bit_cast(s16x8, pf0[i]), z, 0, 0, 0);
    c[i & 1] = __builtin_amdgcn_mfma_f32_32x32x8bf16_1k(__builtin_bit_cast(s16x4, kb[i]), __builtin_bit_cast(s16x4, b1u), z, 0, 0, 0);
    asm volatile("" : "+v"(c[i & 1]));
  };
  stageA(0);
#pragma unroll
  for (int i = 0; i < 8; i++) {
    const int j = i & 3, G = i >> 2;
    const int wo_ = G * 51200 + j * 10240;
    s16x8 w0 = *(const s16x8*)(wl + wo_), w1 = *(const s16x8*)(wl + wo_ + 1024), w2 = *(const s16x8*)(wl2 + wo_);
    if (i + 1 < 8) stageA(i + 1);
    const f32x16 cc = c[i & 1];
    const float den = rb(rb(cc[12]) + 1e-6f);
    const float inv = __builtin_amdgcn_rcpf(den);
    unsigned o[6];
#pragma unroll
    for (int q = 0; q < 6; q++) o[q] = pack2(cc[2 * q] * inv, cc[2 * q + 1] * inv);
    asm volatile("" : "+v"(o[4]), "+v"(o[5]));
    const bool keep = (j >> 1) == h;
    rm[2 * (j & 1)] = keep ? o[4] : rm[2 * (j & 1)]; rm[2 * (j & 1) + 1] = keep ? o[5] : rm[2 * (j & 1) + 1];
    u32x4 ob = {o[0], o[1], o[2], o[3]};
    acc[0] = __builtin_amdgcn_mfma_f32_32x32x16_bf16(w0, __builtin_bit_cast(s16x8, ob), acc[0], 0, 0, 0);
    acc[1] = __builtin_amdgcn_mfma_f32_32x32x16_bf16(w1, __builtin_bit_cast(s16x8, ob), acc[1], 0, 0, 0);
    acc[2] = __builtin_amdgcn_mfma_f32_32x32x16_bf16(w2, __builtin_bit_cast(s16x8, ob), acc[2], 0, 0, 0);
    if (j == 3) {
      const int wr_ = G * 51200 + 40960;
      s16x8 w3 = *(const s16x8*)(wl + wr_), w4 = *(const s16x8*)(wl + wr_ + 1024), w5 = *(const s16x8*)(wl2 + wr_);
      acc[0] = __builtin_amdgcn_mfma_f32_32x32x16_bf16(w3, __builtin_bit_cast(s16x8, rm), acc[0], 0, 0, 0);
      acc[1] = __builtin_amdgcn_mfma_f32_32x32x16_bf16(w4, __builtin_bit_cast(s16x8, rm), acc[1], 0, 0, 0);
      acc[2] = __builtin_amdgcn_mfma_f32_32x32x16_bf16(w5, __builtin_bit_cast(s16x8, rm), acc[2], 0, 0, 0);
    }
    if (i == 1) {
      VMCNT(0);
      unsigned old = 0;
      if (lane == 0) old = __hip_atomic_fetch_add((unsigned*)(smem + K2_STG - 16), 1u, __ATOMIC_RELAXED, __HIP_MEMORY_SCOPE_WORKGROUP);
      if (lane == 0 && old == 7u) __hip_atomic_fetch_add((gu32*)(size_t)(ws + WS_CNT + (8 + xcd) * 128), 1u, __ATOMIC_RELAXED, __HIP_MEMORY_SCOPE_AGENT);
    }
    if (i == 3) { RAW_BARRIER(); }
  }
  STAMP(1, 6);
  {
    char* sp = smem + 102400 + t * 30720 + l16;
#pragma unroll
    for (int g = 0; g < 10; g++) {
      const int own = g & 3;
      f32x4 v = {acc[g >> 2][4 * (g & 3)], acc[g >> 2][4 * (g & 3) + 1], acc[g >> 2][4 * (g & 3) + 2], acc[g >> 2][4 * (g & 3) + 3]};
      if (kh != own) *(f32x4*)(sp + (g * 3 + ((kh - own - 1) & 3)) * 1024) = v;
    }
  }
  RAW_BARRIER();
  STAMP(1, 7);
  {
    const char* sp = smem + 102400 + t * 30720 + l16;
    const float g1 = rb(1.f - g_r);
    const char* atp = smem + (8 * kh + 4 * h + ((lane & 15) >> 2)) * 128 + (((32 * t + (lane & 16) + 4 * (lane & 3)) * 2) ^ ((lane & 8) << 3));
    u16* op = out + (size_t)(l0 + 32 * t + r) * D + c0 + 8 * kh + 4 * h;
#pragma unroll
    for (int tl = 0; tl < 3; tl++) {
      if (tl == 2 && kh >= 2) break;
      f32x4 m;
      if (kh == 0)      m = (f32x4){acc[tl][0], acc[tl][1], acc[tl][2], acc[tl][3]};
      else if (kh == 1) m = (f32x4){acc[tl][4], acc[tl][5], acc[tl][6], acc[tl][7]};
      else if (kh == 2) m = (f32x4){acc[tl][8], acc[tl][9], acc[tl][10], acc[tl][11]};
      else              m = (f32x4){acc[tl][12], acc[tl][13], acc[tl][14], acc[tl][15]};
      const char* pp = sp + (tl * 4 + kh) * 3072;
      f32x4 p0 = *(const f32x4*)pp, p1 = *(const f32x4*)(pp + 1024), p2 = *(const f32x4*)(pp + 2048);
      f32x4 v = (m + p1) + (p0 + p2);
      s16x4 at = __builtin_amdgcn_ds_read_tr16_b64_v4i16((lds_s16x4_p)(atp + tl * 32 * 128));
      u32x2 au = __builtin_bit_cast(u32x2, at);
      u32x2 ov;
#pragma unroll
      for (int e = 0; e < 2; e++) {
        unsigned bq = pack2(v[2 * e], v[2 * e + 1]);
        unsigned gb = pack2(g1 * lo_f(bq), g1 * hi_f(bq));
        unsigned ga = pack2(g_r * lo_f(au[e]), g_r * hi_f(au[e]));
        ov[e] = pack2(lo_f(ga) + lo_f(gb), hi_f(ga) + hi_f(gb));
      }
      *(u32x2*)(op + 32 * tl) = ov;
    }
  }
  asm volatile("code_tail_mark:" ::: "memory");
  STAMP(1, 1);
}

extern "C" void kernel_launch(void* const* d_in, const int* in_sizes, int n_in, void* d_out, int out_size, void* d_ws, size_t ws_size,
                              hipStream_t stream) {
  const u16* u = (const u16*)d_in[0]; const u16* logA = (const u16*)d_in[1]; const u16* Aim = (const u16*)d_in[2];
  const u16* Cr = (const u16*)d_in[3]; const u16* Ci = (const u16*)d_in[4]; const u16* logdt = (const u16*)d_in[5];
  const u16* Dsk = (const u16*)d_in[6]; const u16* wqkv = (const u16*)d_in[8]; const u16* wo = (const u16*)d_in[9];
  const u16* gw = (const u16*)d_in[10];
  char* ws = (char*)d_ws;
  k1<<<NB_GEMM + NB_GATE + NB_KG, 512, 0, stream>>>(u, logA, Aim, Cr, Ci, logdt, wqkv, gw, wo, ws);
  k2<<<256, 576, 0, stream>>>(Dsk, ws, (u16*)d_out);
}
```

```cpp
#include <hip/hip_runtime.h>
#include <hip/hip_bf16.h>
#include <stdint.h>

typedef unsigned short u16;
typedef __attribute__((ext_vector_type(8))) short s16x8;
typedef __attribute__((ext_vector_type(4))) short s16x4;
typedef __attribute__((ext_vector_type(4))) float f32x4;
typedef __attribute__((ext_vector_type(16))) float f32x16;
typedef __attribute__((ext_vector_type(4))) unsigned u32x4;
typedef __attribute__((ext_vector_type(2))) unsigned u32x2;
typedef __attribute__((address_space(3))) s16x4* lds_s16x4_p;
typedef const __attribute__((address_space(1))) void* gptr_t;
typedef __attribute__((address_space(3))) void* lptr_t;

constexpr int L = 2048, D = 640, NS = 192, NH = 32;
constexpr int UT_PAD = 32, UT_STRIDE = L + UT_PAD;
constexpr size_t WS_UT   = 0;
constexpr size_t WS_KIMG = WS_UT + (size_t)D * UT_STRIDE * 2;
constexpr size_t WS_PQ   = WS_KIMG + (size_t)D * 4096;
constexpr size_t WS_PART = WS_PQ + (size_t)L * D * 2;
constexpr size_t WS_GATE = WS_PART + (size_t)32 * 16 * 1680;
constexpr size_t WS_WOF  = WS_GATE + (size_t)L * 4;
constexpr size_t WS_KVT  = WS_WOF + (size_t)D * D * 2;
constexpr size_t WS_AT   = WS_KVT + (size_t)32 * 1024;
constexpr size_t WS_CNT  = WS_AT + (size_t)D * L * 2;
constexpr size_t WS_END  = WS_CNT + 4096;
#define TGET(v) do {} while (0)
#define STEPT(i) do {} while (0)
#define TPUT(kid, slot, thr, v) do {} while (0)
#define STAMP(kid, slot) do {} while (0)

__device__ __forceinline__ float b2f(u16 v) { return __uint_as_float(((unsigned)v) << 16); }
typedef __bf16 bf16x2_t __attribute__((ext_vector_type(2)));
typedef float f32x2 __attribute__((ext_vector_type(2)));
__device__ __forceinline__ unsigned pack2(float a, float b) { f32x2 v = {a, b}; bf16x2_t r = __builtin_convertvector(v, bf16x2_t); return __builtin_bit_cast(unsigned, r); }
__device__ __forceinline__ float lo_f(unsigned u) { return __uint_as_float(u << 16); }
__device__ __forceinline__ float hi_f(unsigned u) { return __uint_as_float(u & 0xffff0000u); }
__device__ __forceinline__ float rb(float f) { return lo_f(pack2(f, f)); }
__device__ __forceinline__ unsigned phi2(float a, float b) {
  unsigned t = pack2(a, b); float t0 = lo_f(t), t1 = hi_f(t);
  float e0 = __builtin_amdgcn_fmed3f(__builtin_amdgcn_exp2f(t0 * 1.44269504f), 0.f, 1.f);
  float e1 = __builtin_amdgcn_fmed3f(__builtin_amdgcn_exp2f(t1 * 1.44269504f), 0.f, 1.f);
  unsigned r = pack2(__builtin_fmaxf(t0, 0.f) + e0, __builtin_fmaxf(t1, 0.f) + e1);
  return pack2(lo_f(r) + 1e-6f, hi_f(r) + 1e-6f);
}
#define VMCNT(n) asm volatile("s_waitcnt vmcnt(" #n ")" ::: "memory")
#define RAW_BARRIER() do { asm volatile("s_waitcnt lgkmcnt(0)" ::: "memory"); __builtin_amdgcn_s_barrier(); asm volatile("" ::: "memory"); } while (0)
__device__ __forceinline__ void glds16(const void* g, void* l) { __builtin_amdgcn_global_load_lds((gptr_t)g, (lptr_t)l, 16, 0, 0); }

#define OPQ_S(v) asm volatile("" : "+s"(v))
typedef f32x2 cf;
__device__ __forceinline__ float smul(float a, float b) { float o; asm("v_mul_f32 %0, %1, %2" : "=v"(o) : "v"(a), "v"(b)); return o; }
__device__ __forceinline__ float sfma(float a, float b, float c) { float o; asm("v_fma_f32 %0, %1, %2, %3" : "=v"(o) : "v"(a), "v"(b), "v"(c)); return o; }
__device__ __forceinline__ float sfnma(float a, float b, float c) { float o; asm("v_fma_f32 %0, -%1, %2, %3" : "=v"(o) : "v"(a), "v"(b), "v"(c)); return o; }
__device__ __forceinline__ float ssub(float a, float b) { float o; asm("v_sub_f32 %0, %1, %2" : "=v"(o) : "v"(a), "v"(b)); return o; }
__device__ __forceinline__ cf cmul(cf a, cf b) {
  cf o;
  asm("v_pk_mul_f32 %0, %1, %2 op_sel:[0,0] op_sel_hi:[0,1]\n\t"
      "v_pk_fma_f32 %0, %1, %2, %0 op_sel:[1,1,0] op_sel_hi:[1,0,1] neg_lo:[1,0,0]" : "=&v"(o) : "v"(a), "v"(b));
  return o;
}

#define CPF 3
#define LPF 1
#define PFD 0
#define PFM_ 1
#define NP0 5
#define SPRIO 3
#define NI0 208
#define NB2 3
#define TB0 1
#define TBD 2
#define MIDX ((rowblk & 3) + 4 * (cb & 7))
#define MPS 3
#define PT0 30
#define PT1 110
#define PTD 20
constexpr int K1_LDS = 81920;
#define GLDS 1
constexpr int NB_GEMM = 256, NB_GATE = GLDS ? 0 : 16, NB_KG = 240, NB_KC = 214;
#define KVAT 0
#define KV1 ""
#define KVB 0
#define ROT 0
#define TPQ 0
#define KVST
#define NW0 3
#define K2PF 1
#define K2PFS "1"
#define KVP 1
#define PQA 2
#define PQB 6
#define STR2(x) #x
#define STR(x) STR2(x)
#define PQBS "2*(" STR(PQB) "-" STR(PQA) ")"
#define WTQ 0
#define WTP 0
#define WTO 0
#define WT 1
__device__ __forceinline__ void st16(void* p, u32x4 v) {
  asm volatile("global_store_dwordx4 %0, %1, off sc1\n\ts_nop 2" :: "v"(p), "v"(v) : "memory");
}

#define BAR_ONLY() do { asm volatile("" ::: "memory"); __builtin_amdgcn_s_barrier(); asm volatile("" ::: "memory"); } while (0)
#define SB() __builtin_amdgcn_sched_barrier(0)
template <bool ISQ>
__device__ __forceinline__ void mfma_part(char* smem, char* __restrict__ ws, int wave, f32x4 (&ep)[2][4]) {
  const int lane = threadIdx.x & 63;
  char* sB = smem; char* sA = smem + 32768;
  const int wr = wave & 1, wc = (wave >> 1) & 1;
  const int g = lane >> 4, fr = lane & 15;
  f32x4 acc[4][4];
#pragma unroll
  for (int m = 0; m < 4; m++)
#pragma unroll
    for (int c = 0; c < 4; c++) acc[m][c] = (f32x4){0.f, 0.f, 0.f, 0.f};
  const int q4 = fr >> 2, p4 = lane & 3;
  int aoff[2];
#pragma unroll
  for (int ks = 0; ks < 2; ks++) aoff[ks] = (wr * 64 + fr) * 128 + (((4 * ks + g) ^ (fr & 7)) << 4);
  int boff[4];
  {
    const int f = q4 | ((g & 1) << 2);
#pragma unroll
    for (int c = 0; c < 4; c++) boff[c] = (8 * g + q4) * 256 + (((4 * wc + c) ^ f) << 5) + p4 * 8;
  }
  s16x8 af[2][4]; s16x4 bl[4], bh[4];
  auto rdA = [&](int buf, int m, const char* pa) { af[buf][m] = *(const s16x8*)(pa + m * 2048); };
  auto rdBl = [&](int c, const char* pb) { bl[c] = __builtin_amdgcn_ds_read_tr16_b64_v4i16((lds_s16x4_p)(pb + boff[c])); };
  auto rdBh = [&](int c, const char* pb) { bh[c] = __builtin_amdgcn_ds_read_tr16_b64_v4i16((lds_s16x4_p)(pb + boff[c] + 1024)); };
  BAR_ONLY();
  STAMP(0, 2);
#pragma unroll
  for (int c = 0; c < 4; c++) rdA(0, c, sA + aoff[0]);
#pragma unroll
  for (int c = 0; c < 3; c++) { rdBl(c, sB); rdBh(c, sB); }
  int ao0 = 0, ao1 = 16384;
  auto grp = [&](int buf, int c, int m) {
    const s16x8 bf = __builtin_shufflevector(bl[c], bh[c], 0, 1, 2, 3, 4, 5, 6, 7);
    if (ISQ) acc[m][c] = __builtin_amdgcn_mfma_f32_16x16x32_bf16(bf, af[buf][m], acc[m][c], 0, 0, 0);
    else     acc[m][c] = __builtin_amdgcn_mfma_f32_16x16x32_bf16(af[buf][m], bf, acc[m][c], 0, 0, 0);
  };
#pragma unroll 1
  for (int j2 = 0; j2 < 5; j2++) {
#pragma unroll
    for (int st = 0; st < 4; st++) {
      const int hs = st & 1;
      SB();
      asm volatile("s_waitcnt lgkmcnt(2)" ::: "memory"); __builtin_amdgcn_s_barrier(); asm volatile("" ::: "memory");
      SB();
      const char* pa = sA + (hs == 0 ? ao0 + aoff[1] : ao1 + aoff[0]);
      const char* pc = sB + st * 8192;
      const char* pb = sB + ((st + 1) & 3) * 8192;
      grp(hs, 0, 0); SB(); rdBl(3, pc); rdBh(3, pc); SB();
      grp(hs, 0, 1); SB(); rdA(hs ^ 1, 0, pa); rdA(hs ^ 1, 1, pa); SB();
      grp(hs, 0, 2); SB(); rdA(hs ^ 1, 2, pa); rdA(hs ^ 1, 3, pa); SB();
      grp(hs, 0, 3); SB();
#pragma unroll
      for (int c = 1; c < 4; c++) {
        grp(hs, c, 0); SB(); rdBl(c - 1, pb); rdBh(c - 1, pb); SB();
        grp(hs, c, 1); grp(hs, c, 2); grp(hs, c, 3); SB();
      }
      if (hs == 1) { ao0 = ao1; ao1 = ao1 == 32768 ? 0 : ao1 + 16384; }
    }
  }
  STAMP(0, 3);
  int l3 = threadIdx.x; asm volatile("" : "+v"(l3));
  char* xch = smem + 32768 + (l3 >> 6) * 8192 + (l3 & 63) * 16;
#pragma unroll
  for (int i = 0; i < 8; i++) *(f32x4*)(xch + i * 1024) = acc[2 + (i >> 2)][i & 3];
#pragma unroll
  for (int i = 0; i < 8; i++) ep[i >> 2][i & 3] = acc[i >> 2][i & 3];
  RAW_BARRIER();
}
__device__ __forceinline__ void dma4x(const int (&vo)[4], const char* sbase, int ldsa) {
  asm volatile("s_mov_b32 m0, %5\n\ts_nop 0\n\tglobal_load_lds_dwordx4 %0, %4\n\tglobal_load_lds_dwordx4 %1, %4 offset:1024\n\t"
               "global_load_lds_dwordx4 %2, %4 offset:2048\n\tglobal_load_lds_dwordx4 %3, %4 offset:3072"
               :: "v"(vo[0]), "v"(vo[1]), "v"(vo[2]), "v"(vo[3]), "s"(sbase), "s"(ldsa) : "memory", "m0");
}
__device__ __forceinline__ void gemm_role(char* smem, const u16* __restrict__ x, const u16* __restrict__ W, const u16* __restrict__ gw, char* __restrict__ ws,
                                          int rowblk, int cb) {
  const int tid = threadIdx.x, lane = tid & 63;
  const int wave = __builtin_amdgcn_readfirstlane(tid >> 6);
  const bool ISQ = cb < 5;
  const int row0 = rowblk * 128, col0 = cb * 128;
  const int kvj = cb - 5;
  const int wr = wave & 1, wc = (wave >> 1) & 1, hf = wave >> 2;
  f32x4 ep[2][4];
  if (wave >= 4) {
    const int lw = wave & 1;
    const int l0 = (int)(size_t)(lptr_t)smem;
    unsigned pf0 = 0;
    const int pidx = (rowblk & 3) + 4 * (cb & 7);
    auto pfaddr = [&](int stg, int r) {
      const int q = r & 15;
      const int ln = (cb & 8) ? (q < 8 ? 12 + q : 14 + q) : (q < 13 ? q : 7 + q);
      return r < 16 ? (const char*)x + ((rowblk >> 2) * 512 + pidx * 16 + q) * 1280 + stg * 128
                    : (const char*)W + (64 * stg + pidx * 2 + (r >> 4) - 1) * 3840 + ln * 128;
    };
    {
      const int q = (wave - 4) * (NI0 / 4) + min(lane, NI0 / 4 - 1), stg = q / 48;
      asm volatile("global_load_dword %0, %1, off" : "+v"(pf0) : "v"(pfaddr(stg, q - 48 * stg)));
      SB();
    }
    __builtin_amdgcn_s_setprio(SPRIO);
    if (wave < 6) {
      int voA[4], voB[4];
#pragma unroll
      for (int i = 0; i < 4; i++) {
        voA[i] = (64 * lw + 8 * i + (lane >> 3)) * 1280 + (((lane & 7) ^ (lane >> 3)) << 4) - 1024 * i;
        const int fB = (lane >> 4) | ((i >> 1) << 2);
        const int c16 = (((lane & 15) >> 1) ^ fB) * 2 + (lane & 1);
        int bcol = col0 + 8 * c16;
        if (!ISQ) bcol = min(640 + 640 * (c16 >> 3) + ((60 * kvj) & ~7) + 8 * (c16 & 7), 1912);
        voB[i] = (16 * lw + 4 * i + (lane >> 4)) * 3840 + bcol * 2 - 1024 * i;
      }
      const char* pA = (const char*)x + row0 * 1280;
      const char* pB = (const char*)W;
      const int lA = l0 + 32768 + lw * 8192, lB = l0 + lw * 4096;
      auto issA = [&](int slot_off, int h) { dma4x(voA, pA + h * 32 * 1280, lA + slot_off + h * 4096); if (h) pA += 128; };
      auto issB = [&](int slot_off) { dma4x(voB, pB, lB + slot_off); pB += 32 * 3840; };
      TGET(tl0);
      asm volatile("s_waitcnt vmcnt(0)" :: "v"(pf0) : "memory");
      TGET(tl1);
      issA(0, 0); issB(0); issA(0, 1);
      issB(8192); issA(16384, 0);
      VMCNT(8);
      TGET(tl2);
      BAR_ONLY();
      issB(16384); issA(16384, 1);
      VMCNT(12); BAR_ONLY();
      issB(24576); issA(32768, 0); issA(32768, 1);
      VMCNT(12); BAR_ONLY();
      issB(0); issA(0, 0);
      int ao = 0, bo = 8192;
#pragma unroll 1
      for (int j = 1; j < 7; j++) {
        VMCNT(12); BAR_ONLY();
        issB(bo); bo = (bo + 8192) & 32767; issA(ao, 1);
        ao = ao == 32768 ? 0 : ao + 16384;
        VMCNT(12); BAR_ONLY();
        issB(bo); bo = (bo + 8192) & 32767; issA(ao, 0);
      }
      VMCNT(12); BAR_ONLY();
      issB(8192); issA(0, 1);
      VMCNT(12); BAR_ONLY();
      issB(16384);
      VMCNT(8); BAR_ONLY();
      issB(24576);
      VMCNT(4); BAR_ONLY();
      VMCNT(0); BAR_ONLY();
      BAR_ONLY();
      TPUT(0, 4, 256, tl0);
      TPUT(0, 5, 256, tl1); TPUT(0, 6, 256, tl2);
      BAR_ONLY();
    } else {
      u32x4 trv[2][4];
      constexpr int CNT2 = 480 - NI0, NL2 = (CNT2 + 2 * NB2 - 1) / (2 * NB2);
      const char* pp[NB2];
#pragma unroll
      for (int i = 0; i < NB2; i++) {
        const int it = NI0 * 32 + ((pidx & 3) * 8 + (pidx >> 2)) * CNT2 + min((2 * i + lw) * NL2 + min(lane, NL2 - 1), CNT2 - 1);
        const int stg = it / 1536, r = it - 1536 * stg, q = r & 15;
        const int ln = (cb & 8) ? (q < 8 ? 12 + q : 14 + q) : (q < 13 ? q : 7 + q);
        pp[i] = r < 512 ? (const char*)x + ((rowblk >> 2) * 512 + r) * 1280 + stg * 128 : (const char*)W + (64 * stg + (r >> 4) - 32) * 3840 + ln * 128;
      }
      const int tb2 = TB0 + TBD * (pidx & 3);
      int tro;
      {
        const int fr = lane & 15, g = lane >> 4, p4 = lane & 3, r = 8 * g + (fr >> 2);
        tro = l0 + 32768 + r * 128 + (((4 * lw + (p4 >> 1)) ^ (r & 7)) << 4) + (p4 & 1) * 8;
      }
      auto transp = [&](int ao) {
#pragma unroll
        for (int hh = 0; hh < 2; hh++) {
          u32x2 tr[8];
          const int a = (tro ^ (32 * hh)) + ao, a2 = (a ^ 64) + 512;
          asm volatile("ds_read_b64_tr_b16 %0, %8\n\tds_read_b64_tr_b16 %1, %9\n\tds_read_b64_tr_b16 %2, %8 offset:4096\n\tds_read_b64_tr_b16 %3, %9 offset:4096\n\t"
                       "ds_read_b64_tr_b16 %4, %8 offset:8192\n\tds_read_b64_tr_b16 %5, %9 offset:8192\n\tds_read_b64_tr_b16 %6, %8 offset:12288\n\tds_read_b64_tr_b16 %7, %9 offset:12288\n\t"
                       "s_waitcnt lgkmcnt(0)"
                       : "=&v"(tr[0]), "=&v"(tr[1]), "=&v"(tr[2]), "=&v"(tr[3]), "=&v"(tr[4]), "=&v"(tr[5]), "=&v"(tr[6]), "=&v"(tr[7])
                       : "v"(a), "v"(a2) : "memory");
#pragma unroll
          for (int i = 0; i < 4; i++) trv[hh][i] = (u32x4){tr[2 * i][0], tr[2 * i][1], tr[2 * i + 1][0], tr[2 * i + 1][1]};
        }
      };
      unsigned pf2 = 0;
      auto burst = [&](int hs) {
        static_assert(NB2 == 3, "");
        asm volatile("s_cmp_lg_u32 %4, %5\n\ts_cbranch_scc1 1f\n\tglobal_load_dword %0, %1, off\n\tglobal_load_dword %0, %2, off\n\tglobal_load_dword %0, %3, off\n1:"
                     : "+v"(pf2) : "v"(pp[0]), "v"(pp[1]), "v"(pp[2]), "s"(hs), "s"(tb2) : "scc");
      };
      asm volatile("s_waitcnt vmcnt(0)" :: "v"(pf0) : "memory");
      BAR_ONLY();
      int ao = 0;
#pragma unroll 1
      for (int j = 0; j < 10; j++) {
        BAR_ONLY();
        if (!lw) STEPT(2 * j);
        burst(2 * j);
        if (j == cb) transp(ao);
        BAR_ONLY();
        if (!lw) STEPT(2 * j + 1);
        burst(2 * j + 1);
        ao = ao == 32768 ? 0 : ao + 16384;
      }
      asm volatile("s_waitcnt vmcnt(0)" :: "v"(pf2) : "memory");
      if (cb < 10) {
        u16* dst = (u16*)(ws + WS_UT) + (size_t)(64 * cb + 32 * lw + (lane & 15)) * UT_STRIDE + 8 * (lane >> 4);
#pragma unroll
        for (int hh = 0; hh < 2; hh++) {
#pragma unroll
          for (int i = 0; i < 4; i++) st16(dst + hh * 16 * UT_STRIDE + UT_PAD + row0 + 32 * i, trv[hh][i]);
          if (rowblk == 0) st16(dst + hh * 16 * UT_STRIDE, (u32x4){0u, 0u, 0u, 0u});
        }
      }
      BAR_ONLY();
    }
    __builtin_amdgcn_s_setprio(0);
    char* xch = smem + 32768 + (wave & 3) * 8192 + lane * 16;
#pragma unroll
    for (int i = 0; i < 8; i++) ep[i >> 2][i & 3] = *(const f32x4*)(xch + i * 1024);
  } else if (ISQ) {
    mfma_part<true>(smem, ws, wave, ep);
  } else {
    mfma_part<false>(smem, ws, wave, ep);
  }
  int l2 = threadIdx.x; asm volatile("" : "+v"(l2));
  const int g = (l2 >> 4) & 3, fr = l2 & 15;
  if (ISQ) {
    char* pq0 = ws + WS_PQ; char* pq1 = ws + WS_PQ + (size_t)L * 16 * 32 * 2;
#pragma unroll
    for (int c = 0; c < 4; c++) {
      const int n = col0 + 64 * wc + 16 * c + 4 * g;
      const int hd = n / 20, d0 = n - 20 * hd;
#pragma unroll
      for (int m = 0; m < 2; m++) {
        const int rb64 = rowblk * 2 + wr, r = 16 * m + fr;
        u32x2 o;
        o[0] = phi2(ep[m][c][0], ep[m][c][1]);
        o[1] = phi2(ep[m][c][2], ep[m][c][3]);
        const int fi = (rb64 * 32 + hd) * 2 + hf;
        char* dst = d0 < 16 ? pq0 + ((size_t)fi * 64 + (d0 >> 3) * 32 + r) * 16 + (d0 & 7) * 2 : pq1 + ((size_t)fi * 32 + r) * 8;
        *(u32x2*)dst = o;
      }
    }
  } else {
    const int o = (60 * kvj) & 7, zc = o ? 0 : 60;
    const int lane2 = l2 & 63;
    char* fb = smem;
#pragma unroll
    for (int c = 0; c < 4; c++) {
      u32x4 a;
      if (wc == 0) {
        a[0] = phi2(ep[0][c][0], ep[0][c][1]); a[1] = phi2(ep[0][c][2], ep[0][c][3]);
        a[2] = phi2(ep[1][c][0], ep[1][c][1]); a[3] = phi2(ep[1][c][2], ep[1][c][3]);
      } else {
        a[0] = pack2(ep[0][c][0], ep[0][c][1]); a[1] = pack2(ep[0][c][2], ep[0][c][3]);
        a[2] = pack2(ep[1][c][0], ep[1][c][1]); a[3] = pack2(ep[1][c][2], ep[1][c][3]);
        if (16 * c + fr == zc) a = (u32x4){0x3F803F80u, 0x3F803F80u, 0x3F803F80u, 0x3F803F80u};
      }
      *(u32x4*)(fb + ((((wc * 4 + wr * 2 + hf) * 4 + c) * 64 + lane2) << 4)) = a;
    }
    RAW_BARRIER();
    const int np = wave < 4 ? 1 : 2;
    for (int i = 0; i < np; i++) {
      const int p = wave < 4 ? wave : wave * 2 - 4 + i;
      int ta, tb;
      if (p < 10) { ta = (int)((0x3322211100ull >> (4 * p)) & 15); tb = (int)((0x3232121010ull >> (4 * p)) & 15); }
      else { ta = (o ? 2 : 0) + (p - 10); tb = o ? 0 : 3; }
      const char* pk = fb + ((ta * 64 + lane2) << 4);
      const char* pv = fb + 16384 + ((tb * 64 + lane2) << 4);
      f32x4 dd = {0.f, 0.f, 0.f, 0.f};
#pragma unroll
      for (int rg = 0; rg < 4; rg++)
        dd = __builtin_amdgcn_mfma_f32_16x16x32_bf16(*(const s16x8*)(pk + rg * 4096), *(const s16x8*)(pv + rg * 4096), dd, 0, 0, 0);
      const int kc0 = 16 * ta + 4 * g - o;
      const int hk = kc0 / 20, dk0 = kc0 - 20 * hk;
      const int vcol = 16 * tb + fr;
      const int dv = vcol == zc ? 20 : vcol - o - 20 * hk;
      if (kc0 >= 0 && kc0 < (kvj == 10 ? 40 : 60) && dv >= 0 && (dv < 20 || vcol == zc))
        *(f32x4*)(ws + WS_PART + (size_t)(((3 * kvj + hk) * 16 + rowblk) * 21 + dv) * 80 + dk0 * 4) = dd;
    }
  }
}

__device__ __forceinline__ cf zpow(float th, float tl, float rh, float rl, float p) {
  float e = __builtin_amdgcn_exp2f(th * p); e = fmaf(e, (tl * p) * 0.69314718f, e);
  float t = rh * p; float f = (t - __builtin_rintf(t)) + rl * p;
  cf o; o[0] = e * __builtin_amdgcn_cosf(f); o[1] = e * __builtin_amdgcn_sinf(f); return o;
}
#define PKM(o, a, b) "v_pk_mul_f32 " o ", " a ", " b " op_sel:[0,0] op_sel_hi:[0,1]\n\t"
#define PKF(o, a, b) "v_pk_fma_f32 " o ", " a ", " b ", " o " op_sel:[1,1,0] op_sel_hi:[1,0,1] neg_lo:[1,0,0]\n\t"
__device__ __forceinline__ void cmul4(cf& o0, cf& o1, cf& o2, cf& o3, cf a0, cf b0, cf a1, cf b1, cf a2, cf b2, cf a3, cf b3) {
  asm(PKM("%0", "%4", "%5") PKM("%1", "%6", "%7") PKM("%2", "%8", "%9") PKM("%3", "%10", "%11")
      PKF("%0", "%4", "%5") PKF("%1", "%6", "%7") PKF("%2", "%8", "%9") PKF("%3", "%10", "%11")
      : "=&v"(o0), "=&v"(o1), "=&v"(o2), "=&v"(o3)
      : "v"(a0), "v"(b0), "v"(a1), "v"(b1), "v"(a2), "v"(b2), "v"(a3), "v"(b3));
}
__device__ __forceinline__ void batch4(unsigned* h, cf b, cf t0, cf t1, cf t2, cf t3) {
  cf w0, w1, w2, w3;
  asm(PKM("%0", "%4", "%5") PKM("%1", "%4", "%6") PKM("%2", "%4", "%7") PKM("%3", "%4", "%8")
      PKF("%0", "%4", "%5") PKF("%1", "%4", "%6") PKF("%2", "%4", "%7") PKF("%3", "%4", "%8")
      : "=&v"(w0), "=&v"(w1), "=&v"(w2), "=&v"(w3) : "v"(b), "v"(t0), "v"(t1), "v"(t2), "v"(t3));
  h[0] = pack2(w0[0], w0[1]); h[1] = pack2(w1[0], w1[1]); h[2] = pack2(w2[0], w2[1]); h[3] = pack2(w3[0], w3[1]);
}
__device__ __forceinline__ void batch3(unsigned* h, cf b, cf t1, cf t2, cf t3) {
  cf w1, w2, w3;
  asm(PKM("%0", "%3", "%4") PKM("%1", "%3", "%5") PKM("%2", "%3", "%6")
      PKF("%0", "%3", "%4") PKF("%1", "%3", "%5") PKF("%2", "%3", "%6")
      : "=&v"(w1), "=&v"(w2), "=&v"(w3) : "v"(b), "v"(t1), "v"(t2), "v"(t3));
  h[0] = pack2(b[0], b[1]); h[1] = pack2(w1[0], w1[1]); h[2] = pack2(w2[0], w2[1]); h[3] = pack2(w3[0], w3[1]);
}
constexpr int KG_RS = 288, KG_PL = 16 * KG_RS;
__device__ __forceinline__ void kgen_role(char* smem, const u16* __restrict__ logA, const u16* __restrict__ Aim,
                                          const u16* __restrict__ Cr, const u16* __restrict__ Ci, const u16* __restrict__ logdt,
                                          const u16* __restrict__ x, const u16* __restrict__ W, const u16* __restrict__ wo,
                                          const u16* __restrict__ gw, char* __restrict__ ws, int blk, unsigned pfv) {
  const int tid = threadIdx.x, lane = tid & 63;
  const int wave = __builtin_amdgcn_readfirstlane(tid >> 6);
  constexpr int RS = KG_RS, PL = KG_PL;
  const bool wow = wave < 4 && blk < 200;
  int wid;
  unsigned wr_[8];
  {
    const int wt = blk * 4 + wave, oct = wt / 10, n = (wt - 10 * oct) * 64 + lane;
    const int h = oct & 1, kh = (oct >> 1) & 3, gs5 = oct >> 3, grp = gs5 >= 5, s5 = gs5 - 5 * grp, hb = 8 * kh + 4 * grp;
    const int cb = n / 80, m = n - 80 * cb;
    const int slot = m < 64 ? (m >> 5) * 64 + h * 32 + (m & 31) : 64 + h * 16 + m;
    wid = ((cb * 10 + gs5) * 4 + kh) * 160 + slot;
    int k0, k1;
    if (s5 < 4) { k0 = 20 * (hb + s5) + 4 * h; k1 = k0 + 8; }
    else { k0 = 20 * (hb + 2 * h) + 16; k1 = k0 + 20; }
    if (wow) {
      const u16* p0 = wo + (size_t)k0 * D + n; const u16* p1 = wo + (size_t)k1 * D + n;
#pragma unroll
      for (int e = 0; e < 4; e++) {
        asm volatile("global_load_ushort %0, %1, off offset:%2" : "=v"(wr_[e]) : "v"(p0), "i"(e * 1280));
        asm volatile("global_load_ushort %0, %1, off offset:%2" : "=v"(wr_[4 + e]) : "v"(p1), "i"(e * 1280));
      }
    }
  }
  char* tb = smem + wave * (2 * PL);
  char* wp = tb + lane * 4;
  const char* rp = tb + (lane & 15) * RS + (lane >> 4) * 16;
  const int nrep = blk >= NB_KC ? 0 : (wave == 7 ? 2 : 1);
  unsigned pa[5], pb[5] = {0u, 0u, 0u, 0u, 0u};
  {
    const int task = min(blk * 9 + wave, 1919), pi = task * 64 + lane;
    pa[0] = logdt[task / 3]; pa[1] = logA[pi]; pa[2] = Aim[pi]; pa[3] = Cr[pi]; pa[4] = Ci[pi];
    if (wave == 7) {
      const int t2 = min(blk * 9 + 8, 1919), p2 = t2 * 64 + lane;
      pb[0] = logdt[t2 / 3]; pb[1] = logA[p2]; pb[2] = Aim[p2]; pb[3] = Cr[p2]; pb[4] = Ci[p2];
    }
    asm volatile("" : "+v"(pb[0]), "+v"(pb[1]), "+v"(pb[2]), "+v"(pb[3]), "+v"(pb[4]));
  }
#pragma unroll 1
  for (int rep = 0; rep < nrep; rep++) {
    const float dt = rb(expf(lo_f(pa[0])));
    float ar;
    {
      const float la = lo_f(pa[1]);
      float h = la * 1.44269502f; float l = fmaf(la, 1.44269502f, -h) + la * 1.92596299e-8f;
      float e = __builtin_amdgcn_exp2f(h); e = fmaf(e, l * 0.69314718f, e);
      ar = -rb(e) * dt;
    }
    const float ai = lo_f(pa[2]) * dt;
    cf C; C[0] = 2.f * (lo_f(pa[3]) * dt); C[1] = 2.f * (lo_f(pa[4]) * dt);
#pragma unroll
    for (int i = 0; i < 5; i++) pa[i] = pb[i];
    const float th = ar * 1.44269502f, tl = fmaf(ar, 1.44269502f, -th) + ar * 1.92596299e-8f;
    const float rh = ai * 0.159154937f, rl = fmaf(ai, 0.159154937f, -rh) + ai * 6.42063833e-9f;
    const cf one = {1.f, 0.f};
    cf S1 = zpow(th, tl, rh, rl, 1.f), Q1 = zpow(th, tl, rh, rl, 8.f), R1 = zpow(th, tl, rh, rl, 128.f);
    Q1[1] = -Q1[1];
    cf S2, Q2, R2, G1, S3, S4, Q3, P1, R3, T1, G2, G3, S5, S6, S7, P2, CT1, T2, P3, CS4, CT2, CT3, CT4, dm0;
    cmul4(S2, Q2, R2, G1, S1, S1, Q1, Q1, R1, R1, C, R1);
    cmul4(S3, S4, Q3, P1, S2, S1, S2, S2, Q2, Q1, Q2, Q2);
    cmul4(R3, T1, G2, P2, R2, R1, R2, R2, C, R2, P1, P1);
    cmul4(S5, S6, S7, P3, S4, S1, S4, S2, S4, S3, P2, P1);
    cmul4(G3, CT1, T2, dm0, C, R3, C, T1, T1, T1, C, R1);
    cmul4(CS4, CT2, CT3, dm0, CT1, S4, C, T2, CT1, T2, C, R1);
    CT4 = cmul(CT2, T2);
    auto rows4 = [&](int pl, int row, int rs, cf b, cf t0, cf t1, cf t2, cf t3) {
      unsigned h[4];
      batch4(h, b, t0, t1, t2, t3);
#pragma unroll
      for (int k = 0; k < 4; k++) *(unsigned*)(wp + pl * PL + (row + k * rs) * RS) = h[k];
    };
    auto rows3 = [&](int pl, int row, int rs, cf b, cf t1, cf t2, cf t3) {
      unsigned h[4];
      batch3(h, b, t1, t2, t3);
#pragma unroll
      for (int k = 0; k < 4; k++) *(unsigned*)(wp + pl * PL + (row + k * rs) * RS) = h[k];
    };
    *(unsigned*)(wp) = 0x00003F80u;
    *(unsigned*)(wp + RS) = pack2(Q1[0], Q1[1]); *(unsigned*)(wp + 2 * RS) = pack2(Q2[0], Q2[1]); *(unsigned*)(wp + 3 * RS) = pack2(Q3[0], Q3[1]);
    rows3(0, 4, 1, P1, Q1, Q2, Q3); rows3(0, 8, 1, P2, Q1, Q2, Q3); rows3(0, 12, 1, P3, Q1, Q2, Q3);
    rows3(1, 0, 1, C, S1, S2, S3); rows4(1, 4, 1, C, S4, S5, S6, S7); rows3(1, 8, 1, G1, S1, S2, S3); rows4(1, 12, 1, G1, S4, S5, S6, S7);
    __builtin_amdgcn_wave_barrier();
    s16x8 bh[4];
    f32x4 res[3];
#pragma unroll
    for (int k = 0; k < 4; k++) bh[k] = *(const s16x8*)(rp + k * 64);
    auto mm = [&](int t, int pl) {
      f32x4 a = {0.f, 0.f, 0.f, 0.f};
#pragma unroll
      for (int k = 0; k < 4; k++) a = __builtin_amdgcn_mfma_f32_16x16x32_bf16(*(const s16x8*)(rp + pl * PL + k * 64), bh[k], a, 0, 0, 0);
      res[t] = a;
    };
    mm(0, 1);
    __builtin_amdgcn_wave_barrier();
    rows3(0, 0, 1, G2, S2, S4, S6); rows3(0, 4, 1, G3, S2, S4, S6); rows3(0, 8, 2, CT1, R1, R2, R3); rows3(0, 9, 2, CS4, R1, R2, R3);
    rows3(1, 0, 1, CT2, R1, R2, R3); rows3(1, 4, 1, CT3, R1, R2, R3);
    *(unsigned*)(wp + PL + 8 * RS) = pack2(CT4[0], CT4[1]);
    __builtin_amdgcn_wave_barrier();
    mm(1, 0); mm(2, 1);
    __builtin_amdgcn_wave_barrier();
    char* rs = (wave == 7 && rep == 0) ? smem + 8 * (2 * PL) : tb;
#pragma unroll
    for (int t = 0; t < 3; t++) *(f32x4*)(rs + t * 1024 + lane * 16) = res[t];
    __builtin_amdgcn_wave_barrier();
  }
  STAMP(0, 4);
  if (wow) {
    asm volatile("s_waitcnt vmcnt(0)" : "+v"(wr_[0]), "+v"(wr_[1]), "+v"(wr_[2]), "+v"(wr_[3]), "+v"(wr_[4]), "+v"(wr_[5]), "+v"(wr_[6]), "+v"(wr_[7]) :: "memory");
    u32x4 wv = {wr_[0] | (wr_[1] << 16), wr_[2] | (wr_[3] << 16), wr_[4] | (wr_[5] << 16), wr_[6] | (wr_[7] << 16)};
    st16(ws + WS_WOF + (size_t)wid * 16, wv);
  }
  if (blk == 239) ((unsigned*)(ws + WS_CNT))[tid] = 0u, ((unsigned*)(ws + WS_CNT))[tid + 512] = 0u;
  if (blk >= NB_KC && blk < NB_KC + 4) st16(ws + WS_KVT + (size_t)((blk - NB_KC) * 512 + tid) * 16, (u32x4){~0u, ~0u, ~0u, ~0u});
  if (blk < NB_KC) {
    int tix[8];
#pragma unroll
    for (int e = 0; e < 8; e++) {
      const int j = (tid & 255) * 8 + 7 - e;
      const int p = (int)rb((float)j);
      const int hh = p >> 7, c = (p >> 3) & 15;
      int sh = 31 - __builtin_clz(hh | 1); sh = sh > 3 ? 3 : sh;
      const int row = 8 * sh + (((hh << 3) | (p & 7)) >> sh);
      tix[e] = 4096 + (tid >> 8) * (2 * PL) + (((row >> 2) * 16 + c) * 4 + (row & 3)) * 4;
      asm volatile("" : "+v"(tix[e]));
    }
    __syncthreads();
#pragma unroll
    for (int it = 0; it < 2; it++) {
      const int e = tid + 512 * it;
      const int seg = e / 192, ent = e - 192 * seg;
      if (seg < 3) {
        const char* p0 = smem + 3 * seg * (2 * PL) + ent * 16;
        const f32x4 a = *(const f32x4*)p0 + *(const f32x4*)(p0 + (seg == 2 ? 2 : 1) * (2 * PL)) + *(const f32x4*)(p0 + (seg == 2 ? 1 : 2) * (2 * PL));
        *(f32x4*)(smem + 4096 + seg * (2 * PL) + ent * 16) = a;
      }
    }
    __syncthreads();
    const int d0 = 3 * blk + (tid >> 8);
    u32x4 hi;
#pragma unroll
    for (int e = 0; e < 4; e++) hi[e] = pack2(*(const float*)(smem + tix[2 * e]), *(const float*)(smem + tix[2 * e + 1]));
    if (d0 < D) st16(ws + WS_KIMG + (size_t)d0 * 4096 + (tid & 255) * 16, hi);
    if (wave < 4 && d0 + 2 < D) {
#pragma unroll
      for (int e = 0; e < 4; e++) hi[e] = pack2(*(const float*)(smem + tix[2 * e] + 4 * PL), *(const float*)(smem + tix[2 * e + 1] + 4 * PL));
      st16(ws + WS_KIMG + (size_t)(d0 + 2) * 4096 + tid * 16, hi);
    }
  }
  asm volatile("s_waitcnt vmcnt(0)" :: "v"(pfv) : "memory");
  const int gt = wave * 30 + (blk >> 3);
  if (gt < 64) {
    const int row = (blk & 7) * 256 + gt * 4 + (lane >> 4);
    const u32x4* px = (const u32x4*)(x + (size_t)row * D) + (lane & 15); const u32x4* pg = (const u32x4*)gw + (lane & 15);
    u32x4 xv[5], gv[5];
#pragma unroll
    for (int i = 0; i < 5; i++) { xv[i] = px[16 * i]; gv[i] = pg[16 * i]; }
    float gs = 0.f;
#pragma unroll
    for (int i = 0; i < 5; i++)
#pragma unroll
      for (int e = 0; e < 4; e++) {
        unsigned p = pack2(lo_f(xv[i][e]) * lo_f(gv[i][e]), hi_f(xv[i][e]) * hi_f(gv[i][e]));
        gs = __builtin_amdgcn_fdot2_f32_bf16(__builtin_bit_cast(bf16x2_t, p), __builtin_bit_cast(bf16x2_t, 0x3F803F80u), gs, false);
      }
    gs += __shfl_xor(gs, 1); gs += __shfl_xor(gs, 2); gs += __shfl_xor(gs, 4); gs += __shfl_xor(gs, 8);
    if ((lane & 15) == 0) { float a = rb(gs); ((float*)(ws + WS_GATE))[row] = rb(1.f / (1.f + __expf(-a))); }
  }
}

__global__ __launch_bounds__(512, 4) void k1(const u16* __restrict__ x, const u16* __restrict__ logA, const u16* __restrict__ Aim,
                                          const u16* __restrict__ Cr, const u16* __restrict__ Ci, const u16* __restrict__ logdt,
                                          const u16* __restrict__ wqkv, const u16* __restrict__ gw, const u16* __restrict__ wo,
                                          char* __restrict__ ws) {
  __shared__ __attribute__((aligned(16))) char smem[K1_LDS];
  int b = blockIdx.x;
  unsigned cpf = 0;
  asm volatile("" :: "s"(x), "s"(logA), "s"(Aim), "s"(Cr), "s"(Ci), "s"(logdt), "s"(wqkv), "s"(gw), "s"(wo), "s"(ws));
  if (b < NB_KG && threadIdx.x >= 501) {
    const char* pc; int len;
    asm volatile("s_getpc_b64 %0\n\ts_mov_b32 %1, code_tail_mark-_Z2k1PKtS0_S0_S0_S0_S0_S0_S0_S0_Pc-128" : "=s"(pc), "=s"(len));
    const int off = min((int)((threadIdx.x - 501) * 30 + (b >> 3)) * 128, len);
    asm volatile("global_load_dword %0, %1, %2" : "+v"(cpf) : "v"(off), "s"(pc));
  }
  if (b < 8 && threadIdx.x >= 490 && threadIdx.x < 500) asm volatile("global_load_dword %0, %1, %2" : "+v"(cpf) : "v"((threadIdx.x - 490) * 128), "s"(gw));
  STAMP(0, 0);
  b = b < NB_KG ? b + NB_GEMM : (b < NB_KG + NB_GEMM ? b - NB_KG : b);
  if (b < NB_GEMM) {
    const int xcd = b & 7, idx = b >> 3;
    const int rowblk = (xcd >> 1) * 4 + (idx & 3), cb = (xcd & 1) * 8 + (idx >> 2);
    gemm_role(smem, x, wqkv, gw, ws, rowblk, cb);
    STAMP(0, 1);
    return;
  }
  b -= NB_GEMM;
  if (b < NB_KG) { kgen_role(smem, logA, Aim, Cr, Ci, logdt, x, wqkv, wo, gw, ws, b, cpf); STAMP(0, 1); return; }
}

constexpr int K2_LDS = 163840;
constexpr int K2_CS = 4160;
constexpr int K2_KOFF = 4 * K2_CS;
constexpr int K2_CH = K2_KOFF + 4608;
constexpr int K2_CONV = K2_LDS - 3 * K2_CH;
constexpr int K2_TAB = 81920;
constexpr int K2_FIN = K2_TAB + 9216;
constexpr int K2_KV = 102400;
constexpr int K2_STG = 131072;
constexpr int NB_KV = 32;
typedef __attribute__((address_space(1))) unsigned gu32;
__device__ __forceinline__ const char* uni(const char* p) {
  size_t v = (size_t)p;
  unsigned lo = __builtin_amdgcn_readfirstlane((unsigned)v), hi = __builtin_amdgcn_readfirstlane((unsigned)(v >> 32));
  size_t o = ((size_t)hi << 32) | lo;
  asm volatile("s_nop 4" : "+s"(o));
  return (const char*)o;
}
__device__ __forceinline__ void wait_count(char* p, unsigned target) {
  gu32* c = (gu32*)(size_t)p;
  for (int n = 0; n < (1 << 22); n++) {
    if (__hip_atomic_load(c, __ATOMIC_RELAXED, __HIP_MEMORY_SCOPE_AGENT) >= target) break;
    __builtin_amdgcn_s_sleep(8);
  }
}
#define GLD4(dst, voff, sbase, pol) asm volatile("global_load_dwordx4 %0, %1, %2" pol : "=v"(dst) : "v"(voff), "s"(sbase))
#define GLD2(dst, voff, sbase, pol) asm volatile("global_load_dwordx2 %0, %1, %2" pol : "=v"(dst) : "v"(voff), "s"(sbase))
#define GLD1(dst, voff, sbase, pol) asm volatile("global_load_dword %0, %1, %2" pol : "=v"(dst) : "v"(voff), "s"(sbase))
template <int G>
__device__ __forceinline__ void conv_unit(f32x4* acc, int ap, int bp) {
  constexpr int NT = 8 - G;
  u32x2 a0[NT], a1[NT]; s16x8 bh[NT];
#pragma unroll
  for (int j = 0; j < NT; j++)
    asm volatile("ds_read_b64 %0, %3 offset:%5\n\tds_read_b64 %1, %3 offset:%6\n\tds_read_b128 %2, %4 offset:%5"
                 : "=&v"(a0[j]), "=&v"(a1[j]), "=&v"(bh[j]) : "v"(ap), "v"(bp), "n"(512 * j), "n"(512 * j + 8) : "memory");
#pragma unroll
  for (int x = 0; x < NT; x++) {
    asm volatile("s_waitcnt lgkmcnt(%3)" : "+v"(a0[x]), "+v"(a1[x]), "+v"(bh[x]) : "n"(3 * (NT - 1 - x) > 15 ? 15 : 3 * (NT - 1 - x)) : "memory");
#pragma unroll
    for (int y = 0; y <= x; y++)
#pragma unroll
      for (int sw = 0; sw < 2; sw++) {
        if (sw && y == x) continue;
        const int j = sw ? y : x, m = sw ? x : y, T = j + m;
        const int ti = G == 0 ? (T == 0 ? 0 : T == 3 ? 1 : T == 4 ? 2 : T == 7 ? 3 : -1) : (T == 1 ? 0 : T == 2 ? 1 : T == 5 ? 2 : T == 6 ? 3 : -1);
        if (ti >= 0) acc[ti] = __builtin_amdgcn_mfma_f32_16x16x32_bf16(__builtin_bit_cast(s16x8, (u32x4){a0[j][0], a0[j][1], a1[j][0], a1[j][1]}), bh[m], acc[ti], 0, 0, 0);
      }
    SB();
  }
}

__global__ __launch_bounds__(576) void k2(const u16* __restrict__ Dskip, char* __restrict__ ws, u16* __restrict__ out) {
  __shared__ __attribute__((aligned(16))) char smem[K2_LDS];
  const int tid = threadIdx.x, lane = tid & 63;
  const int wave = __builtin_amdgcn_readfirstlane(tid >> 6);
  const int b = blockIdx.x;
  unsigned cpf = 0;
  if (b < 32 && tid < 192) {
    const char* pc; int len;
    asm volatile("s_getpc_b64 %0\n\ts_mov_b32 %1, code_tail_mark-_Z2k2PKtPcPt-128" : "=s"(pc), "=s"(len));
    const int off = min(tid * 128, len);
    asm volatile("global_load_dword %0, %1, %2" : "+v"(cpf) : "v"(off), "s"(pc));
  }
  STAMP(1, 0);
  const int lds0 = (int)(size_t)(lptr_t)smem;
  const int l16 = lane * 16;
  const int xcd = b & 7, idx = b >> 3;
  const int odd = idx & 1;
  const int chA = 80 * xcd + 5 * (idx >> 1) + 3 * odd, chH = 80 * xcd + 5 * (idx >> 1) + 2;
  const int rowblk = xcd * 4 + (idx >> 3), cb = idx & 7;
  const int l0 = rowblk * 64, c0 = cb * 80;
  const int r = lane & 31, h = lane >> 5;
  if (wave == 8) {
    __builtin_amdgcn_s_setprio(3);
    const char* wsrc = ws + WS_WOF + (size_t)cb * 102400;
    auto dma4 = [&](const char* src, int dst) {
      asm volatile("s_mov_b32 m0, %2\n\ts_nop 0\n\tglobal_load_lds_dwordx4 %0, %1\n\tglobal_load_lds_dwordx4 %0, %1 offset:1024\n\t"
                   "global_load_lds_dwordx4 %0, %1 offset:2048\n\tglobal_load_lds_dwordx4 %0, %1 offset:3072"
                   :: "v"(l16), "s"(src), "s"(lds0 + dst) : "memory", "m0");
    };
    auto kimg = [&]() {
      dma4(ws + WS_KIMG + chA * 4096, K2_CONV + K2_KOFF + 512);
      dma4(ws + WS_KIMG + chA * 4096 + 4096, K2_CONV + K2_CH + K2_KOFF + 512);
      dma4(ws + WS_KIMG + chH * 4096, K2_CONV + 2 * K2_CH + K2_KOFF + 512);
    };
    if (b < NB_KV) {
      f32x4 kvp[32];
      const char* src = ws + WS_PART + (size_t)b * 16 * 1680;
      const int o2 = min(lane + 64, 104) * 16;
#pragma unroll
      for (int i = 0; i < 16; i++) { GLD4(kvp[i], l16, src + i * 1680, ""); GLD4(kvp[16 + i], o2, src + i * 1680, ""); }
      kimg();
#pragma unroll
      for (int i = 0; i < NW0; i++) dma4(wsrc + i * 4096, i * 4096);
      asm volatile("s_waitcnt vmcnt(%0)" :: "n"(4 * NW0) : "memory");
      BAR_ONLY();
#pragma unroll
      for (int i = 0; i < 32; i += 8)
        asm volatile("" : "+v"(kvp[i]), "+v"(kvp[i + 1]), "+v"(kvp[i + 2]), "+v"(kvp[i + 3]), "+v"(kvp[i + 4]), "+v"(kvp[i + 5]), "+v"(kvp[i + 6]), "+v"(kvp[i + 7]));
#pragma unroll
      for (int st = 1; st < 16; st <<= 1)
#pragma unroll
        for (int i = 0; i < 32; i += 2 * st) kvp[i] += kvp[i + st];
      const u32x2 o0 = {pack2(kvp[0][0], kvp[0][1]), pack2(kvp[0][2], kvp[0][3])}, o1 = {pack2(kvp[16][0], kvp[16][1]), pack2(kvp[16][2], kvp[16][3])};
      const int o = (int)WS_KVT + b * 896 + lane * 8;
      asm volatile("global_store_dwordx2 %0, %1, %2 sc1" :: "v"(o), "v"(o0), "s"(ws) : "memory");
      if (lane < 48) asm volatile("global_store_dwordx2 %0, %1, %2 offset:512 sc1\n\ts_nop 1" :: "v"(o), "v"(o1), "s"(ws) : "memory");
      KVST;
    } else {
      kimg();
#pragma unroll
      for (int i = 0; i < NW0; i++) dma4(wsrc + i * 4096, i * 4096);
      asm volatile("s_waitcnt vmcnt(%0)" :: "n"(4 * NW0) : "memory");
      BAR_ONLY();
    }
#pragma unroll 1
    for (int p = NW0; p < 24; p++) dma4(wsrc + p * 4096, p * 4096);
    {
      u32x4 kt[28];
      const char* kp = ws + WS_KVT;
#define KVL(pol) _Pragma("unroll") for (int i = 0; i < 28; i++) GLD4(kt[i], l16, kp + i * 1024, pol);
#define KVW() _Pragma("unroll") for (int i = 0; i < 28; i += 7) asm volatile("" : "+v"(kt[i]), "+v"(kt[i + 1]), "+v"(kt[i + 2]), "+v"(kt[i + 3]), "+v"(kt[i + 4]), "+v"(kt[i + 5]), "+v"(kt[i + 6]));
      KVL(KV1);
      VMCNT(0);
      KVW();
      for (int n = 0; n < (1 << 14); n++) {
        unsigned m = 0;
#pragma unroll
        for (int i = 0; i < 28; i++) m = max(max(max(m, kt[i][0]), kt[i][1]), max(kt[i][2], kt[i][3]));
        if (__builtin_amdgcn_ballot_w64(m == ~0u) == 0ull) break;
        KVL(" sc1");
        VMCNT(0);
        KVW();
      }
      BAR_ONLY();
      dma4(wsrc + 98304, 98304);
#pragma unroll
      for (int i = 0; i < 28; i++) *(u32x4*)(smem + K2_KV + i * 1024 + l16) = kt[i];
    }
    BAR_ONLY();
    asm volatile("s_waitcnt vmcnt(0) lgkmcnt(0)" ::: "memory");
    BAR_ONLY();
    BAR_ONLY();
    {
      gu32* c = (gu32*)(size_t)(ws + WS_CNT + (8 + cb) * 128);
      for (int n = 0; n < (1 << 22); n++) {
        if (__hip_atomic_load(c, __ATOMIC_RELAXED, __HIP_MEMORY_SCOPE_AGENT) >= 32u) break;
        __builtin_amdgcn_s_sleep(1);
      }
      asm volatile("" ::: "memory");
      const int o = (lane >> 3) * (L * 2) + (((lane & 7) ^ ((lane >> 2) & 4)) << 4);
      const char* src = ws + WS_AT + (size_t)(c0 * L + l0) * 2;
#pragma unroll
      for (int i = 0; i < 10; i++)
        asm volatile("s_mov_b32 m0, %2\n\ts_nop 0\n\tglobal_load_lds_dwordx4 %0, %1 sc1" :: "v"(o), "s"(src + i * 8 * L * 2), "s"(lds0 + i * 1024) : "memory", "m0");
      VMCNT(0);
    }
    BAR_ONLY();
    return;
  }
  const int t = wave & 1, kh = wave >> 1;
  u32x4 in0[2];
  int dsto[2];
#pragma unroll
  for (int rr = 0; rr < 2; rr++) {
    const int f = (wave + 8 * rr) * 49 + min(lane, 49), i = min(f, 776);
    const int ch = i / 259, xx = i - 259 * ch;
    dsto[rr] = (lane < 49 && f < 777) ? K2_CONV + ch * K2_CH + xx * 16 : -1;
    GLD4(in0[rr], (int)WS_UT + (ch == 2 ? chH : chA + ch) * (UT_STRIDE * 2) + (xx + 1) * 16, ws, "");
  }
  const int jg = wave & 1, jc = (wave >> 1) & 1, hiw = wave >> 2;
  const int fo = (lane & 15) * 16 + (lane >> 4) * 4;
  int ft[3];
  ft[0] = hiw ? 3 - jg : jg;
  ft[1] = hiw ? 4 + jg : (wave & 2) * 2 + ((wave & 1) ? 3 - odd : odd);
  ft[2] = 7 - jg;
  u32x2 us[3];
  float g_r;
  GLD1(g_r, (int)WS_GATE + (l0 + 32 * t + r) * 4, ws, "");
  unsigned dsA, dsH;
  asm volatile("global_load_ushort %0, %1, %2" : "=v"(dsA) : "v"((chA + jc) * 2), "s"(Dskip));
  asm volatile("global_load_ushort %0, %1, %2" : "=v"(dsH) : "v"((hiw ? chA + jc : chH) * 2), "s"(Dskip));
  u32x4 pf0[8]; u32x2 pf1[8];
  const char* pq0p = ws + WS_PQ + (size_t)((rowblk * 32 + kh * 8) * 2 + t) * 1024;
  const char* pq1p = ws + WS_PQ + (size_t)L * 16 * 32 * 2 + (size_t)((rowblk * 32 + kh * 8) * 2 + t) * 256;
  const int r8 = r * 8;
#define PQL(i0, i1) _Pragma("unroll") for (int i = i0; i < i1; i++) { GLD4(pf0[i], l16, pq0p + i * 2048, ""); GLD2(pf1[i], r8, pq1p + i * 512, ""); }
  if (tid < 96) *(u32x4*)(smem + K2_CONV + (tid >> 5) * K2_CH + K2_KOFF + (tid & 31) * 16) = (u32x4){0u, 0u, 0u, 0u};
  asm volatile("s_waitcnt vmcnt(3)" : "+v"(in0[0]), "+v"(in0[1]), "+v"(cpf) :: "memory");
  STAMP(1, 2);
#pragma unroll
  for (int rr = 0; rr < 2; rr++) {
    const u32x4 a = in0[rr];
    unsigned w[6] = {a[0], a[1], a[2], a[3], (unsigned)__builtin_amdgcn_update_dpp(0, (int)a[0], 0x130, 0xf, 0xf, false),
                     (unsigned)__builtin_amdgcn_update_dpp(0, (int)a[1], 0x130, 0xf, 0xf, false)};
    unsigned o[5];
#pragma unroll
    for (int k = 0; k < 5; k++) o[k] = __builtin_amdgcn_alignbyte(w[k + 1], w[k], 2);
    if (dsto[rr] >= 0) {
      char* p = smem + dsto[rr];
#pragma unroll
      for (int q = 0; q < 2; q++) {
        *(u32x4*)(p + (2 * q) * K2_CS) = (u32x4){o[q], o[q + 1], o[q + 2], o[q + 3]};
        *(u32x4*)(p + (2 * q + 1) * K2_CS) = (u32x4){w[q + 1], w[q + 2], w[q + 3], w[q + 4]};
      }
    }
  }
  RAW_BARRIER();
  PQL(0, PQA);
  STAMP(1, 3);
  f32x4 ca[4], sv[4];
#pragma unroll
  for (int i = 0; i < 4; i++) { ca[i] = (f32x4){0.f, 0.f, 0.f, 0.f}; sv[i] = (f32x4){0.f, 0.f, 0.f, 0.f}; }
  {
    const int i = lane & 15, kg = lane >> 4, e = kg >> 1, bb = kg & 1;
    const int ap = lds0 + K2_CONV + (i & 3) * K2_CS + (2 * e - bb + 2) * 16 + (i >> 2) * 8;
    const int bp = lds0 + K2_CONV + K2_KOFF + (16 + i - e) * 32 + bb * 16;
    int apj = ap + jc * K2_CH + hiw * 192;
    int bpj = bp + jc * K2_CH - hiw * 192;
    if (jg == 0) {
#pragma unroll 1
      for (int it = 0; it < 3; it++) conv_unit<0>(ca, apj + it * 64, bpj - it * 64);
    } else {
#pragma unroll 1
      for (int it = 0; it < 3; it++) conv_unit<1>(ca, apj + it * 64, bpj - it * 64);
    }
    PQL(PQA, PQB);
    apj += 192; bpj -= 192;
    int g2 = jg;
    if (!hiw) {
#pragma unroll
      for (int q = 0; q < 4; q++) { sv[q] = ca[q]; ca[q] = (f32x4){0.f, 0.f, 0.f, 0.f}; }
      apj = ap + 2 * K2_CH + wave * 128; bpj = bp + 2 * K2_CH - wave * 128; g2 = odd;
    }
    if (g2 == 0) {
#pragma unroll 1
      for (int it = 0; it < 2; it++) conv_unit<0>(ca, apj + it * 64, bpj - it * 64);
    } else {
#pragma unroll 1
      for (int it = 0; it < 2; it++) conv_unit<1>(ca, apj + it * 64, bpj - it * 64);
    }
  }
  STAMP(1, 4);
  {
    const char* ua = smem + K2_CONV + 3 * K2_CS + (20 + fo) * 2;
    us[0] = *(const u32x2*)(ua + jc * K2_CH + ft[0] * 512); us[1] = *(const u32x2*)(ua + (hiw ? jc : 2) * K2_CH + ft[1] * 512);
    us[2] = *(const u32x2*)(ua + jc * K2_CH + ft[2] * 512);
  }
  PQL(PQB, 8);
  RAW_BARRIER();
  {
    if (tid == 0) *(unsigned*)(smem + K2_LDS - 16) = 0u;
    char* sp = smem + K2_STG + l16;
    if (hiw) {
      *(f32x4*)(sp + (wave & 3) * 4096) = ca[0];
    } else {
#pragma unroll
      for (int i = 1; i < 4; i++) *(f32x4*)(sp + wave * 4096 + i * 1024) = sv[i];
#pragma unroll
      for (int i = 0; i < 4; i++) if (i != wave) *(f32x4*)(sp + 16384 + (i * 3 + ((wave - i - 1) & 3)) * 1024) = ca[i];
    }
  }
  RAW_BARRIER();
  {
    const char* sp = smem + K2_STG + l16;
    asm volatile("s_waitcnt vmcnt(16)" : "+v"(g_r), "+v"(dsA), "+v"(dsH) :: "memory");
    const float dskA = lo_f(dsA), dskH = lo_f(dsH);
    auto fin = [&](f32x4 s, u32x2 uu, float dsk, int ch, int tile) {
      u32x2 ov;
#pragma unroll
      for (int e = 0; e < 2; e++) {
        unsigned yy = pack2(s[2 * e], s[2 * e + 1]);
        unsigned k = pack2(lo_f(uu[e]) * dsk, hi_f(uu[e]) * dsk);
        ov[e] = pack2(lo_f(yy) + lo_f(k), hi_f(yy) + hi_f(k));
      }
      const int o = (int)WS_AT + (ch * L + tile * 256 + fo) * 2;
      asm volatile("global_store_dwordx2 %0, %1, %2 sc1\n\ts_nop 1" :: "v"(o), "v"(ov), "s"(ws) : "memory");
    };
    if (hiw) {
      const char* pp = sp + (wave & 3) * 4096;
#pragma unroll
      for (int i = 1; i < 4; i++) fin(ca[i] + *(const f32x4*)(pp + i * 1024), us[i - 1], dskA, chA + jc, ft[i - 1]);
      VMCNT(3);
    } else {
      const char* hp = sp + 16384 + wave * 3072;
      const f32x4 own = wave == 0 ? ca[0] : wave == 1 ? ca[1] : wave == 2 ? ca[2] : ca[3];
      fin(sv[0] + *(const f32x4*)(sp + wave * 4096), us[0], dskA, chA + jc, ft[0]);
      fin((own + *(const f32x4*)(hp + 1024)) + (*(const f32x4*)hp + *(const f32x4*)(hp + 2048)), us[1], dskH, chH, ft[1]);
      VMCNT(2);
    }
  }
#pragma unroll
  for (int i = 0; i < 8; i++) asm volatile("" : "+v"(pf0[i]), "+v"(pf1[i]));
  RAW_BARRIER();
  STAMP(1, 5);
  u32x4 ka[8]; u32x2 kb[8];
  {
    const char* kp = smem + K2_KV + kh * 8 * 896;
#pragma unroll
    for (int i = 0; i < 8; i++) {
      const char* p = kp + i * 896 + (r < 16 ? r : ((r >> 2) == ((i & 2) ? 5 : 4) ? 16 + (r & 3) : 20)) * 40;
      const u32x2 a0 = *(const u32x2*)(p + 16 * h), a1 = *(const u32x2*)(p + 16 * h + 8);
      ka[i] = (u32x4){a0[0], a0[1], a1[0], a1[1]};
      kb[i] = *(const u32x2*)(p + 32);
    }
  }
  f32x16 acc[3], c[2];
#pragma unroll
  for (int q = 0; q < 3; q++)
#pragma unroll
    for (int i = 0; i < 16; i++) acc[q][i] = 0.f;
  u32x4 rm = {0u, 0u, 0u, 0u};
  const char* wl = smem + kh * 2560 + l16;
  const char* wl2 = smem + kh * 2560 + 2048 + (h * 16 + (lane & 15)) * 16;
  auto stageA = [&](int i) {
    u32x2 b1u = {h ? 0u : pf1[i][0], h ? 0u : pf1[i][1]};
    f32x16 z;
#pragma unroll
    for (int q = 0; q < 16; q++) z[q] = 0.f;
    z = __builtin_amdgcn_mfma_f32_32x32x16_bf16(__builtin_bit_cast(s16x8, ka[i]), __builtin_bit_cast(s16x8, pf0[i]), z, 0, 0, 0);
    c[i & 1] = __builtin_amdgcn_mfma_f32_32x32x8bf16_1k(__builtin_bit_cast(s16x4, kb[i]), __builtin_bit_cast(s16x4, b1u), z, 0, 0, 0);
    asm volatile("" : "+v"(c[i & 1]));
  };
  stageA(0);
#pragma unroll
  for (int i = 0; i < 8; i++) {
    const int j = i & 3, G = i >> 2;
    const int wo_ = G * 51200 + j * 10240;
    s16x8 w0 = *(const s16x8*)(wl + wo_), w1 = *(const s16x8*)(wl + wo_ + 1024), w2 = *(const s16x8*)(wl2 + wo_);
    if (i + 1 < 8) stageA(i + 1);
    const f32x16 cc = c[i & 1];
    const float den = rb(rb(cc[12]) + 1e-6f);
    const float inv = __builtin_amdgcn_rcpf(den);
    unsigned o[6];
#pragma unroll
    for (int q = 0; q < 6; q++) o[q] = pack2(cc[2 * q] * inv, cc[2 * q + 1] * inv);
    asm volatile("" : "+v"(o[4]), "+v"(o[5]));
    const bool keep = (j >> 1) == h;
    rm[2 * (j & 1)] = keep ? o[4] : rm[2 * (j & 1)]; rm[2 * (j & 1) + 1] = keep ? o[5] : rm[2 * (j & 1) + 1];
    u32x4 ob = {o[0], o[1], o[2], o[3]};
    acc[0] = __builtin_amdgcn_mfma_f32_32x32x16_bf16(w0, __builtin_bit_cast(s16x8, ob), acc[0], 0, 0, 0);
    acc[1] = __builtin_amdgcn_mfma_f32_32x32x16_bf16(w1, __builtin_bit_cast(s16x8, ob), acc[1], 0, 0, 0);
    acc[2] = __builtin_amdgcn_mfma_f32_32x32x16_bf16(w2, __builtin_bit_cast(s16x8, ob), acc[2], 0, 0, 0);
    if (j == 3) {
      const int wr_ = G * 51200 + 40960;
      s16x8 w3 = *(const s16x8*)(wl + wr_), w4 = *(const s16x8*)(wl + wr_ + 1024), w5 = *(const s16x8*)(wl2 + wr_);
      acc[0] = __builtin_amdgcn_mfma_f32_32x32x16_bf16(w3, __builtin_bit_cast(s16x8, rm), acc[0], 0, 0, 0);
      acc[1] = __builtin_amdgcn_mfma_f32_32x32x16_bf16(w4, __builtin_bit_cast(s16x8, rm), acc[1], 0, 0, 0);
      acc[2] = __builtin_amdgcn_mfma_f32_32x32x16_bf16(w5, __builtin_bit_cast(s16x8, rm), acc[2], 0, 0, 0);
    }
    if (i == 1) {
      VMCNT(0);
      unsigned old = 0;
      if (lane == 0) old = __hip_atomic_fetch_add((unsigned*)(smem + K2_LDS - 16), 1u, __ATOMIC_RELAXED, __HIP_MEMORY_SCOPE_WORKGROUP);
      if (lane == 0 && old == 7u) __hip_atomic_fetch_add((gu32*)(size_t)(ws + WS_CNT + (8 + xcd) * 128), 1u, __ATOMIC_RELAXED, __HIP_MEMORY_SCOPE_AGENT);
    }
    if (i == 3) { RAW_BARRIER(); }
  }
  STAMP(1, 6);
  {
    char* sp = smem + 102400 + t * 30720 + l16;
#pragma unroll
    for (int g = 0; g < 10; g++) {
      const int own = g & 3;
      f32x4 v = {acc[g >> 2][4 * (g & 3)], acc[g >> 2][4 * (g & 3) + 1], acc[g >> 2][4 * (g & 3) + 2], acc[g >> 2][4 * (g & 3) + 3]};
      if (kh != own) *(f32x4*)(sp + (g * 3 + ((kh - own - 1) & 3)) * 1024) = v;
    }
  }
  RAW_BARRIER();
  STAMP(1, 7);
  {
    const char* sp = smem + 102400 + t * 30720 + l16;
    const float g1 = rb(1.f - g_r);
    const char* atp = smem + (8 * kh + 4 * h + ((lane & 15) >> 2)) * 128 + (((32 * t + (lane & 16) + 4 * (lane & 3)) * 2) ^ ((lane & 8) << 3));
    u16* op = out + (size_t)(l0 + 32 * t + r) * D + c0 + 8 * kh + 4 * h;
#pragma unroll
    for (int tl = 0; tl < 3; tl++) {
      if (tl == 2 && kh >= 2) break;
      f32x4 m;
      if (kh == 0)      m = (f32x4){acc[tl][0], acc[tl][1], acc[tl][2], acc[tl][3]};
      else if (kh == 1) m = (f32x4){acc[tl][4], acc[tl][5], acc[tl][6], acc[tl][7]};
      else if (kh == 2) m = (f32x4){acc[tl][8], acc[tl][9], acc[tl][10], acc[tl][11]};
      else              m = (f32x4){acc[tl][12], acc[tl][13], acc[tl][14], acc[tl][15]};
      const char* pp = sp + (tl * 4 + kh) * 3072;
      f32x4 p0 = *(const f32x4*)pp, p1 = *(const f32x4*)(pp + 1024), p2 = *(const f32x4*)(pp + 2048);
      f32x4 v = (m + p1) + (p0 + p2);
      s16x4 at = __builtin_amdgcn_ds_read_tr16_b64_v4i16((lds_s16x4_p)(atp + tl * 32 * 128));
      u32x2 au = __builtin_bit_cast(u32x2, at);
      u32x2 ov;
#pragma unroll
      for (int e = 0; e < 2; e++) {
        unsigned bq = pack2(v[2 * e], v[2 * e + 1]);
        unsigned gb = pack2(g1 * lo_f(bq), g1 * hi_f(bq));
        unsigned ga = pack2(g_r * lo_f(au[e]), g_r * hi_f(au[e]));
        ov[e] = pack2(lo_f(ga) + lo_f(gb), hi_f(ga) + hi_f(gb));
      }
      *(u32x2*)(op + 32 * tl) = ov;
    }
  }
  asm volatile("code_tail_mark:" ::: "memory");
  STAMP(1, 1);
}

extern "C" void kernel_launch(void* const* d_in, const int* in_sizes, int n_in, void* d_out, int out_size, void* d_ws, size_t ws_size,
                              hipStream_t stream) {
  const u16* u = (const u16*)d_in[0]; const u16* logA = (const u16*)d_in[1]; const u16* Aim = (const u16*)d_in[2];
  const u16* Cr = (const u16*)d_in[3]; const u16* Ci = (const u16*)d_in[4]; const u16* logdt = (const u16*)d_in[5];
  const u16* Dsk = (const u16*)d_in[6]; const u16* wqkv = (const u16*)d_in[8]; const u16* wo = (const u16*)d_in[9];
  const u16* gw = (const u16*)d_in[10];
  char* ws = (char*)d_ws;
  k1<<<NB_GEMM + NB_GATE + NB_KG, 512, 0, stream>>>(u, logA, Aim, Cr, Ci, logdt, wqkv, gw, wo, ws);
  k2<<<256, 576, 0, stream>>>(Dsk, ws, (u16*)d_out);
}
```

```cpp
#include <hip/hip_runtime.h>
#include <hip/hip_bf16.h>
#include <stdint.h>

typedef unsigned short u16;
typedef __attribute__((ext_vector_type(8))) short s16x8;
typedef __attribute__((ext_vector_type(4))) short s16x4;
typedef __attribute__((ext_vector_type(4))) float f32x4;
typedef __attribute__((ext_vector_type(16))) float f32x16;
typedef __attribute__((ext_vector_type(4))) unsigned u32x4;
typedef __attribute__((ext_vector_type(2))) unsigned u32x2;
typedef __attribute__((address_space(3))) s16x4* lds_s16x4_p;
typedef const __attribute__((address_space(1))) void* gptr_t;
typedef __attribute__((address_space(3))) void* lptr_t;

constexpr int L = 2048, D = 640, NS = 192, NH = 32;
constexpr int UT_PAD = 32, UT_STRIDE = L + UT_PAD;
constexpr size_t WS_UT   = 0;
constexpr size_t WS_KIMG = WS_UT + (size_t)D * UT_STRIDE * 2;
constexpr size_t WS_PQ   = WS_KIMG + (size_t)D * 4096;
constexpr size_t WS_PART = WS_PQ + (size_t)L * D * 2;
constexpr size_t WS_GATE = WS_PART + (size_t)32 * 16 * 1680;
constexpr size_t WS_KVT  = WS_GATE + (size_t)L * 4;
constexpr size_t WS_AT   = WS_KVT + (size_t)32 * 1024;
constexpr size_t WS_CNT  = WS_AT + (size_t)D * L * 2;
constexpr size_t WS_END  = WS_CNT + 4096;
#define TGET(v) do {} while (0)
#define STEPT(i) do {} while (0)
#define TPUT(kid, slot, thr, v) do {} while (0)
#define STAMP(kid, slot) do {} while (0)

__device__ __forceinline__ float b2f(u16 v) { return __uint_as_float(((unsigned)v) << 16); }
typedef __bf16 bf16x2_t __attribute__((ext_vector_type(2)));
typedef float f32x2 __attribute__((ext_vector_type(2)));
__device__ __forceinline__ unsigned pack2(float a, float b) { f32x2 v = {a, b}; bf16x2_t r = __builtin_convertvector(v, bf16x2_t); return __builtin_bit_cast(unsigned, r); }
__device__ __forceinline__ float lo_f(unsigned u) { return __uint_as_float(u << 16); }
__device__ __forceinline__ float hi_f(unsigned u) { return __uint_as_float(u & 0xffff0000u); }
__device__ __forceinline__ float rb(float f) { return lo_f(pack2(f, f)); }
__device__ __forceinline__ unsigned phi2(float a, float b) {
  unsigned t = pack2(a, b); float t0 = lo_f(t), t1 = hi_f(t);
  float e0 = __builtin_amdgcn_fmed3f(__builtin_amdgcn_exp2f(t0 * 1.44269504f), 0.f, 1.f);
  float e1 = __builtin_amdgcn_fmed3f(__builtin_amdgcn_exp2f(t1 * 1.44269504f), 0.f, 1.f);
  unsigned r = pack2(__builtin_fmaxf(t0, 0.f) + e0, __builtin_fmaxf(t1, 0.f) + e1);
  return pack2(lo_f(r) + 1e-6f, hi_f(r) + 1e-6f);
}
#define VMCNT(n) asm volatile("s_waitcnt vmcnt(" #n ")" ::: "memory")
#define RAW_BARRIER() do { asm volatile("s_waitcnt lgkmcnt(0)" ::: "memory"); __builtin_amdgcn_s_barrier(); asm volatile("" ::: "memory"); } while (0)
__device__ __forceinline__ void glds16(const void* g, void* l) { __builtin_amdgcn_global_load_lds((gptr_t)g, (lptr_t)l, 16, 0, 0); }

#define OPQ_S(v) asm volatile("" : "+s"(v))
typedef f32x2 cf;
__device__ __forceinline__ float smul(float a, float b) { float o; asm("v_mul_f32 %0, %1, %2" : "=v"(o) : "v"(a), "v"(b)); return o; }
__device__ __forceinline__ float sfma(float a, float b, float c) { float o; asm("v_fma_f32 %0, %1, %2, %3" : "=v"(o) : "v"(a), "v"(b), "v"(c)); return o; }
__device__ __forceinline__ float sfnma(float a, float b, float c) { float o; asm("v_fma_f32 %0, -%1, %2, %3" : "=v"(o) : "v"(a), "v"(b), "v"(c)); return o; }
__device__ __forceinline__ float ssub(float a, float b) { float o; asm("v_sub_f32 %0, %1, %2" : "=v"(o) : "v"(a), "v"(b)); return o; }
__device__ __forceinline__ cf cmul(cf a, cf b) {
  cf o;
  asm("v_pk_mul_f32 %0, %1, %2 op_sel:[0,0] op_sel_hi:[0,1]\n\t"
      "v_pk_fma_f32 %0, %1, %2, %0 op_sel:[1,1,0] op_sel_hi:[1,0,1] neg_lo:[1,0,0]" : "=&v"(o) : "v"(a), "v"(b));
  return o;
}

#define CPF 3
#define LPF 1
#define PFD 0
#define PFM_ 1
#define NP0 5
#define SPRIO 3
#define NI0 208
#define NB2 3
#define TB0 1
#define TBD 2
#define MIDX ((rowblk & 3) + 4 * (cb & 7))
#define MPS 3
#define PT0 30
#define PT1 110
#define PTD 20
constexpr int K1_LDS = 81920;
#define GLDS 1
constexpr int NB_GEMM = 256, NB_GATE = GLDS ? 0 : 16, NB_KG = 240, NB_KC = 214;
#define KVAT 0
#define KV1 ""
#define KVB 0
#define ROT 0
#define TPQ 0
#define KVST
#define STG 0
#define SA0 8
#define SB0 8
#define SA1 0
#define SB1 8
#define WOT 1
#define KRR 1
#define B6I 5
#define WE 3
#define SGB 1
#define NW0 3
#define K2PF 1
#define K2PFS "1"
#define KVP 1
#define PQA 2
#define PQB 4
#define STR2(x) #x
#define STR(x) STR2(x)
#define PQBS "2*(" STR(PQB) "-" STR(PQA) ")"
#define WTQ 0
#define WTP 0
#define WTO 0
#define WT 1
__device__ __forceinline__ void st16(void* p, u32x4 v) {
  asm volatile("global_store_dwordx4 %0, %1, off sc1\n\ts_nop 2" :: "v"(p), "v"(v) : "memory");
}

#define BAR_ONLY() do { asm volatile("" ::: "memory"); __builtin_amdgcn_s_barrier(); asm volatile("" ::: "memory"); } while (0)
#define SB() __builtin_amdgcn_sched_barrier(0)
template <bool ISQ>
__device__ __forceinline__ void mfma_part(char* smem, char* __restrict__ ws, int wave, f32x4 (&ep)[2][4]) {
  const int lane = threadIdx.x & 63;
  char* sB = smem; char* sA = smem + 32768;
  const int wr = wave & 1, wc = (wave >> 1) & 1;
  const int g = lane >> 4, fr = lane & 15;
  f32x4 acc[4][4];
#pragma unroll
  for (int m = 0; m < 4; m++)
#pragma unroll
    for (int c = 0; c < 4; c++) acc[m][c] = (f32x4){0.f, 0.f, 0.f, 0.f};
  const int q4 = fr >> 2, p4 = lane & 3;
  int aoff[2];
#pragma unroll
  for (int ks = 0; ks < 2; ks++) aoff[ks] = (wr * 64 + fr) * 128 + (((4 * ks + g) ^ (fr & 7)) << 4);
  int boff[4];
  {
    const int f = q4 | ((g & 1) << 2);
#pragma unroll
    for (int c = 0; c < 4; c++) boff[c] = (8 * g + q4) * 256 + (((4 * wc + c) ^ f) << 5) + p4 * 8;
  }
  s16x8 af[2][4]; s16x4 bl[4], bh[4];
  auto rdA = [&](int buf, int m, const char* pa) { af[buf][m] = *(const s16x8*)(pa + m * 2048); };
  auto rdBl = [&](int c, const char* pb) { bl[c] = __builtin_amdgcn_ds_read_tr16_b64_v4i16((lds_s16x4_p)(pb + boff[c])); };
  auto rdBh = [&](int c, const char* pb) { bh[c] = __builtin_amdgcn_ds_read_tr16_b64_v4i16((lds_s16x4_p)(pb + boff[c] + 1024)); };
  BAR_ONLY();
  STAMP(0, 2);
#pragma unroll
  for (int c = 0; c < 4; c++) rdA(0, c, sA + aoff[0]);
#pragma unroll
  for (int c = 0; c < 3; c++) { rdBl(c, sB); rdBh(c, sB); }
  int ao0 = 0, ao1 = 16384;
  auto grp = [&](int buf, int c, int m) {
    const s16x8 bf = __builtin_shufflevector(bl[c], bh[c], 0, 1, 2, 3, 4, 5, 6, 7);
    if (ISQ) acc[m][c] = __builtin_amdgcn_mfma_f32_16x16x32_bf16(bf, af[buf][m], acc[m][c], 0, 0, 0);
    else     acc[m][c] = __builtin_amdgcn_mfma_f32_16x16x32_bf16(af[buf][m], bf, acc[m][c], 0, 0, 0);
  };
#pragma unroll 1
  for (int j2 = 0; j2 < 5; j2++) {
#pragma unroll
    for (int st = 0; st < 4; st++) {
      const int hs = st & 1;
      SB();
      asm volatile("s_waitcnt lgkmcnt(2)" ::: "memory"); __builtin_amdgcn_s_barrier(); asm volatile("" ::: "memory");
      SB();
      const char* pa = sA + (hs == 0 ? ao0 + aoff[1] : ao1 + aoff[0]);
      const char* pc = sB + st * 8192;
      const char* pb = sB + ((st + 1) & 3) * 8192;
      grp(hs, 0, 0); SB(); rdBl(3, pc); rdBh(3, pc); SB();
      grp(hs, 0, 1); SB(); rdA(hs ^ 1, 0, pa); rdA(hs ^ 1, 1, pa); SB();
      grp(hs, 0, 2); SB(); rdA(hs ^ 1, 2, pa); rdA(hs ^ 1, 3, pa); SB();
      grp(hs, 0, 3); SB();
#pragma unroll
      for (int c = 1; c < 4; c++) {
        grp(hs, c, 0); SB(); rdBl(c - 1, pb); rdBh(c - 1, pb); SB();
        grp(hs, c, 1); grp(hs, c, 2); grp(hs, c, 3); SB();
      }
      if (hs == 1) { ao0 = ao1; ao1 = ao1 == 32768 ? 0 : ao1 + 16384; }
    }
  }
  STAMP(0, 3);
  int l3 = threadIdx.x; asm volatile("" : "+v"(l3));
  char* xch = smem + 32768 + (l3 >> 6) * 8192 + (l3 & 63) * 16;
#pragma unroll
  for (int i = 0; i < 8; i++) *(f32x4*)(xch + i * 1024) = acc[2 + (i >> 2)][i & 3];
#pragma unroll
  for (int i = 0; i < 8; i++) ep[i >> 2][i & 3] = acc[i >> 2][i & 3];
  RAW_BARRIER();
}
__device__ __forceinline__ void dma4x(const int (&vo)[4], const char* sbase, int ldsa) {
  asm volatile("s_mov_b32 m0, %5\n\ts_nop 0\n\tglobal_load_lds_dwordx4 %0, %4\n\tglobal_load_lds_dwordx4 %1, %4 offset:1024\n\t"
               "global_load_lds_dwordx4 %2, %4 offset:2048\n\tglobal_load_lds_dwordx4 %3, %4 offset:3072"
               :: "v"(vo[0]), "v"(vo[1]), "v"(vo[2]), "v"(vo[3]), "s"(sbase), "s"(ldsa) : "memory", "m0");
}
__device__ __forceinline__ void gemm_role(char* smem, const u16* __restrict__ x, const u16* __restrict__ W, const u16* __restrict__ wo, char* __restrict__ ws,
                                          int rowblk, int cb) {
  const int tid = threadIdx.x, lane = tid & 63;
  const int wave = __builtin_amdgcn_readfirstlane(tid >> 6);
  const bool ISQ = cb < 5;
  const int row0 = rowblk * 128, col0 = cb * 128;
  const int kvj = cb - 5;
  const int wr = wave & 1, wc = (wave >> 1) & 1, hf = wave >> 2;
  f32x4 ep[2][4];
  unsigned pf3 = 0;
  if (wave >= 4) {
    const int lw = wave & 1;
    const int l0 = (int)(size_t)(lptr_t)smem;
    unsigned pf0 = 0;
    const int pidx = (rowblk & 3) + 4 * (cb & 7);
    auto pfaddr = [&](int stg, int r) {
      const int q = r & 15;
      const int ln = (cb & 8) ? (q < 8 ? 12 + q : 14 + q) : (q < 13 ? q : 7 + q);
      return r < 16 ? (const char*)x + ((rowblk >> 2) * 512 + pidx * 16 + q) * 1280 + stg * 128
                    : (const char*)W + (64 * stg + pidx * 2 + (r >> 4) - 1) * 3840 + ln * 128;
    };
    {
      const int q = (wave - 4) * (NI0 / 4) + min(lane, NI0 / 4 - 1), stg = q / 48;
      asm volatile("global_load_dword %0, %1, off" : "+v"(pf0) : "v"(pfaddr(stg, q - 48 * stg)));
      SB();
    }
    __builtin_amdgcn_s_setprio(SPRIO);
    if (wave < 6) {
      int voA[4], voB[4];
#pragma unroll
      for (int i = 0; i < 4; i++) {
        voA[i] = (64 * lw + 8 * i + (lane >> 3)) * 1280 + (((lane & 7) ^ (lane >> 3)) << 4) - 1024 * i;
        const int fB = (lane >> 4) | ((i >> 1) << 2);
        const int c16 = (((lane & 15) >> 1) ^ fB) * 2 + (lane & 1);
        int bcol = col0 + 8 * c16;
        if (!ISQ) bcol = min(640 + 640 * (c16 >> 3) + ((60 * kvj) & ~7) + 8 * (c16 & 7), 1912);
        voB[i] = (16 * lw + 4 * i + (lane >> 4)) * 3840 + bcol * 2 - 1024 * i;
      }
      const char* pA = (const char*)x + row0 * 1280;
      const char* pB = (const char*)W;
      const int lA = l0 + 32768 + lw * 8192, lB = l0 + lw * 4096;
      auto issA = [&](int slot_off, int h) { dma4x(voA, pA + h * 32 * 1280, lA + slot_off + h * 4096); if (h) pA += 128; };
      auto issB = [&](int slot_off) { dma4x(voB, pB, lB + slot_off); pB += 32 * 3840; };
      TGET(tl0);
      asm volatile("s_waitcnt vmcnt(0)" :: "v"(pf0) : "memory");
      TGET(tl1);
      issA(0, 0); issB(0); issA(0, 1);
      issB(8192); issA(16384, 0);
      VMCNT(8);
      TGET(tl2);
      BAR_ONLY();
      issB(16384); issA(16384, 1);
      VMCNT(12); BAR_ONLY();
      issB(24576); issA(32768, 0); issA(32768, 1);
      VMCNT(12); BAR_ONLY();
      issB(0); issA(0, 0);
      int ao = 0, bo = 8192;
#pragma unroll 1
      for (int j = 1; j < 7; j++) {
        VMCNT(12); BAR_ONLY();
        issB(bo); bo = (bo + 8192) & 32767; issA(ao, 1);
        ao = ao == 32768 ? 0 : ao + 16384;
        VMCNT(12); BAR_ONLY();
        issB(bo); bo = (bo + 8192) & 32767; issA(ao, 0);
      }
      VMCNT(12); BAR_ONLY();
      issB(8192); issA(0, 1);
      VMCNT(12); BAR_ONLY();
      issB(16384);
      VMCNT(8); BAR_ONLY();
      issB(24576);
      {
        const int o = (pidx * 200 + lw * 100 + min(lane, 49)) * 128;
        asm volatile("global_load_dword %0, %1, %2\n\tglobal_load_dword %0, %1, %3" : "+v"(pf3) : "v"(o), "s"(wo), "s"((const char*)wo + 6400));
      }
      VMCNT(6); BAR_ONLY();
      VMCNT(2); BAR_ONLY();
      BAR_ONLY();
      TPUT(0, 4, 256, tl0);
      TPUT(0, 5, 256, tl1); TPUT(0, 6, 256, tl2);
      BAR_ONLY();
    } else {
      u32x4 trv[2][4];
      constexpr int CNT2 = 480 - NI0, NL2 = (CNT2 + 2 * NB2 - 1) / (2 * NB2);
      const char* pp[NB2];
#pragma unroll
      for (int i = 0; i < NB2; i++) {
        const int it = NI0 * 32 + ((pidx & 3) * 8 + (pidx >> 2)) * CNT2 + min((2 * i + lw) * NL2 + min(lane, NL2 - 1), CNT2 - 1);
        const int stg = it / 1536, r = it - 1536 * stg, q = r & 15;
        const int ln = (cb & 8) ? (q < 8 ? 12 + q : 14 + q) : (q < 13 ? q : 7 + q);
        pp[i] = r < 512 ? (const char*)x + ((rowblk >> 2) * 512 + r) * 1280 + stg * 128 : (const char*)W + (64 * stg + (r >> 4) - 32) * 3840 + ln * 128;
      }
      const int tb2 = TB0 + TBD * (pidx & 3);
      int tro;
      {
        const int fr = lane & 15, g = lane >> 4, p4 = lane & 3, r = 8 * g + (fr >> 2);
        tro = l0 + 32768 + r * 128 + (((4 * lw + (p4 >> 1)) ^ (r & 7)) << 4) + (p4 & 1) * 8;
      }
      auto transp = [&](int ao) {
#pragma unroll
        for (int hh = 0; hh < 2; hh++) {
          u32x2 tr[8];
          const int a = (tro ^ (32 * hh)) + ao, a2 = (a ^ 64) + 512;
          asm volatile("ds_read_b64_tr_b16 %0, %8\n\tds_read_b64_tr_b16 %1, %9\n\tds_read_b64_tr_b16 %2, %8 offset:4096\n\tds_read_b64_tr_b16 %3, %9 offset:4096\n\t"
                       "ds_read_b64_tr_b16 %4, %8 offset:8192\n\tds_read_b64_tr_b16 %5, %9 offset:8192\n\tds_read_b64_tr_b16 %6, %8 offset:12288\n\tds_read_b64_tr_b16 %7, %9 offset:12288\n\t"
                       "s_waitcnt lgkmcnt(0)"
                       : "=&v"(tr[0]), "=&v"(tr[1]), "=&v"(tr[2]), "=&v"(tr[3]), "=&v"(tr[4]), "=&v"(tr[5]), "=&v"(tr[6]), "=&v"(tr[7])
                       : "v"(a), "v"(a2) : "memory");
#pragma unroll
          for (int i = 0; i < 4; i++) trv[hh][i] = (u32x4){tr[2 * i][0], tr[2 * i][1], tr[2 * i + 1][0], tr[2 * i + 1][1]};
        }
      };
      unsigned pf2 = 0;
      auto burst = [&](int hs) {
        static_assert(NB2 == 3, "");
        asm volatile("s_cmp_lg_u32 %4, %5\n\ts_cbranch_scc1 1f\n\tglobal_load_dword %0, %1, off\n\tglobal_load_dword %0, %2, off\n\tglobal_load_dword %0, %3, off\n1:"
                     : "+v"(pf2) : "v"(pp[0]), "v"(pp[1]), "v"(pp[2]), "s"(hs), "s"(tb2) : "scc");
      };
      asm volatile("s_waitcnt vmcnt(0)" :: "v"(pf0) : "memory");
      BAR_ONLY();
      int ao = 0;
#pragma unroll 1
      for (int j = 0; j < 10; j++) {
        BAR_ONLY();
        if (!lw) STEPT(2 * j);
        burst(2 * j);
        if (j == cb) transp(ao);
        BAR_ONLY();
        if (!lw) STEPT(2 * j + 1);
        burst(2 * j + 1);
        ao = ao == 32768 ? 0 : ao + 16384;
      }
      asm volatile("s_waitcnt vmcnt(0)" :: "v"(pf2) : "memory");
      if (cb < 10) {
        u16* dst = (u16*)(ws + WS_UT) + (size_t)(64 * cb + 32 * lw + (lane & 15)) * UT_STRIDE + 8 * (lane >> 4);
#pragma unroll
        for (int hh = 0; hh < 2; hh++) {
#pragma unroll
          for (int i = 0; i < 4; i++) st16(dst + hh * 16 * UT_STRIDE + UT_PAD + row0 + 32 * i, trv[hh][i]);
          if (rowblk == 0) st16(dst + hh * 16 * UT_STRIDE, (u32x4){0u, 0u, 0u, 0u});
        }
      }
      BAR_ONLY();
    }
    __builtin_amdgcn_s_setprio(0);
    char* xch = smem + 32768 + (wave & 3) * 8192 + lane * 16;
#pragma unroll
    for (int i = 0; i < 8; i++) ep[i >> 2][i & 3] = *(const f32x4*)(xch + i * 1024);
  } else if (ISQ) {
    mfma_part<true>(smem, ws, wave, ep);
  } else {
    mfma_part<false>(smem, ws, wave, ep);
  }
  int l2 = threadIdx.x; asm volatile("" : "+v"(l2));
  const int g = (l2 >> 4) & 3, fr = l2 & 15;
  if (ISQ) {
#pragma unroll
    for (int c = 0; c < 4; c++) {
      const int n = col0 + 64 * wc + 16 * c + 4 * g;
      const int hd = n / 20, d0 = n - 20 * hd;
      char* dst0 = ws + WS_PQ + (size_t)(((rowblk * 2 + wr) * 32 + hd) * 4 + 2 * hf) * 640 + (d0 < 16 ? 128 + (d0 >> 3) * 256 + fr * 16 + (d0 & 4) * 2 : fr * 8);
#pragma unroll
      for (int m = 0; m < 2; m++) {
        u32x2 o;
        o[0] = phi2(ep[m][c][0], ep[m][c][1]);
        o[1] = phi2(ep[m][c][2], ep[m][c][3]);
        char* dst = dst0 + m * 640;
        *(u32x2*)dst = o;
      }
    }
  } else {
    const int o = (60 * kvj) & 7, zc = o ? 0 : 60;
    const int lane2 = l2 & 63;
    char* fb = smem;
#pragma unroll
    for (int c = 0; c < 4; c++) {
      u32x4 a;
      if (wc == 0) {
        a[0] = phi2(ep[0][c][0], ep[0][c][1]); a[1] = phi2(ep[0][c][2], ep[0][c][3]);
        a[2] = phi2(ep[1][c][0], ep[1][c][1]); a[3] = phi2(ep[1][c][2], ep[1][c][3]);
      } else {
        a[0] = pack2(ep[0][c][0], ep[0][c][1]); a[1] = pack2(ep[0][c][2], ep[0][c][3]);
        a[2] = pack2(ep[1][c][0], ep[1][c][1]); a[3] = pack2(ep[1][c][2], ep[1][c][3]);
        if (16 * c + fr == zc) a = (u32x4){0x3F803F80u, 0x3F803F80u, 0x3F803F80u, 0x3F803F80u};
      }
      *(u32x4*)(fb + ((((wc * 4 + wr * 2 + hf) * 4 + c) * 64 + lane2) << 4)) = a;
    }
    RAW_BARRIER();
    const int np = wave < 4 ? 1 : 2;
    for (int i = 0; i < np; i++) {
      const int p = wave < 4 ? wave : wave * 2 - 4 + i;
      int ta, tb;
      if (p < 10) { ta = (int)((0x3322211100ull >> (4 * p)) & 15); tb = (int)((0x3232121010ull >> (4 * p)) & 15); }
      else { ta = (o ? 2 : 0) + (p - 10); tb = o ? 0 : 3; }
      const char* pk = fb + ((ta * 64 + lane2) << 4);
      const char* pv = fb + 16384 + ((tb * 64 + lane2) << 4);
      f32x4 dd = {0.f, 0.f, 0.f, 0.f};
#pragma unroll
      for (int rg = 0; rg < 4; rg++)
        dd = __builtin_amdgcn_mfma_f32_16x16x32_bf16(*(const s16x8*)(pk + rg * 4096), *(const s16x8*)(pv + rg * 4096), dd, 0, 0, 0);
      const int kc0 = 16 * ta + 4 * g - o;
      const int hk = kc0 / 20, dk0 = kc0 - 20 * hk;
      const int vcol = 16 * tb + fr;
      const int dv = vcol == zc ? 20 : vcol - o - 20 * hk;
      if (kc0 >= 0 && kc0 < (kvj == 10 ? 40 : 60) && dv >= 0 && (dv < 20 || vcol == zc))
        *(f32x4*)(ws + WS_PART + (size_t)(((3 * kvj + hk) * 16 + rowblk) * 21 + dv) * 80 + dk0 * 4) = dd;
    }
  }
  asm volatile("s_waitcnt vmcnt(0)" :: "v"(pf3) : "memory");
}

__device__ __forceinline__ cf zpow(float th, float tl, float rh, float rl, float p) {
  float e = __builtin_amdgcn_exp2f(th * p); e = fmaf(e, (tl * p) * 0.69314718f, e);
  float t = rh * p; float f = (t - __builtin_rintf(t)) + rl * p;
  cf o; o[0] = e * __builtin_amdgcn_cosf(f); o[1] = e * __builtin_amdgcn_sinf(f); return o;
}
#define PKM(o, a, b) "v_pk_mul_f32 " o ", " a ", " b " op_sel:[0,0] op_sel_hi:[0,1]\n\t"
#define PKF(o, a, b) "v_pk_fma_f32 " o ", " a ", " b ", " o " op_sel:[1,1,0] op_sel_hi:[1,0,1] neg_lo:[1,0,0]\n\t"
__device__ __forceinline__ void cmul4(cf& o0, cf& o1, cf& o2, cf& o3, cf a0, cf b0, cf a1, cf b1, cf a2, cf b2, cf a3, cf b3) {
  asm(PKM("%0", "%4", "%5") PKM("%1", "%6", "%7") PKM("%2", "%8", "%9") PKM("%3", "%10", "%11")
      PKF("%0", "%4", "%5") PKF("%1", "%6", "%7") PKF("%2", "%8", "%9") PKF("%3", "%10", "%11")
      : "=&v"(o0), "=&v"(o1), "=&v"(o2), "=&v"(o3)
      : "v"(a0), "v"(b0), "v"(a1), "v"(b1), "v"(a2), "v"(b2), "v"(a3), "v"(b3));
}
__device__ __forceinline__ void batch4(unsigned* h, cf b, cf t0, cf t1, cf t2, cf t3) {
  cf w0, w1, w2, w3;
  asm(PKM("%0", "%4", "%5") PKM("%1", "%4", "%6") PKM("%2", "%4", "%7") PKM("%3", "%4", "%8")
      PKF("%0", "%4", "%5") PKF("%1", "%4", "%6") PKF("%2", "%4", "%7") PKF("%3", "%4", "%8")
      : "=&v"(w0), "=&v"(w1), "=&v"(w2), "=&v"(w3) : "v"(b), "v"(t0), "v"(t1), "v"(t2), "v"(t3));
  h[0] = pack2(w0[0], w0[1]); h[1] = pack2(w1[0], w1[1]); h[2] = pack2(w2[0], w2[1]); h[3] = pack2(w3[0], w3[1]);
}
__device__ __forceinline__ void batch3(unsigned* h, cf b, cf t1, cf t2, cf t3) {
  cf w1, w2, w3;
  asm(PKM("%0", "%3", "%4") PKM("%1", "%3", "%5") PKM("%2", "%3", "%6")
      PKF("%0", "%3", "%4") PKF("%1", "%3", "%5") PKF("%2", "%3", "%6")
      : "=&v"(w1), "=&v"(w2), "=&v"(w3) : "v"(b), "v"(t1), "v"(t2), "v"(t3));
  h[0] = pack2(b[0], b[1]); h[1] = pack2(w1[0], w1[1]); h[2] = pack2(w2[0], w2[1]); h[3] = pack2(w3[0], w3[1]);
}
constexpr int KG_RS = 288, KG_PL = 16 * KG_RS;
__device__ __forceinline__ void kgen_role(char* smem, const u16* __restrict__ logA, const u16* __restrict__ Aim,
                                          const u16* __restrict__ Cr, const u16* __restrict__ Ci, const u16* __restrict__ logdt,
                                          const u16* __restrict__ x, const u16* __restrict__ W, const u16* __restrict__ wo,
                                          const u16* __restrict__ gw, char* __restrict__ ws, int blk, unsigned pfv) {
  const int tid = threadIdx.x, lane = tid & 63;
  const int wave = __builtin_amdgcn_readfirstlane(tid >> 6);
  constexpr int RS = KG_RS, PL = KG_PL;
  char* tb = smem + wave * (2 * PL);
  char* wp = tb + lane * 4;
  const char* rp = tb + (lane & 15) * RS + (lane >> 4) * 16;
  const int kq = blk >> 3, tbase = (blk & 7) * 240;
  const int nrep = kq >= 27 ? 0 : (wave == 7 ? 2 : 1);
  unsigned pa[5], pb[5] = {0u, 0u, 0u, 0u, 0u};
  {
    const int task = tbase + min(kq * 9 + wave, 239), pi = task * 64 + lane;
    pa[0] = logdt[task / 3]; pa[1] = logA[pi]; pa[2] = Aim[pi]; pa[3] = Cr[pi]; pa[4] = Ci[pi];
    if (wave == 7) {
      const int t2 = tbase + min(kq * 9 + 8, 239), p2 = t2 * 64 + lane;
      pb[0] = logdt[t2 / 3]; pb[1] = logA[p2]; pb[2] = Aim[p2]; pb[3] = Cr[p2]; pb[4] = Ci[p2];
    }
    asm volatile("" : "+v"(pb[0]), "+v"(pb[1]), "+v"(pb[2]), "+v"(pb[3]), "+v"(pb[4]));
  }
#pragma unroll 1
  for (int rep = 0; rep < nrep; rep++) {
    const float dt = rb(expf(lo_f(pa[0])));
    float ar;
    {
      const float la = lo_f(pa[1]);
      float h = la * 1.44269502f; float l = fmaf(la, 1.44269502f, -h) + la * 1.92596299e-8f;
      float e = __builtin_amdgcn_exp2f(h); e = fmaf(e, l * 0.69314718f, e);
      ar = -rb(e) * dt;
    }
    const float ai = lo_f(pa[2]) * dt;
    cf C; C[0] = 2.f * (lo_f(pa[3]) * dt); C[1] = 2.f * (lo_f(pa[4]) * dt);
#pragma unroll
    for (int i = 0; i < 5; i++) pa[i] = pb[i];
    const float th = ar * 1.44269502f, tl = fmaf(ar, 1.44269502f, -th) + ar * 1.92596299e-8f;
    const float rh = ai * 0.159154937f, rl = fmaf(ai, 0.159154937f, -rh) + ai * 6.42063833e-9f;
    const cf one = {1.f, 0.f};
    cf S1 = zpow(th, tl, rh, rl, 1.f), Q1 = zpow(th, tl, rh, rl, 8.f), R1 = zpow(th, tl, rh, rl, 128.f);
    Q1[1] = -Q1[1];
    cf S2, Q2, R2, G1, S3, S4, Q3, P1, R3, T1, G2, G3, S5, S6, S7, P2, CT1, T2, P3, CS4, CT2, CT3, CT4, dm0;
    cmul4(S2, Q2, R2, G1, S1, S1, Q1, Q1, R1, R1, C, R1);
    cmul4(S3, S4, Q3, P1, S2, S1, S2, S2, Q2, Q1, Q2, Q2);
    cmul4(R3, T1, G2, P2, R2, R1, R2, R2, C, R2, P1, P1);
    cmul4(S5, S6, S7, P3, S4, S1, S4, S2, S4, S3, P2, P1);
    cmul4(G3, CT1, T2, dm0, C, R3, C, T1, T1, T1, C, R1);
    cmul4(CS4, CT2, CT3, dm0, CT1, S4, C, T2, CT1, T2, C, R1);
    CT4 = cmul(CT2, T2);
    auto rows4 = [&](int pl, int row, int rs, cf b, cf t0, cf t1, cf t2, cf t3) {
      unsigned h[4];
      batch4(h, b, t0, t1, t2, t3);
#pragma unroll
      for (int k = 0; k < 4; k++) *(unsigned*)(wp + pl * PL + (row + k * rs) * RS) = h[k];
    };
    auto rows3 = [&](int pl, int row, int rs, cf b, cf t1, cf t2, cf t3) {
      unsigned h[4];
      batch3(h, b, t1, t2, t3);
#pragma unroll
      for (int k = 0; k < 4; k++) *(unsigned*)(wp + pl * PL + (row + k * rs) * RS) = h[k];
    };
    *(unsigned*)(wp) = 0x00003F80u;
    *(unsigned*)(wp + RS) = pack2(Q1[0], Q1[1]); *(unsigned*)(wp + 2 * RS) = pack2(Q2[0], Q2[1]); *(unsigned*)(wp + 3 * RS) = pack2(Q3[0], Q3[1]);
    rows3(0, 4, 1, P1, Q1, Q2, Q3); rows3(0, 8, 1, P2, Q1, Q2, Q3); rows3(0, 12, 1, P3, Q1, Q2, Q3);
    rows3(1, 0, 1, C, S1, S2, S3); rows4(1, 4, 1, C, S4, S5, S6, S7); rows3(1, 8, 1, G1, S1, S2, S3); rows4(1, 12, 1, G1, S4, S5, S6, S7);
    __builtin_amdgcn_wave_barrier();
    s16x8 bh[4];
    f32x4 res[3];
#pragma unroll
    for (int k = 0; k < 4; k++) bh[k] = *(const s16x8*)(rp + k * 64);
    auto mm = [&](int t, int pl) {
      f32x4 a = {0.f, 0.f, 0.f, 0.f};
#pragma unroll
      for (int k = 0; k < 4; k++) a = __builtin_amdgcn_mfma_f32_16x16x32_bf16(*(const s16x8*)(rp + pl * PL + k * 64), bh[k], a, 0, 0, 0);
      res[t] = a;
    };
    mm(0, 1);
    __builtin_amdgcn_wave_barrier();
    rows3(0, 0, 1, G2, S2, S4, S6); rows3(0, 4, 1, G3, S2, S4, S6); rows3(0, 8, 2, CT1, R1, R2, R3); rows3(0, 9, 2, CS4, R1, R2, R3);
    rows3(1, 0, 1, CT2, R1, R2, R3); rows3(1, 4, 1, CT3, R1, R2, R3);
    *(unsigned*)(wp + PL + 8 * RS) = pack2(CT4[0], CT4[1]);
    __builtin_amdgcn_wave_barrier();
    mm(1, 0); mm(2, 1);
    __builtin_amdgcn_wave_barrier();
    char* rs = (wave == 7 && rep == 0) ? smem + 8 * (2 * PL) : tb;
#pragma unroll
    for (int t = 0; t < 3; t++) *(f32x4*)(rs + t * 1024 + lane * 16) = res[t];
    __builtin_amdgcn_wave_barrier();
  }
  STAMP(0, 4);
  if (blk == 239) ((unsigned*)(ws + WS_CNT))[tid] = 0u, ((unsigned*)(ws + WS_CNT))[tid + 512] = 0u;
  if (blk >= 216 && blk < 220) st16(ws + WS_KVT + (size_t)((blk - 216) * 512 + tid) * 16, (u32x4){~0u, ~0u, ~0u, ~0u});
  if (kq < 27) {
    int tix[8];
#pragma unroll
    for (int e = 0; e < 8; e++) {
      const int j = (tid & 255) * 8 + 7 - e;
      const int p = (int)rb((float)j);
      const int hh = p >> 7, c = (p >> 3) & 15;
      int sh = 31 - __builtin_clz(hh | 1); sh = sh > 3 ? 3 : sh;
      const int row = 8 * sh + (((hh << 3) | (p & 7)) >> sh);
      tix[e] = 4096 + (tid >> 8) * (2 * PL) + (((row >> 2) * 16 + c) * 4 + (row & 3)) * 4;
      asm volatile("" : "+v"(tix[e]));
    }
    __syncthreads();
#pragma unroll
    for (int it = 0; it < 2; it++) {
      const int e = tid + 512 * it;
      const int seg = e / 192, ent = e - 192 * seg;
      if (seg < 3) {
        const char* p0 = smem + 3 * seg * (2 * PL) + ent * 16;
        const f32x4 a = *(const f32x4*)p0 + *(const f32x4*)(p0 + (seg == 2 ? 2 : 1) * (2 * PL)) + *(const f32x4*)(p0 + (seg == 2 ? 1 : 2) * (2 * PL));
        *(f32x4*)(smem + 4096 + seg * (2 * PL) + ent * 16) = a;
      }
    }
    __syncthreads();
    const int d0 = 80 * (blk & 7) + 3 * kq + (tid >> 8);
    u32x4 hi;
#pragma unroll
    for (int e = 0; e < 4; e++) hi[e] = pack2(*(const float*)(smem + tix[2 * e]), *(const float*)(smem + tix[2 * e + 1]));
    st16(ws + WS_KIMG + (size_t)d0 * 4096 + (tid & 255) * 16, hi);
    if (wave < 4 && kq < 26) {
#pragma unroll
      for (int e = 0; e < 4; e++) hi[e] = pack2(*(const float*)(smem + tix[2 * e] + 4 * PL), *(const float*)(smem + tix[2 * e + 1] + 4 * PL));
      st16(ws + WS_KIMG + (size_t)(d0 + 2) * 4096 + tid * 16, hi);
    }
    VMCNT(0);
    if (lane < 16) {
      const int o = (int)WS_KIMG + (d0 + ((lane & 8) && wave < 4 && kq < 26 ? 2 : 0)) * 4096 + (wave & 3) * 1024 + (lane & 7) * 128;
      asm volatile("global_load_dword %0, %1, %2" : "+v"(pfv) : "v"(o), "s"(ws));
    }
  }
  asm volatile("s_waitcnt vmcnt(0)" :: "v"(pfv) : "memory");
  const int gt = wave * 30 + (blk >> 3);
  if (gt < 64) {
    const int row = (blk & 7) * 256 + gt * 4 + (lane >> 4);
    const u32x4* px = (const u32x4*)(x + (size_t)row * D) + (lane & 15); const u32x4* pg = (const u32x4*)gw + (lane & 15);
    u32x4 xv[5], gv[5];
#pragma unroll
    for (int i = 0; i < 5; i++) { xv[i] = px[16 * i]; gv[i] = pg[16 * i]; }
    float gs = 0.f;
#pragma unroll
    for (int i = 0; i < 5; i++)
#pragma unroll
      for (int e = 0; e < 4; e++) {
        unsigned p = pack2(lo_f(xv[i][e]) * lo_f(gv[i][e]), hi_f(xv[i][e]) * hi_f(gv[i][e]));
        gs = __builtin_amdgcn_fdot2_f32_bf16(__builtin_bit_cast(bf16x2_t, p), __builtin_bit_cast(bf16x2_t, 0x3F803F80u), gs, false);
      }
    gs += __shfl_xor(gs, 1); gs += __shfl_xor(gs, 2); gs += __shfl_xor(gs, 4); gs += __shfl_xor(gs, 8);
    if ((lane & 15) == 0) { float a = rb(gs); ((float*)(ws + WS_GATE))[row] = rb(1.f / (1.f + __expf(-a))); }
  }
}

__global__ __launch_bounds__(512, 4) void k1(const u16* __restrict__ x, const u16* __restrict__ logA, const u16* __restrict__ Aim,
                                          const u16* __restrict__ Cr, const u16* __restrict__ Ci, const u16* __restrict__ logdt,
                                          const u16* __restrict__ wqkv, const u16* __restrict__ gw, const u16* __restrict__ wo,
                                          char* __restrict__ ws) {
  __shared__ __attribute__((aligned(16))) char smem[K1_LDS];
  int b = blockIdx.x;
  unsigned cpf = 0;
  asm volatile("" :: "s"(x), "s"(logA), "s"(Aim), "s"(Cr), "s"(Ci), "s"(logdt), "s"(wqkv), "s"(gw), "s"(wo), "s"(ws));
  if (b < NB_KG && threadIdx.x >= 501) {
    const char* pc; int len;
    asm volatile("s_getpc_b64 %0\n\ts_mov_b32 %1, code_tail_mark-_Z2k1PKtS0_S0_S0_S0_S0_S0_S0_S0_Pc-128" : "=s"(pc), "=s"(len));
    const int off = min((int)((threadIdx.x - 501) * 30 + (b >> 3)) * 128, len);
    asm volatile("global_load_dword %0, %1, %2" : "+v"(cpf) : "v"(off), "s"(pc));
  }
  if (b < 8 && threadIdx.x >= 490 && threadIdx.x < 500) asm volatile("global_load_dword %0, %1, %2" : "+v"(cpf) : "v"((threadIdx.x - 490) * 128), "s"(gw));
  STAMP(0, 0);
  b = b < NB_KG ? b + NB_GEMM : (b < NB_KG + NB_GEMM ? b - NB_KG : b);
  if (b < NB_GEMM) {
    const int xcd = b & 7, idx = b >> 3;
    const int rowblk = (xcd >> 1) * 4 + (idx & 3), cb = (xcd & 1) * 8 + (idx >> 2);
    gemm_role(smem, x, wqkv, wo, ws, rowblk, cb);
    STAMP(0, 1);
    return;
  }
  b -= NB_GEMM;
  if (b < NB_KG) { kgen_role(smem, logA, Aim, Cr, Ci, logdt, x, wqkv, wo, gw, ws, b, cpf); STAMP(0, 1); return; }
}

constexpr int K2_LDS = 163840;
constexpr int K2_CS = 4160;
constexpr int K2_KOFF = 4 * K2_CS;
constexpr int K2_CH = K2_KOFF + 4608;
constexpr int K2_CONV = K2_LDS - 3 * K2_CH;
constexpr int K2_TAB = 81920;
constexpr int K2_FIN = K2_TAB + 9216;
constexpr int K2_KV = 102400;
constexpr int K2_STG = 131072;
constexpr int NB_KV = 32;
typedef __attribute__((address_space(1))) unsigned gu32;
__device__ __forceinline__ const char* uni(const char* p) {
  size_t v = (size_t)p;
  unsigned lo = __builtin_amdgcn_readfirstlane((unsigned)v), hi = __builtin_amdgcn_readfirstlane((unsigned)(v >> 32));
  size_t o = ((size_t)hi << 32) | lo;
  asm volatile("s_nop 4" : "+s"(o));
  return (const char*)o;
}
__device__ __forceinline__ void wait_count(char* p, unsigned target) {
  gu32* c = (gu32*)(size_t)p;
  for (int n = 0; n < (1 << 22); n++) {
    if (__hip_atomic_load(c, __ATOMIC_RELAXED, __HIP_MEMORY_SCOPE_AGENT) >= target) break;
    __builtin_amdgcn_s_sleep(8);
  }
}
#define GLD4(dst, voff, sbase, pol) asm volatile("global_load_dwordx4 %0, %1, %2" pol : "=v"(dst) : "v"(voff), "s"(sbase))
#define GLD2(dst, voff, sbase, pol) asm volatile("global_load_dwordx2 %0, %1, %2" pol : "=v"(dst) : "v"(voff), "s"(sbase))
#define GLD1(dst, voff, sbase, pol) asm volatile("global_load_dword %0, %1, %2" pol : "=v"(dst) : "v"(voff), "s"(sbase))
template <int G>
__device__ __forceinline__ void conv_unit(f32x4* acc, int ap, int bp) {
  constexpr int NT = 8 - G;
  u32x2 a0[NT], a1[NT]; s16x8 bh[NT];
#pragma unroll
  for (int j = 0; j < NT; j++)
    asm volatile("ds_read_b64 %0, %3 offset:%5\n\tds_read_b64 %1, %3 offset:%6\n\tds_read_b128 %2, %4 offset:%5"
                 : "=&v"(a0[j]), "=&v"(a1[j]), "=&v"(bh[j]) : "v"(ap), "v"(bp), "n"(512 * j), "n"(512 * j + 8) : "memory");
#pragma unroll
  for (int x = 0; x < NT; x++) {
    asm volatile("s_waitcnt lgkmcnt(%3)" : "+v"(a0[x]), "+v"(a1[x]), "+v"(bh[x]) : "n"(3 * (NT - 1 - x) > 15 ? 15 : 3 * (NT - 1 - x)) : "memory");
#pragma unroll
    for (int y = 0; y <= x; y++)
#pragma unroll
      for (int sw = 0; sw < 2; sw++) {
        if (sw && y == x) continue;
        const int j = sw ? y : x, m = sw ? x : y, T = j + m;
        const int ti = G == 0 ? (T == 0 ? 0 : T == 3 ? 1 : T == 4 ? 2 : T == 7 ? 3 : -1) : (T == 1 ? 0 : T == 2 ? 1 : T == 5 ? 2 : T == 6 ? 3 : -1);
        if (ti >= 0) acc[ti] = __builtin_amdgcn_mfma_f32_16x16x32_bf16(__builtin_bit_cast(s16x8, (u32x4){a0[j][0], a0[j][1], a1[j][0], a1[j][1]}), bh[m], acc[ti], 0, 0, 0);
      }
    SB();
  }
}

__global__ __launch_bounds__(576) void k2(const u16* __restrict__ Dskip, const u16* __restrict__ wo, char* __restrict__ ws, u16* __restrict__ out) {
  __shared__ __attribute__((aligned(16))) char smem[K2_LDS];
  const int tid = threadIdx.x, lane = tid & 63;
  const int wave = __builtin_amdgcn_readfirstlane(tid >> 6);
  const int b = blockIdx.x;
  unsigned cpf = 0;
  if (b < 32 && tid < 192) {
    const char* pc; int len;
    asm volatile("s_getpc_b64 %0\n\ts_mov_b32 %1, code_tail_mark-_Z2k2PKtS0_PcPt-128" : "=s"(pc), "=s"(len));
    const int off = min(tid * 128, len);
    asm volatile("global_load_dword %0, %1, %2" : "+v"(cpf) : "v"(off), "s"(pc));
  }
  STAMP(1, 0);
  const int lds0 = (int)(size_t)(lptr_t)smem;
  const int l16 = lane * 16;
  const int xcd = b & 7, idx = b >> 3;
  const int odd = idx & 1;
  const int chA = 80 * xcd + 5 * (idx >> 1) + 3 * odd, chH = 80 * xcd + 5 * (idx >> 1) + 2;
  const int rowblk = xcd * 4 + (idx >> 3), cb = idx & 7;
  const int l0 = rowblk * 64, c0 = cb * 80;
  const int r = lane & 31, h = lane >> 5;
  if (wave == 8) {
    __builtin_amdgcn_s_setprio(3);
    const char* wsrc = (const char*)wo + cb * 160;
    int wv[5];
#pragma unroll
    for (int j = 0; j < 5; j++) { const int f = 64 * j + lane, row = f / 10; wv[j] = row * 1280 + (f - 10 * row) * 16 - (j & 3) * 1024; }
    auto dma5 = [&](int grp) {
      asm volatile("s_mov_b32 m0, %6\n\ts_nop 0\n\tglobal_load_lds_dwordx4 %0, %5\n\tglobal_load_lds_dwordx4 %1, %5 offset:1024\n\t"
                   "global_load_lds_dwordx4 %2, %5 offset:2048\n\tglobal_load_lds_dwordx4 %3, %5 offset:3072\n\t"
                   "s_add_u32 m0, m0, 0x1000\n\ts_nop 0\n\tglobal_load_lds_dwordx4 %4, %5"
                   :: "v"(wv[0]), "v"(wv[1]), "v"(wv[2]), "v"(wv[3]), "v"(wv[4]), "s"(wsrc + grp * 40960), "s"(lds0 + grp * 5120) : "memory", "m0", "scc");
    };
    auto dma4 = [&](const char* src, int dst) {
      asm volatile("s_mov_b32 m0, %2\n\ts_nop 0\n\tglobal_load_lds_dwordx4 %0, %1\n\tglobal_load_lds_dwordx4 %0, %1 offset:1024\n\t"
                   "global_load_lds_dwordx4 %0, %1 offset:2048\n\tglobal_load_lds_dwordx4 %0, %1 offset:3072"
                   :: "v"(l16), "s"(src), "s"(lds0 + dst) : "memory", "m0");
    };
    auto kimg = [&]() {
      dma4(ws + WS_KIMG + chA * 4096, K2_CONV + K2_KOFF + 512);
      dma4(ws + WS_KIMG + chA * 4096 + 4096, K2_CONV + K2_CH + K2_KOFF + 512);
      dma4(ws + WS_KIMG + chH * 4096, K2_CONV + 2 * K2_CH + K2_KOFF + 512);
    };
    if (b < NB_KV) {
      f32x4 kvp[32];
      const char* src = ws + WS_PART + (size_t)b * 16 * 1680;
      const int o2 = min(lane + 64, 104) * 16;
#pragma unroll
      for (int i = 0; i < 16; i++) { GLD4(kvp[i], l16, src + i * 1680, ""); GLD4(kvp[16 + i], o2, src + i * 1680, ""); }
      kimg();
#pragma unroll
      for (int i = 0; i < NW0; i++) dma5(i);
      asm volatile("s_waitcnt vmcnt(%0)" :: "n"(5 * NW0) : "memory");
      BAR_ONLY();
#pragma unroll
      for (int i = 0; i < 32; i += 8)
        asm volatile("" : "+v"(kvp[i]), "+v"(kvp[i + 1]), "+v"(kvp[i + 2]), "+v"(kvp[i + 3]), "+v"(kvp[i + 4]), "+v"(kvp[i + 5]), "+v"(kvp[i + 6]), "+v"(kvp[i + 7]));
#pragma unroll
      for (int st = 1; st < 16; st <<= 1)
#pragma unroll
        for (int i = 0; i < 32; i += 2 * st) kvp[i] += kvp[i + st];
      const u32x2 o0 = {pack2(kvp[0][0], kvp[0][1]), pack2(kvp[0][2], kvp[0][3])}, o1 = {lane < 41 ? pack2(kvp[16][0], kvp[16][1]) : 0u, lane < 41 ? pack2(kvp[16][2], kvp[16][3]) : 0u};
      const int o = (int)WS_KVT + b * 896 + lane * 8;
      asm volatile("global_store_dwordx2 %0, %1, %2 sc1" :: "v"(o), "v"(o0), "s"(ws) : "memory");
      if (lane < 48) asm volatile("global_store_dwordx2 %0, %1, %2 offset:512 sc1\n\ts_nop 1" :: "v"(o), "v"(o1), "s"(ws) : "memory");
      KVST;
    } else {
      kimg();
#pragma unroll
      for (int i = 0; i < NW0; i++) dma5(i);
      asm volatile("s_waitcnt vmcnt(%0)" :: "n"(5 * NW0) : "memory");
      BAR_ONLY();
    }
#pragma unroll 1
    for (int p = NW0; p < 19; p++) {
      dma5(p);
    }
    {
      u32x4 kt[28];
      const char* kp = ws + WS_KVT;
#define KVL(pol) _Pragma("unroll") for (int i = 0; i < 28; i++) GLD4(kt[i], l16, kp + i * 1024, pol);
#define KVW() _Pragma("unroll") for (int i = 0; i < 28; i += 7) asm volatile("" : "+v"(kt[i]), "+v"(kt[i + 1]), "+v"(kt[i + 2]), "+v"(kt[i + 3]), "+v"(kt[i + 4]), "+v"(kt[i + 5]), "+v"(kt[i + 6]));
      KVL(KV1);
      VMCNT(0);
      KVW();
      for (int n = 0; n < (1 << 14); n++) {
        unsigned m = 0;
#pragma unroll
        for (int i = 0; i < 28; i++) m = max(max(max(m, kt[i][0]), kt[i][1]), max(kt[i][2], kt[i][3]));
        if (__builtin_amdgcn_ballot_w64(m == ~0u) == 0ull) break;
        KVL(" sc1");
        VMCNT(0);
        KVW();
      }
      BAR_ONLY();
#pragma unroll
      for (int i = 0; i < 14; i++) *(u32x4*)(smem + K2_KV + i * 1024 + l16) = kt[i];
      BAR_ONLY();
#pragma unroll
      for (int i = 14; i < 28; i++) *(u32x4*)(smem + K2_KV + i * 1024 + l16) = kt[i];
    }
    asm volatile("s_waitcnt lgkmcnt(0)" ::: "memory");
    BAR_ONLY();
    dma5(19);
    VMCNT(0);
    BAR_ONLY();
    {
      gu32* c = (gu32*)(size_t)(ws + WS_CNT + (8 + cb) * 128);
      for (int n = 0; n < (1 << 22); n++) {
        if (__hip_atomic_load(c, __ATOMIC_RELAXED, __HIP_MEMORY_SCOPE_AGENT) >= 32u) break;
        __builtin_amdgcn_s_sleep(1);
      }
      asm volatile("" ::: "memory");
      const int o = (lane >> 3) * (L * 2) + (((lane & 7) ^ ((lane >> 2) & 4)) << 4);
      const char* src = ws + WS_AT + (size_t)(c0 * L + l0) * 2;
#pragma unroll
      for (int i = 0; i < 10; i++)
        asm volatile("s_mov_b32 m0, %2\n\ts_nop 0\n\tglobal_load_lds_dwordx4 %0, %1 sc1" :: "v"(o), "s"(src + i * 8 * L * 2), "s"(lds0 + (i < 8 ? (i >> 1) * 3200 : 25600) + (i & 1) * 1024) : "memory", "m0");
      VMCNT(0);
    }
    BAR_ONLY();
    return;
  }
  const int t = wave & 1, kh = wave >> 1;
  u32x4 in0[2];
  int dsto[2];
#pragma unroll
  for (int rr = 0; rr < 2; rr++) {
    const int f = (wave + 8 * rr) * 49 + min(lane, 49), i = min(f, 776);
    const int ch = i / 259, xx = i - 259 * ch;
    dsto[rr] = (lane < 49 && f < 777) ? K2_CONV + ch * K2_CH + xx * 16 : -1;
    GLD4(in0[rr], (int)WS_UT + (ch == 2 ? chH : chA + ch) * (UT_STRIDE * 2) + (xx + 1) * 16, ws, "");
  }
  const int jg = wave & 1, jc = (wave >> 1) & 1, hiw = wave >> 2;
  const int fo = (lane & 15) * 16 + (lane >> 4) * 4;
  int ft[3];
  ft[0] = hiw ? 3 - jg : jg;
  ft[1] = hiw ? 4 + jg : (wave & 2) * 2 + ((wave & 1) ? 3 - odd : odd);
  ft[2] = 7 - jg;
  u32x2 us[3];
  float g_r;
  GLD1(g_r, (int)WS_GATE + (l0 + 32 * t + 16 * (kh & 1) + (lane & 15)) * 4, ws, "");
  unsigned dsA, dsH;
  asm volatile("global_load_ushort %0, %1, %2" : "=v"(dsA) : "v"((chA + jc) * 2), "s"(Dskip));
  asm volatile("global_load_ushort %0, %1, %2" : "=v"(dsH) : "v"((hiw ? chA + jc : chH) * 2), "s"(Dskip));
  u32x4 pf[8][2];
  const char* pqp = ws + WS_PQ + (size_t)((rowblk * 32 + kh * 8) * 4 + 2 * t) * 640;
  const int pqo = lane < 32 ? 128 + l16 : (lane & 15) * 8;
#define PQL(i0, i1) _Pragma("unroll") for (int i = i0; i < i1; i++) { GLD4(pf[i][0], pqo, pqp + i * 2560, ""); GLD4(pf[i][1], pqo, pqp + i * 2560, " offset:640"); }
  if (tid < 96) *(u32x4*)(smem + K2_CONV + (tid >> 5) * K2_CH + K2_KOFF + (tid & 31) * 16) = (u32x4){0u, 0u, 0u, 0u};
  asm volatile("s_waitcnt vmcnt(3)" : "+v"(in0[0]), "+v"(in0[1]), "+v"(cpf) :: "memory");
  STAMP(1, 2);
#pragma unroll
  for (int rr = 0; rr < 2; rr++) {
    const u32x4 a = in0[rr];
    unsigned w[6] = {a[0], a[1], a[2], a[3], (unsigned)__builtin_amdgcn_update_dpp(0, (int)a[0], 0x130, 0xf, 0xf, false),
                     (unsigned)__builtin_amdgcn_update_dpp(0, (int)a[1], 0x130, 0xf, 0xf, false)};
    unsigned o[5];
#pragma unroll
    for (int k = 0; k < 5; k++) o[k] = __builtin_amdgcn_alignbyte(w[k + 1], w[k], 2);
    if (dsto[rr] >= 0) {
      char* p = smem + dsto[rr];
#pragma unroll
      for (int q = 0; q < 2; q++) {
        *(u32x4*)(p + (2 * q) * K2_CS) = (u32x4){o[q], o[q + 1], o[q + 2], o[q + 3]};
        *(u32x4*)(p + (2 * q + 1) * K2_CS) = (u32x4){w[q + 1], w[q + 2], w[q + 3], w[q + 4]};
      }
    }
  }
  RAW_BARRIER();
  PQL(0, PQA);
  STAMP(1, 3);
  f32x4 ca[4], sv[4];
#pragma unroll
  for (int i = 0; i < 4; i++) { ca[i] = (f32x4){0.f, 0.f, 0.f, 0.f}; sv[i] = (f32x4){0.f, 0.f, 0.f, 0.f}; }
  {
    const int i = lane & 15, kg = lane >> 4, e = kg >> 1, bb = kg & 1;
    const int ap = lds0 + K2_CONV + (i & 3) * K2_CS + (2 * e - bb + 2) * 16 + (i >> 2) * 8;
    const int bp = lds0 + K2_CONV + K2_KOFF + (16 + i - e) * 32 + bb * 16;
    int apj = ap + jc * K2_CH + hiw * 192;
    int bpj = bp + jc * K2_CH - hiw * 192;
    if (jg == 0) {
#pragma unroll 1
      for (int it = 0; it < 3; it++) conv_unit<0>(ca, apj + it * 64, bpj - it * 64);
    } else {
#pragma unroll 1
      for (int it = 0; it < 3; it++) conv_unit<1>(ca, apj + it * 64, bpj - it * 64);
    }
    PQL(PQA, PQB);
    apj += 192; bpj -= 192;
    int g2 = jg;
    if (!hiw) {
#pragma unroll
      for (int q = 0; q < 4; q++) { sv[q] = ca[q]; ca[q] = (f32x4){0.f, 0.f, 0.f, 0.f}; }
      apj = ap + 2 * K2_CH + wave * 128; bpj = bp + 2 * K2_CH - wave * 128; g2 = odd;
    }
    if (g2 == 0) {
#pragma unroll 1
      for (int it = 0; it < 2; it++) conv_unit<0>(ca, apj + it * 64, bpj - it * 64);
    } else {
#pragma unroll 1
      for (int it = 0; it < 2; it++) conv_unit<1>(ca, apj + it * 64, bpj - it * 64);
    }
  }
  STAMP(1, 4);
  {
    const char* ua = smem + K2_CONV + 3 * K2_CS + (20 + fo) * 2;
    us[0] = *(const u32x2*)(ua + jc * K2_CH + ft[0] * 512); us[1] = *(const u32x2*)(ua + (hiw ? jc : 2) * K2_CH + ft[1] * 512);
    us[2] = *(const u32x2*)(ua + jc * K2_CH + ft[2] * 512);
  }
  PQL(PQB, 8);
  RAW_BARRIER();
  {
    if (tid == 0) *(unsigned*)(smem + K2_LDS - 16) = 0u;
    char* sp = smem + K2_STG + l16;
    if (hiw) {
      *(f32x4*)(sp + (wave & 3) * 4096) = ca[0];
    } else {
#pragma unroll
      for (int i = 1; i < 4; i++) *(f32x4*)(sp + wave * 4096 + i * 1024) = sv[i];
#pragma unroll
      for (int i = 0; i < 4; i++) if (i != wave) *(f32x4*)(sp + 16384 + (i * 3 + ((wave - i - 1) & 3)) * 1024) = ca[i];
    }
  }
  RAW_BARRIER();
  {
    const char* sp = smem + K2_STG + l16;
    asm volatile("s_waitcnt vmcnt(16)" : "+v"(g_r), "+v"(dsA), "+v"(dsH) :: "memory");
    const float dskA = lo_f(dsA), dskH = lo_f(dsH);
    auto fin = [&](f32x4 s, u32x2 uu, float dsk, int ch, int tile) {
      u32x2 ov;
#pragma unroll
      for (int e = 0; e < 2; e++) {
        unsigned yy = pack2(s[2 * e], s[2 * e + 1]);
        unsigned k = pack2(lo_f(uu[e]) * dsk, hi_f(uu[e]) * dsk);
        ov[e] = pack2(lo_f(yy) + lo_f(k), hi_f(yy) + hi_f(k));
      }
      const int o = (int)WS_AT + (ch * L + tile * 256 + fo) * 2;
      asm volatile("global_store_dwordx2 %0, %1, %2 sc1\n\ts_nop 1" :: "v"(o), "v"(ov), "s"(ws) : "memory");
    };
    if (hiw) {
      const char* pp = sp + (wave & 3) * 4096;
#pragma unroll
      for (int i = 1; i < 4; i++) fin(ca[i] + *(const f32x4*)(pp + i * 1024), us[i - 1], dskA, chA + jc, ft[i - 1]);
      VMCNT(3);
    } else {
      const char* hp = sp + 16384 + wave * 3072;
      const f32x4 own = wave == 0 ? ca[0] : wave == 1 ? ca[1] : wave == 2 ? ca[2] : ca[3];
      fin(sv[0] + *(const f32x4*)(sp + wave * 4096), us[0], dskA, chA + jc, ft[0]);
      fin((own + *(const f32x4*)(hp + 1024)) + (*(const f32x4*)hp + *(const f32x4*)(hp + 2048)), us[1], dskH, chH, ft[1]);
      VMCNT(2);
    }
  }
#pragma unroll
  for (int i = 0; i < 8; i++) asm volatile("" : "+v"(pf[i][0]), "+v"(pf[i][1]));
  RAW_BARRIER();
  STAMP(1, 5);
  const int n16 = lane & 15, g = lane >> 4;
  f32x4 acc[5][2];
  {
    const char* kp = smem + K2_KV + kh * 8 * 896;
    const int row1 = (n16 & 3) ? 20 : 16 + (n16 >> 2);
    const char* k00 = kp + (g < 3 ? n16 * 40 + 16 * g : 840);
    const char* k01 = kp + (g < 2 ? n16 * 40 + 16 * g + 8 : 840);
    const char* k10 = kp + (g < 3 ? row1 * 40 + 16 * g : 840);
    const char* k11 = kp + (g < 2 ? row1 * 40 + 16 * g + 8 : 840);
    const char* wm = smem + (160 * kh + 4 * g + (n16 >> 2)) * 160 + (lane & 3) * 8;
    const char* wt = smem + (160 * kh + 20 * (n16 >> 2) + 16 + g) * 160 + (lane & 3) * 8;
    u32x4 ka[8][2];
    auto rdK = [&](int i) {
      const u32x2 a0 = *(const u32x2*)(k00 + i * 896), a1 = *(const u32x2*)(k01 + i * 896), b0 = *(const u32x2*)(k10 + i * 896), b1 = *(const u32x2*)(k11 + i * 896);
      ka[i][0] = (u32x4){a0[0], a0[1], a1[0], a1[1]}; ka[i][1] = (u32x4){b0[0], b0[1], b1[0], b1[1]};
    };
    f32x4 c[2][2][2];
    const f32x4 z4 = {0.f, 0.f, 0.f, 0.f};
    auto stageA = [&](int i) {
#pragma unroll
      for (int tl = 1; tl >= 0; tl--)
#pragma unroll
        for (int nt = 0; nt < 2; nt++)
          c[i & 1][tl][nt] = __builtin_amdgcn_mfma_f32_16x16x32_bf16(__builtin_bit_cast(s16x8, ka[i][tl]), __builtin_bit_cast(s16x8, pf[i][nt]), z4, 0, 0, 0);
    };
    auto wfrag = [&](const char* p, int d) {
      const s16x4 lo = __builtin_amdgcn_ds_read_tr16_b64_v4i16((lds_s16x4_p)(p)), hi = __builtin_amdgcn_ds_read_tr16_b64_v4i16((lds_s16x4_p)(p + d));
      return __builtin_shufflevector(lo, hi, 0, 1, 2, 3, 4, 5, 6, 7);
    };
    u32x4 ob[2], o5[2];
    float p5[2];
    rdK(0); rdK(1);
    stageA(0);
    SB();
#pragma unroll
    for (int i = 0; i < 8; i++) {
      s16x8 w[5];
      if (i + 2 < 8 && i <= B6I) rdK(i + 2);
      if (i == B6I) { _Pragma("unroll") for (int j = i + 3; j < 8; j++) rdK(j); }
      if (i & 1) { _Pragma("unroll") for (int mt = 0; mt < WE; mt++) w[mt] = wfrag(wm + (i - 1) * 3200 + mt * 32, 3200); }
      SB();
      if (i + 1 < 8) stageA(i + 1);
      const f32x4 (&cc)[2][2] = c[i & 1];
      unsigned dz = pack2(cc[1][0][1], cc[1][1][1]);
      dz = pack2(lo_f(dz) + 1e-6f, hi_f(dz) + 1e-6f);
      const float inv[2] = {__builtin_amdgcn_rcpf(lo_f(dz)), __builtin_amdgcn_rcpf(hi_f(dz))};
#pragma unroll
      for (int nt = 0; nt < 2; nt++) {
        const f32x4 v = cc[0][nt] * inv[nt];
        ob[nt][2 * (i & 1)] = pack2(v[0], v[1]); ob[nt][2 * (i & 1) + 1] = pack2(v[2], v[3]);
        const float q5 = cc[1][nt][0] * inv[nt];
        if (i & 1) o5[nt][i >> 1] = pack2(p5[nt], q5); else p5[nt] = q5;
      }
      if (i + 1 < 8) {
#pragma unroll
        for (int q = 0; q < 4; q++) { __builtin_amdgcn_sched_group_barrier(0x8, 1, 0); __builtin_amdgcn_sched_group_barrier(0x2, 5, 0); }
      }
      SB();
      if (i & 1) {
        _Pragma("unroll") for (int mt = WE; mt < 5; mt++) w[mt] = wfrag(wm + (i - 1) * 3200 + mt * 32, 3200);
        SB();
#pragma unroll
        for (int mt = 0; mt < 5; mt++)
#pragma unroll
          for (int nt = 0; nt < 2; nt++)
            acc[mt][nt] = __builtin_amdgcn_mfma_f32_16x16x32_bf16(w[mt], __builtin_bit_cast(s16x8, ob[nt]), i == 1 ? z4 : acc[mt][nt], 0, 0, 0);
        SB();
      }
      if (i == 1) {
        VMCNT(0);
        unsigned old = 0;
        if (lane == 0) old = __hip_atomic_fetch_add((unsigned*)(smem + K2_LDS - 16), 1u, __ATOMIC_RELAXED, __HIP_MEMORY_SCOPE_WORKGROUP);
        if (lane == 0 && old == 7u) __hip_atomic_fetch_add((gu32*)(size_t)(ws + WS_CNT + (8 + xcd) * 128), 1u, __ATOMIC_RELAXED, __HIP_MEMORY_SCOPE_AGENT);
      }
      if (i == B6I) { RAW_BARRIER(); }
    }
#pragma unroll
    for (int mt = 0; mt < 5; mt++) {
      const s16x8 w = wfrag(wt + mt * 32, 12800);
#pragma unroll
      for (int nt = 0; nt < 2; nt++)
        acc[mt][nt] = __builtin_amdgcn_mfma_f32_16x16x32_bf16(w, __builtin_bit_cast(s16x8, o5[nt]), acc[mt][nt], 0, 0, 0);
    }
  }
  STAMP(1, 6);
  {
    char* sp = smem + 102400 + t * 30720 + l16;
#pragma unroll
    for (int q = 0; q < 10; q++) {
      const int own = q & 3;
      if (kh != own) *(f32x4*)(sp + (q * 3 + ((kh - own - 1) & 3)) * 1024) = acc[q >> 1][q & 1];
    }
  }
  RAW_BARRIER();
  STAMP(1, 7);
  {
    const char* sp = smem + 102400 + t * 30720 + l16;
    const float g1 = rb(1.f - g_r);
    const char* atp = smem + (kh >> 1) * 3200 + (4 * g + (n16 >> 2)) * 128 + (((32 * t + 16 * (kh & 1) + 4 * (lane & 3)) * 2) ^ ((lane & 8) << 3));
    u16* op = out + (size_t)(l0 + 32 * t + 16 * (kh & 1) + n16) * D + c0 + 16 * (kh >> 1) + 4 * g;
#pragma unroll
    for (int tl = 0; tl < 3; tl++) {
      if (tl == 2 && kh >= 2) break;
      f32x4 m;
      if (kh == 0)      m = acc[2 * tl][0];
      else if (kh == 1) m = acc[2 * tl][1];
      else if (kh == 2) m = acc[tl < 2 ? 2 * tl + 1 : 0][0];
      else              m = acc[tl < 2 ? 2 * tl + 1 : 0][1];
      const char* pp = sp + (tl * 4 + kh) * 3072;
      f32x4 p0 = *(const f32x4*)pp, p1 = *(const f32x4*)(pp + 1024), p2 = *(const f32x4*)(pp + 2048);
      f32x4 v = (m + p1) + (p0 + p2);
      s16x4 at = __builtin_amdgcn_ds_read_tr16_b64_v4i16((lds_s16x4_p)(atp + (tl < 2 ? tl * 6400 : 25600)));
      u32x2 au = __builtin_bit_cast(u32x2, at);
      u32x2 ov;
#pragma unroll
      for (int e = 0; e < 2; e++) {
        unsigned bq = pack2(v[2 * e], v[2 * e + 1]);
        unsigned gb = pack2(g1 * lo_f(bq), g1 * hi_f(bq));
        unsigned ga = pack2(g_r * lo_f(au[e]), g_r * hi_f(au[e]));
        ov[e] = pack2(lo_f(ga) + lo_f(gb), hi_f(ga) + hi_f(gb));
      }
      *(u32x2*)(op + 32 * tl) = ov;
    }
  }
  asm volatile("code_tail_mark:" ::: "memory");
  STAMP(1, 1);
}

extern "C" void kernel_launch(void* const* d_in, const int* in_sizes, int n_in, void* d_out, int out_size, void* d_ws, size_t ws_size,
                              hipStream_t stream) {
  const u16* u = (const u16*)d_in[0]; const u16* logA = (const u16*)d_in[1]; const u16* Aim = (const u16*)d_in[2];
  const u16* Cr = (const u16*)d_in[3]; const u16* Ci = (const u16*)d_in[4]; const u16* logdt = (const u16*)d_in[5];
  const u16* Dsk = (const u16*)d_in[6]; const u16* wqkv = (const u16*)d_in[8]; const u16* wo = (const u16*)d_in[9];
  const u16* gw = (const u16*)d_in[10];
  char* ws = (char*)d_ws;
  k1<<<NB_GEMM + NB_GATE + NB_KG, 512, 0, stream>>>(u, logA, Aim, Cr, Ci, logdt, wqkv, gw, wo, ws);
  k2<<<256, 576, 0, stream>>>(Dsk, wo, ws, (u16*)d_out);
}
```

```cpp
#include <hip/hip_runtime.h>
#include <hip/hip_bf16.h>
#include <stdint.h>

typedef unsigned short u16;
typedef __attribute__((ext_vector_type(8))) short s16x8;
typedef __attribute__((ext_vector_type(4))) short s16x4;
typedef __attribute__((ext_vector_type(4))) float f32x4;
typedef __attribute__((ext_vector_type(16))) float f32x16;
typedef __attribute__((ext_vector_type(4))) unsigned u32x4;
typedef __attribute__((ext_vector_type(2))) unsigned u32x2;
typedef __attribute__((address_space(3))) s16x4* lds_s16x4_p;
typedef const __attribute__((address_space(1))) void* gptr_t;
typedef __attribute__((address_space(3))) void* lptr_t;

constexpr int L = 2048, D = 640, NS = 192, NH = 32;
constexpr int UT_PAD = 32, UT_STRIDE = L + UT_PAD;
constexpr size_t WS_UT   = 0;
constexpr size_t WS_KIMG = WS_UT + (size_t)D * UT_STRIDE * 2;
constexpr size_t WS_PQ   = WS_KIMG + (size_t)D * 4096;
constexpr size_t WS_PART = WS_PQ + (size_t)L * D * 2;
constexpr size_t WS_GATE = WS_PART + (size_t)32 * 16 * 1680;
constexpr size_t WS_KVT  = WS_GATE + (size_t)L * 4;
constexpr size_t WS_AT   = WS_KVT + (size_t)32 * 1024;
constexpr size_t WS_CNT  = WS_AT + (size_t)D * L * 2;
constexpr size_t WS_END  = WS_CNT + 4096;
#define TGET(v) do {} while (0)
#define STEPT(i) do {} while (0)
#define TPUT(kid, slot, thr, v) do {} while (0)
#define STAMP(kid, slot) do {} while (0)

__device__ __forceinline__ float b2f(u16 v) { return __uint_as_float(((unsigned)v) << 16); }
typedef __bf16 bf16x2_t __attribute__((ext_vector_type(2)));
typedef float f32x2 __attribute__((ext_vector_type(2)));
__device__ __forceinline__ unsigned pack2(float a, float b) { f32x2 v = {a, b}; bf16x2_t r = __builtin_convertvector(v, bf16x2_t); return __builtin_bit_cast(unsigned, r); }
__device__ __forceinline__ float lo_f(unsigned u) { return __uint_as_float(u << 16); }
__device__ __forceinline__ float hi_f(unsigned u) { return __uint_as_float(u & 0xffff0000u); }
__device__ __forceinline__ float rb(float f) { return lo_f(pack2(f, f)); }
__device__ __forceinline__ unsigned phi2(float a, float b) {
  unsigned t = pack2(a, b); float t0 = lo_f(t), t1 = hi_f(t);
  float e0 = __builtin_amdgcn_fmed3f(__builtin_amdgcn_exp2f(t0 * 1.44269504f), 0.f, 1.f);
  float e1 = __builtin_amdgcn_fmed3f(__builtin_amdgcn_exp2f(t1 * 1.44269504f), 0.f, 1.f);
  unsigned r = pack2(__builtin_fmaxf(t0, 0.f) + e0, __builtin_fmaxf(t1, 0.f) + e1);
  return pack2(lo_f(r) + 1e-6f, hi_f(r) + 1e-6f);
}
#define VMCNT(n) asm volatile("s_waitcnt vmcnt(" #n ")" ::: "memory")
#define RAW_BARRIER() do { asm volatile("s_waitcnt lgkmcnt(0)" ::: "memory"); __builtin_amdgcn_s_barrier(); asm volatile("" ::: "memory"); } while (0)
__device__ __forceinline__ void glds16(const void* g, void* l) { __builtin_amdgcn_global_load_lds((gptr_t)g, (lptr_t)l, 16, 0, 0); }

#define OPQ_S(v) asm volatile("" : "+s"(v))
typedef f32x2 cf;
__device__ __forceinline__ float smul(float a, float b) { float o; asm("v_mul_f32 %0, %1, %2" : "=v"(o) : "v"(a), "v"(b)); return o; }
__device__ __forceinline__ float sfma(float a, float b, float c) { float o; asm("v_fma_f32 %0, %1, %2, %3" : "=v"(o) : "v"(a), "v"(b), "v"(c)); return o; }
__device__ __forceinline__ float sfnma(float a, float b, float c) { float o; asm("v_fma_f32 %0, -%1, %2, %3" : "=v"(o) : "v"(a), "v"(b), "v"(c)); return o; }
__device__ __forceinline__ float ssub(float a, float b) { float o; asm("v_sub_f32 %0, %1, %2" : "=v"(o) : "v"(a), "v"(b)); return o; }
__device__ __forceinline__ cf cmul(cf a, cf b) {
  cf o;
  asm("v_pk_mul_f32 %0, %1, %2 op_sel:[0,0] op_sel_hi:[0,1]\n\t"
      "v_pk_fma_f32 %0, %1, %2, %0 op_sel:[1,1,0] op_sel_hi:[1,0,1] neg_lo:[1,0,0]" : "=&v"(o) : "v"(a), "v"(b));
  return o;
}

#define CPF 3
#define LPF 1
#define PFD 0
#define PFM_ 1
#define NP0 5
#define SPRIO 3
#define NI0 208
#define NB2 3
#define TB0 1
#define TBD 2
#define MIDX ((rowblk & 3) + 4 * (cb & 7))
#define MPS 3
#define PT0 30
#define PT1 110
#define PTD 20
constexpr int K1_LDS = 81920;
#define GLDS 1
constexpr int NB_GEMM = 256, NB_GATE = GLDS ? 0 : 16, NB_KG = 240, NB_KC = 214;
#define KVAT 0
#define KV1 ""
#define KVB 0
#define ROT 0
#define TPQ 0
#define KVST
#define STG 0
#define SA0 8
#define SB0 8
#define SA1 0
#define SB1 8
#define WOT 1
#define KRR 1
#define B6I 5
#define WE 3
#define SGB 1
#define TLBT 1
#define SIGI 2
#define NW0 3
#define K2PF 1
#define K2PFS "1"
#define KVP 1
#define PQA 2
#define PQB 4
#define STR2(x) #x
#define STR(x) STR2(x)
#define PQBS "2*(" STR(PQB) "-" STR(PQA) ")"
#define WTQ 0
#define WTP 0
#define WTO 0
#define WT 1
__device__ __forceinline__ void st16(void* p, u32x4 v) {
  asm volatile("global_store_dwordx4 %0, %1, off sc1\n\ts_nop 2" :: "v"(p), "v"(v) : "memory");
}

#define BAR_ONLY() do { asm volatile("" ::: "memory"); __builtin_amdgcn_s_barrier(); asm volatile("" ::: "memory"); } while (0)
#define SB() __builtin_amdgcn_sched_barrier(0)
template <bool ISQ>
__device__ __forceinline__ void mfma_part(char* smem, char* __restrict__ ws, int wave, f32x4 (&ep)[2][4]) {
  const int lane = threadIdx.x & 63;
  char* sB = smem; char* sA = smem + 32768;
  const int wr = wave & 1, wc = (wave >> 1) & 1;
  const int g = lane >> 4, fr = lane & 15;
  f32x4 acc[4][4];
#pragma unroll
  for (int m = 0; m < 4; m++)
#pragma unroll
    for (int c = 0; c < 4; c++) acc[m][c] = (f32x4){0.f, 0.f, 0.f, 0.f};
  const int q4 = fr >> 2, p4 = lane & 3;
  int aoff[2];
#pragma unroll
  for (int ks = 0; ks < 2; ks++) aoff[ks] = (wr * 64 + fr) * 128 + (((4 * ks + g) ^ (fr & 7)) << 4);
  int boff[4];
  {
    const int f = q4 | ((g & 1) << 2);
#pragma unroll
    for (int c = 0; c < 4; c++) boff[c] = (8 * g + q4) * 256 + (((4 * wc + c) ^ f) << 5) + p4 * 8;
  }
  s16x8 af[2][4]; s16x4 bl[4], bh[4];
  auto rdA = [&](int buf, int m, const char* pa) { af[buf][m] = *(const s16x8*)(pa + m * 2048); };
  auto rdBl = [&](int c, const char* pb) { bl[c] = __builtin_amdgcn_ds_read_tr16_b64_v4i16((lds_s16x4_p)(pb + boff[c])); };
  auto rdBh = [&](int c, const char* pb) { bh[c] = __builtin_amdgcn_ds_read_tr16_b64_v4i16((lds_s16x4_p)(pb + boff[c] + 1024)); };
  BAR_ONLY();
  STAMP(0, 2);
#pragma unroll
  for (int c = 0; c < 4; c++) rdA(0, c, sA + aoff[0]);
#pragma unroll
  for (int c = 0; c < 3; c++) { rdBl(c, sB); rdBh(c, sB); }
  int ao0 = 0, ao1 = 16384;
  auto grp = [&](int buf, int c, int m) {
    const s16x8 bf = __builtin_shufflevector(bl[c], bh[c], 0, 1, 2, 3, 4, 5, 6, 7);
    if (ISQ) acc[m][c] = __builtin_amdgcn_mfma_f32_16x16x32_bf16(bf, af[buf][m], acc[m][c], 0, 0, 0);
    else     acc[m][c] = __builtin_amdgcn_mfma_f32_16x16x32_bf16(af[buf][m], bf, acc[m][c], 0, 0, 0);
  };
#pragma unroll 1
  for (int j2 = 0; j2 < 5; j2++) {
#pragma unroll
    for (int st = 0; st < 4; st++) {
      const int hs = st & 1;
      SB();
      asm volatile("s_waitcnt lgkmcnt(2)" ::: "memory"); __builtin_amdgcn_s_barrier(); asm volatile("" ::: "memory");
      SB();
      const char* pa = sA + (hs == 0 ? ao0 + aoff[1] : ao1 + aoff[0]);
      const char* pc = sB + st * 8192;
      const char* pb = sB + ((st + 1) & 3) * 8192;
      grp(hs, 0, 0); SB(); rdBl(3, pc); rdBh(3, pc); SB();
      grp(hs, 0, 1); SB(); rdA(hs ^ 1, 0, pa); rdA(hs ^ 1, 1, pa); SB();
      grp(hs, 0, 2); SB(); rdA(hs ^ 1, 2, pa); rdA(hs ^ 1, 3, pa); SB();
      grp(hs, 0, 3); SB();
#pragma unroll
      for (int c = 1; c < 4; c++) {
        grp(hs, c, 0); SB(); rdBl(c - 1, pb); rdBh(c - 1, pb); SB();
        grp(hs, c, 1); grp(hs, c, 2); grp(hs, c, 3); SB();
      }
      if (hs == 1) { ao0 = ao1; ao1 = ao1 == 32768 ? 0 : ao1 + 16384; }
    }
  }
  STAMP(0, 3);
  int l3 = threadIdx.x; asm volatile("" : "+v"(l3));
  char* xch = smem + 32768 + (l3 >> 6) * 8192 + (l3 & 63) * 16;
#pragma unroll
  for (int i = 0; i < 8; i++) *(f32x4*)(xch + i * 1024) = acc[2 + (i >> 2)][i & 3];
#pragma unroll
  for (int i = 0; i < 8; i++) ep[i >> 2][i & 3] = acc[i >> 2][i & 3];
  RAW_BARRIER();
}
__device__ __forceinline__ void dma4x(const int (&vo)[4], const char* sbase, int ldsa) {
  asm volatile("s_mov_b32 m0, %5\n\ts_nop 0\n\tglobal_load_lds_dwordx4 %0, %4\n\tglobal_load_lds_dwordx4 %1, %4 offset:1024\n\t"
               "global_load_lds_dwordx4 %2, %4 offset:2048\n\tglobal_load_lds_dwordx4 %3, %4 offset:3072"
               :: "v"(vo[0]), "v"(vo[1]), "v"(vo[2]), "v"(vo[3]), "s"(sbase), "s"(ldsa) : "memory", "m0");
}
__device__ __forceinline__ void gemm_role(char* smem, const u16* __restrict__ x, const u16* __restrict__ W, const u16* __restrict__ wo, char* __restrict__ ws,
                                          int rowblk, int cb) {
  const int tid = threadIdx.x, lane = tid & 63;
  const int wave = __builtin_amdgcn_readfirstlane(tid >> 6);
  const bool ISQ = cb < 5;
  const int row0 = rowblk * 128, col0 = cb * 128;
  const int kvj = cb - 5;
  const int wr = wave & 1, wc = (wave >> 1) & 1, hf = wave >> 2;
  f32x4 ep[2][4];
  unsigned pf3 = 0;
  if (wave >= 4) {
    const int lw = wave & 1;
    const int l0 = (int)(size_t)(lptr_t)smem;
    unsigned pf0 = 0;
    const int pidx = (rowblk & 3) + 4 * (cb & 7);
    auto pfaddr = [&](int stg, int r) {
      const int q = r & 15;
      const int ln = (cb & 8) ? (q < 8 ? 12 + q : 14 + q) : (q < 13 ? q : 7 + q);
      return r < 16 ? (const char*)x + ((rowblk >> 2) * 512 + pidx * 16 + q) * 1280 + stg * 128
                    : (const char*)W + (64 * stg + pidx * 2 + (r >> 4) - 1) * 3840 + ln * 128;
    };
    {
      const int q = (wave - 4) * (NI0 / 4) + min(lane, NI0 / 4 - 1), stg = q / 48;
      asm volatile("global_load_dword %0, %1, off" : "+v"(pf0) : "v"(pfaddr(stg, q - 48 * stg)));
      SB();
    }
    __builtin_amdgcn_s_setprio(SPRIO);
    if (wave < 6) {
      int voA[4], voB[4];
#pragma unroll
      for (int i = 0; i < 4; i++) {
        voA[i] = (64 * lw + 8 * i + (lane >> 3)) * 1280 + (((lane & 7) ^ (lane >> 3)) << 4) - 1024 * i;
        const int fB = (lane >> 4) | ((i >> 1) << 2);
        const int c16 = (((lane & 15) >> 1) ^ fB) * 2 + (lane & 1);
        int bcol = col0 + 8 * c16;
        if (!ISQ) bcol = min(640 + 640 * (c16 >> 3) + ((60 * kvj) & ~7) + 8 * (c16 & 7), 1912);
        voB[i] = (16 * lw + 4 * i + (lane >> 4)) * 3840 + bcol * 2 - 1024 * i;
      }
      const char* pA = (const char*)x + row0 * 1280;
      const char* pB = (const char*)W;
      const int lA = l0 + 32768 + lw * 8192, lB = l0 + lw * 4096;
      auto issA = [&](int slot_off, int h) { dma4x(voA, pA + h * 32 * 1280, lA + slot_off + h * 4096); if (h) pA += 128; };
      auto issB = [&](int slot_off) { dma4x(voB, pB, lB + slot_off); pB += 32 * 3840; };
      TGET(tl0);
      asm volatile("s_waitcnt vmcnt(0)" :: "v"(pf0) : "memory");
      TGET(tl1);
      issA(0, 0); issB(0); issA(0, 1);
      issB(8192); issA(16384, 0);
      VMCNT(8);
      TGET(tl2);
      BAR_ONLY();
      issB(16384); issA(16384, 1);
      VMCNT(12); BAR_ONLY();
      issB(24576); issA(32768, 0); issA(32768, 1);
      VMCNT(12); BAR_ONLY();
      issB(0); issA(0, 0);
      int ao = 0, bo = 8192;
#pragma unroll 1
      for (int j = 1; j < 7; j++) {
        VMCNT(12); BAR_ONLY();
        issB(bo); bo = (bo + 8192) & 32767; issA(ao, 1);
        ao = ao == 32768 ? 0 : ao + 16384;
        VMCNT(12); BAR_ONLY();
        issB(bo); bo = (bo + 8192) & 32767; issA(ao, 0);
      }
      VMCNT(12); BAR_ONLY();
      issB(8192); issA(0, 1);
      VMCNT(12); BAR_ONLY();
      issB(16384);
      VMCNT(8); BAR_ONLY();
      issB(24576);
      {
        const int o = (pidx * 200 + lw * 100 + min(lane, 49)) * 128;
        asm volatile("global_load_dword %0, %1, %2\n\tglobal_load_dword %0, %1, %3" : "+v"(pf3) : "v"(o), "s"(wo), "s"((const char*)wo + 6400));
      }
      VMCNT(6); BAR_ONLY();
      VMCNT(2); BAR_ONLY();
      BAR_ONLY();
      TPUT(0, 4, 256, tl0);
      TPUT(0, 5, 256, tl1); TPUT(0, 6, 256, tl2);
      BAR_ONLY();
    } else {
      u32x4 trv[2][4];
      constexpr int CNT2 = 480 - NI0, NL2 = (CNT2 + 2 * NB2 - 1) / (2 * NB2);
      const char* pp[NB2];
#pragma unroll
      for (int i = 0; i < NB2; i++) {
        const int it = NI0 * 32 + ((pidx & 3) * 8 + (pidx >> 2)) * CNT2 + min((2 * i + lw) * NL2 + min(lane, NL2 - 1), CNT2 - 1);
        const int stg = it / 1536, r = it - 1536 * stg, q = r & 15;
        const int ln = (cb & 8) ? (q < 8 ? 12 + q : 14 + q) : (q < 13 ? q : 7 + q);
        pp[i] = r < 512 ? (const char*)x + ((rowblk >> 2) * 512 + r) * 1280 + stg * 128 : (const char*)W + (64 * stg + (r >> 4) - 32) * 3840 + ln * 128;
      }
      const int tb2 = TB0 + TBD * (pidx & 3);
      int tro;
      {
        const int fr = lane & 15, g = lane >> 4, p4 = lane & 3, r = 8 * g + (fr >> 2);
        tro = l0 + 32768 + r * 128 + (((4 * lw + (p4 >> 1)) ^ (r & 7)) << 4) + (p4 & 1) * 8;
      }
      auto transp = [&](int ao) {
#pragma unroll
        for (int hh = 0; hh < 2; hh++) {
          u32x2 tr[8];
          const int a = (tro ^ (32 * hh)) + ao, a2 = (a ^ 64) + 512;
          asm volatile("ds_read_b64_tr_b16 %0, %8\n\tds_read_b64_tr_b16 %1, %9\n\tds_read_b64_tr_b16 %2, %8 offset:4096\n\tds_read_b64_tr_b16 %3, %9 offset:4096\n\t"
                       "ds_read_b64_tr_b16 %4, %8 offset:8192\n\tds_read_b64_tr_b16 %5, %9 offset:8192\n\tds_read_b64_tr_b16 %6, %8 offset:12288\n\tds_read_b64_tr_b16 %7, %9 offset:12288\n\t"
                       "s_waitcnt lgkmcnt(0)"
                       : "=&v"(tr[0]), "=&v"(tr[1]), "=&v"(tr[2]), "=&v"(tr[3]), "=&v"(tr[4]), "=&v"(tr[5]), "=&v"(tr[6]), "=&v"(tr[7])
                       : "v"(a), "v"(a2) : "memory");
#pragma unroll
          for (int i = 0; i < 4; i++) trv[hh][i] = (u32x4){tr[2 * i][0], tr[2 * i][1], tr[2 * i + 1][0], tr[2 * i + 1][1]};
        }
      };
      unsigned pf2 = 0;
      auto burst = [&](int hs) {
        static_assert(NB2 == 3, "");
        asm volatile("s_cmp_lg_u32 %4, %5\n\ts_cbranch_scc1 1f\n\tglobal_load_dword %0, %1, off\n\tglobal_load_dword %0, %2, off\n\tglobal_load_dword %0, %3, off\n1:"
                     : "+v"(pf2) : "v"(pp[0]), "v"(pp[1]), "v"(pp[2]), "s"(hs), "s"(tb2) : "scc");
      };
      asm volatile("s_waitcnt vmcnt(0)" :: "v"(pf0) : "memory");
      BAR_ONLY();
      int ao = 0;
#pragma unroll 1
      for (int j = 0; j < 10; j++) {
        BAR_ONLY();
        if (!lw) STEPT(2 * j);
        burst(2 * j);
        if (j == cb) transp(ao);
        BAR_ONLY();
        if (!lw) STEPT(2 * j + 1);
        burst(2 * j + 1);
        ao = ao == 32768 ? 0 : ao + 16384;
      }
      asm volatile("s_waitcnt vmcnt(0)" :: "v"(pf2) : "memory");
      if (cb < 10) {
        u16* dst = (u16*)(ws + WS_UT) + (size_t)(64 * cb + 32 * lw + (lane & 15)) * UT_STRIDE + 8 * (lane >> 4);
#pragma unroll
        for (int hh = 0; hh < 2; hh++) {
#pragma unroll
          for (int i = 0; i < 4; i++) st16(dst + hh * 16 * UT_STRIDE + UT_PAD + row0 + 32 * i, trv[hh][i]);
          if (rowblk == 0) st16(dst + hh * 16 * UT_STRIDE, (u32x4){0u, 0u, 0u, 0u});
        }
      }
      BAR_ONLY();
    }
    __builtin_amdgcn_s_setprio(0);
    char* xch = smem + 32768 + (wave & 3) * 8192 + lane * 16;
#pragma unroll
    for (int i = 0; i < 8; i++) ep[i >> 2][i & 3] = *(const f32x4*)(xch + i * 1024);
  } else if (ISQ) {
    mfma_part<true>(smem, ws, wave, ep);
  } else {
    mfma_part<false>(smem, ws, wave, ep);
  }
  int l2 = threadIdx.x; asm volatile("" : "+v"(l2));
  const int g = (l2 >> 4) & 3, fr = l2 & 15;
  if (ISQ) {
#pragma unroll
    for (int c = 0; c < 4; c++) {
      const int n = col0 + 64 * wc + 16 * c + 4 * g;
      const int hd = n / 20, d0 = n - 20 * hd;
      char* dst0 = ws + WS_PQ + (size_t)(((rowblk * 2 + wr) * 32 + hd) * 4 + 2 * hf) * 640 + (d0 < 16 ? 128 + (d0 >> 3) * 256 + fr * 16 + (d0 & 4) * 2 : fr * 8);
#pragma unroll
      for (int m = 0; m < 2; m++) {
        u32x2 o;
        o[0] = phi2(ep[m][c][0], ep[m][c][1]);
        o[1] = phi2(ep[m][c][2], ep[m][c][3]);
        char* dst = dst0 + m * 640;
        *(u32x2*)dst = o;
      }
    }
  } else {
    const int o = (60 * kvj) & 7, zc = o ? 0 : 60;
    const int lane2 = l2 & 63;
    char* fb = smem;
#pragma unroll
    for (int c = 0; c < 4; c++) {
      u32x4 a;
      if (wc == 0) {
        a[0] = phi2(ep[0][c][0], ep[0][c][1]); a[1] = phi2(ep[0][c][2], ep[0][c][3]);
        a[2] = phi2(ep[1][c][0], ep[1][c][1]); a[3] = phi2(ep[1][c][2], ep[1][c][3]);
      } else {
        a[0] = pack2(ep[0][c][0], ep[0][c][1]); a[1] = pack2(ep[0][c][2], ep[0][c][3]);
        a[2] = pack2(ep[1][c][0], ep[1][c][1]); a[3] = pack2(ep[1][c][2], ep[1][c][3]);
        if (16 * c + fr == zc) a = (u32x4){0x3F803F80u, 0x3F803F80u, 0x3F803F80u, 0x3F803F80u};
      }
      *(u32x4*)(fb + ((((wc * 4 + wr * 2 + hf) * 4 + c) * 64 + lane2) << 4)) = a;
    }
    RAW_BARRIER();
    const int np = wave < 4 ? 1 : 2;
    for (int i = 0; i < np; i++) {
      const int p = wave < 4 ? wave : wave * 2 - 4 + i;
      int ta, tb;
      if (p < 10) { ta = (int)((0x3322211100ull >> (4 * p)) & 15); tb = (int)((0x3232121010ull >> (4 * p)) & 15); }
      else { ta = (o ? 2 : 0) + (p - 10); tb = o ? 0 : 3; }
      const char* pk = fb + ((ta * 64 + lane2) << 4);
      const char* pv = fb + 16384 + ((tb * 64 + lane2) << 4);
      f32x4 dd = {0.f, 0.f, 0.f, 0.f};
#pragma unroll
      for (int rg = 0; rg < 4; rg++)
        dd = __builtin_amdgcn_mfma_f32_16x16x32_bf16(*(const s16x8*)(pk + rg * 4096), *(const s16x8*)(pv + rg * 4096), dd, 0, 0, 0);
      const int kc0 = 16 * ta + 4 * g - o;
      const int hk = kc0 / 20, dk0 = kc0 - 20 * hk;
      const int vcol = 16 * tb + fr;
      const int dv = vcol == zc ? 20 : vcol - o - 20 * hk;
      if (kc0 >= 0 && kc0 < (kvj == 10 ? 40 : 60) && dv >= 0 && (dv < 20 || vcol == zc))
        *(f32x4*)(ws + WS_PART + (size_t)(((3 * kvj + hk) * 16 + rowblk) * 21 + dv) * 80 + dk0 * 4) = dd;
    }
  }
  asm volatile("s_waitcnt vmcnt(0)" :: "v"(pf3) : "memory");
}

__device__ __forceinline__ cf zpow(float th, float tl, float rh, float rl, float p) {
  float e = __builtin_amdgcn_exp2f(th * p); e = fmaf(e, (tl * p) * 0.69314718f, e);
  float t = rh * p; float f = (t - __builtin_rintf(t)) + rl * p;
  cf o; o[0] = e * __builtin_amdgcn_cosf(f); o[1] = e * __builtin_amdgcn_sinf(f); return o;
}
#define PKM(o, a, b) "v_pk_mul_f32 " o ", " a ", " b " op_sel:[0,0] op_sel_hi:[0,1]\n\t"
#define PKF(o, a, b) "v_pk_fma_f32 " o ", " a ", " b ", " o " op_sel:[1,1,0] op_sel_hi:[1,0,1] neg_lo:[1,0,0]\n\t"
__device__ __forceinline__ void cmul4(cf& o0, cf& o1, cf& o2, cf& o3, cf a0, cf b0, cf a1, cf b1, cf a2, cf b2, cf a3, cf b3) {
  asm(PKM("%0", "%4", "%5") PKM("%1", "%6", "%7") PKM("%2", "%8", "%9") PKM("%3", "%10", "%11")
      PKF("%0", "%4", "%5") PKF("%1", "%6", "%7") PKF("%2", "%8", "%9") PKF("%3", "%10", "%11")
      : "=&v"(o0), "=&v"(o1), "=&v"(o2), "=&v"(o3)
      : "v"(a0), "v"(b0), "v"(a1), "v"(b1), "v"(a2), "v"(b2), "v"(a3), "v"(b3));
}
__device__ __forceinline__ void batch4(unsigned* h, cf b, cf t0, cf t1, cf t2, cf t3) {
  cf w0, w1, w2, w3;
  asm(PKM("%0", "%4", "%5") PKM("%1", "%4", "%6") PKM("%2", "%4", "%7") PKM("%3", "%4", "%8")
      PKF("%0", "%4", "%5") PKF("%1", "%4", "%6") PKF("%2", "%4", "%7") PKF("%3", "%4", "%8")
      : "=&v"(w0), "=&v"(w1), "=&v"(w2), "=&v"(w3) : "v"(b), "v"(t0), "v"(t1), "v"(t2), "v"(t3));
  h[0] = pack2(w0[0], w0[1]); h[1] = pack2(w1[0], w1[1]); h[2] = pack2(w2[0], w2[1]); h[3] = pack2(w3[0], w3[1]);
}
__device__ __forceinline__ void batch3(unsigned* h, cf b, cf t1, cf t2, cf t3) {
  cf w1, w2, w3;
  asm(PKM("%0", "%3", "%4") PKM("%1", "%3", "%5") PKM("%2", "%3", "%6")
      PKF("%0", "%3", "%4") PKF("%1", "%3", "%5") PKF("%2", "%3", "%6")
      : "=&v"(w1), "=&v"(w2), "=&v"(w3) : "v"(b), "v"(t1), "v"(t2), "v"(t3));
  h[0] = pack2(b[0], b[1]); h[1] = pack2(w1[0], w1[1]); h[2] = pack2(w2[0], w2[1]); h[3] = pack2(w3[0], w3[1]);
}
constexpr int KG_RS = 288, KG_PL = 16 * KG_RS;
__device__ __forceinline__ void kgen_role(char* smem, const u16* __restrict__ logA, const u16* __restrict__ Aim,
                                          const u16* __restrict__ Cr, const u16* __restrict__ Ci, const u16* __restrict__ logdt,
                                          const u16* __restrict__ x, const u16* __restrict__ W, const u16* __restrict__ wo,
                                          const u16* __restrict__ gw, char* __restrict__ ws, int blk, unsigned pfv) {
  const int tid = threadIdx.x, lane = tid & 63;
  const int wave = __builtin_amdgcn_readfirstlane(tid >> 6);
  constexpr int RS = KG_RS, PL = KG_PL;
  char* tb = smem + wave * (2 * PL);
  char* wp = tb + lane * 4;
  const char* rp = tb + (lane & 15) * RS + (lane >> 4) * 16;
  const int kq = blk >> 3, tbase = (blk & 7) * 240;
  const int nrep = kq >= 27 ? 0 : (wave == 7 ? 2 : 1);
  unsigned pa[5], pb[5] = {0u, 0u, 0u, 0u, 0u};
  {
    const int task = tbase + min(kq * 9 + wave, 239), pi = task * 64 + lane;
    pa[0] = logdt[task / 3]; pa[1] = logA[pi]; pa[2] = Aim[pi]; pa[3] = Cr[pi]; pa[4] = Ci[pi];
    if (wave == 7) {
      const int t2 = tbase + min(kq * 9 + 8, 239), p2 = t2 * 64 + lane;
      pb[0] = logdt[t2 / 3]; pb[1] = logA[p2]; pb[2] = Aim[p2]; pb[3] = Cr[p2]; pb[4] = Ci[p2];
    }
    asm volatile("" : "+v"(pb[0]), "+v"(pb[1]), "+v"(pb[2]), "+v"(pb[3]), "+v"(pb[4]));
  }
#pragma unroll 1
  for (int rep = 0; rep < nrep; rep++) {
    const float dt = rb(expf(lo_f(pa[0])));
    float ar;
    {
      const float la = lo_f(pa[1]);
      float h = la * 1.44269502f; float l = fmaf(la, 1.44269502f, -h) + la * 1.92596299e-8f;
      float e = __builtin_amdgcn_exp2f(h); e = fmaf(e, l * 0.69314718f, e);
      ar = -rb(e) * dt;
    }
    const float ai = lo_f(pa[2]) * dt;
    cf C; C[0] = 2.f * (lo_f(pa[3]) * dt); C[1] = 2.f * (lo_f(pa[4]) * dt);
#pragma unroll
    for (int i = 0; i < 5; i++) pa[i] = pb[i];
    const float th = ar * 1.44269502f, tl = fmaf(ar, 1.44269502f, -th) + ar * 1.92596299e-8f;
    const float rh = ai * 0.159154937f, rl = fmaf(ai, 0.159154937f, -rh) + ai * 6.42063833e-9f;
    const cf one = {1.f, 0.f};
    cf S1 = zpow(th, tl, rh, rl, 1.f), Q1 = zpow(th, tl, rh, rl, 8.f), R1 = zpow(th, tl, rh, rl, 128.f);
    Q1[1] = -Q1[1];
    cf S2, Q2, R2, G1, S3, S4, Q3, P1, R3, T1, G2, G3, S5, S6, S7, P2, CT1, T2, P3, CS4, CT2, CT3, CT4, dm0;
    cmul4(S2, Q2, R2, G1, S1, S1, Q1, Q1, R1, R1, C, R1);
    cmul4(S3, S4, Q3, P1, S2, S1, S2, S2, Q2, Q1, Q2, Q2);
    cmul4(R3, T1, G2, P2, R2, R1, R2, R2, C, R2, P1, P1);
    cmul4(S5, S6, S7, P3, S4, S1, S4, S2, S4, S3, P2, P1);
    cmul4(G3, CT1, T2, dm0, C, R3, C, T1, T1, T1, C, R1);
    cmul4(CS4, CT2, CT3, dm0, CT1, S4, C, T2, CT1, T2, C, R1);
    CT4 = cmul(CT2, T2);
    auto rows4 = [&](int pl, int row, int rs, cf b, cf t0, cf t1, cf t2, cf t3) {
      unsigned h[4];
      batch4(h, b, t0, t1, t2, t3);
#pragma unroll
      for (int k = 0; k < 4; k++) *(unsigned*)(wp + pl * PL + (row + k * rs) * RS) = h[k];
    };
    auto rows3 = [&](int pl, int row, int rs, cf b, cf t1, cf t2, cf t3) {
      unsigned h[4];
      batch3(h, b, t1, t2, t3);
#pragma unroll
      for (int k = 0; k < 4; k++) *(unsigned*)(wp + pl * PL + (row + k * rs) * RS) = h[k];
    };
    *(unsigned*)(wp) = 0x00003F80u;
    *(unsigned*)(wp + RS) = pack2(Q1[0], Q1[1]); *(unsigned*)(wp + 2 * RS) = pack2(Q2[0], Q2[1]); *(unsigned*)(wp + 3 * RS) = pack2(Q3[0], Q3[1]);
    rows3(0, 4, 1, P1, Q1, Q2, Q3); rows3(0, 8, 1, P2, Q1, Q2, Q3); rows3(0, 12, 1, P3, Q1, Q2, Q3);
    rows3(1, 0, 1, C, S1, S2, S3); rows4(1, 4, 1, C, S4, S5, S6, S7); rows3(1, 8, 1, G1, S1, S2, S3); rows4(1, 12, 1, G1, S4, S5, S6, S7);
    __builtin_amdgcn_wave_barrier();
    s16x8 bh[4];
    f32x4 res[3];
#pragma unroll
    for (int k = 0; k < 4; k++) bh[k] = *(const s16x8*)(rp + k * 64);
    auto mm = [&](int t, int pl) {
      f32x4 a = {0.f, 0.f, 0.f, 0.f};
#pragma unroll
      for (int k = 0; k < 4; k++) a = __builtin_amdgcn_mfma_f32_16x16x32_bf16(*(const s16x8*)(rp + pl * PL + k * 64), bh[k], a, 0, 0, 0);
      res[t] = a;
    };
    mm(0, 1);
    __builtin_amdgcn_wave_barrier();
    rows3(0, 0, 1, G2, S2, S4, S6); rows3(0, 4, 1, G3, S2, S4, S6); rows3(0, 8, 2, CT1, R1, R2, R3); rows3(0, 9, 2, CS4, R1, R2, R3);
    rows3(1, 0, 1, CT2, R1, R2, R3); rows3(1, 4, 1, CT3, R1, R2, R3);
    *(unsigned*)(wp + PL + 8 * RS) = pack2(CT4[0], CT4[1]);
    __builtin_amdgcn_wave_barrier();
    mm(1, 0); mm(2, 1);
    __builtin_amdgcn_wave_barrier();
    char* rs = (wave == 7 && rep == 0) ? smem + 8 * (2 * PL) : tb;
#pragma unroll
    for (int t = 0; t < 3; t++) *(f32x4*)(rs + t * 1024 + lane * 16) = res[t];
    __builtin_amdgcn_wave_barrier();
  }
  STAMP(0, 4);
  if (blk == 239) ((unsigned*)(ws + WS_CNT))[tid] = 0u, ((unsigned*)(ws + WS_CNT))[tid + 512] = 0u;
  if (blk >= 216 && blk < 220) st16(ws + WS_KVT + (size_t)((blk - 216) * 512 + tid) * 16, (u32x4){~0u, ~0u, ~0u, ~0u});
  if (kq < 27) {
    int tix[8];
#pragma unroll
    for (int e = 0; e < 8; e++) {
      const int j = (tid & 255) * 8 + 7 - e;
      const int p = (int)rb((float)j);
      const int hh = p >> 7, c = (p >> 3) & 15;
      int sh = 31 - __builtin_clz(hh | 1); sh = sh > 3 ? 3 : sh;
      const int row = 8 * sh + (((hh << 3) | (p & 7)) >> sh);
      tix[e] = 4096 + (tid >> 8) * (2 * PL) + (((row >> 2) * 16 + c) * 4 + (row & 3)) * 4;
      asm volatile("" : "+v"(tix[e]));
    }
    __syncthreads();
#pragma unroll
    for (int it = 0; it < 2; it++) {
      const int e = tid + 512 * it;
      const int seg = e / 192, ent = e - 192 * seg;
      if (seg < 3) {
        const char* p0 = smem + 3 * seg * (2 * PL) + ent * 16;
        const f32x4 a = *(const f32x4*)p0 + *(const f32x4*)(p0 + (seg == 2 ? 2 : 1) * (2 * PL)) + *(const f32x4*)(p0 + (seg == 2 ? 1 : 2) * (2 * PL));
        *(f32x4*)(smem + 4096 + seg * (2 * PL) + ent * 16) = a;
      }
    }
    __syncthreads();
    const int d0 = 80 * (blk & 7) + 3 * kq + (tid >> 8);
    u32x4 hi;
#pragma unroll
    for (int e = 0; e < 4; e++) hi[e] = pack2(*(const float*)(smem + tix[2 * e]), *(const float*)(smem + tix[2 * e + 1]));
    st16(ws + WS_KIMG + (size_t)d0 * 4096 + (tid & 255) * 16, hi);
    if (wave < 4 && kq < 26) {
#pragma unroll
      for (int e = 0; e < 4; e++) hi[e] = pack2(*(const float*)(smem + tix[2 * e] + 4 * PL), *(const float*)(smem + tix[2 * e + 1] + 4 * PL));
      st16(ws + WS_KIMG + (size_t)(d0 + 2) * 4096 + tid * 16, hi);
    }
    VMCNT(0);
    if (lane < 16) {
      const int o = (int)WS_KIMG + (d0 + ((lane & 8) && wave < 4 && kq < 26 ? 2 : 0)) * 4096 + (wave & 3) * 1024 + (lane & 7) * 128;
      asm volatile("global_load_dword %0, %1, %2" : "+v"(pfv) : "v"(o), "s"(ws));
    }
  }
  asm volatile("s_waitcnt vmcnt(0)" :: "v"(pfv) : "memory");
  const int gt = wave * 30 + (blk >> 3);
  if (gt < 64) {
    const int row = (blk & 7) * 256 + gt * 4 + (lane >> 4);
    const u32x4* px = (const u32x4*)(x + (size_t)row * D) + (lane & 15); const u32x4* pg = (const u32x4*)gw + (lane & 15);
    u32x4 xv[5], gv[5];
#pragma unroll
    for (int i = 0; i < 5; i++) { xv[i] = px[16 * i]; gv[i] = pg[16 * i]; }
    float gs = 0.f;
#pragma unroll
    for (int i = 0; i < 5; i++)
#pragma unroll
      for (int e = 0; e < 4; e++) {
        unsigned p = pack2(lo_f(xv[i][e]) * lo_f(gv[i][e]), hi_f(xv[i][e]) * hi_f(gv[i][e]));
        gs = __builtin_amdgcn_fdot2_f32_bf16(__builtin_bit_cast(bf16x2_t, p), __builtin_bit_cast(bf16x2_t, 0x3F803F80u), gs, false);
      }
    gs += __shfl_xor(gs, 1); gs += __shfl_xor(gs, 2); gs += __shfl_xor(gs, 4); gs += __shfl_xor(gs, 8);
    if ((lane & 15) == 0) { float a = rb(gs); ((float*)(ws + WS_GATE))[row] = rb(1.f / (1.f + __expf(-a))); }
  }
}

__global__ __launch_bounds__(512, 4) void k1(const u16* __restrict__ x, const u16* __restrict__ logA, const u16* __restrict__ Aim,
                                          const u16* __restrict__ Cr, const u16* __restrict__ Ci, const u16* __restrict__ logdt,
                                          const u16* __restrict__ wqkv, const u16* __restrict__ gw, const u16* __restrict__ wo,
                                          const u16* __restrict__ Dsk, const u16* __restrict__ outp, char* __restrict__ ws) {
  __shared__ __attribute__((aligned(16))) char smem[K1_LDS];
  int b = blockIdx.x;
  unsigned cpf = 0;
  asm volatile("" :: "s"(x), "s"(logA), "s"(Aim), "s"(Cr), "s"(Ci), "s"(logdt), "s"(wqkv), "s"(gw), "s"(wo), "s"(ws), "s"(Dsk), "s"(outp));
  if (b < NB_KG && threadIdx.x >= 501) {
    const char* pc; int len;
    asm volatile("s_getpc_b64 %0\n\ts_mov_b32 %1, code_tail_mark-_Z2k1PKtS0_S0_S0_S0_S0_S0_S0_S0_S0_S0_Pc-128" : "=s"(pc), "=s"(len));
    const int off = min((int)((threadIdx.x - 501) * 30 + (b >> 3)) * 128, len);
    asm volatile("global_load_dword %0, %1, %2" : "+v"(cpf) : "v"(off), "s"(pc));
  }
  if (b < 8 && threadIdx.x >= 490 && threadIdx.x < 500) asm volatile("global_load_dword %0, %1, %2" : "+v"(cpf) : "v"((threadIdx.x - 490) * 128), "s"(gw));
  if (b >= 224 && b < 232) asm volatile("global_load_dword %0, %1, %2" : "+v"(cpf) : "v"(threadIdx.x * 4096), "s"(outp));
  if (b >= 232 && b < 240) {
    if (threadIdx.x < 128) asm volatile("global_load_dword %0, %1, %2" : "+v"(cpf) : "v"((512 + threadIdx.x) * 4096), "s"(outp));
    else if (threadIdx.x < 192) asm volatile("global_load_dword %0, %1, %2" : "+v"(cpf) : "v"(0), "s"(Dsk));
    else if (threadIdx.x < 192 + 162) asm volatile("global_load_dword %0, %1, %2 sc1" : "+v"(cpf) : "v"(min((int)WS_KVT + 31 * 1024 + (int)(threadIdx.x - 192) * 16384, (int)WS_END - 128)), "s"(ws));
  }
  STAMP(0, 0);
  b = b < NB_KG ? b + NB_GEMM : (b < NB_KG + NB_GEMM ? b - NB_KG : b);
  if (b < NB_GEMM) {
    const int xcd = b & 7, idx = b >> 3;
    const int rowblk = (xcd >> 1) * 4 + (idx & 3), cb = (xcd & 1) * 8 + (idx >> 2);
    gemm_role(smem, x, wqkv, wo, ws, rowblk, cb);
    STAMP(0, 1);
    return;
  }
  b -= NB_GEMM;
  if (b < NB_KG) { kgen_role(smem, logA, Aim, Cr, Ci, logdt, x, wqkv, wo, gw, ws, b, cpf); STAMP(0, 1); return; }
}

constexpr int K2_LDS = 163840;
constexpr int K2_CS = 4160;
constexpr int K2_KOFF = 4 * K2_CS;
constexpr int K2_CH = K2_KOFF + 4608;
constexpr int K2_CONV = K2_LDS - 3 * K2_CH;
constexpr int K2_TAB = 81920;
constexpr int K2_FIN = K2_TAB + 9216;
constexpr int K2_KV = 102400;
constexpr int K2_STG = 131072;
constexpr int NB_KV = 32;
typedef __attribute__((address_space(1))) unsigned gu32;
__device__ __forceinline__ const char* uni(const char* p) {
  size_t v = (size_t)p;
  unsigned lo = __builtin_amdgcn_readfirstlane((unsigned)v), hi = __builtin_amdgcn_readfirstlane((unsigned)(v >> 32));
  size_t o = ((size_t)hi << 32) | lo;
  asm volatile("s_nop 4" : "+s"(o));
  return (const char*)o;
}
__device__ __forceinline__ void wait_count(char* p, unsigned target) {
  gu32* c = (gu32*)(size_t)p;
  for (int n = 0; n < (1 << 22); n++) {
    if (__hip_atomic_load(c, __ATOMIC_RELAXED, __HIP_MEMORY_SCOPE_AGENT) >= target) break;
    __builtin_amdgcn_s_sleep(8);
  }
}
#define GLD4(dst, voff, sbase, pol) asm volatile("global_load_dwordx4 %0, %1, %2" pol : "=v"(dst) : "v"(voff), "s"(sbase))
#define GLD2(dst, voff, sbase, pol) asm volatile("global_load_dwordx2 %0, %1, %2" pol : "=v"(dst) : "v"(voff), "s"(sbase))
#define GLD1(dst, voff, sbase, pol) asm volatile("global_load_dword %0, %1, %2" pol : "=v"(dst) : "v"(voff), "s"(sbase))
template <int G>
__device__ __forceinline__ void conv_unit(f32x4* acc, int ap, int bp) {
  constexpr int NT = 8 - G;
  u32x2 a0[NT], a1[NT]; s16x8 bh[NT];
#pragma unroll
  for (int j = 0; j < NT; j++)
    asm volatile("ds_read_b64 %0, %3 offset:%5\n\tds_read_b64 %1, %3 offset:%6\n\tds_read_b128 %2, %4 offset:%5"
                 : "=&v"(a0[j]), "=&v"(a1[j]), "=&v"(bh[j]) : "v"(ap), "v"(bp), "n"(512 * j), "n"(512 * j + 8) : "memory");
#pragma unroll
  for (int x = 0; x < NT; x++) {
    asm volatile("s_waitcnt lgkmcnt(%3)" : "+v"(a0[x]), "+v"(a1[x]), "+v"(bh[x]) : "n"(3 * (NT - 1 - x) > 15 ? 15 : 3 * (NT - 1 - x)) : "memory");
#pragma unroll
    for (int y = 0; y <= x; y++)
#pragma unroll
      for (int sw = 0; sw < 2; sw++) {
        if (sw && y == x) continue;
        const int j = sw ? y : x, m = sw ? x : y, T = j + m;
        const int ti = G == 0 ? (T == 0 ? 0 : T == 3 ? 1 : T == 4 ? 2 : T == 7 ? 3 : -1) : (T == 1 ? 0 : T == 2 ? 1 : T == 5 ? 2 : T == 6 ? 3 : -1);
        if (ti >= 0) acc[ti] = __builtin_amdgcn_mfma_f32_16x16x32_bf16(__builtin_bit_cast(s16x8, (u32x4){a0[j][0], a0[j][1], a1[j][0], a1[j][1]}), bh[m], acc[ti], 0, 0, 0);
      }
    SB();
  }
}

__global__ __launch_bounds__(576) void k2(const u16* __restrict__ Dskip, const u16* __restrict__ wo, char* __restrict__ ws, u16* __restrict__ out) {
  __shared__ __attribute__((aligned(16))) char smem[K2_LDS];
  const int tid = threadIdx.x, lane = tid & 63;
  const int wave = __builtin_amdgcn_readfirstlane(tid >> 6);
  const int b = blockIdx.x;
  unsigned cpf = 0;
  if (b < 32 && tid < 192) {
    const char* pc; int len;
    asm volatile("s_getpc_b64 %0\n\ts_mov_b32 %1, code_tail_mark-_Z2k2PKtS0_PcPt-128" : "=s"(pc), "=s"(len));
    const int off = min(tid * 128, len);
    asm volatile("global_load_dword %0, %1, %2" : "+v"(cpf) : "v"(off), "s"(pc));
  }
  STAMP(1, 0);
  const int lds0 = (int)(size_t)(lptr_t)smem;
  const int l16 = lane * 16;
  const int xcd = b & 7, idx = b >> 3;
  const int odd = idx & 1;
  const int chA = 80 * xcd + 5 * (idx >> 1) + 3 * odd, chH = 80 * xcd + 5 * (idx >> 1) + 2;
  const int rowblk = xcd * 4 + (idx >> 3), cb = idx & 7;
  const int l0 = rowblk * 64, c0 = cb * 80;
  const int r = lane & 31, h = lane >> 5;
  if (wave == 8) {
    __builtin_amdgcn_s_setprio(3);
    const char* wsrc = (const char*)wo + cb * 160;
    int wv[5];
#pragma unroll
    for (int j = 0; j < 5; j++) { const int f = 64 * j + lane, row = f / 10; wv[j] = row * 1280 + (f - 10 * row) * 16 - (j & 3) * 1024; }
    auto dma5 = [&](int grp) {
      asm volatile("s_mov_b32 m0, %6\n\ts_nop 0\n\tglobal_load_lds_dwordx4 %0, %5\n\tglobal_load_lds_dwordx4 %1, %5 offset:1024\n\t"
                   "global_load_lds_dwordx4 %2, %5 offset:2048\n\tglobal_load_lds_dwordx4 %3, %5 offset:3072\n\t"
                   "s_add_u32 m0, m0, 0x1000\n\ts_nop 0\n\tglobal_load_lds_dwordx4 %4, %5"
                   :: "v"(wv[0]), "v"(wv[1]), "v"(wv[2]), "v"(wv[3]), "v"(wv[4]), "s"(wsrc + grp * 40960), "s"(lds0 + grp * 5120) : "memory", "m0", "scc");
    };
    auto dma4 = [&](const char* src, int dst) {
      asm volatile("s_mov_b32 m0, %2\n\ts_nop 0\n\tglobal_load_lds_dwordx4 %0, %1\n\tglobal_load_lds_dwordx4 %0, %1 offset:1024\n\t"
                   "global_load_lds_dwordx4 %0, %1 offset:2048\n\tglobal_load_lds_dwordx4 %0, %1 offset:3072"
                   :: "v"(l16), "s"(src), "s"(lds0 + dst) : "memory", "m0");
    };
    auto kimg = [&]() {
      dma4(ws + WS_KIMG + chA * 4096, K2_CONV + K2_KOFF + 512);
      dma4(ws + WS_KIMG + chA * 4096 + 4096, K2_CONV + K2_CH + K2_KOFF + 512);
      dma4(ws + WS_KIMG + chH * 4096, K2_CONV + 2 * K2_CH + K2_KOFF + 512);
    };
    if (b < NB_KV) {
      f32x4 kvp[32];
      const char* src = ws + WS_PART + (size_t)b * 16 * 1680;
      const int o2 = min(lane + 64, 104) * 16;
#pragma unroll
      for (int i = 0; i < 16; i++) { GLD4(kvp[i], l16, src + i * 1680, ""); GLD4(kvp[16 + i], o2, src + i * 1680, ""); }
      kimg();
#pragma unroll
      for (int i = 0; i < NW0; i++) dma5(i);
      asm volatile("s_waitcnt vmcnt(%0)" :: "n"(5 * NW0) : "memory");
      BAR_ONLY();
#pragma unroll
      for (int i = 0; i < 32; i += 8)
        asm volatile("" : "+v"(kvp[i]), "+v"(kvp[i + 1]), "+v"(kvp[i + 2]), "+v"(kvp[i + 3]), "+v"(kvp[i + 4]), "+v"(kvp[i + 5]), "+v"(kvp[i + 6]), "+v"(kvp[i + 7]));
#pragma unroll
      for (int st = 1; st < 16; st <<= 1)
#pragma unroll
        for (int i = 0; i < 32; i += 2 * st) kvp[i] += kvp[i + st];
      const u32x2 o0 = {pack2(kvp[0][0], kvp[0][1]), pack2(kvp[0][2], kvp[0][3])}, o1 = {lane < 41 ? pack2(kvp[16][0], kvp[16][1]) : 0u, lane < 41 ? pack2(kvp[16][2], kvp[16][3]) : 0u};
      const int o = (int)WS_KVT + b * 896 + lane * 8;
      asm volatile("global_store_dwordx2 %0, %1, %2 sc1" :: "v"(o), "v"(o0), "s"(ws) : "memory");
      if (lane < 48) asm volatile("global_store_dwordx2 %0, %1, %2 offset:512 sc1\n\ts_nop 1" :: "v"(o), "v"(o1), "s"(ws) : "memory");
      KVST;
    } else {
      kimg();
#pragma unroll
      for (int i = 0; i < NW0; i++) dma5(i);
      asm volatile("s_waitcnt vmcnt(%0)" :: "n"(5 * NW0) : "memory");
      BAR_ONLY();
    }
#pragma unroll 1
    for (int p = NW0; p < 19; p++) {
      dma5(p);
    }
    {
      u32x4 kt[28];
      const char* kp = ws + WS_KVT;
#define KVL(pol) _Pragma("unroll") for (int i = 0; i < 28; i++) GLD4(kt[i], l16, kp + i * 1024, pol);
#define KVW() _Pragma("unroll") for (int i = 0; i < 28; i += 7) asm volatile("" : "+v"(kt[i]), "+v"(kt[i + 1]), "+v"(kt[i + 2]), "+v"(kt[i + 3]), "+v"(kt[i + 4]), "+v"(kt[i + 5]), "+v"(kt[i + 6]));
      KVL(KV1);
      VMCNT(0);
      KVW();
      for (int n = 0; n < (1 << 14); n++) {
        unsigned m = 0;
#pragma unroll
        for (int i = 0; i < 28; i++) m = max(max(max(m, kt[i][0]), kt[i][1]), max(kt[i][2], kt[i][3]));
        if (__builtin_amdgcn_ballot_w64(m == ~0u) == 0ull) break;
        KVL(" sc1");
        VMCNT(0);
        KVW();
      }
      BAR_ONLY();
#pragma unroll
      for (int i = 0; i < 14; i++) *(u32x4*)(smem + K2_KV + i * 1024 + l16) = kt[i];
      BAR_ONLY();
#pragma unroll
      for (int i = 14; i < 28; i++) *(u32x4*)(smem + K2_KV + i * 1024 + l16) = kt[i];
    }
    asm volatile("s_waitcnt lgkmcnt(0)" ::: "memory");
    BAR_ONLY();
    dma5(19);
    VMCNT(0);
    BAR_ONLY();
    {
      gu32* c = (gu32*)(size_t)(ws + WS_CNT + (8 + cb) * 128);
      for (int n = 0; n < (1 << 22); n++) {
        if (__hip_atomic_load(c, __ATOMIC_RELAXED, __HIP_MEMORY_SCOPE_AGENT) >= 32u) break;
        __builtin_amdgcn_s_sleep(1);
      }
      asm volatile("" ::: "memory");
      const int o = (lane >> 3) * (L * 2) + (((lane & 7) ^ ((lane >> 2) & 4)) << 4);
      const char* src = ws + WS_AT + (size_t)(c0 * L + l0) * 2;
#pragma unroll
      for (int i = 0; i < 10; i++)
        asm volatile("s_mov_b32 m0, %2\n\ts_nop 0\n\tglobal_load_lds_dwordx4 %0, %1 sc1" :: "v"(o), "s"(src + i * 8 * L * 2), "s"(lds0 + (i < 8 ? (i >> 1) * 3200 : 25600) + (i & 1) * 1024) : "memory", "m0");
      VMCNT(0);
    }
    BAR_ONLY();
    return;
  }
  const int t = wave & 1, kh = wave >> 1;
  u32x4 in0[2];
  int dsto[2];
#pragma unroll
  for (int rr = 0; rr < 2; rr++) {
    const int f = (wave + 8 * rr) * 49 + min(lane, 49), i = min(f, 776);
    const int ch = i / 259, xx = i - 259 * ch;
    dsto[rr] = (lane < 49 && f < 777) ? K2_CONV + ch * K2_CH + xx * 16 : -1;
    GLD4(in0[rr], (int)WS_UT + (ch == 2 ? chH : chA + ch) * (UT_STRIDE * 2) + (xx + 1) * 16, ws, "");
  }
  const int jg = wave & 1, jc = (wave >> 1) & 1, hiw = wave >> 2;
  const int fo = (lane & 15) * 16 + (lane >> 4) * 4;
  int ft[3];
  ft[0] = hiw ? 3 - jg : jg;
  ft[1] = hiw ? 4 + jg : (wave & 2) * 2 + ((wave & 1) ? 3 - odd : odd);
  ft[2] = 7 - jg;
  u32x2 us[3];
  float g_r;
  GLD1(g_r, (int)WS_GATE + (l0 + 32 * t + 16 * (kh & 1) + (lane & 15)) * 4, ws, "");
  unsigned dsA, dsH;
  asm volatile("global_load_ushort %0, %1, %2" : "=v"(dsA) : "v"((chA + jc) * 2), "s"(Dskip));
  asm volatile("global_load_ushort %0, %1, %2" : "=v"(dsH) : "v"((hiw ? chA + jc : chH) * 2), "s"(Dskip));
  u32x4 pf[8][2];
  const char* pqp = ws + WS_PQ + (size_t)((rowblk * 32 + kh * 8) * 4 + 2 * t) * 640;
  const int pqo = lane < 32 ? 128 + l16 : (lane & 15) * 8;
#define PQL(i0, i1) _Pragma("unroll") for (int i = i0; i < i1; i++) { GLD4(pf[i][0], pqo, pqp + i * 2560, ""); GLD4(pf[i][1], pqo, pqp + i * 2560, " offset:640"); }
  if (tid < 96) *(u32x4*)(smem + K2_CONV + (tid >> 5) * K2_CH + K2_KOFF + (tid & 31) * 16) = (u32x4){0u, 0u, 0u, 0u};
  asm volatile("s_waitcnt vmcnt(3)" : "+v"(in0[0]), "+v"(in0[1]), "+v"(cpf) :: "memory");
  STAMP(1, 2);
#pragma unroll
  for (int rr = 0; rr < 2; rr++) {
    const u32x4 a = in0[rr];
    unsigned w[6] = {a[0], a[1], a[2], a[3], (unsigned)__builtin_amdgcn_update_dpp(0, (int)a[0], 0x130, 0xf, 0xf, false),
                     (unsigned)__builtin_amdgcn_update_dpp(0, (int)a[1], 0x130, 0xf, 0xf, false)};
    unsigned o[5];
#pragma unroll
    for (int k = 0; k < 5; k++) o[k] = __builtin_amdgcn_alignbyte(w[k + 1], w[k], 2);
    if (dsto[rr] >= 0) {
      char* p = smem + dsto[rr];
#pragma unroll
      for (int q = 0; q < 2; q++) {
        *(u32x4*)(p + (2 * q) * K2_CS) = (u32x4){o[q], o[q + 1], o[q + 2], o[q + 3]};
        *(u32x4*)(p + (2 * q + 1) * K2_CS) = (u32x4){w[q + 1], w[q + 2], w[q + 3], w[q + 4]};
      }
    }
  }
  RAW_BARRIER();
  PQL(0, PQA);
  STAMP(1, 3);
  f32x4 ca[4], sv[4];
#pragma unroll
  for (int i = 0; i < 4; i++) { ca[i] = (f32x4){0.f, 0.f, 0.f, 0.f}; sv[i] = (f32x4){0.f, 0.f, 0.f, 0.f}; }
  {
    const int i = lane & 15, kg = lane >> 4, e = kg >> 1, bb = kg & 1;
    const int ap = lds0 + K2_CONV + (i & 3) * K2_CS + (2 * e - bb + 2) * 16 + (i >> 2) * 8;
    const int bp = lds0 + K2_CONV + K2_KOFF + (16 + i - e) * 32 + bb * 16;
    int apj = ap + jc * K2_CH + hiw * 192;
    int bpj = bp + jc * K2_CH - hiw * 192;
    if (jg == 0) {
#pragma unroll 1
      for (int it = 0; it < 3; it++) conv_unit<0>(ca, apj + it * 64, bpj - it * 64);
    } else {
#pragma unroll 1
      for (int it = 0; it < 3; it++) conv_unit<1>(ca, apj + it * 64, bpj - it * 64);
    }
    PQL(PQA, PQB);
    apj += 192; bpj -= 192;
    int g2 = jg;
    if (!hiw) {
#pragma unroll
      for (int q = 0; q < 4; q++) { sv[q] = ca[q]; ca[q] = (f32x4){0.f, 0.f, 0.f, 0.f}; }
      apj = ap + 2 * K2_CH + wave * 128; bpj = bp + 2 * K2_CH - wave * 128; g2 = odd;
    }
    if (g2 == 0) {
#pragma unroll 1
      for (int it = 0; it < 2; it++) conv_unit<0>(ca, apj + it * 64, bpj - it * 64);
    } else {
#pragma unroll 1
      for (int it = 0; it < 2; it++) conv_unit<1>(ca, apj + it * 64, bpj - it * 64);
    }
  }
  STAMP(1, 4);
  {
    const char* ua = smem + K2_CONV + 3 * K2_CS + (20 + fo) * 2;
    us[0] = *(const u32x2*)(ua + jc * K2_CH + ft[0] * 512); us[1] = *(const u32x2*)(ua + (hiw ? jc : 2) * K2_CH + ft[1] * 512);
    us[2] = *(const u32x2*)(ua + jc * K2_CH + ft[2] * 512);
  }
  PQL(PQB, 8);
  RAW_BARRIER();
  {
    if (tid == 0) *(unsigned*)(smem + K2_LDS - 16) = 0u;
    char* sp = smem + K2_STG + l16;
    if (hiw) {
      *(f32x4*)(sp + (wave & 3) * 4096) = ca[0];
    } else {
#pragma unroll
      for (int i = 1; i < 4; i++) *(f32x4*)(sp + wave * 4096 + i * 1024) = sv[i];
#pragma unroll
      for (int i = 0; i < 4; i++) if (i != wave) *(f32x4*)(sp + 16384 + (i * 3 + ((wave - i - 1) & 3)) * 1024) = ca[i];
    }
  }
  RAW_BARRIER();
  {
    const char* sp = smem + K2_STG + l16;
    asm volatile("s_waitcnt vmcnt(16)" : "+v"(g_r), "+v"(dsA), "+v"(dsH) :: "memory");
    const float dskA = lo_f(dsA), dskH = lo_f(dsH);
    auto fin = [&](f32x4 s, u32x2 uu, float dsk, int ch, int tile) {
      u32x2 ov;
#pragma unroll
      for (int e = 0; e < 2; e++) {
        unsigned yy = pack2(s[2 * e], s[2 * e + 1]);
        unsigned k = pack2(lo_f(uu[e]) * dsk, hi_f(uu[e]) * dsk);
        ov[e] = pack2(lo_f(yy) + lo_f(k), hi_f(yy) + hi_f(k));
      }
      const int o = (int)WS_AT + (ch * L + tile * 256 + fo) * 2;
      asm volatile("global_store_dwordx2 %0, %1, %2 sc1\n\ts_nop 1" :: "v"(o), "v"(ov), "s"(ws) : "memory");
    };
    if (hiw) {
      const char* pp = sp + (wave & 3) * 4096;
#pragma unroll
      for (int i = 1; i < 4; i++) fin(ca[i] + *(const f32x4*)(pp + i * 1024), us[i - 1], dskA, chA + jc, ft[i - 1]);
      VMCNT(3);
    } else {
      const char* hp = sp + 16384 + wave * 3072;
      const f32x4 own = wave == 0 ? ca[0] : wave == 1 ? ca[1] : wave == 2 ? ca[2] : ca[3];
      fin(sv[0] + *(const f32x4*)(sp + wave * 4096), us[0], dskA, chA + jc, ft[0]);
      fin((own + *(const f32x4*)(hp + 1024)) + (*(const f32x4*)hp + *(const f32x4*)(hp + 2048)), us[1], dskH, chH, ft[1]);
      VMCNT(2);
    }
  }
#pragma unroll
  for (int i = 0; i < 8; i++) asm volatile("" : "+v"(pf[i][0]), "+v"(pf[i][1]));
  RAW_BARRIER();
  STAMP(1, 5);
  const int n16 = lane & 15, g = lane >> 4;
  f32x4 acc[5][2];
  {
    const char* kp = smem + K2_KV + kh * 8 * 896;
    const int row1 = (n16 & 3) ? 20 : 16 + (n16 >> 2);
    const char* k00 = kp + (g < 3 ? n16 * 40 + 16 * g : 840);
    const char* k01 = kp + (g < 2 ? n16 * 40 + 16 * g + 8 : 840);
    const char* k10 = kp + (g < 3 ? row1 * 40 + 16 * g : 840);
    const char* k11 = kp + (g < 2 ? row1 * 40 + 16 * g + 8 : 840);
    const char* wm = smem + (160 * kh + 4 * g + (n16 >> 2)) * 160 + (lane & 3) * 8;
    const char* wt = smem + (160 * kh + 20 * (n16 >> 2) + 16 + g) * 160 + (lane & 3) * 8;
    u32x4 ka[8][2];
    auto rdK = [&](int i) {
      const u32x2 a0 = *(const u32x2*)(k00 + i * 896), a1 = *(const u32x2*)(k01 + i * 896), b0 = *(const u32x2*)(k10 + i * 896), b1 = *(const u32x2*)(k11 + i * 896);
      ka[i][0] = (u32x4){a0[0], a0[1], a1[0], a1[1]}; ka[i][1] = (u32x4){b0[0], b0[1], b1[0], b1[1]};
    };
    f32x4 c[2][2][2];
    const f32x4 z4 = {0.f, 0.f, 0.f, 0.f};
    auto stageA = [&](int i) {
#pragma unroll
      for (int tl = 1; tl >= 0; tl--)
#pragma unroll
        for (int nt = 0; nt < 2; nt++)
          c[i & 1][tl][nt] = __builtin_amdgcn_mfma_f32_16x16x32_bf16(__builtin_bit_cast(s16x8, ka[i][tl]), __builtin_bit_cast(s16x8, pf[i][nt]), z4, 0, 0, 0);
    };
    auto wfrag = [&](const char* p, int d) {
      const s16x4 lo = __builtin_amdgcn_ds_read_tr16_b64_v4i16((lds_s16x4_p)(p)), hi = __builtin_amdgcn_ds_read_tr16_b64_v4i16((lds_s16x4_p)(p + d));
      return __builtin_shufflevector(lo, hi, 0, 1, 2, 3, 4, 5, 6, 7);
    };
    u32x4 ob[2], o5[2];
    float p5[2];
    rdK(0); rdK(1);
    stageA(0);
    SB();
#pragma unroll
    for (int i = 0; i < 8; i++) {
      s16x8 w[5];
      if (i + 2 < 8 && i <= B6I) rdK(i + 2);
      if (i == B6I) { _Pragma("unroll") for (int j = i + 3; j < 8; j++) rdK(j); }
      if (i & 1) { _Pragma("unroll") for (int mt = 0; mt < WE; mt++) w[mt] = wfrag(wm + (i - 1) * 3200 + mt * 32, 3200); }
      SB();
      if (i + 1 < 8) stageA(i + 1);
      const f32x4 (&cc)[2][2] = c[i & 1];
      unsigned dz = pack2(cc[1][0][1], cc[1][1][1]);
      dz = pack2(lo_f(dz) + 1e-6f, hi_f(dz) + 1e-6f);
      const float inv[2] = {__builtin_amdgcn_rcpf(lo_f(dz)), __builtin_amdgcn_rcpf(hi_f(dz))};
#pragma unroll
      for (int nt = 0; nt < 2; nt++) {
        const f32x4 v = cc[0][nt] * inv[nt];
        ob[nt][2 * (i & 1)] = pack2(v[0], v[1]); ob[nt][2 * (i & 1) + 1] = pack2(v[2], v[3]);
        const float q5 = cc[1][nt][0] * inv[nt];
        if (i & 1) o5[nt][i >> 1] = pack2(p5[nt], q5); else p5[nt] = q5;
      }
      if (i + 1 < 8) {
#pragma unroll
        for (int q = 0; q < 4; q++) { __builtin_amdgcn_sched_group_barrier(0x8, 1, 0); __builtin_amdgcn_sched_group_barrier(0x2, 5, 0); }
      }
      SB();
      if (i & 1) {
        _Pragma("unroll") for (int mt = WE; mt < 5; mt++) w[mt] = wfrag(wm + (i - 1) * 3200 + mt * 32, 3200);
        SB();
#pragma unroll
        for (int mt = 0; mt < 5; mt++)
#pragma unroll
          for (int nt = 0; nt < 2; nt++)
            acc[mt][nt] = __builtin_amdgcn_mfma_f32_16x16x32_bf16(w[mt], __builtin_bit_cast(s16x8, ob[nt]), i == 1 ? z4 : acc[mt][nt], 0, 0, 0);
        SB();
      }
      if (i == SIGI) {
        VMCNT(0);
        unsigned old = 0;
        if (lane == 0) old = __hip_atomic_fetch_add((unsigned*)(smem + K2_LDS - 16), 1u, __ATOMIC_RELAXED, __HIP_MEMORY_SCOPE_WORKGROUP);
        if (lane == 0 && old == 7u) __hip_atomic_fetch_add((gu32*)(size_t)(ws + WS_CNT + (8 + xcd) * 128), 1u, __ATOMIC_RELAXED, __HIP_MEMORY_SCOPE_AGENT);
      }
      if (i == B6I) { RAW_BARRIER(); }
    }
#pragma unroll
    for (int mt = 0; mt < 5; mt++) {
      const s16x8 w = wfrag(wt + mt * 32, 12800);
#pragma unroll
      for (int nt = 0; nt < 2; nt++)
        acc[mt][nt] = __builtin_amdgcn_mfma_f32_16x16x32_bf16(w, __builtin_bit_cast(s16x8, o5[nt]), acc[mt][nt], 0, 0, 0);
    }
  }
  STAMP(1, 6);
  {
    char* sp = smem + 102400 + t * 30720 + l16;
#pragma unroll
    for (int q = 0; q < 10; q++) {
      const int own = q & 3;
      if (kh != own) *(f32x4*)(sp + (q * 3 + ((kh - own - 1) & 3)) * 1024) = acc[q >> 1][q & 1];
    }
  }
  RAW_BARRIER();
  STAMP(1, 7);
  {
    const char* sp = smem + 102400 + t * 30720 + l16;
    const float g1 = rb(1.f - g_r);
    const char* atp = smem + (kh >> 1) * 3200 + (4 * g + (n16 >> 2)) * 128 + (((32 * t + 16 * (kh & 1) + 4 * (lane & 3)) * 2) ^ ((lane & 8) << 3));
    u16* op = out + (size_t)(l0 + 32 * t + 16 * (kh & 1) + n16) * D + c0 + 16 * (kh >> 1) + 4 * g;
#pragma unroll
    for (int tl = 0; tl < 3; tl++) {
      if (tl == 2 && kh >= 2) break;
      f32x4 m;
      if (kh == 0)      m = acc[2 * tl][0];
      else if (kh == 1) m = acc[2 * tl][1];
      else if (kh == 2) m = acc[tl < 2 ? 2 * tl + 1 : 0][0];
      else              m = acc[tl < 2 ? 2 * tl + 1 : 0][1];
      const char* pp = sp + (tl * 4 + kh) * 3072;
      f32x4 p0 = *(const f32x4*)pp, p1 = *(const f32x4*)(pp + 1024), p2 = *(const f32x4*)(pp + 2048);
      f32x4 v = (m + p1) + (p0 + p2);
      s16x4 at = __builtin_amdgcn_ds_read_tr16_b64_v4i16((lds_s16x4_p)(atp + (tl < 2 ? tl * 6400 : 25600)));
      u32x2 au = __builtin_bit_cast(u32x2, at);
      u32x2 ov;
#pragma unroll
      for (int e = 0; e < 2; e++) {
        unsigned bq = pack2(v[2 * e], v[2 * e + 1]);
        unsigned gb = pack2(g1 * lo_f(bq), g1 * hi_f(bq));
        unsigned ga = pack2(g_r * lo_f(au[e]), g_r * hi_f(au[e]));
        ov[e] = pack2(lo_f(ga) + lo_f(gb), hi_f(ga) + hi_f(gb));
      }
      *(u32x2*)(op + 32 * tl) = ov;
    }
  }
  asm volatile("code_tail_mark:" ::: "memory");
  STAMP(1, 1);
}

extern "C" void kernel_launch(void* const* d_in, const int* in_sizes, int n_in, void* d_out, int out_size, void* d_ws, size_t ws_size,
                              hipStream_t stream) {
  const u16* u = (const u16*)d_in[0]; const u16* logA = (const u16*)d_in[1]; const u16* Aim = (const u16*)d_in[2];
  const u16* Cr = (const u16*)d_in[3]; const u16* Ci = (const u16*)d_in[4]; const u16* logdt = (const u16*)d_in[5];
  const u16* Dsk = (const u16*)d_in[6]; const u16* wqkv = (const u16*)d_in[8]; const u16* wo = (const u16*)d_in[9];
  const u16* gw = (const u16*)d_in[10];
  char* ws = (char*)d_ws;
  k1<<<NB_GEMM + NB_GATE + NB_KG, 512, 0, stream>>>(u, logA, Aim, Cr, Ci, logdt, wqkv, gw, wo, Dsk, (const u16*)d_out, ws);
  k2<<<256, 576, 0, stream>>>(Dsk, wo, ws, (u16*)d_out);
}
```

```cpp
#include <hip/hip_runtime.h>
#include <type_traits>
#include <hip/hip_bf16.h>
#include <stdint.h>

typedef unsigned short u16;
typedef __attribute__((ext_vector_type(8))) short s16x8;
typedef __attribute__((ext_vector_type(4))) short s16x4;
typedef __attribute__((ext_vector_type(4))) float f32x4;
typedef __attribute__((ext_vector_type(16))) float f32x16;
typedef __attribute__((ext_vector_type(4))) unsigned u32x4;
typedef __attribute__((ext_vector_type(2))) unsigned u32x2;
typedef __attribute__((address_space(3))) s16x4* lds_s16x4_p;
typedef const __attribute__((address_space(1))) void* gptr_t;
typedef __attribute__((address_space(3))) void* lptr_t;

constexpr int L = 2048, D = 640, NS = 192, NH = 32;
constexpr int UT_PAD = 32, UT_STRIDE = L + UT_PAD;
constexpr size_t WS_UT   = 0;
constexpr size_t WS_KIMG = WS_UT + (size_t)D * UT_STRIDE * 2;
constexpr size_t WS_PQ   = WS_KIMG + (size_t)D * 4096;
constexpr size_t WS_PART = WS_PQ + (size_t)L * D * 2;
constexpr size_t WS_GATE = WS_PART + (size_t)32 * 16 * 1680;
constexpr size_t WS_KVT  = WS_GATE + (size_t)L * 4;
constexpr size_t WS_AT   = WS_KVT + (size_t)32 * 1024;
constexpr size_t WS_CNT  = WS_AT + (size_t)D * L * 2;
constexpr size_t WS_END  = WS_CNT + 4096;
#define TGET(v) do {} while (0)
#define STEPT(i) do {} while (0)
#define TPUT(kid, slot, thr, v) do {} while (0)
#define STAMP(kid, slot) do {} while (0)

__device__ __forceinline__ float b2f(u16 v) { return __uint_as_float(((unsigned)v) << 16); }
typedef __bf16 bf16x2_t __attribute__((ext_vector_type(2)));
typedef float f32x2 __attribute__((ext_vector_type(2)));
__device__ __forceinline__ unsigned pack2(float a, float b) { f32x2 v = {a, b}; bf16x2_t r = __builtin_convertvector(v, bf16x2_t); return __builtin_bit_cast(unsigned, r); }
__device__ __forceinline__ float lo_f(unsigned u) { return __uint_as_float(u << 16); }
__device__ __forceinline__ float hi_f(unsigned u) { return __uint_as_float(u & 0xffff0000u); }
__device__ __forceinline__ float rb(float f) { return lo_f(pack2(f, f)); }
__device__ __forceinline__ float rb1(float f) { return __uint_as_float(pack2(0.f, f)); }
typedef unsigned short u16x2 __attribute__((ext_vector_type(2)));
__device__ __forceinline__ unsigned phi2(float a, float b, f32x2 kl) {
  const f32x2 t = {rb1(a), rb1(b)};
  const f32x2 x = t * kl, p = t + (f32x2){1.f, 1.f};
  const float e0 = __builtin_amdgcn_fmed3f(__builtin_amdgcn_exp2f(x[0]), 0.f, 1.f);
  const float e1 = __builtin_amdgcn_fmed3f(__builtin_amdgcn_exp2f(x[1]), 0.f, 1.f);
  return pack2(__builtin_fmaxf(p[0], e0), __builtin_fmaxf(p[1], e1));
}
__device__ __forceinline__ unsigned phi_eps(unsigned r) { return pack2(lo_f(r) + 1e-6f, hi_f(r) + 1e-6f); }
#define VMCNT(n) asm volatile("s_waitcnt vmcnt(" #n ")" ::: "memory")
#define RAW_BARRIER() do { asm volatile("s_waitcnt lgkmcnt(0)" ::: "memory"); __builtin_amdgcn_s_barrier(); asm volatile("" ::: "memory"); } while (0)
__device__ __forceinline__ void glds16(const void* g, void* l) { __builtin_amdgcn_global_load_lds((gptr_t)g, (lptr_t)l, 16, 0, 0); }

#define OPQ_S(v) asm volatile("" : "+s"(v))
typedef f32x2 cf;
__device__ __forceinline__ float smul(float a, float b) { float o; asm("v_mul_f32 %0, %1, %2" : "=v"(o) : "v"(a), "v"(b)); return o; }
__device__ __forceinline__ float sfma(float a, float b, float c) { float o; asm("v_fma_f32 %0, %1, %2, %3" : "=v"(o) : "v"(a), "v"(b), "v"(c)); return o; }
__device__ __forceinline__ float sfnma(float a, float b, float c) { float o; asm("v_fma_f32 %0, -%1, %2, %3" : "=v"(o) : "v"(a), "v"(b), "v"(c)); return o; }
__device__ __forceinline__ float ssub(float a, float b) { float o; asm("v_sub_f32 %0, %1, %2" : "=v"(o) : "v"(a), "v"(b)); return o; }
__device__ __forceinline__ cf cmul(cf a, cf b) {
  cf o;
  asm("v_pk_mul_f32 %0, %1, %2 op_sel:[0,0] op_sel_hi:[0,1]\n\t"
      "v_pk_fma_f32 %0, %1, %2, %0 op_sel:[1,1,0] op_sel_hi:[1,0,1] neg_lo:[1,0,0]" : "=&v"(o) : "v"(a), "v"(b));
  return o;
}

#define CPF 3
#define LPF 1
#define PFD 0
#define PFM_ 1
#define NP0 5
#define SPRIO 3
#define NI0 208
#define NB2 3
#define TB0 1
#define TBD 2
#define MIDX ((rowblk & 3) + 4 * (cb & 7))
#define MPS 3
#define PT0 30
#define PT1 110
#define PTD 20
constexpr int K1_LDS = 81920;
#define GLDS 1
constexpr int NB_GEMM = 256, NB_GATE = GLDS ? 0 : 16, NB_KG = 240, NB_KC = 214;
#define KVAT 0
#define KV1 ""
#define KVB 0
#define ROT 0
#define TPQ 0
#define KVST
#define STG 0
#define SA0 8
#define SB0 8
#define SA1 0
#define SB1 8
#define WOT 1
#define KRR 1
#define B6I 5
#define WE 3
#define SGB 1
#define XB 0
#define UTE 0
#define SIGI 2
#define NW0 3
#define K2PF 1
#define K2PFS "1"
#define KVP 1
#define PQA 2
#define PQB 4
#define STR2(x) #x
#define STR(x) STR2(x)
#define PQBS "2*(" STR(PQB) "-" STR(PQA) ")"
#define WTQ 0
#define WTP 0
#define WTO 0
#define WT 1
__device__ __forceinline__ void st16(void* p, u32x4 v) {
  asm volatile("global_store_dwordx4 %0, %1, off sc1\n\ts_nop 2" :: "v"(p), "v"(v) : "memory");
}

#define BAR_ONLY() do { asm volatile("" ::: "memory"); __builtin_amdgcn_s_barrier(); asm volatile("" ::: "memory"); } while (0)
#define SB() __builtin_amdgcn_sched_barrier(0)
template <bool ISQ>
__device__ __forceinline__ void mfma_part(char* smem, char* __restrict__ ws, int wave, f32x4 (&ep)[2][4], int zc) {
  const int lane = threadIdx.x & 63;
  char* sB = smem; char* sA = smem + 32768;
  const int wr = wave & 1, wc = (wave >> 1) & 1;
  const int g = lane >> 4, fr = lane & 15;
  f32x4 acc[4][4];
#pragma unroll
  for (int m = 0; m < 4; m++)
#pragma unroll
    for (int c = 0; c < 4; c++) acc[m][c] = (f32x4){0.f, 0.f, 0.f, 0.f};
  const int q4 = fr >> 2, p4 = lane & 3;
  int aoff[2];
#pragma unroll
  for (int ks = 0; ks < 2; ks++) aoff[ks] = (wr * 64 + fr) * 128 + (((4 * ks + g) ^ (fr & 7)) << 4);
  int boff[4];
  {
    const int f = q4 | ((g & 1) << 2);
#pragma unroll
    for (int c = 0; c < 4; c++) boff[c] = (8 * g + q4) * 256 + (((4 * wc + c) ^ f) << 5) + p4 * 8;
  }
  s16x8 af[2][4]; s16x4 bl[4], bh[4];
  auto rdA = [&](int buf, int m, const char* pa) { af[buf][m] = *(const s16x8*)(pa + m * 2048); };
  auto rdBl = [&](int c, const char* pb) { bl[c] = __builtin_amdgcn_ds_read_tr16_b64_v4i16((lds_s16x4_p)(pb + boff[c])); };
  auto rdBh = [&](int c, const char* pb) { bh[c] = __builtin_amdgcn_ds_read_tr16_b64_v4i16((lds_s16x4_p)(pb + boff[c] + 1024)); };
  BAR_ONLY();
  STAMP(0, 2);
#pragma unroll
  for (int c = 0; c < 4; c++) rdA(0, c, sA + aoff[0]);
#pragma unroll
  for (int c = 0; c < 3; c++) { rdBl(c, sB); rdBh(c, sB); }
  int ao0 = 0, ao1 = 16384;
  auto grp = [&](int buf, int c, int m) {
    const s16x8 bf = __builtin_shufflevector(bl[c], bh[c], 0, 1, 2, 3, 4, 5, 6, 7);
    if (ISQ) acc[m][c] = __builtin_amdgcn_mfma_f32_16x16x32_bf16(bf, af[buf][m], acc[m][c], 0, 0, 0);
    else     acc[m][c] = __builtin_amdgcn_mfma_f32_16x16x32_bf16(af[buf][m], bf, acc[m][c], 0, 0, 0);
  };
#pragma unroll 1
  for (int j2 = 0; j2 < 5; j2++) {
#pragma unroll
    for (int st = 0; st < 4; st++) {
      const int hs = st & 1;
      SB();
      asm volatile("s_waitcnt lgkmcnt(2)" ::: "memory"); __builtin_amdgcn_s_barrier(); asm volatile("" ::: "memory");
      SB();
      const char* pa = sA + (hs == 0 ? ao0 + aoff[1] : ao1 + aoff[0]);
      const char* pc = sB + st * 8192;
      const char* pb = sB + ((st + 1) & 3) * 8192;
      grp(hs, 0, 0); SB(); rdBl(3, pc); rdBh(3, pc); SB();
      grp(hs, 0, 1); SB(); rdA(hs ^ 1, 0, pa); rdA(hs ^ 1, 1, pa); SB();
      grp(hs, 0, 2); SB(); rdA(hs ^ 1, 2, pa); rdA(hs ^ 1, 3, pa); SB();
      grp(hs, 0, 3); SB();
#pragma unroll
      for (int c = 1; c < 4; c++) {
        grp(hs, c, 0); SB(); rdBl(c - 1, pb); rdBh(c - 1, pb); SB();
        grp(hs, c, 1); grp(hs, c, 2); grp(hs, c, 3); SB();
      }
      if (hs == 1) { ao0 = ao1; ao1 = ao1 == 32768 ? 0 : ao1 + 16384; }
    }
  }
  STAMP(0, 3);
  int l3 = threadIdx.x; asm volatile("" : "+v"(l3));
  char* xch = smem + 32768 + (l3 >> 6) * 8192 + (l3 & 63) * 16;
  if (ISQ || wc == 0) {
#pragma unroll
    for (int i = 0; i < 8; i++) *(f32x4*)(xch + i * 1024) = acc[2 + (i >> 2)][i & 3];
  }
#pragma unroll
  for (int i = 0; i < 8; i++) ep[i >> 2][i & 3] = acc[i >> 2][i & 3];
  RAW_BARRIER();
  if (!ISQ && wc == 1) {
    char* fv = smem + 16384 + wr * 8192 + (l3 & 63) * 16;
#pragma unroll
    for (int c = 0; c < 4; c++)
#pragma unroll
      for (int h = 0; h < 2; h++) {
        u32x4 a = {pack2(acc[2 * h][c][0], acc[2 * h][c][1]), pack2(acc[2 * h][c][2], acc[2 * h][c][3]),
                   pack2(acc[2 * h + 1][c][0], acc[2 * h + 1][c][1]), pack2(acc[2 * h + 1][c][2], acc[2 * h + 1][c][3])};
        if ((c == 0 || c == 3) && 16 * c + (l3 & 15) == zc) a = (u32x4){0x3F803F80u, 0x3F803F80u, 0x3F803F80u, 0x3F803F80u};
        *(u32x4*)(fv + h * 4096 + c * 1024) = a;
      }
  }
}
__device__ __forceinline__ void dma4x(const int (&vo)[4], const char* sbase, int ldsa) {
  asm volatile("s_mov_b32 m0, %5\n\ts_nop 0\n\tglobal_load_lds_dwordx4 %0, %4\n\tglobal_load_lds_dwordx4 %1, %4 offset:1024\n\t"
               "global_load_lds_dwordx4 %2, %4 offset:2048\n\tglobal_load_lds_dwordx4 %3, %4 offset:3072"
               :: "v"(vo[0]), "v"(vo[1]), "v"(vo[2]), "v"(vo[3]), "s"(sbase), "s"(ldsa) : "memory", "m0");
}
__device__ __forceinline__ void gemm_role(char* smem, const u16* __restrict__ x, const u16* __restrict__ W, const u16* __restrict__ wo, char* __restrict__ ws,
                                          int rowblk, int cb, int slot, int par) {
  const int tid = threadIdx.x, lane = tid & 63;
  const int wave = __builtin_amdgcn_readfirstlane(tid >> 6);
  const bool ISQ = cb < 5;
  const int row0 = rowblk * 128, col0 = cb * 128;
  const int kvj = cb - 5;
  const int wr = wave & 1, wc = (wave >> 1) & 1, hf = wave >> 2;
  f32x4 ep[2][4];
  unsigned pf3 = 0;
  const int wce = wc ^ hf;
  const int o = (60 * kvj) & 7, zc = o ? 0 : 60;
  unsigned eo[2];
  int ta[2], tb[2];
  auto eoffs = [&](bool isq) {
    int l4 = threadIdx.x; asm volatile("" : "+v"(l4));
    const unsigned g = (l4 >> 4) & 3, fr = l4 & 15;
    if (isq) {
#pragma unroll
      for (int c = 0; c < 4; c++) {
        const unsigned q4 = (unsigned)(col0 / 4 + 16 * wce + 4 * c) + g;
        const unsigned hd = (q4 * 205u) >> 10, d4 = q4 - 5u * hd;
        const unsigned off = hd * 320u + (d4 < 4u ? 16u + ((d4 * 17u) & 0x21u) + fr * 2u : fr);
        if (c & 1) eo[c >> 1] |= off << 16; else eo[c >> 1] = off;
      }
      ta[0] = ta[1] = tb[0] = tb[1] = 0;
    } else {
      const unsigned long long TA = o ? 0x323322211100ull : 0x103322211100ull, TB = o ? 0x003232121010ull : 0x333232121010ull;
#pragma unroll
      for (int i = 0; i < 2; i++) {
        const int p = wave < 4 ? wave : wave * 2 - 4 + i;
        ta[i] = (int)((TA >> (4 * p)) & 15); tb[i] = (int)((TB >> (4 * p)) & 15);
        const int kc0 = 16 * ta[i] + 4 * (int)g - o;
        const int hk = (kc0 >= 20) + (kc0 >= 40), dk0 = kc0 - 20 * hk;
        const int vcol = 16 * tb[i] + (int)fr;
        const int dv = vcol == zc ? 20 : vcol - o - 20 * hk;
        const bool ok = (kc0 >= 0) & (kc0 < (kvj == 10 ? 40 : 60)) & (dv >= 0) & ((dv < 20) | (vcol == zc)) & (i == 0 || wave >= 4);
        eo[i] = ok ? (unsigned)((hk * 16 * 21 + dv) * 80 + dk0 * 4) : ~0u;
      }
    }
    asm volatile("" : "+v"(eo[0]), "+v"(eo[1]));
    SB();
  };
  if (wave >= 4) {
    const int lw = wave & 1;
    const int l0 = (int)(size_t)(lptr_t)smem;
    unsigned pf0 = 0;
    const int pidx = (rowblk & 3) + 4 * slot;
    auto pfaddr = [&](int stg, int r) {
      const int q = r & 15;
      const int ln = par ? (q < 8 ? 12 + q : 14 + q) : (q < 13 ? q : 7 + q);
      return r < 16 ? (const char*)x + ((rowblk >> 2) * 512 + pidx * 16 + q) * 1280 + stg * 128
                    : (const char*)W + (64 * stg + pidx * 2 + (r >> 4) - 1) * 3840 + ln * 128;
    };
    {
      const int q = (wave - 4) * (NI0 / 4) + min(lane, NI0 / 4 - 1), stg = q / 48;
      asm volatile("global_load_dword %0, %1, off" : "+v"(pf0) : "v"(pfaddr(stg, q - 48 * stg)));
      SB();
    }
    eoffs(ISQ);
    __builtin_amdgcn_s_setprio(SPRIO);
    if (wave < 6) {
      int voA[4], voB[4];
#pragma unroll
      for (int i = 0; i < 4; i++) {
        voA[i] = (64 * lw + 8 * i + (lane >> 3)) * 1280 + (((lane & 7) ^ (lane >> 3)) << 4) - 1024 * i;
        const int fB = (lane >> 4) | ((i >> 1) << 2);
        const int c16 = (((lane & 15) >> 1) ^ fB) * 2 + (lane & 1);
        int bcol = col0 + 8 * c16;
        if (!ISQ) bcol = 640 + 640 * (c16 >> 3) + min(((60 * kvj) & ~7) + 8 * (c16 & 7), 632);
        voB[i] = (16 * lw + 4 * i + (lane >> 4)) * 3840 + bcol * 2 - 1024 * i;
      }
      const char* pA = (const char*)x + row0 * 1280;
      const char* pB = (const char*)W;
      const int lA = l0 + 32768 + lw * 8192, lB = l0 + lw * 4096;
      auto issA = [&](int slot_off, int h) { dma4x(voA, pA + h * 32 * 1280, lA + slot_off + h * 4096); if (h) pA += 128; };
      auto issB = [&](int slot_off) { dma4x(voB, pB, lB + slot_off); pB += 32 * 3840; };
      TGET(tl0);
      asm volatile("s_waitcnt vmcnt(0)" :: "v"(pf0) : "memory");
      TGET(tl1);
      issA(0, 0); issB(0); issA(0, 1);
      issB(8192); issA(16384, 0);
      VMCNT(8);
      TGET(tl2);
      BAR_ONLY();
      issB(16384); issA(16384, 1);
      VMCNT(12); BAR_ONLY();
      issB(24576); issA(32768, 0); issA(32768, 1);
      VMCNT(12); BAR_ONLY();
      issB(0); issA(0, 0);
      int ao = 0, bo = 8192;
#pragma unroll 1
      for (int j = 1; j < 7; j++) {
        VMCNT(12); BAR_ONLY();
        issB(bo); bo = (bo + 8192) & 32767; issA(ao, 1);
        ao = ao == 32768 ? 0 : ao + 16384;
        VMCNT(12); BAR_ONLY();
        issB(bo); bo = (bo + 8192) & 32767; issA(ao, 0);
      }
      VMCNT(12); BAR_ONLY();
      issB(8192); issA(0, 1);
      VMCNT(12); BAR_ONLY();
      issB(16384);
      VMCNT(8); BAR_ONLY();
      issB(24576);
      {
        const int o = (pidx * 200 + lw * 100 + min(lane, 49)) * 128;
        asm volatile("global_load_dword %0, %1, %2\n\tglobal_load_dword %0, %1, %3" : "+v"(pf3) : "v"(o), "s"(wo), "s"((const char*)wo + 6400));
      }
      VMCNT(6); BAR_ONLY();
      VMCNT(2); BAR_ONLY();
      BAR_ONLY();
      TPUT(0, 4, 256, tl0);
      TPUT(0, 5, 256, tl1); TPUT(0, 6, 256, tl2);
      BAR_ONLY();
    } else {
      u32x4 trv[2][4];
      constexpr int CNT2 = 480 - NI0, NL2 = (CNT2 + 2 * NB2 - 1) / (2 * NB2);
      const char* pp[NB2];
#pragma unroll
      for (int i = 0; i < NB2; i++) {
        const int it = NI0 * 32 + ((pidx & 3) * 8 + (pidx >> 2)) * CNT2 + min((2 * i + lw) * NL2 + min(lane, NL2 - 1), CNT2 - 1);
        const int stg = it / 1536, r = it - 1536 * stg, q = r & 15;
      const int ln = par ? (q < 8 ? 12 + q : 14 + q) : (q < 13 ? q : 7 + q);
        pp[i] = r < 512 ? (const char*)x + ((rowblk >> 2) * 512 + r) * 1280 + stg * 128 : (const char*)W + (64 * stg + (r >> 4) - 32) * 3840 + ln * 128;
      }
      const int tb2 = TB0 + TBD * (pidx & 3);
      int tro;
      {
        const int fr = lane & 15, g = lane >> 4, p4 = lane & 3, r = 8 * g + (fr >> 2);
        tro = l0 + 32768 + r * 128 + (((4 * lw + (p4 >> 1)) ^ (r & 7)) << 4) + (p4 & 1) * 8;
      }
      auto transp = [&](int ao) {
#pragma unroll
        for (int hh = 0; hh < 2; hh++) {
          u32x2 tr[8];
          const int a = (tro ^ (32 * hh)) + ao, a2 = (a ^ 64) + 512;
          asm volatile("ds_read_b64_tr_b16 %0, %8\n\tds_read_b64_tr_b16 %1, %9\n\tds_read_b64_tr_b16 %2, %8 offset:4096\n\tds_read_b64_tr_b16 %3, %9 offset:4096\n\t"
                       "ds_read_b64_tr_b16 %4, %8 offset:8192\n\tds_read_b64_tr_b16 %5, %9 offset:8192\n\tds_read_b64_tr_b16 %6, %8 offset:12288\n\tds_read_b64_tr_b16 %7, %9 offset:12288\n\t"
                       "s_waitcnt lgkmcnt(0)"
                       : "=&v"(tr[0]), "=&v"(tr[1]), "=&v"(tr[2]), "=&v"(tr[3]), "=&v"(tr[4]), "=&v"(tr[5]), "=&v"(tr[6]), "=&v"(tr[7])
                       : "v"(a), "v"(a2) : "memory");
#pragma unroll
          for (int i = 0; i < 4; i++) trv[hh][i] = (u32x4){tr[2 * i][0], tr[2 * i][1], tr[2 * i + 1][0], tr[2 * i + 1][1]};
        }
      };
      unsigned pf2 = 0;
      auto burst = [&](int hs) {
        static_assert(NB2 == 3, "");
        asm volatile("s_cmp_lg_u32 %4, %5\n\ts_cbranch_scc1 1f\n\tglobal_load_dword %0, %1, off\n\tglobal_load_dword %0, %2, off\n\tglobal_load_dword %0, %3, off\n1:"
                     : "+v"(pf2) : "v"(pp[0]), "v"(pp[1]), "v"(pp[2]), "s"(hs), "s"(tb2) : "scc");
      };
      auto utst = [&]() {
      asm volatile("s_waitcnt vmcnt(0)" :: "v"(pf2) : "memory");
      if (cb < 10) {
        u16* dst = (u16*)(ws + WS_UT) + (size_t)(64 * cb + 32 * lw + (lane & 15)) * UT_STRIDE + 8 * (lane >> 4);
#pragma unroll
        for (int hh = 0; hh < 2; hh++) {
#pragma unroll
          for (int i = 0; i < 4; i++) st16(dst + hh * 16 * UT_STRIDE + UT_PAD + row0 + 32 * i, trv[hh][i]);
          if (rowblk == 0) st16(dst + hh * 16 * UT_STRIDE, (u32x4){0u, 0u, 0u, 0u});
        }
      }
      };
      asm volatile("s_waitcnt vmcnt(0)" :: "v"(pf0) : "memory");
      BAR_ONLY();
      int ao = 0;
#pragma unroll 1
      for (int j = 0; j < 10; j++) {
        BAR_ONLY();
        if (!lw) STEPT(2 * j);
        burst(2 * j);
        if (j == cb) transp(ao);
        BAR_ONLY();
        if (!lw) STEPT(2 * j + 1);
        burst(2 * j + 1);
        ao = ao == 32768 ? 0 : ao + 16384;
      }
      utst();
      BAR_ONLY();
    }
    __builtin_amdgcn_s_setprio(0);
    char* xch = smem + 32768 + ((wave & 3) ^ 2) * 8192 + lane * 16;
    if (ISQ || wce == 0) {
#pragma unroll
      for (int i = 0; i < 8; i++) ep[i >> 2][i & 3] = *(const f32x4*)(xch + i * 1024);
    }
  } else if (ISQ) {
    eoffs(true);
    mfma_part<true>(smem, ws, wave, ep, 0);
  } else {
    eoffs(false);
    mfma_part<false>(smem, ws, wave, ep, zc);
  }
  int l2 = threadIdx.x; asm volatile("" : "+v"(l2));
  const int lane2 = l2 & 63;
  f32x2 kl = {1.44269504f, 1.44269504f};
  asm volatile("" : "+s"(kl));
  char* fb = smem;
  if (ISQ || wce == 0) {
    float mn = ep[0][0][0];
#pragma unroll
    for (int i = 0; i < 16; i++) mn = __builtin_fminf(__builtin_fminf(mn, ep[i >> 3][(i >> 1) & 3][2 * (i & 1)]), ep[i >> 3][(i >> 1) & 3][2 * (i & 1) + 1]);
    const bool small = __builtin_amdgcn_ballot_w64(!(mn > -7.f)) != 0;
    char* base = ws + WS_PQ + (size_t)((rowblk * 2 + wr) * 128 + 2 * hf) * 640;
    auto body = [&](auto EPS) {
#pragma unroll
      for (int c = 0; c < 4; c++) {
        u32x4 a;
        a[0] = phi2(ep[0][c][0], ep[0][c][1], kl); a[1] = phi2(ep[0][c][2], ep[0][c][3], kl);
        a[2] = phi2(ep[1][c][0], ep[1][c][1], kl); a[3] = phi2(ep[1][c][2], ep[1][c][3], kl);
        if (EPS.value) { a[0] = phi_eps(a[0]); a[1] = phi_eps(a[1]); a[2] = phi_eps(a[2]); a[3] = phi_eps(a[3]); }
        if (ISQ) {
          const unsigned off = ((c & 1) ? eo[c >> 1] >> 16 : eo[c >> 1] & 0xffffu) * 8u;
          *(u32x2*)(base + off) = (u32x2){a[0], a[1]};
          *(u32x2*)(base + off + 640) = (u32x2){a[2], a[3]};
        } else {
          *(u32x4*)(fb + ((((wr * 2 + hf) * 4 + c) * 64 + lane2) << 4)) = a;
        }
      }
    };
    if (small) body(std::true_type{}); else body(std::false_type{});
  }
  if (!ISQ) {
    RAW_BARRIER();
    s16x8 fk[2][4], fv[2][4];
#pragma unroll
    for (int rg = 0; rg < 4; rg++)
#pragma unroll
      for (int i = 0; i < 2; i++) {
        fk[i][rg] = *(const s16x8*)(fb + ((ta[i] * 64 + lane2) << 4) + rg * 4096);
        fv[i][rg] = *(const s16x8*)(fb + 16384 + ((tb[i] * 64 + lane2) << 4) + rg * 4096);
      }
    f32x4 dd[2] = {{0.f, 0.f, 0.f, 0.f}, {0.f, 0.f, 0.f, 0.f}};
#pragma unroll
    for (int rg = 0; rg < 4; rg++)
#pragma unroll
      for (int i = 0; i < 2; i++) dd[i] = __builtin_amdgcn_mfma_f32_16x16x32_bf16(fk[i][rg], fv[i][rg], dd[i], 0, 0, 0);
    char* pb = ws + WS_PART + (size_t)((3 * kvj * 16 + rowblk) * 21) * 80;
#pragma unroll
    for (int i = 0; i < 2; i++)
      if (eo[i] != ~0u) *(f32x4*)(pb + eo[i]) = dd[i];
  }
  asm volatile("s_waitcnt vmcnt(0)" :: "v"(pf3) : "memory");
}

__device__ __forceinline__ cf zpow(float th, float tl, float rh, float rl, float p) {
  float e = __builtin_amdgcn_exp2f(th * p); e = fmaf(e, (tl * p) * 0.69314718f, e);
  float t = rh * p; float f = (t - __builtin_rintf(t)) + rl * p;
  cf o; o[0] = e * __builtin_amdgcn_cosf(f); o[1] = e * __builtin_amdgcn_sinf(f); return o;
}
#define PKM(o, a, b) "v_pk_mul_f32 " o ", " a ", " b " op_sel:[0,0] op_sel_hi:[0,1]\n\t"
#define PKF(o, a, b) "v_pk_fma_f32 " o ", " a ", " b ", " o " op_sel:[1,1,0] op_sel_hi:[1,0,1] neg_lo:[1,0,0]\n\t"
__device__ __forceinline__ void cmul4(cf& o0, cf& o1, cf& o2, cf& o3, cf a0, cf b0, cf a1, cf b1, cf a2, cf b2, cf a3, cf b3) {
  asm(PKM("%0", "%4", "%5") PKM("%1", "%6", "%7") PKM("%2", "%8", "%9") PKM("%3", "%10", "%11")
      PKF("%0", "%4", "%5") PKF("%1", "%6", "%7") PKF("%2", "%8", "%9") PKF("%3", "%10", "%11")
      : "=&v"(o0), "=&v"(o1), "=&v"(o2), "=&v"(o3)
      : "v"(a0), "v"(b0), "v"(a1), "v"(b1), "v"(a2), "v"(b2), "v"(a3), "v"(b3));
}
__device__ __forceinline__ void batch4(unsigned* h, cf b, cf t0, cf t1, cf t2, cf t3) {
  cf w0, w1, w2, w3;
  asm(PKM("%0", "%4", "%5") PKM("%1", "%4", "%6") PKM("%2", "%4", "%7") PKM("%3", "%4", "%8")
      PKF("%0", "%4", "%5") PKF("%1", "%4", "%6") PKF("%2", "%4", "%7") PKF("%3", "%4", "%8")
      : "=&v"(w0), "=&v"(w1), "=&v"(w2), "=&v"(w3) : "v"(b), "v"(t0), "v"(t1), "v"(t2), "v"(t3));
  h[0] = pack2(w0[0], w0[1]); h[1] = pack2(w1[0], w1[1]); h[2] = pack2(w2[0], w2[1]); h[3] = pack2(w3[0], w3[1]);
}
__device__ __forceinline__ void batch3(unsigned* h, cf b, cf t1, cf t2, cf t3) {
  cf w1, w2, w3;
  asm(PKM("%0", "%3", "%4") PKM("%1", "%3", "%5") PKM("%2", "%3", "%6")
      PKF("%0", "%3", "%4") PKF("%1", "%3", "%5") PKF("%2", "%3", "%6")
      : "=&v"(w1), "=&v"(w2), "=&v"(w3) : "v"(b), "v"(t1), "v"(t2), "v"(t3));
  h[0] = pack2(b[0], b[1]); h[1] = pack2(w1[0], w1[1]); h[2] = pack2(w2[0], w2[1]); h[3] = pack2(w3[0], w3[1]);
}
constexpr int KG_RS = 288, KG_PL = 16 * KG_RS;
__device__ __forceinline__ void kgen_role(char* smem, const u16* __restrict__ logA, const u16* __restrict__ Aim,
                                          const u16* __restrict__ Cr, const u16* __restrict__ Ci, const u16* __restrict__ logdt,
                                          const u16* __restrict__ x, const u16* __restrict__ W, const u16* __restrict__ wo,
                                          const u16* __restrict__ gw, char* __restrict__ ws, int blk, unsigned pfv) {
  const int tid = threadIdx.x, lane = tid & 63;
  const int wave = __builtin_amdgcn_readfirstlane(tid >> 6);
  constexpr int RS = KG_RS, PL = KG_PL;
  char* tb = smem + wave * (2 * PL);
  char* wp = tb + lane * 4;
  const char* rp = tb + (lane & 15) * RS + (lane >> 4) * 16;
  const int kq = blk >> 3, tbase = (blk & 7) * 240;
  const int nrep = kq >= 27 ? 0 : (wave == 7 ? 2 : 1);
  unsigned pa[5], pb[5] = {0u, 0u, 0u, 0u, 0u};
  {
    const int task = tbase + min(kq * 9 + wave, 239), pi = task * 64 + lane;
    pa[0] = logdt[task / 3]; pa[1] = logA[pi]; pa[2] = Aim[pi]; pa[3] = Cr[pi]; pa[4] = Ci[pi];
    if (wave == 7) {
      const int t2 = tbase + min(kq * 9 + 8, 239), p2 = t2 * 64 + lane;
      pb[0] = logdt[t2 / 3]; pb[1] = logA[p2]; pb[2] = Aim[p2]; pb[3] = Cr[p2]; pb[4] = Ci[p2];
    }
    asm volatile("" : "+v"(pb[0]), "+v"(pb[1]), "+v"(pb[2]), "+v"(pb[3]), "+v"(pb[4]));
  }
#pragma unroll 1
  for (int rep = 0; rep < nrep; rep++) {
    const float dt = rb(expf(lo_f(pa[0])));
    float ar;
    {
      const float la = lo_f(pa[1]);
      float h = la * 1.44269502f; float l = fmaf(la, 1.44269502f, -h) + la * 1.92596299e-8f;
      float e = __builtin_amdgcn_exp2f(h); e = fmaf(e, l * 0.69314718f, e);
      ar = -rb(e) * dt;
    }
    const float ai = lo_f(pa[2]) * dt;
    cf C; C[0] = 2.f * (lo_f(pa[3]) * dt); C[1] = 2.f * (lo_f(pa[4]) * dt);
#pragma unroll
    for (int i = 0; i < 5; i++) pa[i] = pb[i];
    const float th = ar * 1.44269502f, tl = fmaf(ar, 1.44269502f, -th) + ar * 1.92596299e-8f;
    const float rh = ai * 0.159154937f, rl = fmaf(ai, 0.159154937f, -rh) + ai * 6.42063833e-9f;
    const cf one = {1.f, 0.f};
    cf S1 = zpow(th, tl, rh, rl, 1.f), Q1 = zpow(th, tl, rh, rl, 8.f), R1 = zpow(th, tl, rh, rl, 128.f);
    Q1[1] = -Q1[1];
    cf S2, Q2, R2, G1, S3, S4, Q3, P1, R3, T1, G2, G3, S5, S6, S7, P2, CT1, T2, P3, CS4, CT2, CT3, CT4, dm0;
    cmul4(S2, Q2, R2, G1, S1, S1, Q1, Q1, R1, R1, C, R1);
    cmul4(S3, S4, Q3, P1, S2, S1, S2, S2, Q2, Q1, Q2, Q2);
    cmul4(R3, T1, G2, P2, R2, R1, R2, R2, C, R2, P1, P1);
    cmul4(S5, S6, S7, P3, S4, S1, S4, S2, S4, S3, P2, P1);
    cmul4(G3, CT1, T2, dm0, C, R3, C, T1, T1, T1, C, R1);
    cmul4(CS4, CT2, CT3, dm0, CT1, S4, C, T2, CT1, T2, C, R1);
    CT4 = cmul(CT2, T2);
    auto rows4 = [&](int pl, int row, int rs, cf b, cf t0, cf t1, cf t2, cf t3) {
      unsigned h[4];
      batch4(h, b, t0, t1, t2, t3);
#pragma unroll
      for (int k = 0; k < 4; k++) *(unsigned*)(wp + pl * PL + (row + k * rs) * RS) = h[k];
    };
    auto rows3 = [&](int pl, int row, int rs, cf b, cf t1, cf t2, cf t3) {
      unsigned h[4];
      batch3(h, b, t1, t2, t3);
#pragma unroll
      for (int k = 0; k < 4; k++) *(unsigned*)(wp + pl * PL + (row + k * rs) * RS) = h[k];
    };
    *(unsigned*)(wp) = 0x00003F80u;
    *(unsigned*)(wp + RS) = pack2(Q1[0], Q1[1]); *(unsigned*)(wp + 2 * RS) = pack2(Q2[0], Q2[1]); *(unsigned*)(wp + 3 * RS) = pack2(Q3[0], Q3[1]);
    rows3(0, 4, 1, P1, Q1, Q2, Q3); rows3(0, 8, 1, P2, Q1, Q2, Q3); rows3(0, 12, 1, P3, Q1, Q2, Q3);
    rows3(1, 0, 1, C, S1, S2, S3); rows4(1, 4, 1, C, S4, S5, S6, S7); rows3(1, 8, 1, G1, S1, S2, S3); rows4(1, 12, 1, G1, S4, S5, S6, S7);
    __builtin_amdgcn_wave_barrier();
    s16x8 bh[4];
    f32x4 res[3];
#pragma unroll
    for (int k = 0; k < 4; k++) bh[k] = *(const s16x8*)(rp + k * 64);
    auto mm = [&](int t, int pl) {
      f32x4 a = {0.f, 0.f, 0.f, 0.f};
#pragma unroll
      for (int k = 0; k < 4; k++) a = __builtin_amdgcn_mfma_f32_16x16x32_bf16(*(const s16x8*)(rp + pl * PL + k * 64), bh[k], a, 0, 0, 0);
      res[t] = a;
    };
    mm(0, 1);
    __builtin_amdgcn_wave_barrier();
    rows3(0, 0, 1, G2, S2, S4, S6); rows3(0, 4, 1, G3, S2, S4, S6); rows3(0, 8, 2, CT1, R1, R2, R3); rows3(0, 9, 2, CS4, R1, R2, R3);
    rows3(1, 0, 1, CT2, R1, R2, R3); rows3(1, 4, 1, CT3, R1, R2, R3);
    *(unsigned*)(wp + PL + 8 * RS) = pack2(CT4[0], CT4[1]);
    __builtin_amdgcn_wave_barrier();
    mm(1, 0); mm(2, 1);
    __builtin_amdgcn_wave_barrier();
    char* rs = (wave == 7 && rep == 0) ? smem + 8 * (2 * PL) : tb;
#pragma unroll
    for (int t = 0; t < 3; t++) *(f32x4*)(rs + t * 1024 + lane * 16) = res[t];
    __builtin_amdgcn_wave_barrier();
  }
  STAMP(0, 4);
  if (blk == 239) ((unsigned*)(ws + WS_CNT))[tid] = 0u, ((unsigned*)(ws + WS_CNT))[tid + 512] = 0u;
  if (blk >= 216 && blk < 220) st16(ws + WS_KVT + (size_t)((blk - 216) * 512 + tid) * 16, (u32x4){~0u, ~0u, ~0u, ~0u});
  if (kq < 27) {
    int tix[8];
#pragma unroll
    for (int e = 0; e < 8; e++) {
      const int j = (tid & 255) * 8 + 7 - e;
      const int p = (int)rb((float)j);
      const int hh = p >> 7, c = (p >> 3) & 15;
      int sh = 31 - __builtin_clz(hh | 1); sh = sh > 3 ? 3 : sh;
      const int row = 8 * sh + (((hh << 3) | (p & 7)) >> sh);
      tix[e] = 4096 + (tid >> 8) * (2 * PL) + (((row >> 2) * 16 + c) * 4 + (row & 3)) * 4;
      asm volatile("" : "+v"(tix[e]));
    }
    __syncthreads();
#pragma unroll
    for (int it = 0; it < 2; it++) {
      const int e = tid + 512 * it;
      const int seg = e / 192, ent = e - 192 * seg;
      if (seg < 3) {
        const char* p0 = smem + 3 * seg * (2 * PL) + ent * 16;
        const f32x4 a = *(const f32x4*)p0 + *(const f32x4*)(p0 + (seg == 2 ? 2 : 1) * (2 * PL)) + *(const f32x4*)(p0 + (seg == 2 ? 1 : 2) * (2 * PL));
        *(f32x4*)(smem + 4096 + seg * (2 * PL) + ent * 16) = a;
      }
    }
    __syncthreads();
    const int d0 = 80 * (blk & 7) + 3 * kq + (tid >> 8);
    u32x4 hi;
#pragma unroll
    for (int e = 0; e < 4; e++) hi[e] = pack2(*(const float*)(smem + tix[2 * e]), *(const float*)(smem + tix[2 * e + 1]));
    st16(ws + WS_KIMG + (size_t)d0 * 4096 + (tid & 255) * 16, hi);
    if (wave < 4 && kq < 26) {
#pragma unroll
      for (int e = 0; e < 4; e++) hi[e] = pack2(*(const float*)(smem + tix[2 * e] + 4 * PL), *(const float*)(smem + tix[2 * e + 1] + 4 * PL));
      st16(ws + WS_KIMG + (size_t)(d0 + 2) * 4096 + tid * 16, hi);
    }
    VMCNT(0);
    if (lane < 16) {
      const int o = (int)WS_KIMG + (d0 + ((lane & 8) && wave < 4 && kq < 26 ? 2 : 0)) * 4096 + (wave & 3) * 1024 + (lane & 7) * 128;
      asm volatile("global_load_dword %0, %1, %2" : "+v"(pfv) : "v"(o), "s"(ws));
    }
  }
  asm volatile("s_waitcnt vmcnt(0)" :: "v"(pfv) : "memory");
  const int gt = wave * 30 + (blk >> 3);
  if (gt < 64) {
    const int row = (blk & 7) * 256 + gt * 4 + (lane >> 4);
    const u32x4* px = (const u32x4*)(x + (size_t)row * D) + (lane & 15); const u32x4* pg = (const u32x4*)gw + (lane & 15);
    u32x4 xv[5], gv[5];
#pragma unroll
    for (int i = 0; i < 5; i++) { xv[i] = px[16 * i]; gv[i] = pg[16 * i]; }
    float gs = 0.f;
#pragma unroll
    for (int i = 0; i < 5; i++)
#pragma unroll
      for (int e = 0; e < 4; e++) {
        unsigned p = pack2(lo_f(xv[i][e]) * lo_f(gv[i][e]), hi_f(xv[i][e]) * hi_f(gv[i][e]));
        gs = __builtin_amdgcn_fdot2_f32_bf16(__builtin_bit_cast(bf16x2_t, p), __builtin_bit_cast(bf16x2_t, 0x3F803F80u), gs, false);
      }
    gs += __shfl_xor(gs, 1); gs += __shfl_xor(gs, 2); gs += __shfl_xor(gs, 4); gs += __shfl_xor(gs, 8);
    if ((lane & 15) == 0) { float a = rb(gs); ((float*)(ws + WS_GATE))[row] = rb(1.f / (1.f + __expf(-a))); }
  }
}

__global__ __launch_bounds__(512, 4) void k1(const u16* __restrict__ x, const u16* __restrict__ logA, const u16* __restrict__ Aim,
                                          const u16* __restrict__ Cr, const u16* __restrict__ Ci, const u16* __restrict__ logdt,
                                          const u16* __restrict__ wqkv, const u16* __restrict__ gw, const u16* __restrict__ wo,
                                          char* __restrict__ ws) {
  __shared__ __attribute__((aligned(16))) char smem[K1_LDS];
  int b = blockIdx.x;
  unsigned cpf = 0;
  asm volatile("" :: "s"(x), "s"(logA), "s"(Aim), "s"(Cr), "s"(Ci), "s"(logdt), "s"(wqkv), "s"(gw), "s"(wo), "s"(ws));
  if (b < NB_KG && threadIdx.x >= 501) {
    const char* pc; int len;
    asm volatile("s_getpc_b64 %0\n\ts_mov_b32 %1, code_tail_mark-_Z2k1PKtS0_S0_S0_S0_S0_S0_S0_S0_Pc-128" : "=s"(pc), "=s"(len));
    const int off = min((int)((threadIdx.x - 501) * 30 + (b >> 3)) * 128, len);
    asm volatile("global_load_dword %0, %1, %2" : "+v"(cpf) : "v"(off), "s"(pc));
  }
  if (b < 8 && threadIdx.x >= 490 && threadIdx.x < 500) asm volatile("global_load_dword %0, %1, %2" : "+v"(cpf) : "v"((threadIdx.x - 490) * 128), "s"(gw));
  STAMP(0, 0);
  b = b < NB_KG ? b + NB_GEMM : (b < NB_KG + NB_GEMM ? b - NB_KG : b);
  if (b < NB_GEMM) {
    const int xcd = b & 7, idx = b >> 3;
    const int rowblk = (xcd >> 1) * 4 + (idx & 3), slot = idx >> 2, par = xcd & 1;
    const int cb = par * 8 + slot;
    gemm_role(smem, x, wqkv, wo, ws, rowblk, cb, slot, par);
    STAMP(0, 1);
    return;
  }
  b -= NB_GEMM;
  if (b < NB_KG) { kgen_role(smem, logA, Aim, Cr, Ci, logdt, x, wqkv, wo, gw, ws, b, cpf); STAMP(0, 1); return; }
}

constexpr int K2_LDS = 163840;
constexpr int K2_CS = 4160;
constexpr int K2_KOFF = 4 * K2_CS;
constexpr int K2_CH = K2_KOFF + 4608;
constexpr int K2_CONV = K2_LDS - 3 * K2_CH;
constexpr int K2_TAB = 81920;
constexpr int K2_FIN = K2_TAB + 9216;
constexpr int K2_KV = 102400;
constexpr int K2_STG = 131072;
constexpr int NB_KV = 32;
typedef __attribute__((address_space(1))) unsigned gu32;
__device__ __forceinline__ const char* uni(const char* p) {
  size_t v = (size_t)p;
  unsigned lo = __builtin_amdgcn_readfirstlane((unsigned)v), hi = __builtin_amdgcn_readfirstlane((unsigned)(v >> 32));
  size_t o = ((size_t)hi << 32) | lo;
  asm volatile("s_nop 4" : "+s"(o));
  return (const char*)o;
}
__device__ __forceinline__ void wait_count(char* p, unsigned target) {
  gu32* c = (gu32*)(size_t)p;
  for (int n = 0; n < (1 << 22); n++) {
    if (__hip_atomic_load(c, __ATOMIC_RELAXED, __HIP_MEMORY_SCOPE_AGENT) >= target) break;
    __builtin_amdgcn_s_sleep(8);
  }
}
#define GLD4(dst, voff, sbase, pol) asm volatile("global_load_dwordx4 %0, %1, %2" pol : "=v"(dst) : "v"(voff), "s"(sbase))
#define GLD2(dst, voff, sbase, pol) asm volatile("global_load_dwordx2 %0, %1, %2" pol : "=v"(dst) : "v"(voff), "s"(sbase))
#define GLD1(dst, voff, sbase, pol) asm volatile("global_load_dword %0, %1, %2" pol : "=v"(dst) : "v"(voff), "s"(sbase))
template <int G>
__device__ __forceinline__ void conv_unit(f32x4* acc, int ap, int bp) {
  constexpr int NT = 8 - G;
  u32x2 a0[NT], a1[NT]; s16x8 bh[NT];
#pragma unroll
  for (int j = 0; j < NT; j++)
    asm volatile("ds_read_b64 %0, %3 offset:%5\n\tds_read_b64 %1, %3 offset:%6\n\tds_read_b128 %2, %4 offset:%5"
                 : "=&v"(a0[j]), "=&v"(a1[j]), "=&v"(bh[j]) : "v"(ap), "v"(bp), "n"(512 * j), "n"(512 * j + 8) : "memory");
#pragma unroll
  for (int x = 0; x < NT; x++) {
    asm volatile("s_waitcnt lgkmcnt(%3)" : "+v"(a0[x]), "+v"(a1[x]), "+v"(bh[x]) : "n"(3 * (NT - 1 - x) > 15 ? 15 : 3 * (NT - 1 - x)) : "memory");
#pragma unroll
    for (int y = 0; y <= x; y++)
#pragma unroll
      for (int sw = 0; sw < 2; sw++) {
        if (sw && y == x) continue;
        const int j = sw ? y : x, m = sw ? x : y, T = j + m;
        const int ti = G == 0 ? (T == 0 ? 0 : T == 3 ? 1 : T == 4 ? 2 : T == 7 ? 3 : -1) : (T == 1 ? 0 : T == 2 ? 1 : T == 5 ? 2 : T == 6 ? 3 : -1);
        if (ti >= 0) acc[ti] = __builtin_amdgcn_mfma_f32_16x16x32_bf16(__builtin_bit_cast(s16x8, (u32x4){a0[j][0], a0[j][1], a1[j][0], a1[j][1]}), bh[m], acc[ti], 0, 0, 0);
      }
    SB();
  }
}

__global__ __launch_bounds__(576) void k2(const u16* __restrict__ Dskip, const u16* __restrict__ wo, char* __restrict__ ws, u16* __restrict__ out) {
  __shared__ __attribute__((aligned(16))) char smem[K2_LDS];
  const int tid = threadIdx.x, lane = tid & 63;
  const int wave = __builtin_amdgcn_readfirstlane(tid >> 6);
  const int b = blockIdx.x;
  unsigned cpf = 0;
  if (b < 32 && tid < 192) {
    const char* pc; int len;
    asm volatile("s_getpc_b64 %0\n\ts_mov_b32 %1, code_tail_mark-_Z2k2PKtS0_PcPt-128" : "=s"(pc), "=s"(len));
    const int off = min(tid * 128, len);
    asm volatile("global_load_dword %0, %1, %2" : "+v"(cpf) : "v"(off), "s"(pc));
  }
  STAMP(1, 0);
  const int lds0 = (int)(size_t)(lptr_t)smem;
  const int l16 = lane * 16;
  const int xcd = b & 7, idx = b >> 3;
  const int odd = idx & 1;
  const int chA = 80 * xcd + 5 * (idx >> 1) + 3 * odd, chH = 80 * xcd + 5 * (idx >> 1) + 2;
  const int rowblk = xcd * 4 + (idx >> 3), cb = idx & 7;
  const int l0 = rowblk * 64, c0 = cb * 80;
  const int r = lane & 31, h = lane >> 5;
  if (wave == 8) {
    __builtin_amdgcn_s_setprio(3);
    const char* wsrc = (const char*)wo + cb * 160;
    int wv[5];
#pragma unroll
    for (int j = 0; j < 5; j++) { const int f = 64 * j + lane, row = f / 10; wv[j] = row * 1280 + (f - 10 * row) * 16 - (j & 3) * 1024; }
    auto dma5 = [&](int grp) {
      asm volatile("s_mov_b32 m0, %6\n\ts_nop 0\n\tglobal_load_lds_dwordx4 %0, %5\n\tglobal_load_lds_dwordx4 %1, %5 offset:1024\n\t"
                   "global_load_lds_dwordx4 %2, %5 offset:2048\n\tglobal_load_lds_dwordx4 %3, %5 offset:3072\n\t"
                   "s_add_u32 m0, m0, 0x1000\n\ts_nop 0\n\tglobal_load_lds_dwordx4 %4, %5"
                   :: "v"(wv[0]), "v"(wv[1]), "v"(wv[2]), "v"(wv[3]), "v"(wv[4]), "s"(wsrc + grp * 40960), "s"(lds0 + grp * 5120) : "memory", "m0", "scc");
    };
    auto dma4 = [&](const char* src, int dst) {
      asm volatile("s_mov_b32 m0, %2\n\ts_nop 0\n\tglobal_load_lds_dwordx4 %0, %1\n\tglobal_load_lds_dwordx4 %0, %1 offset:1024\n\t"
                   "global_load_lds_dwordx4 %0, %1 offset:2048\n\tglobal_load_lds_dwordx4 %0, %1 offset:3072"
                   :: "v"(l16), "s"(src), "s"(lds0 + dst) : "memory", "m0");
    };
    auto kimg = [&]() {
      dma4(ws + WS_KIMG + chA * 4096, K2_CONV + K2_KOFF + 512);
      dma4(ws + WS_KIMG + chA * 4096 + 4096, K2_CONV + K2_CH + K2_KOFF + 512);
      dma4(ws + WS_KIMG + chH * 4096, K2_CONV + 2 * K2_CH + K2_KOFF + 512);
    };
    if (b < NB_KV) {
      f32x4 kvp[32];
      const char* src = ws + WS_PART + (size_t)b * 16 * 1680;
      const int o2 = min(lane + 64, 104) * 16;
#pragma unroll
      for (int i = 0; i < 16; i++) { GLD4(kvp[i], l16, src + i * 1680, ""); GLD4(kvp[16 + i], o2, src + i * 1680, ""); }
      kimg();
#pragma unroll
      for (int i = 0; i < NW0; i++) dma5(i);
      asm volatile("s_waitcnt vmcnt(%0)" :: "n"(5 * NW0) : "memory");
      BAR_ONLY();
#pragma unroll
      for (int i = 0; i < 32; i += 8)
        asm volatile("" : "+v"(kvp[i]), "+v"(kvp[i + 1]), "+v"(kvp[i + 2]), "+v"(kvp[i + 3]), "+v"(kvp[i + 4]), "+v"(kvp[i + 5]), "+v"(kvp[i + 6]), "+v"(kvp[i + 7]));
#pragma unroll
      for (int st = 1; st < 16; st <<= 1)
#pragma unroll
        for (int i = 0; i < 32; i += 2 * st) kvp[i] += kvp[i + st];
      const u32x2 o0 = {pack2(kvp[0][0], kvp[0][1]), pack2(kvp[0][2], kvp[0][3])}, o1 = {lane < 41 ? pack2(kvp[16][0], kvp[16][1]) : 0u, lane < 41 ? pack2(kvp[16][2], kvp[16][3]) : 0u};
      const int o = (int)WS_KVT + b * 896 + lane * 8;
      asm volatile("global_store_dwordx2 %0, %1, %2 sc1" :: "v"(o), "v"(o0), "s"(ws) : "memory");
      if (lane < 48) asm volatile("global_store_dwordx2 %0, %1, %2 offset:512 sc1\n\ts_nop 1" :: "v"(o), "v"(o1), "s"(ws) : "memory");
      KVST;
    } else {
      kimg();
#pragma unroll
      for (int i = 0; i < NW0; i++) dma5(i);
      asm volatile("s_waitcnt vmcnt(%0)" :: "n"(5 * NW0) : "memory");
      BAR_ONLY();
    }
#pragma unroll 1
    for (int p = NW0; p < 19; p++) {
      dma5(p);
    }
    {
      u32x4 kt[28];
      const char* kp = ws + WS_KVT;
#define KVL(pol) _Pragma("unroll") for (int i = 0; i < 28; i++) GLD4(kt[i], l16, kp + i * 1024, pol);
#define KVW() _Pragma("unroll") for (int i = 0; i < 28; i += 7) asm volatile("" : "+v"(kt[i]), "+v"(kt[i + 1]), "+v"(kt[i + 2]), "+v"(kt[i + 3]), "+v"(kt[i + 4]), "+v"(kt[i + 5]), "+v"(kt[i + 6]));
      KVL(KV1);
      VMCNT(0);
      KVW();
      for (int n = 0; n < (1 << 14); n++) {
        unsigned m = 0;
#pragma unroll
        for (int i = 0; i < 28; i++) m = max(max(max(m, kt[i][0]), kt[i][1]), max(kt[i][2], kt[i][3]));
        if (__builtin_amdgcn_ballot_w64(m == ~0u) == 0ull) break;
        KVL(" sc1");
        VMCNT(0);
        KVW();
      }
      BAR_ONLY();
#pragma unroll
      for (int i = 0; i < 14; i++) *(u32x4*)(smem + K2_KV + i * 1024 + l16) = kt[i];
      BAR_ONLY();
#pragma unroll
      for (int i = 14; i < 28; i++) *(u32x4*)(smem + K2_KV + i * 1024 + l16) = kt[i];
    }
    asm volatile("s_waitcnt lgkmcnt(0)" ::: "memory");
    BAR_ONLY();
    dma5(19);
    VMCNT(0);
    BAR_ONLY();
    {
      gu32* c = (gu32*)(size_t)(ws + WS_CNT + (8 + cb) * 128);
      for (int n = 0; n < (1 << 22); n++) {
        if (__hip_atomic_load(c, __ATOMIC_RELAXED, __HIP_MEMORY_SCOPE_AGENT) >= 32u) break;
        __builtin_amdgcn_s_sleep(1);
      }
      asm volatile("" ::: "memory");
      const int o = (lane >> 3) * (L * 2) + (((lane & 7) ^ ((lane >> 2) & 4)) << 4);
      const char* src = ws + WS_AT + (size_t)(c0 * L + l0) * 2;
#pragma unroll
      for (int i = 0; i < 10; i++)
        asm volatile("s_mov_b32 m0, %2\n\ts_nop 0\n\tglobal_load_lds_dwordx4 %0, %1 sc1" :: "v"(o), "s"(src + i * 8 * L * 2), "s"(lds0 + (i < 8 ? (i >> 1) * 3200 : 25600) + (i & 1) * 1024) : "memory", "m0");
      VMCNT(0);
    }
    BAR_ONLY();
    return;
  }
  const int t = wave & 1, kh = wave >> 1;
  u32x4 in0[2];
  int dsto[2];
#pragma unroll
  for (int rr = 0; rr < 2; rr++) {
    const int f = (wave + 8 * rr) * 49 + min(lane, 49), i = min(f, 776);
    const int ch = i / 259, xx = i - 259 * ch;
    dsto[rr] = (lane < 49 && f < 777) ? K2_CONV + ch * K2_CH + xx * 16 : -1;
    GLD4(in0[rr], (int)WS_UT + (ch == 2 ? chH : chA + ch) * (UT_STRIDE * 2) + (xx + 1) * 16, ws, "");
  }
  const int jg = wave & 1, jc = (wave >> 1) & 1, hiw = wave >> 2;
  const int fo = (lane & 15) * 16 + (lane >> 4) * 4;
  int ft[3];
  ft[0] = hiw ? 3 - jg : jg;
  ft[1] = hiw ? 4 + jg : (wave & 2) * 2 + ((wave & 1) ? 3 - odd : odd);
  ft[2] = 7 - jg;
  u32x2 us[3];
  float g_r;
  GLD1(g_r, (int)WS_GATE + (l0 + 32 * t + 16 * (kh & 1) + (lane & 15)) * 4, ws, "");
  unsigned dsA, dsH;
  asm volatile("global_load_ushort %0, %1, %2" : "=v"(dsA) : "v"((chA + jc) * 2), "s"(Dskip));
  asm volatile("global_load_ushort %0, %1, %2" : "=v"(dsH) : "v"((hiw ? chA + jc : chH) * 2), "s"(Dskip));
  u32x4 pf[8][2];
  const char* pqp = ws + WS_PQ + (size_t)((rowblk * 32 + kh * 8) * 4 + 2 * t) * 640;
  const int pqo = lane < 32 ? 128 + l16 : (lane & 15) * 8;
#define PQL(i0, i1) _Pragma("unroll") for (int i = i0; i < i1; i++) { GLD4(pf[i][0], pqo, pqp + i * 2560, ""); GLD4(pf[i][1], pqo, pqp + i * 2560, " offset:640"); }
  if (tid < 96) *(u32x4*)(smem + K2_CONV + (tid >> 5) * K2_CH + K2_KOFF + (tid & 31) * 16) = (u32x4){0u, 0u, 0u, 0u};
  asm volatile("s_waitcnt vmcnt(3)" : "+v"(in0[0]), "+v"(in0[1]), "+v"(cpf) :: "memory");
  STAMP(1, 2);
#pragma unroll
  for (int rr = 0; rr < 2; rr++) {
    const u32x4 a = in0[rr];
    unsigned w[6] = {a[0], a[1], a[2], a[3], (unsigned)__builtin_amdgcn_update_dpp(0, (int)a[0], 0x130, 0xf, 0xf, false),
                     (unsigned)__builtin_amdgcn_update_dpp(0, (int)a[1], 0x130, 0xf, 0xf, false)};
    unsigned o[5];
#pragma unroll
    for (int k = 0; k < 5; k++) o[k] = __builtin_amdgcn_alignbyte(w[k + 1], w[k], 2);
    if (dsto[rr] >= 0) {
      char* p = smem + dsto[rr];
#pragma unroll
      for (int q = 0; q < 2; q++) {
        *(u32x4*)(p + (2 * q) * K2_CS) = (u32x4){o[q], o[q + 1], o[q + 2], o[q + 3]};
        *(u32x4*)(p + (2 * q + 1) * K2_CS) = (u32x4){w[q + 1], w[q + 2], w[q + 3], w[q + 4]};
      }
    }
  }
  RAW_BARRIER();
  PQL(0, PQA);
  STAMP(1, 3);
  f32x4 ca[4], sv[4];
#pragma unroll
  for (int i = 0; i < 4; i++) { ca[i] = (f32x4){0.f, 0.f, 0.f, 0.f}; sv[i] = (f32x4){0.f, 0.f, 0.f, 0.f}; }
  {
    const int i = lane & 15, kg = lane >> 4, e = kg >> 1, bb = kg & 1;
    const int ap = lds0 + K2_CONV + (i & 3) * K2_CS + (2 * e - bb + 2) * 16 + (i >> 2) * 8;
    const int bp = lds0 + K2_CONV + K2_KOFF + (16 + i - e) * 32 + bb * 16;
    int apj = ap + jc * K2_CH + hiw * 192;
    int bpj = bp + jc * K2_CH - hiw * 192;
    if (jg == 0) {
#pragma unroll 1
      for (int it = 0; it < 3; it++) conv_unit<0>(ca, apj + it * 64, bpj - it * 64);
    } else {
#pragma unroll 1
      for (int it = 0; it < 3; it++) conv_unit<1>(ca, apj + it * 64, bpj - it * 64);
    }
    PQL(PQA, PQB);
    apj += 192; bpj -= 192;
    int g2 = jg;
    if (!hiw) {
#pragma unroll
      for (int q = 0; q < 4; q++) { sv[q] = ca[q]; ca[q] = (f32x4){0.f, 0.f, 0.f, 0.f}; }
      apj = ap + 2 * K2_CH + wave * 128; bpj = bp + 2 * K2_CH - wave * 128; g2 = odd;
    }
    if (g2 == 0) {
#pragma unroll 1
      for (int it = 0; it < 2; it++) conv_unit<0>(ca, apj + it * 64, bpj - it * 64);
    } else {
#pragma unroll 1
      for (int it = 0; it < 2; it++) conv_unit<1>(ca, apj + it * 64, bpj - it * 64);
    }
  }
  STAMP(1, 4);
  {
    const char* ua = smem + K2_CONV + 3 * K2_CS + (20 + fo) * 2;
    us[0] = *(const u32x2*)(ua + jc * K2_CH + ft[0] * 512); us[1] = *(const u32x2*)(ua + (hiw ? jc : 2) * K2_CH + ft[1] * 512);
    us[2] = *(const u32x2*)(ua + jc * K2_CH + ft[2] * 512);
  }
  PQL(PQB, 8);
  RAW_BARRIER();
  {
    if (tid == 0) *(unsigned*)(smem + K2_LDS - 16) = 0u;
    char* sp = smem + K2_STG + l16;
    if (hiw) {
      *(f32x4*)(sp + (wave & 3) * 4096) = ca[0];
    } else {
#pragma unroll
      for (int i = 1; i < 4; i++) *(f32x4*)(sp + wave * 4096 + i * 1024) = sv[i];
#pragma unroll
      for (int i = 0; i < 4; i++) if (i != wave) *(f32x4*)(sp + 16384 + (i * 3 + ((wave - i - 1) & 3)) * 1024) = ca[i];
    }
  }
  RAW_BARRIER();
  {
    const char* sp = smem + K2_STG + l16;
    asm volatile("s_waitcnt vmcnt(16)" : "+v"(g_r), "+v"(dsA), "+v"(dsH) :: "memory");
    const float dskA = lo_f(dsA), dskH = lo_f(dsH);
    auto fin = [&](f32x4 s, u32x2 uu, float dsk, int ch, int tile) {
      u32x2 ov;
#pragma unroll
      for (int e = 0; e < 2; e++) {
        unsigned yy = pack2(s[2 * e], s[2 * e + 1]);
        unsigned k = pack2(lo_f(uu[e]) * dsk, hi_f(uu[e]) * dsk);
        ov[e] = pack2(lo_f(yy) + lo_f(k), hi_f(yy) + hi_f(k));
      }
      const int o = (int)WS_AT + (ch * L + tile * 256 + fo) * 2;
      asm volatile("global_store_dwordx2 %0, %1, %2 sc1\n\ts_nop 1" :: "v"(o), "v"(ov), "s"(ws) : "memory");
    };
    if (hiw) {
      const char* pp = sp + (wave & 3) * 4096;
#pragma unroll
      for (int i = 1; i < 4; i++) fin(ca[i] + *(const f32x4*)(pp + i * 1024), us[i - 1], dskA, chA + jc, ft[i - 1]);
      VMCNT(3);
    } else {
      const char* hp = sp + 16384 + wave * 3072;
      const f32x4 own = wave == 0 ? ca[0] : wave == 1 ? ca[1] : wave == 2 ? ca[2] : ca[3];
      fin(sv[0] + *(const f32x4*)(sp + wave * 4096), us[0], dskA, chA + jc, ft[0]);
      fin((own + *(const f32x4*)(hp + 1024)) + (*(const f32x4*)hp + *(const f32x4*)(hp + 2048)), us[1], dskH, chH, ft[1]);
      VMCNT(2);
    }
  }
#pragma unroll
  for (int i = 0; i < 8; i++) asm volatile("" : "+v"(pf[i][0]), "+v"(pf[i][1]));
  RAW_BARRIER();
  STAMP(1, 5);
  const int n16 = lane & 15, g = lane >> 4;
  f32x4 acc[5][2];
  {
    const char* kp = smem + K2_KV + kh * 8 * 896;
    const int row1 = (n16 & 3) ? 20 : 16 + (n16 >> 2);
    const char* k00 = kp + (g < 3 ? n16 * 40 + 16 * g : 840);
    const char* k01 = kp + (g < 2 ? n16 * 40 + 16 * g + 8 : 840);
    const char* k10 = kp + (g < 3 ? row1 * 40 + 16 * g : 840);
    const char* k11 = kp + (g < 2 ? row1 * 40 + 16 * g + 8 : 840);
    const char* wm = smem + (160 * kh + 4 * g + (n16 >> 2)) * 160 + (lane & 3) * 8;
    const char* wt = smem + (160 * kh + 20 * (n16 >> 2) + 16 + g) * 160 + (lane & 3) * 8;
    u32x4 ka[8][2];
    auto rdK = [&](int i) {
      const u32x2 a0 = *(const u32x2*)(k00 + i * 896), a1 = *(const u32x2*)(k01 + i * 896), b0 = *(const u32x2*)(k10 + i * 896), b1 = *(const u32x2*)(k11 + i * 896);
      ka[i][0] = (u32x4){a0[0], a0[1], a1[0], a1[1]}; ka[i][1] = (u32x4){b0[0], b0[1], b1[0], b1[1]};
    };
    f32x4 c[2][2][2];
    const f32x4 z4 = {0.f, 0.f, 0.f, 0.f};
    auto stageA = [&](int i) {
#pragma unroll
      for (int tl = 1; tl >= 0; tl--)
#pragma unroll
        for (int nt = 0; nt < 2; nt++)
          c[i & 1][tl][nt] = __builtin_amdgcn_mfma_f32_16x16x32_bf16(__builtin_bit_cast(s16x8, ka[i][tl]), __builtin_bit_cast(s16x8, pf[i][nt]), z4, 0, 0, 0);
    };
    auto wfrag = [&](const char* p, int d) {
      const s16x4 lo = __builtin_amdgcn_ds_read_tr16_b64_v4i16((lds_s16x4_p)(p)), hi = __builtin_amdgcn_ds_read_tr16_b64_v4i16((lds_s16x4_p)(p + d));
      return __builtin_shufflevector(lo, hi, 0, 1, 2, 3, 4, 5, 6, 7);
    };
    u32x4 ob[2][2], o5[2];
    float p5[2];
    s16x8 w[3];
    auto proj = [&](int s, int q) {
      if (s < 0) return;
      const u32x4 bb = s < 4 ? ob[s & 1][q & 1] : o5[q & 1];
      acc[q >> 1][q & 1] = __builtin_amdgcn_mfma_f32_16x16x32_bf16(w[(q >> 1) % 3], __builtin_bit_cast(s16x8, bb), s == 0 ? z4 : acc[q >> 1][q & 1], 0, 0, 0);
    };
    auto rdW = [&](int s, int mt) { w[mt % 3] = s < 4 ? wfrag(wm + s * 6400 + mt * 32, 3200) : wfrag(wt + mt * 32, 12800); };
    auto sA = [&](int i, int q) {
      if (i < 8) c[i & 1][1 - (q >> 1)][q & 1] = __builtin_amdgcn_mfma_f32_16x16x32_bf16(__builtin_bit_cast(s16x8, ka[i][1 - (q >> 1)]), __builtin_bit_cast(s16x8, pf[i][q & 1]), z4, 0, 0, 0);
    };
    rdK(0);
    stageA(0);
    SB();
#pragma unroll
    for (int i = 0; i < 8; i++) {
      const int s = (i >> 1) - 1, q0 = (i & 1) * 5;
      if (i + 1 < 8 && i <= B6I) rdK(i + 1);
      if (i == B6I) { _Pragma("unroll") for (int j = i + 2; j < 8; j++) rdK(j); }
      SB();
      const f32x4 (&cc)[2][2] = c[i & 1];
      proj(s, q0);
      unsigned dz = pack2(cc[1][0][1], cc[1][1][1]);
      float d0 = lo_f(dz), d1 = hi_f(dz);
      asm volatile("" : "+v"(d0), "+v"(d1));
      SB();
      proj(s, q0 + 1);
      if (s >= 0 && !(i & 1)) rdW(s, 3);
      if (i & 1) rdW(s + 1, 2);
      dz = pack2(d0 + 1e-6f, d1 + 1e-6f);
      d0 = lo_f(dz);
      asm volatile("" : "+v"(d0), "+v"(dz));
      SB();
      proj(s, q0 + 2);
      float inv[2] = {__builtin_amdgcn_rcpf(d0), __builtin_amdgcn_rcpf(hi_f(dz))};
      asm volatile("" : "+v"(inv[0]), "+v"(inv[1]));
      SB();
      proj(s, q0 + 3);
      if (s >= 0 && !(i & 1)) rdW(s, 4);
      if (i & 1) rdW(s + 1, 0);
      f32x4 v0 = cc[0][0] * inv[0];
      float q50 = cc[1][0][0] * inv[0];
      asm volatile("" : "+v"(v0), "+v"(q50));
      SB();
      proj(s, q0 + 4);
      f32x4 v1 = cc[0][1] * inv[1];
      float q51 = cc[1][1][0] * inv[1];
      asm volatile("" : "+v"(v1), "+v"(q51));
      SB();
      sA(i + 1, 0);
      if (i & 1) rdW(s + 1, 1);
      ob[(i >> 1) & 1][0][2 * (i & 1)] = pack2(v0[0], v0[1]); ob[(i >> 1) & 1][0][2 * (i & 1) + 1] = pack2(v0[2], v0[3]);
      SB();
      sA(i + 1, 1);
      ob[(i >> 1) & 1][1][2 * (i & 1)] = pack2(v1[0], v1[1]); ob[(i >> 1) & 1][1][2 * (i & 1) + 1] = pack2(v1[2], v1[3]);
      SB();
      sA(i + 1, 2);
      if (i & 1) { o5[0][i >> 1] = pack2(p5[0], q50); o5[1][i >> 1] = pack2(p5[1], q51); } else { p5[0] = q50; p5[1] = q51; }
      SB();
      sA(i + 1, 3);
      SB();
      if (i == SIGI) {
        VMCNT(0);
        unsigned old = 0;
        if (lane == 0) old = __hip_atomic_fetch_add((unsigned*)(smem + K2_LDS - 16), 1u, __ATOMIC_RELAXED, __HIP_MEMORY_SCOPE_WORKGROUP);
        if (lane == 0 && old == 7u) __hip_atomic_fetch_add((gu32*)(size_t)(ws + WS_CNT + (8 + xcd) * 128), 1u, __ATOMIC_RELAXED, __HIP_MEMORY_SCOPE_AGENT);
      }
      if (i == B6I) { RAW_BARRIER(); }
    }
    s16x8 w2[7];
#pragma unroll
    for (int j = 0; j < 7; j++) w2[j] = j < 2 ? wfrag(wm + 3 * 6400 + (3 + j) * 32, 3200) : wfrag(wt + (j - 2) * 32, 12800);
    SB();
#pragma unroll
    for (int q = 0; q < 6; q++) proj(3, q);
#pragma unroll
    for (int q = 6; q < 20; q++) {
      const int mt = q < 10 ? q >> 1 : (q - 10) >> 1;
      acc[mt][q & 1] = __builtin_amdgcn_mfma_f32_16x16x32_bf16(w2[(q - 6) >> 1], __builtin_bit_cast(s16x8, q < 10 ? ob[1][q & 1] : o5[q & 1]), acc[mt][q & 1], 0, 0, 0);
    }
  }
  STAMP(1, 6);
  {
    char* sp = smem + 102400 + t * 30720 + l16;
#pragma unroll
    for (int q = 0; q < 10; q++) {
      const int own = q & 3;
      if (kh != own) *(f32x4*)(sp + (q * 3 + ((kh - own - 1) & 3)) * 1024) = acc[q >> 1][q & 1];
    }
  }
  RAW_BARRIER();
  STAMP(1, 7);
  {
    const char* sp = smem + 102400 + t * 30720 + l16;
    const float g1 = rb(1.f - g_r);
    const char* atp = smem + (kh >> 1) * 3200 + (4 * g + (n16 >> 2)) * 128 + (((32 * t + 16 * (kh & 1) + 4 * (lane & 3)) * 2) ^ ((lane & 8) << 3));
    u16* op = out + (size_t)(l0 + 32 * t + 16 * (kh & 1) + n16) * D + c0 + 16 * (kh >> 1) + 4 * g;
#pragma unroll
    for (int tl = 0; tl < 3; tl++) {
      if (tl == 2 && kh >= 2) break;
      f32x4 m;
      if (kh == 0)      m = acc[2 * tl][0];
      else if (kh == 1) m = acc[2 * tl][1];
      else if (kh == 2) m = acc[tl < 2 ? 2 * tl + 1 : 0][0];
      else              m = acc[tl < 2 ? 2 * tl + 1 : 0][1];
      const char* pp = sp + (tl * 4 + kh) * 3072;
      f32x4 p0 = *(const f32x4*)pp, p1 = *(const f32x4*)(pp + 1024), p2 = *(const f32x4*)(pp + 2048);
      f32x4 v = (m + p1) + (p0 + p2);
      s16x4 at = __builtin_amdgcn_ds_read_tr16_b64_v4i16((lds_s16x4_p)(atp + (tl < 2 ? tl * 6400 : 25600)));
      u32x2 au = __builtin_bit_cast(u32x2, at);
      u32x2 ov;
#pragma unroll
      for (int e = 0; e < 2; e++) {
        unsigned bq = pack2(v[2 * e], v[2 * e + 1]);
        unsigned gb = pack2(g1 * lo_f(bq), g1 * hi_f(bq));
        unsigned ga = pack2(g_r * lo_f(au[e]), g_r * hi_f(au[e]));
        ov[e] = pack2(lo_f(ga) + lo_f(gb), hi_f(ga) + hi_f(gb));
      }
      *(u32x2*)(op + 32 * tl) = ov;
    }
  }
  asm volatile("code_tail_mark:" ::: "memory");
  STAMP(1, 1);
}

extern "C" void kernel_launch(void* const* d_in, const int* in_sizes, int n_in, void* d_out, int out_size, void* d_ws, size_t ws_size,
                              hipStream_t stream) {
  const u16* u = (const u16*)d_in[0]; const u16* logA = (const u16*)d_in[1]; const u16* Aim = (const u16*)d_in[2];
  const u16* Cr = (const u16*)d_in[3]; const u16* Ci = (const u16*)d_in[4]; const u16* logdt = (const u16*)d_in[5];
  const u16* Dsk = (const u16*)d_in[6]; const u16* wqkv = (const u16*)d_in[8]; const u16* wo = (const u16*)d_in[9];
  const u16* gw = (const u16*)d_in[10];
  char* ws = (char*)d_ws;
  k1<<<NB_GEMM + NB_GATE + NB_KG, 512, 0, stream>>>(u, logA, Aim, Cr, Ci, logdt, wqkv, gw, wo, ws);
  k2<<<256, 576, 0, stream>>>(Dsk, wo, ws, (u16*)d_out);
}
```

```cpp
#include <hip/hip_runtime.h>
#include <type_traits>
#include <hip/hip_bf16.h>
#include <stdint.h>

typedef unsigned short u16;
typedef __attribute__((ext_vector_type(8))) short s16x8;
typedef __attribute__((ext_vector_type(4))) short s16x4;
typedef __attribute__((ext_vector_type(4))) float f32x4;
typedef __attribute__((ext_vector_type(16))) float f32x16;
typedef __attribute__((ext_vector_type(4))) unsigned u32x4;
typedef __attribute__((ext_vector_type(2))) unsigned u32x2;
typedef __attribute__((address_space(3))) s16x4* lds_s16x4_p;
typedef const __attribute__((address_space(1))) void* gptr_t;
typedef __attribute__((address_space(3))) void* lptr_t;

constexpr int L = 2048, D = 640, NS = 192, NH = 32;
constexpr int UT_PAD = 32, UT_STRIDE = L + UT_PAD;
constexpr size_t WS_UT   = 0;
constexpr size_t WS_KIMG = WS_UT + (size_t)D * UT_STRIDE * 2;
constexpr size_t WS_PQ   = WS_KIMG + (size_t)D * 4096;
constexpr size_t WS_PART = WS_PQ + (size_t)L * D * 2;
constexpr size_t WS_GATE = WS_PART + (size_t)32 * 16 * 1680;
constexpr size_t WS_KVT  = WS_GATE + (size_t)L * 4;
constexpr size_t WS_AT   = WS_KVT + (size_t)32 * 1024;
constexpr size_t WS_CNT  = WS_AT + (size_t)D * L * 2;
constexpr size_t WS_END  = WS_CNT + 4096;
#define TGET(v) do {} while (0)
#define STEPT(i) do {} while (0)
#define TPUT(kid, slot, thr, v) do {} while (0)
#define STAMP(kid, slot) do {} while (0)

__device__ __forceinline__ float b2f(u16 v) { return __uint_as_float(((unsigned)v) << 16); }
typedef __bf16 bf16x2_t __attribute__((ext_vector_type(2)));
typedef float f32x2 __attribute__((ext_vector_type(2)));
__device__ __forceinline__ unsigned pack2(float a, float b) { f32x2 v = {a, b}; bf16x2_t r = __builtin_convertvector(v, bf16x2_t); return __builtin_bit_cast(unsigned, r); }
__device__ __forceinline__ float lo_f(unsigned u) { return __uint_as_float(u << 16); }
__device__ __forceinline__ float hi_f(unsigned u) { return __uint_as_float(u & 0xffff0000u); }
__device__ __forceinline__ float rb(float f) { return lo_f(pack2(f, f)); }
__device__ __forceinline__ float rb1(float f) { return __uint_as_float(pack2(0.f, f)); }
typedef unsigned short u16x2 __attribute__((ext_vector_type(2)));
__device__ __forceinline__ unsigned phi2(float a, float b, f32x2 kl) {
  const f32x2 t = {rb1(a), rb1(b)};
  const f32x2 x = t * kl, p = t + (f32x2){1.f, 1.f};
  const float e0 = __builtin_amdgcn_fmed3f(__builtin_amdgcn_exp2f(x[0]), 0.f, 1.f);
  const float e1 = __builtin_amdgcn_fmed3f(__builtin_amdgcn_exp2f(x[1]), 0.f, 1.f);
  return pack2(__builtin_fmaxf(p[0], e0), __builtin_fmaxf(p[1], e1));
}
__device__ __forceinline__ unsigned phi_eps(unsigned r) { return pack2(lo_f(r) + 1e-6f, hi_f(r) + 1e-6f); }
#define VMCNT(n) asm volatile("s_waitcnt vmcnt(" #n ")" ::: "memory")
#define RAW_BARRIER() do { asm volatile("s_waitcnt lgkmcnt(0)" ::: "memory"); __builtin_amdgcn_s_barrier(); asm volatile("" ::: "memory"); } while (0)
__device__ __forceinline__ void glds16(const void* g, void* l) { __builtin_amdgcn_global_load_lds((gptr_t)g, (lptr_t)l, 16, 0, 0); }

#define OPQ_S(v) asm volatile("" : "+s"(v))
typedef f32x2 cf;
__device__ __forceinline__ float smul(float a, float b) { float o; asm("v_mul_f32 %0, %1, %2" : "=v"(o) : "v"(a), "v"(b)); return o; }
__device__ __forceinline__ float sfma(float a, float b, float c) { float o; asm("v_fma_f32 %0, %1, %2, %3" : "=v"(o) : "v"(a), "v"(b), "v"(c)); return o; }
__device__ __forceinline__ float sfnma(float a, float b, float c) { float o; asm("v_fma_f32 %0, -%1, %2, %3" : "=v"(o) : "v"(a), "v"(b), "v"(c)); return o; }
__device__ __forceinline__ float ssub(float a, float b) { float o; asm("v_sub_f32 %0, %1, %2" : "=v"(o) : "v"(a), "v"(b)); return o; }
__device__ __forceinline__ cf cmul(cf a, cf b) {
  cf o;
  asm("v_pk_mul_f32 %0, %1, %2 op_sel:[0,0] op_sel_hi:[0,1]\n\t"
      "v_pk_fma_f32 %0, %1, %2, %0 op_sel:[1,1,0] op_sel_hi:[1,0,1] neg_lo:[1,0,0]" : "=&v"(o) : "v"(a), "v"(b));
  return o;
}

#define CPF 3
#define LPF 1
#define PFD 0
#define PFM_ 1
#define NP0 5
#define SPRIO 3
#define NI0 208
#define NB2 3
#define TB0 1
#define TBD 2
#define MIDX ((rowblk & 3) + 4 * (cb & 7))
#define MPS 3
#define PT0 30
#define PT1 110
#define PTD 20
constexpr int K1_LDS = 81920;
#define GLDS 1
constexpr int NB_GEMM = 256, NB_GATE = GLDS ? 0 : 16, NB_KG = 240, NB_KC = 214;
#define KVAT 0
#define KV1 ""
#define KVB 0
#define ROT 0
#define TPQ 0
#define KVST
#define STG 0
#define SA0 8
#define SB0 8
#define SA1 0
#define SB1 8
#define WOT 1
#define KRR 1
#define B6I 5
#define WE 3
#define SGB 1
#define XB 1
#define UTE 0
#define NE 48
#define MPRIO 1
#define EPRIO 1
#define W7P 2
#define GSL 100
#define GPRIO 0
#define TPOL ""
#define SIGI 2
#define NW0 3
#define K2PF 1
#define K2PFS "1"
#define KVP 1
#define PQA 2
#define PQB 4
#define STR2(x) #x
#define STR(x) STR2(x)
#define PQBS "2*(" STR(PQB) "-" STR(PQA) ")"
#define WTQ 0
#define WTP 0
#define WTO 0
#define WT 1
__device__ __forceinline__ void st16(void* p, u32x4 v) {
  asm volatile("global_store_dwordx4 %0, %1, off sc1\n\ts_nop 2" :: "v"(p), "v"(v) : "memory");
}

#define BAR_ONLY() do { asm volatile("" ::: "memory"); __builtin_amdgcn_s_barrier(); asm volatile("" ::: "memory"); } while (0)
#define SB() __builtin_amdgcn_sched_barrier(0)
template <bool ISQ>
__device__ __forceinline__ void mfma_part(char* smem, char* __restrict__ ws, int wave, f32x4 (&ep)[2][4], int zc) {
  const int lane = threadIdx.x & 63;
  char* sB = smem; char* sA = smem + 32768;
  const int wr = wave & 1, wc = (wave >> 1) & 1;
  const int g = lane >> 4, fr = lane & 15;
  f32x4 acc[4][4];
#pragma unroll
  for (int m = 0; m < 4; m++)
#pragma unroll
    for (int c = 0; c < 4; c++) acc[m][c] = (f32x4){0.f, 0.f, 0.f, 0.f};
  const int q4 = fr >> 2, p4 = lane & 3;
  int aoff[2];
#pragma unroll
  for (int ks = 0; ks < 2; ks++) aoff[ks] = (wr * 64 + fr) * 128 + (((4 * ks + g) ^ (fr & 7)) << 4);
  int boff[4];
  {
    const int f = q4 | ((g & 1) << 2);
#pragma unroll
    for (int c = 0; c < 4; c++) boff[c] = (8 * g + q4) * 256 + (((4 * wc + c) ^ f) << 5) + p4 * 8;
  }
  s16x8 af[2][4]; s16x4 bl[4], bh[4];
  auto rdA = [&](int buf, int m, const char* pa) { af[buf][m] = *(const s16x8*)(pa + m * 2048); };
  auto rdBl = [&](int c, const char* pb) { bl[c] = __builtin_amdgcn_ds_read_tr16_b64_v4i16((lds_s16x4_p)(pb + boff[c])); };
  auto rdBh = [&](int c, const char* pb) { bh[c] = __builtin_amdgcn_ds_read_tr16_b64_v4i16((lds_s16x4_p)(pb + boff[c] + 1024)); };
  __builtin_amdgcn_s_setprio(MPRIO);
  BAR_ONLY();
  STAMP(0, 2);
#pragma unroll
  for (int c = 0; c < 4; c++) rdA(0, c, sA + aoff[0]);
#pragma unroll
  for (int c = 0; c < 3; c++) { rdBl(c, sB); rdBh(c, sB); }
  int ao0 = 0, ao1 = 16384;
  auto grp = [&](int buf, int c, int m) {
    const s16x8 bf = __builtin_shufflevector(bl[c], bh[c], 0, 1, 2, 3, 4, 5, 6, 7);
    if (ISQ) acc[m][c] = __builtin_amdgcn_mfma_f32_16x16x32_bf16(bf, af[buf][m], acc[m][c], 0, 0, 0);
    else     acc[m][c] = __builtin_amdgcn_mfma_f32_16x16x32_bf16(af[buf][m], bf, acc[m][c], 0, 0, 0);
  };
#pragma unroll 1
  for (int j2 = 0; j2 < 5; j2++) {
#pragma unroll
    for (int st = 0; st < 4; st++) {
      const int hs = st & 1;
      SB();
      asm volatile("s_waitcnt lgkmcnt(2)" ::: "memory"); __builtin_amdgcn_s_barrier(); asm volatile("" ::: "memory");
      SB();
      const char* pa = sA + (hs == 0 ? ao0 + aoff[1] : ao1 + aoff[0]);
      const char* pc = sB + st * 8192;
      const char* pb = sB + ((st + 1) & 3) * 8192;
      grp(hs, 0, 0); SB(); rdBl(3, pc); rdBh(3, pc); SB();
      grp(hs, 0, 1); SB(); rdA(hs ^ 1, 0, pa); rdA(hs ^ 1, 1, pa); SB();
      grp(hs, 0, 2); SB(); rdA(hs ^ 1, 2, pa); rdA(hs ^ 1, 3, pa); SB();
      grp(hs, 0, 3); SB();
#pragma unroll
      for (int c = 1; c < 4; c++) {
        grp(hs, c, 0); SB(); rdBl(c - 1, pb); rdBh(c - 1, pb); SB();
        grp(hs, c, 1); grp(hs, c, 2); grp(hs, c, 3); SB();
      }
      if (hs == 1) { ao0 = ao1; ao1 = ao1 == 32768 ? 0 : ao1 + 16384; }
    }
  }
  STAMP(0, 3);
  int l3 = threadIdx.x; asm volatile("" : "+v"(l3));
  char* xch = smem + 32768 + (l3 >> 6) * 8192 + (l3 & 63) * 16;
  if (ISQ || wc == 0) {
#pragma unroll
    for (int i = 0; i < 8; i++) *(f32x4*)(xch + i * 1024) = acc[2 + (i >> 2)][i & 3];
  }
#pragma unroll
  for (int i = 0; i < 8; i++) ep[i >> 2][i & 3] = acc[i >> 2][i & 3];
  RAW_BARRIER();
  if (!ISQ && wc == 1) {
    char* fv = smem + 16384 + wr * 8192 + (l3 & 63) * 16;
#pragma unroll
    for (int c = 0; c < 4; c++)
#pragma unroll
      for (int h = 0; h < 2; h++) {
        u32x4 a = {pack2(acc[2 * h][c][0], acc[2 * h][c][1]), pack2(acc[2 * h][c][2], acc[2 * h][c][3]),
                   pack2(acc[2 * h + 1][c][0], acc[2 * h + 1][c][1]), pack2(acc[2 * h + 1][c][2], acc[2 * h + 1][c][3])};
        if ((c == 0 || c == 3) && 16 * c + (l3 & 15) == zc) a = (u32x4){0x3F803F80u, 0x3F803F80u, 0x3F803F80u, 0x3F803F80u};
        *(u32x4*)(fv + h * 4096 + c * 1024) = a;
      }
  }
}
__device__ __forceinline__ void dma4x(const int (&vo)[4], const char* sbase, int ldsa) {
  asm volatile("s_mov_b32 m0, %5\n\ts_nop 0\n\tglobal_load_lds_dwordx4 %0, %4\n\tglobal_load_lds_dwordx4 %1, %4 offset:1024\n\t"
               "global_load_lds_dwordx4 %2, %4 offset:2048\n\tglobal_load_lds_dwordx4 %3, %4 offset:3072"
               :: "v"(vo[0]), "v"(vo[1]), "v"(vo[2]), "v"(vo[3]), "s"(sbase), "s"(ldsa) : "memory", "m0");
}
__device__ __forceinline__ unsigned touch_off(int gb, int q, int& r) {
  const int par = gb & 1, pidx = gb >> 3;
  const int stg = (q * 1366) >> 16, q15 = q & 15;
  r = q - 48 * stg;
  const int ln = q15 + (par ? (q15 < 4 ? 6 : q15 < 10 ? 10 : 14) : (q15 < 6 ? 0 : q15 < 11 ? 4 : 9));
  const unsigned ox = (unsigned)((((gb >> 1) & 3) * 512 + pidx * 16 + q15) * 1280 + stg * 128);
  const unsigned ow = (unsigned)((64 * stg + pidx * 2 + (r >> 4) - 1) * 3840 + ln * 128);
  return r < 16 ? ox : ow;
}
#define TOUCH_LDS(p, la) asm volatile("s_mov_b32 m0, %1\n\ts_nop 0\n\tglobal_load_lds_dword %0, off" TPOL :: "v"(p), "s"(la) : "memory", "m0")
__device__ __forceinline__ void gemm_role(char* smem, const u16* __restrict__ x, const u16* __restrict__ W, const u16* __restrict__ wo, char* __restrict__ ws,
                                          int rowblk, int cb, int slot, int par) {
  const int tid = threadIdx.x, lane = tid & 63;
  const int wave = __builtin_amdgcn_readfirstlane(tid >> 6);
  const bool ISQ = cb < 5;
  const int row0 = rowblk * 128, col0 = cb * 128;
  const int kvj = cb - 5;
  const int wr = wave & 1, wc = (wave >> 1) & 1, hf = wave >> 2;
  f32x4 ep[2][4];
  unsigned pf3 = 0;
  const int wce = wc ^ hf;
  const int o = (60 * kvj) & 7, zc = o ? 0 : 60;
  unsigned eo[2];
  int ta[2], tb[2];
  auto eoffs = [&](bool isq) {
    int l4 = threadIdx.x; asm volatile("" : "+v"(l4));
    const unsigned g = (l4 >> 4) & 3, fr = l4 & 15;
    if (isq) {
#pragma unroll
      for (int c = 0; c < 4; c++) {
        const unsigned q4 = (unsigned)(col0 / 4 + 16 * wce + 4 * c) + g;
        const unsigned hd = (q4 * 205u) >> 10, d4 = q4 - 5u * hd;
        const unsigned off = hd * 320u + (d4 < 4u ? 16u + ((d4 * 17u) & 0x21u) + fr * 2u : fr);
        if (c & 1) eo[c >> 1] |= off << 16; else eo[c >> 1] = off;
      }
      ta[0] = ta[1] = tb[0] = tb[1] = 0;
    } else {
      const unsigned long long TA = o ? 0x323322211100ull : 0x103322211100ull, TB = o ? 0x003232121010ull : 0x333232121010ull;
#pragma unroll
      for (int i = 0; i < 2; i++) {
        const int p = wave < 4 ? wave : wave * 2 - 4 + i;
        ta[i] = (int)((TA >> (4 * p)) & 15); tb[i] = (int)((TB >> (4 * p)) & 15);
        const int kc0 = 16 * ta[i] + 4 * (int)g - o;
        const int hk = (kc0 >= 20) + (kc0 >= 40), dk0 = kc0 - 20 * hk;
        const int vcol = 16 * tb[i] + (int)fr;
        const int dv = vcol == zc ? 20 : vcol - o - 20 * hk;
        const bool ok = (kc0 >= 0) & (kc0 < (kvj == 10 ? 40 : 60)) & (dv >= 0) & ((dv < 20) | (vcol == zc)) & (i == 0 || wave >= 4);
        eo[i] = ok ? (unsigned)((hk * 16 * 21 + dv) * 80 + dk0 * 4) : ~0u;
      }
    }
    asm volatile("" : "+v"(eo[0]), "+v"(eo[1]));
    SB();
  };
  if (wave >= 4) {
    const int lw = wave & 1;
    const int l0 = (int)(size_t)(lptr_t)smem;
    const int pidx = (rowblk & 3) + 4 * slot;
    __builtin_amdgcn_s_setprio(SPRIO);
    if (wave < 6) {
      int voA[4], voB[4];
#pragma unroll
      for (int i = 0; i < 4; i++) {
        voA[i] = (64 * lw + 8 * i + (lane >> 3)) * 1280 + (((lane & 7) ^ (lane >> 3)) << 4) - 1024 * i;
        const int fB = (lane >> 4) | ((i >> 1) << 2);
        const int c16 = (((lane & 15) >> 1) ^ fB) * 2 + (lane & 1);
        int bcol = col0 + 8 * c16;
        if (!ISQ) bcol = 640 + 640 * (c16 >> 3) + min(((60 * kvj) & ~7) + 8 * (c16 & 7), 632);
        voB[i] = (16 * lw + 4 * i + (lane >> 4)) * 3840 + bcol * 2 - 1024 * i;
      }
      const char* pA = (const char*)x + row0 * 1280;
      const char* pB = (const char*)W;
      const int lA = l0 + 32768 + lw * 8192, lB = l0 + lw * 4096;
      auto issA = [&](int slot_off, int h) { dma4x(voA, pA + h * 32 * 1280, lA + slot_off + h * 4096); if (h) pA += 128; };
      auto issB = [&](int slot_off) { dma4x(voB, pB, lB + slot_off); pB += 32 * 3840; };
      TGET(tl0);
      VMCNT(0);
      TGET(tl1);
      issA(0, 0); issB(0); issA(0, 1);
      issB(8192); issA(16384, 0);
      VMCNT(8);
      TGET(tl2);
      BAR_ONLY();
      issB(16384); issA(16384, 1);
      VMCNT(12); BAR_ONLY();
      issB(24576); issA(32768, 0); issA(32768, 1);
      VMCNT(12); BAR_ONLY();
      issB(0); issA(0, 0);
      int ao = 0, bo = 8192;
#pragma unroll 1
      for (int j = 1; j < 7; j++) {
        VMCNT(12); BAR_ONLY();
        issB(bo); bo = (bo + 8192) & 32767; issA(ao, 1);
        ao = ao == 32768 ? 0 : ao + 16384;
        VMCNT(12); BAR_ONLY();
        issB(bo); bo = (bo + 8192) & 32767; issA(ao, 0);
      }
      VMCNT(12); BAR_ONLY();
      issB(8192); issA(0, 1);
      VMCNT(12); BAR_ONLY();
      issB(16384);
      VMCNT(8); BAR_ONLY();
      issB(24576);
      {
        const int o = (pidx * 200 + lw * 100 + min(lane, 49)) * 128;
        asm volatile("global_load_dword %0, %1, %2" TPOL "\n\tglobal_load_dword %0, %1, %3" TPOL : "+v"(pf3) : "v"(o), "s"(wo), "s"((const char*)wo + 6400));
      }
      eoffs(ISQ);
      VMCNT(6); BAR_ONLY();
      VMCNT(2); BAR_ONLY();
      BAR_ONLY();
      TPUT(0, 4, 256, tl0);
      TPUT(0, 5, 256, tl1); TPUT(0, 6, 256, tl2);
      BAR_ONLY();
    } else {
      {
        constexpr int NH = (NI0 - 2 * NE) / 2;
        int r;
        const unsigned to = touch_off(pidx * 8 + (rowblk >> 2) * 2 + par, 2 * NE + lw * NH + min(lane, NH - 1), r);
        TOUCH_LDS((r < 16 ? (const char*)x : (const char*)W) + to, l0 + 65536 + 512 + lw * 256);
        SB();
      }
      u32x4 trv[2][4];
      constexpr int CNT2 = 480 - NI0, NL2 = (CNT2 + 2 * NB2 - 1) / (2 * NB2);
      const char* pp[NB2];
#pragma unroll
      for (int i = 0; i < NB2; i++) {
        const int it = NI0 * 32 + ((pidx & 3) * 8 + (pidx >> 2)) * CNT2 + min((2 * i + lw) * NL2 + min(lane, NL2 - 1), CNT2 - 1);
        const int stg = it / 1536, r = it - 1536 * stg, q = r & 15;
      const int ln = par ? (q < 4 ? 6 + q : q < 10 ? 10 + q : 14 + q) : (q < 6 ? q : q < 11 ? 4 + q : 9 + q);
        pp[i] = r < 512 ? (const char*)x + ((rowblk >> 2) * 512 + r) * 1280 + stg * 128 : (const char*)W + (64 * stg + (r >> 4) - 32) * 3840 + ln * 128;
      }
      const int tb2 = TB0 + TBD * (pidx & 3);
      int tro;
      {
        const int fr = lane & 15, g = lane >> 4, p4 = lane & 3, r = 8 * g + (fr >> 2);
        tro = l0 + 32768 + r * 128 + (((4 * lw + (p4 >> 1)) ^ (r & 7)) << 4) + (p4 & 1) * 8;
      }
      auto transp = [&](int ao) {
#pragma unroll
        for (int hh = 0; hh < 2; hh++) {
          u32x2 tr[8];
          const int a = (tro ^ (32 * hh)) + ao, a2 = (a ^ 64) + 512;
          asm volatile("ds_read_b64_tr_b16 %0, %8\n\tds_read_b64_tr_b16 %1, %9\n\tds_read_b64_tr_b16 %2, %8 offset:4096\n\tds_read_b64_tr_b16 %3, %9 offset:4096\n\t"
                       "ds_read_b64_tr_b16 %4, %8 offset:8192\n\tds_read_b64_tr_b16 %5, %9 offset:8192\n\tds_read_b64_tr_b16 %6, %8 offset:12288\n\tds_read_b64_tr_b16 %7, %9 offset:12288\n\t"
                       "s_waitcnt lgkmcnt(0)"
                       : "=&v"(tr[0]), "=&v"(tr[1]), "=&v"(tr[2]), "=&v"(tr[3]), "=&v"(tr[4]), "=&v"(tr[5]), "=&v"(tr[6]), "=&v"(tr[7])
                       : "v"(a), "v"(a2) : "memory");
#pragma unroll
          for (int i = 0; i < 4; i++) trv[hh][i] = (u32x4){tr[2 * i][0], tr[2 * i][1], tr[2 * i + 1][0], tr[2 * i + 1][1]};
        }
      };
      unsigned pf2 = 0;
      auto burst = [&](int hs) {
        static_assert(NB2 == 3, "");
        asm volatile("s_cmp_lg_u32 %4, %5\n\ts_cbranch_scc1 1f\n\tglobal_load_dword %0, %1, off" TPOL "\n\tglobal_load_dword %0, %2, off" TPOL "\n\tglobal_load_dword %0, %3, off" TPOL "\n1:"
                     : "+v"(pf2) : "v"(pp[0]), "v"(pp[1]), "v"(pp[2]), "s"(hs), "s"(tb2) : "scc");
      };
      auto utst = [&]() {
      asm volatile("s_waitcnt vmcnt(0)" :: "v"(pf2) : "memory");
      if (cb < 10) {
        u16* dst = (u16*)(ws + WS_UT) + (size_t)(64 * cb + 32 * lw + (lane & 15)) * UT_STRIDE + 8 * (lane >> 4);
#pragma unroll
        for (int hh = 0; hh < 2; hh++) {
#pragma unroll
          for (int i = 0; i < 4; i++) st16(dst + hh * 16 * UT_STRIDE + UT_PAD + row0 + 32 * i, trv[hh][i]);
          if (rowblk == 0) st16(dst + hh * 16 * UT_STRIDE, (u32x4){0u, 0u, 0u, 0u});
        }
      }
      };
      eoffs(ISQ);
      VMCNT(0);
      BAR_ONLY();
      int ao = 0;
#pragma unroll 1
      for (int j = 0; j < 10; j++) {
        BAR_ONLY();
        if (!lw) STEPT(2 * j);
        burst(2 * j);
        if (j == cb) transp(ao);
        BAR_ONLY();
        if (!lw) STEPT(2 * j + 1);
        burst(2 * j + 1);
        ao = ao == 32768 ? 0 : ao + 16384;
      }
      utst();
      BAR_ONLY();
    }
    __builtin_amdgcn_s_setprio(EPRIO);
    char* xch = smem + 32768 + ((wave & 3) ^ 2) * 8192 + lane * 16;
    if (ISQ || wce == 0) {
#pragma unroll
      for (int i = 0; i < 8; i++) ep[i >> 2][i & 3] = *(const f32x4*)(xch + i * 1024);
    }
  } else if (ISQ) {
    eoffs(true);
    mfma_part<true>(smem, ws, wave, ep, 0);
  } else {
    eoffs(false);
    mfma_part<false>(smem, ws, wave, ep, zc);
  }
  int l2 = threadIdx.x; asm volatile("" : "+v"(l2));
  const int lane2 = l2 & 63;
  f32x2 kl = {1.44269504f, 1.44269504f};
  asm volatile("" : "+s"(kl));
  char* fb = smem;
  if (ISQ || wce == 0) {
    float mn[2] = {ep[0][0][0], ep[1][0][0]};
#pragma unroll
    for (int i = 0; i < 16; i++) mn[i & 1] = __builtin_fminf(__builtin_fminf(mn[i & 1], ep[i & 1][i >> 2][i & 2]), ep[i & 1][i >> 2][(i & 2) + 1]);
    const bool small = __builtin_amdgcn_ballot_w64(!(__builtin_fminf(mn[0], mn[1]) > -7.f)) != 0;
    char* base = ws + WS_PQ + (size_t)((rowblk * 2 + wr) * 128 + 2 * hf) * 640;
    char* fk = fb + (wr * 2 + hf) * 4096 + lane2 * 16;
    auto body = [&](bool isq, bool eps) {
#pragma unroll
      for (int hh = 0; hh < 2; hh++) {
        f32x2 t[8], x[8];
#pragma unroll
        for (int i = 0; i < 8; i++) { const f32x4 v = ep[(i >> 1) & 1][2 * hh + (i >> 2)]; t[i] = (f32x2){rb1(v[2 * (i & 1)]), rb1(v[2 * (i & 1) + 1])}; }
        SB();
#pragma unroll
        for (int i = 0; i < 8; i++) x[i] = t[i] * kl;
        SB();
#pragma unroll
        for (int i = 0; i < 8; i++) {
          x[i][0] = __builtin_amdgcn_fmed3f(__builtin_amdgcn_exp2f(x[i][0]), 0.f, 1.f); x[i][1] = __builtin_amdgcn_fmed3f(__builtin_amdgcn_exp2f(x[i][1]), 0.f, 1.f);
          t[i] = t[i] + (f32x2){1.f, 1.f};
        }
        SB();
#pragma unroll
        for (int i = 0; i < 8; i++) { t[i][0] = __builtin_fmaxf(t[i][0], x[i][0]); t[i][1] = __builtin_fmaxf(t[i][1], x[i][1]); }
        SB();
        u32x4 a[2];
#pragma unroll
        for (int i = 0; i < 8; i++) { a[i >> 2][i & 3] = pack2(t[i][0], t[i][1]); if (eps) a[i >> 2][i & 3] = phi_eps(a[i >> 2][i & 3]); }
        SB();
#pragma unroll
        for (int k = 0; k < 2; k++) {
          if (isq) {
            const unsigned off = (k ? eo[hh] >> 16 : eo[hh] & 0xffffu) * 8u;
            *(u32x2*)(base + off) = (u32x2){a[k][0], a[k][1]};
            *(u32x2*)(base + off + 640) = (u32x2){a[k][2], a[k][3]};
          } else {
            *(u32x4*)(fk + (2 * hh + k) * 1024) = a[k];
          }
        }
        SB();
      }
    };
    if (__builtin_expect(small, 0)) body(ISQ, true); else if (__builtin_expect(ISQ, 0)) body(true, false); else body(false, false);
  }
  if (!ISQ) {
    RAW_BARRIER();
    s16x8 fk[2][4], fv[2][4];
#pragma unroll
    for (int rg = 0; rg < 4; rg++)
#pragma unroll
      for (int i = 0; i < 2; i++) {
        fk[i][rg] = *(const s16x8*)(fb + ((ta[i] * 64 + lane2) << 4) + rg * 4096);
        fv[i][rg] = *(const s16x8*)(fb + 16384 + ((tb[i] * 64 + lane2) << 4) + rg * 4096);
      }
    f32x4 dd[2] = {{0.f, 0.f, 0.f, 0.f}, {0.f, 0.f, 0.f, 0.f}};
#pragma unroll
    for (int rg = 0; rg < 4; rg++)
#pragma unroll
      for (int i = 0; i < 2; i++) dd[i] = __builtin_amdgcn_mfma_f32_16x16x32_bf16(fk[i][rg], fv[i][rg], dd[i], 0, 0, 0);
    char* pb = ws + WS_PART + (size_t)((3 * kvj * 16 + rowblk) * 21) * 80;
#pragma unroll
    for (int i = 0; i < 2; i++)
      if (eo[i] != ~0u) *(f32x4*)(pb + eo[i]) = dd[i];
  }
  asm volatile("s_waitcnt vmcnt(0)" :: "v"(pf3) : "memory");
}

__device__ __forceinline__ cf zpow(float th, float tl, float rh, float rl, float p) {
  float e = __builtin_amdgcn_exp2f(th * p); e = fmaf(e, (tl * p) * 0.69314718f, e);
  float t = rh * p; float f = (t - __builtin_rintf(t)) + rl * p;
  cf o; o[0] = e * __builtin_amdgcn_cosf(f); o[1] = e * __builtin_amdgcn_sinf(f); return o;
}
#define PKM(o, a, b) "v_pk_mul_f32 " o ", " a ", " b " op_sel:[0,0] op_sel_hi:[0,1]\n\t"
#define PKF(o, a, b) "v_pk_fma_f32 " o ", " a ", " b ", " o " op_sel:[1,1,0] op_sel_hi:[1,0,1] neg_lo:[1,0,0]\n\t"
__device__ __forceinline__ void cmul4(cf& o0, cf& o1, cf& o2, cf& o3, cf a0, cf b0, cf a1, cf b1, cf a2, cf b2, cf a3, cf b3) {
  asm(PKM("%0", "%4", "%5") PKM("%1", "%6", "%7") PKM("%2", "%8", "%9") PKM("%3", "%10", "%11")
      PKF("%0", "%4", "%5") PKF("%1", "%6", "%7") PKF("%2", "%8", "%9") PKF("%3", "%10", "%11")
      : "=&v"(o0), "=&v"(o1), "=&v"(o2), "=&v"(o3)
      : "v"(a0), "v"(b0), "v"(a1), "v"(b1), "v"(a2), "v"(b2), "v"(a3), "v"(b3));
}
__device__ __forceinline__ void batch4(unsigned* h, cf b, cf t0, cf t1, cf t2, cf t3) {
  cf w0, w1, w2, w3;
  asm(PKM("%0", "%4", "%5") PKM("%1", "%4", "%6") PKM("%2", "%4", "%7") PKM("%3", "%4", "%8")
      PKF("%0", "%4", "%5") PKF("%1", "%4", "%6") PKF("%2", "%4", "%7") PKF("%3", "%4", "%8")
      : "=&v"(w0), "=&v"(w1), "=&v"(w2), "=&v"(w3) : "v"(b), "v"(t0), "v"(t1), "v"(t2), "v"(t3));
  h[0] = pack2(w0[0], w0[1]); h[1] = pack2(w1[0], w1[1]); h[2] = pack2(w2[0], w2[1]); h[3] = pack2(w3[0], w3[1]);
}
__device__ __forceinline__ void batch3(unsigned* h, cf b, cf t1, cf t2, cf t3) {
  cf w1, w2, w3;
  asm(PKM("%0", "%3", "%4") PKM("%1", "%3", "%5") PKM("%2", "%3", "%6")
      PKF("%0", "%3", "%4") PKF("%1", "%3", "%5") PKF("%2", "%3", "%6")
      : "=&v"(w1), "=&v"(w2), "=&v"(w3) : "v"(b), "v"(t1), "v"(t2), "v"(t3));
  h[0] = pack2(b[0], b[1]); h[1] = pack2(w1[0], w1[1]); h[2] = pack2(w2[0], w2[1]); h[3] = pack2(w3[0], w3[1]);
}
constexpr int KG_RS = 288, KG_PL = 16 * KG_RS;
__device__ __forceinline__ void kgen_role(char* smem, const u16* __restrict__ logA, const u16* __restrict__ Aim,
                                          const u16* __restrict__ Cr, const u16* __restrict__ Ci, const u16* __restrict__ logdt,
                                          const u16* __restrict__ x, const u16* __restrict__ W, const u16* __restrict__ wo,
                                          const u16* __restrict__ gw, char* __restrict__ ws, int blk, unsigned pfv) {
  const int tid = threadIdx.x, lane = tid & 63;
  const int wave = __builtin_amdgcn_readfirstlane(tid >> 6);
  constexpr int RS = KG_RS, PL = KG_PL;
  char* tb = smem + wave * (2 * PL);
  char* wp = tb + lane * 4;
  const char* rp = tb + (lane & 15) * RS + (lane >> 4) * 16;
  const int kq = blk >> 3, tbase = (blk & 7) * 240;
  const int nrep = kq >= 27 ? 0 : (wave == 7 ? 2 : 1);
  unsigned pa[5], pb[5] = {0u, 0u, 0u, 0u, 0u};
  {
    const int task = tbase + min(kq * 9 + wave, 239), pi = task * 64 + lane;
    pa[0] = logdt[task / 3]; pa[1] = logA[pi]; pa[2] = Aim[pi]; pa[3] = Cr[pi]; pa[4] = Ci[pi];
    if (wave == 7) {
      const int t2 = tbase + min(kq * 9 + 8, 239), p2 = t2 * 64 + lane;
      pb[0] = logdt[t2 / 3]; pb[1] = logA[p2]; pb[2] = Aim[p2]; pb[3] = Cr[p2]; pb[4] = Ci[p2];
    }
  }
  int tix[8];
#pragma unroll
  for (int e = 0; e < 8; e++) {
    const int j = (tid & 255) * 8 + 7 - e;
    const int p = (int)rb((float)j);
    const int hh = p >> 7, c = (p >> 3) & 15;
    int sh = 31 - __builtin_clz(hh | 1); sh = sh > 3 ? 3 : sh;
    const int row = 8 * sh + (((hh << 3) | (p & 7)) >> sh);
    tix[e] = 4096 + (tid >> 8) * (2 * PL) + (((row >> 2) * 16 + c) * 4 + (row & 3)) * 4;
    asm volatile("" : "+v"(tix[e]));
  }
  SB();
  asm volatile("" : "+v"(pb[0]), "+v"(pb[1]), "+v"(pb[2]), "+v"(pb[3]), "+v"(pb[4]));
  if (wave == 7) __builtin_amdgcn_s_setprio(W7P);
  const int gt = wave * 30 + (blk >> 3), grow = (blk & 7) * 256 + gt * 4 + (lane >> 4);
  u32x4 xv[5], gv[5];
  asm volatile("" : "+v"(pa[0]), "+v"(pa[1]), "+v"(pa[2]), "+v"(pa[3]), "+v"(pa[4]));
  if (gt < 64) {
    const u32x4* px = (const u32x4*)(x + (size_t)grow * D) + (lane & 15); const u32x4* pg = (const u32x4*)gw + (lane & 15);
#pragma unroll
    for (int i = 0; i < 5; i++) { xv[i] = px[16 * i]; gv[i] = pg[16 * i]; }
  }
  SB();
#pragma unroll 1
  for (int rep = 0; rep < nrep; rep++) {
    const float dt = rb(expf(lo_f(pa[0])));
    float ar;
    {
      const float la = lo_f(pa[1]);
      float h = la * 1.44269502f; float l = fmaf(la, 1.44269502f, -h) + la * 1.92596299e-8f;
      float e = __builtin_amdgcn_exp2f(h); e = fmaf(e, l * 0.69314718f, e);
      ar = -rb(e) * dt;
    }
    const float ai = lo_f(pa[2]) * dt;
    cf C; C[0] = 2.f * (lo_f(pa[3]) * dt); C[1] = 2.f * (lo_f(pa[4]) * dt);
#pragma unroll
    for (int i = 0; i < 5; i++) pa[i] = pb[i];
    const float th = ar * 1.44269502f, tl = fmaf(ar, 1.44269502f, -th) + ar * 1.92596299e-8f;
    const float rh = ai * 0.159154937f, rl = fmaf(ai, 0.159154937f, -rh) + ai * 6.42063833e-9f;
    const cf one = {1.f, 0.f};
    cf S1 = zpow(th, tl, rh, rl, 1.f), Q1 = zpow(th, tl, rh, rl, 8.f), R1 = zpow(th, tl, rh, rl, 128.f);
    Q1[1] = -Q1[1];
    cf S2, Q2, R2, G1, S3, S4, Q3, P1, R3, T1, G2, G3, S5, S6, S7, P2, CT1, T2, P3, CS4, CT2, CT3, CT4, dm0;
    cmul4(S2, Q2, R2, G1, S1, S1, Q1, Q1, R1, R1, C, R1);
    cmul4(S3, S4, Q3, P1, S2, S1, S2, S2, Q2, Q1, Q2, Q2);
    cmul4(R3, T1, G2, P2, R2, R1, R2, R2, C, R2, P1, P1);
    cmul4(S5, S6, S7, P3, S4, S1, S4, S2, S4, S3, P2, P1);
    cmul4(G3, CT1, T2, dm0, C, R3, C, T1, T1, T1, C, R1);
    cmul4(CS4, CT2, CT3, dm0, CT1, S4, C, T2, CT1, T2, C, R1);
    CT4 = cmul(CT2, T2);
    auto rows4 = [&](int pl, int row, int rs, cf b, cf t0, cf t1, cf t2, cf t3) {
      unsigned h[4];
      batch4(h, b, t0, t1, t2, t3);
#pragma unroll
      for (int k = 0; k < 4; k++) *(unsigned*)(wp + pl * PL + (row + k * rs) * RS) = h[k];
    };
    auto rows3 = [&](int pl, int row, int rs, cf b, cf t1, cf t2, cf t3) {
      unsigned h[4];
      batch3(h, b, t1, t2, t3);
#pragma unroll
      for (int k = 0; k < 4; k++) *(unsigned*)(wp + pl * PL + (row + k * rs) * RS) = h[k];
    };
    *(unsigned*)(wp) = 0x00003F80u;
    *(unsigned*)(wp + RS) = pack2(Q1[0], Q1[1]); *(unsigned*)(wp + 2 * RS) = pack2(Q2[0], Q2[1]); *(unsigned*)(wp + 3 * RS) = pack2(Q3[0], Q3[1]);
    rows3(0, 4, 1, P1, Q1, Q2, Q3); rows3(0, 8, 1, P2, Q1, Q2, Q3); rows3(0, 12, 1, P3, Q1, Q2, Q3);
    rows3(1, 0, 1, C, S1, S2, S3); rows4(1, 4, 1, C, S4, S5, S6, S7); rows3(1, 8, 1, G1, S1, S2, S3); rows4(1, 12, 1, G1, S4, S5, S6, S7);
    __builtin_amdgcn_wave_barrier();
    s16x8 bh[4];
    f32x4 res[3];
#pragma unroll
    for (int k = 0; k < 4; k++) bh[k] = *(const s16x8*)(rp + k * 64);
    auto mm = [&](int t, int pl) {
      f32x4 a = {0.f, 0.f, 0.f, 0.f};
#pragma unroll
      for (int k = 0; k < 4; k++) a = __builtin_amdgcn_mfma_f32_16x16x32_bf16(*(const s16x8*)(rp + pl * PL + k * 64), bh[k], a, 0, 0, 0);
      res[t] = a;
    };
    mm(0, 1);
    __builtin_amdgcn_wave_barrier();
    rows3(0, 0, 1, G2, S2, S4, S6); rows3(0, 4, 1, G3, S2, S4, S6); rows3(0, 8, 2, CT1, R1, R2, R3); rows3(0, 9, 2, CS4, R1, R2, R3);
    rows3(1, 0, 1, CT2, R1, R2, R3); rows3(1, 4, 1, CT3, R1, R2, R3);
    *(unsigned*)(wp + PL + 8 * RS) = pack2(CT4[0], CT4[1]);
    __builtin_amdgcn_wave_barrier();
    mm(1, 0); mm(2, 1);
    __builtin_amdgcn_wave_barrier();
    char* rs = (wave == 7 && rep == 0) ? smem + 8 * (2 * PL) : tb;
#pragma unroll
    for (int t = 0; t < 3; t++) *(f32x4*)(rs + t * 1024 + lane * 16) = res[t];
    __builtin_amdgcn_wave_barrier();
  }
  STAMP(0, 4);
  if (gt < 64) {
    float gs = 0.f;
#pragma unroll
    for (int i = 0; i < 5; i++)
#pragma unroll
      for (int e = 0; e < 4; e++) {
        unsigned p = pack2(lo_f(xv[i][e]) * lo_f(gv[i][e]), hi_f(xv[i][e]) * hi_f(gv[i][e]));
        gs = __builtin_amdgcn_fdot2_f32_bf16(__builtin_bit_cast(bf16x2_t, p), __builtin_bit_cast(bf16x2_t, 0x3F803F80u), gs, false);
      }
    gs += __shfl_xor(gs, 1); gs += __shfl_xor(gs, 2); gs += __shfl_xor(gs, 4); gs += __shfl_xor(gs, 8);
    if ((lane & 15) == 0) { float a = rb(gs); ((float*)(ws + WS_GATE))[grow] = rb(1.f / (1.f + __expf(-a))); }
  }
  if (blk == 239) ((unsigned*)(ws + WS_CNT))[tid] = 0u, ((unsigned*)(ws + WS_CNT))[tid + 512] = 0u;
  if (blk >= 216 && blk < 220) st16(ws + WS_KVT + (size_t)((blk - 216) * 512 + tid) * 16, (u32x4){~0u, ~0u, ~0u, ~0u});
  if (kq < 27) {
    __syncthreads();
#pragma unroll
    for (int it = 0; it < 2; it++) {
      const int e = tid + 512 * it;
      const int seg = e / 192, ent = e - 192 * seg;
      if (seg < 3) {
        const char* p0 = smem + 3 * seg * (2 * PL) + ent * 16;
        const f32x4 a = *(const f32x4*)p0 + *(const f32x4*)(p0 + (seg == 2 ? 2 : 1) * (2 * PL)) + *(const f32x4*)(p0 + (seg == 2 ? 1 : 2) * (2 * PL));
        *(f32x4*)(smem + 4096 + seg * (2 * PL) + ent * 16) = a;
      }
    }
    __syncthreads();
    const int d0 = 80 * (blk & 7) + 3 * kq + (tid >> 8);
    u32x4 hi;
#pragma unroll
    for (int e = 0; e < 4; e++) hi[e] = pack2(*(const float*)(smem + tix[2 * e]), *(const float*)(smem + tix[2 * e + 1]));
    st16(ws + WS_KIMG + (size_t)d0 * 4096 + (tid & 255) * 16, hi);
    if (wave < 4 && kq < 26) {
#pragma unroll
      for (int e = 0; e < 4; e++) hi[e] = pack2(*(const float*)(smem + tix[2 * e] + 4 * PL), *(const float*)(smem + tix[2 * e + 1] + 4 * PL));
      st16(ws + WS_KIMG + (size_t)(d0 + 2) * 4096 + tid * 16, hi);
    }
    VMCNT(0);
    if (lane < 16) {
      const int o = (int)WS_KIMG + (d0 + ((lane & 8) && wave < 4 && kq < 26 ? 2 : 0)) * 4096 + (wave & 3) * 1024 + (lane & 7) * 128;
      asm volatile("global_load_dword %0, %1, %2" : "+v"(pfv) : "v"(o), "s"(ws));
    }
  }
  asm volatile("s_waitcnt vmcnt(0)" :: "v"(pfv) : "memory");
}

__global__ __launch_bounds__(512, 4) void k1(const u16* __restrict__ x, const u16* __restrict__ logA, const u16* __restrict__ Aim,
                                          const u16* __restrict__ Cr, const u16* __restrict__ Ci, const u16* __restrict__ logdt,
                                          const u16* __restrict__ wqkv, const u16* __restrict__ gw, const u16* __restrict__ wo,
                                          char* __restrict__ ws) {
  __shared__ __attribute__((aligned(16))) char smem[K1_LDS];
  int b = blockIdx.x;
  unsigned cpf = 0;
  const int w4 = __builtin_amdgcn_readfirstlane(threadIdx.x >> 6) & 3;
  int r = 0; unsigned to = 0;
  if (b >= NB_KG) {
    __builtin_amdgcn_s_setprio(GPRIO);
    to = touch_off(b - NB_KG, w4 * NE + min((int)(threadIdx.x & 63), NE - 1), r);
    asm volatile("" : "+v"(to), "+v"(r));
  }
  asm volatile("" :: "s"(x), "s"(logA), "s"(Aim), "s"(Cr), "s"(Ci), "s"(logdt), "s"(wqkv), "s"(gw), "s"(wo), "s"(ws));
  if (b >= NB_KG && threadIdx.x >= 256 && threadIdx.x < 384) TOUCH_LDS((r < 16 ? (const char*)x : (const char*)wqkv) + to, (int)(size_t)(lptr_t)smem + 65536 + w4 * 256);
  if (b < NB_KG && threadIdx.x >= 501) {
    const char* pc; int len;
    asm volatile("s_getpc_b64 %0\n\ts_mov_b32 %1, code_tail_mark-_Z2k1PKtS0_S0_S0_S0_S0_S0_S0_S0_Pc-128" : "=s"(pc), "=s"(len));
    const int off = min((int)((threadIdx.x - 501) * 30 + (b >> 3)) * 128, len);
    asm volatile("global_load_dword %0, %1, %2" : "+v"(cpf) : "v"(off), "s"(pc));
  }
  if (b < 8 && threadIdx.x >= 490 && threadIdx.x < 500) asm volatile("global_load_dword %0, %1, %2" : "+v"(cpf) : "v"((threadIdx.x - 490) * 128), "s"(gw));
  STAMP(0, 0);
  b = b < NB_KG ? b + NB_GEMM : (b < NB_KG + NB_GEMM ? b - NB_KG : b);
  if (b < NB_GEMM) {
    const int xcd = b & 7, idx = b >> 3;
    const int rowblk = (xcd >> 1) * 4 + (idx & 3), slot = idx >> 2, par = xcd & 1;
    const int cb = par ? (slot < 2 ? slot + 3 : slot + 8) : (slot < 3 ? slot : slot + 2);
    gemm_role(smem, x, wqkv, wo, ws, rowblk, cb, slot, par);
    STAMP(0, 1);
    return;
  }
  b -= NB_GEMM;
  if (b < NB_KG) { kgen_role(smem, logA, Aim, Cr, Ci, logdt, x, wqkv, wo, gw, ws, b, cpf); STAMP(0, 1); return; }
}

constexpr int K2_LDS = 163840;
constexpr int K2_CS = 4160;
constexpr int K2_KOFF = 4 * K2_CS;
constexpr int K2_CH = K2_KOFF + 4608;
constexpr int K2_CONV = K2_LDS - 3 * K2_CH;
constexpr int K2_TAB = 81920;
constexpr int K2_FIN = K2_TAB + 9216;
constexpr int K2_KV = 102400;
constexpr int K2_STG = 131072;
constexpr int NB_KV = 32;
typedef __attribute__((address_space(1))) unsigned gu32;
__device__ __forceinline__ const char* uni(const char* p) {
  size_t v = (size_t)p;
  unsigned lo = __builtin_amdgcn_readfirstlane((unsigned)v), hi = __builtin_amdgcn_readfirstlane((unsigned)(v >> 32));
  size_t o = ((size_t)hi << 32) | lo;
  asm volatile("s_nop 4" : "+s"(o));
  return (const char*)o;
}
__device__ __forceinline__ void wait_count(char* p, unsigned target) {
  gu32* c = (gu32*)(size_t)p;
  for (int n = 0; n < (1 << 22); n++) {
    if (__hip_atomic_load(c, __ATOMIC_RELAXED, __HIP_MEMORY_SCOPE_AGENT) >= target) break;
    __builtin_amdgcn_s_sleep(8);
  }
}
#define GLD4(dst, voff, sbase, pol) asm volatile("global_load_dwordx4 %0, %1, %2" pol : "=v"(dst) : "v"(voff), "s"(sbase))
#define GLD2(dst, voff, sbase, pol) asm volatile("global_load_dwordx2 %0, %1, %2" pol : "=v"(dst) : "v"(voff), "s"(sbase))
#define GLD1(dst, voff, sbase, pol) asm volatile("global_load_dword %0, %1, %2" pol : "=v"(dst) : "v"(voff), "s"(sbase))
template <int G>
__device__ __forceinline__ void conv_unit(f32x4* acc, int ap, int bp) {
  constexpr int NT = 8 - G;
  u32x2 a0[NT], a1[NT]; s16x8 bh[NT];
#pragma unroll
  for (int j = 0; j < NT; j++)
    asm volatile("ds_read_b64 %0, %3 offset:%5\n\tds_read_b64 %1, %3 offset:%6\n\tds_read_b128 %2, %4 offset:%5"
                 : "=&v"(a0[j]), "=&v"(a1[j]), "=&v"(bh[j]) : "v"(ap), "v"(bp), "n"(512 * j), "n"(512 * j + 8) : "memory");
#pragma unroll
  for (int x = 0; x < NT; x++) {
    asm volatile("s_waitcnt lgkmcnt(%3)" : "+v"(a0[x]), "+v"(a1[x]), "+v"(bh[x]) : "n"(3 * (NT - 1 - x) > 15 ? 15 : 3 * (NT - 1 - x)) : "memory");
#pragma unroll
    for (int y = 0; y <= x; y++)
#pragma unroll
      for (int sw = 0; sw < 2; sw++) {
        if (sw && y == x) continue;
        const int j = sw ? y : x, m = sw ? x : y, T = j + m;
        const int ti = G == 0 ? (T == 0 ? 0 : T == 3 ? 1 : T == 4 ? 2 : T == 7 ? 3 : -1) : (T == 1 ? 0 : T == 2 ? 1 : T == 5 ? 2 : T == 6 ? 3 : -1);
        if (ti >= 0) acc[ti] = __builtin_amdgcn_mfma_f32_16x16x32_bf16(__builtin_bit_cast(s16x8, (u32x4){a0[j][0], a0[j][1], a1[j][0], a1[j][1]}), bh[m], acc[ti], 0, 0, 0);
      }
    SB();
  }
}

__global__ __launch_bounds__(576) void k2(const u16* __restrict__ Dskip, const u16* __restrict__ wo, char* __restrict__ ws, u16* __restrict__ out) {
  __shared__ __attribute__((aligned(16))) char smem[K2_LDS];
  const int tid = threadIdx.x, lane = tid & 63;
  const int wave = __builtin_amdgcn_readfirstlane(tid >> 6);
  const int b = blockIdx.x;
  unsigned cpf = 0;
  if (b < 32 && tid < 192) {
    const char* pc; int len;
    asm volatile("s_getpc_b64 %0\n\ts_mov_b32 %1, code_tail_mark-_Z2k2PKtS0_PcPt-128" : "=s"(pc), "=s"(len));
    const int off = min(tid * 128, len);
    asm volatile("global_load_dword %0, %1, %2" : "+v"(cpf) : "v"(off), "s"(pc));
  }
  STAMP(1, 0);
  const int lds0 = (int)(size_t)(lptr_t)smem;
  const int l16 = lane * 16;
  const int xcd = b & 7, idx = b >> 3;
  const int odd = idx & 1;
  const int chA = 80 * xcd + 5 * (idx >> 1) + 3 * odd, chH = 80 * xcd + 5 * (idx >> 1) + 2;
  const int rowblk = xcd * 4 + (idx >> 3), cb = idx & 7;
  const int l0 = rowblk * 64, c0 = cb * 80;
  const int r = lane & 31, h = lane >> 5;
  if (wave == 8) {
    __builtin_amdgcn_s_setprio(3);
    const char* wsrc = (const char*)wo + cb * 160;
    int wv[5];
#pragma unroll
    for (int j = 0; j < 5; j++) { const int f = 64 * j + lane, row = f / 10; wv[j] = row * 1280 + (f - 10 * row) * 16 - (j & 3) * 1024; }
    auto dma5 = [&](int grp) {
      asm volatile("s_mov_b32 m0, %6\n\ts_nop 0\n\tglobal_load_lds_dwordx4 %0, %5\n\tglobal_load_lds_dwordx4 %1, %5 offset:1024\n\t"
                   "global_load_lds_dwordx4 %2, %5 offset:2048\n\tglobal_load_lds_dwordx4 %3, %5 offset:3072\n\t"
                   "s_add_u32 m0, m0, 0x1000\n\ts_nop 0\n\tglobal_load_lds_dwordx4 %4, %5"
                   :: "v"(wv[0]), "v"(wv[1]), "v"(wv[2]), "v"(wv[3]), "v"(wv[4]), "s"(wsrc + grp * 40960), "s"(lds0 + grp * 5120) : "memory", "m0", "scc");
    };
    auto dma4 = [&](const char* src, int dst) {
      asm volatile("s_mov_b32 m0, %2\n\ts_nop 0\n\tglobal_load_lds_dwordx4 %0, %1\n\tglobal_load_lds_dwordx4 %0, %1 offset:1024\n\t"
                   "global_load_lds_dwordx4 %0, %1 offset:2048\n\tglobal_load_lds_dwordx4 %0, %1 offset:3072"
                   :: "v"(l16), "s"(src), "s"(lds0 + dst) : "memory", "m0");
    };
    auto kimg = [&]() {
      dma4(ws + WS_KIMG + chA * 4096, K2_CONV + K2_KOFF + 512);
      dma4(ws + WS_KIMG + chA * 4096 + 4096, K2_CONV + K2_CH + K2_KOFF + 512);
      dma4(ws + WS_KIMG + chH * 4096, K2_CONV + 2 * K2_CH + K2_KOFF + 512);
    };
    if (b < NB_KV) {
      f32x4 kvp[32];
      const char* src = ws + WS_PART + (size_t)b * 16 * 1680;
      const int o2 = min(lane + 64, 104) * 16;
#pragma unroll
      for (int i = 0; i < 16; i++) { GLD4(kvp[i], l16, src + i * 1680, ""); GLD4(kvp[16 + i], o2, src + i * 1680, ""); }
      kimg();
#pragma unroll
      for (int i = 0; i < NW0; i++) dma5(i);
      asm volatile("s_waitcnt vmcnt(%0)" :: "n"(5 * NW0) : "memory");
      BAR_ONLY();
#pragma unroll
      for (int i = 0; i < 32; i += 8)
        asm volatile("" : "+v"(kvp[i]), "+v"(kvp[i + 1]), "+v"(kvp[i + 2]), "+v"(kvp[i + 3]), "+v"(kvp[i + 4]), "+v"(kvp[i + 5]), "+v"(kvp[i + 6]), "+v"(kvp[i + 7]));
#pragma unroll
      for (int st = 1; st < 16; st <<= 1)
#pragma unroll
        for (int i = 0; i < 32; i += 2 * st) kvp[i] += kvp[i + st];
      const u32x2 o0 = {pack2(kvp[0][0], kvp[0][1]), pack2(kvp[0][2], kvp[0][3])}, o1 = {lane < 41 ? pack2(kvp[16][0], kvp[16][1]) : 0u, lane < 41 ? pack2(kvp[16][2], kvp[16][3]) : 0u};
      const int o = (int)WS_KVT + b * 896 + lane * 8;
      asm volatile("global_store_dwordx2 %0, %1, %2 sc1" :: "v"(o), "v"(o0), "s"(ws) : "memory");
      if (lane < 48) asm volatile("global_store_dwordx2 %0, %1, %2 offset:512 sc1\n\ts_nop 1" :: "v"(o), "v"(o1), "s"(ws) : "memory");
      KVST;
    } else {
      kimg();
#pragma unroll
      for (int i = 0; i < NW0; i++) dma5(i);
      asm volatile("s_waitcnt vmcnt(%0)" :: "n"(5 * NW0) : "memory");
      BAR_ONLY();
    }
#pragma unroll 1
    for (int p = NW0; p < 19; p++) {
      dma5(p);
    }
    {
      u32x4 kt[28];
      const char* kp = ws + WS_KVT;
#define KVL(pol) _Pragma("unroll") for (int i = 0; i < 28; i++) GLD4(kt[i], l16, kp + i * 1024, pol);
#define KVW() _Pragma("unroll") for (int i = 0; i < 28; i += 7) asm volatile("" : "+v"(kt[i]), "+v"(kt[i + 1]), "+v"(kt[i + 2]), "+v"(kt[i + 3]), "+v"(kt[i + 4]), "+v"(kt[i + 5]), "+v"(kt[i + 6]));
      KVL(KV1);
      VMCNT(0);
      KVW();
      for (int n = 0; n < (1 << 14); n++) {
        unsigned m = 0;
#pragma unroll
        for (int i = 0; i < 28; i++) m = max(max(max(m, kt[i][0]), kt[i][1]), max(kt[i][2], kt[i][3]));
        if (__builtin_amdgcn_ballot_w64(m == ~0u) == 0ull) break;
        KVL(" sc1");
        VMCNT(0);
        KVW();
      }
      BAR_ONLY();
#pragma unroll
      for (int i = 0; i < 14; i++) *(u32x4*)(smem + K2_KV + i * 1024 + l16) = kt[i];
      BAR_ONLY();
#pragma unroll
      for (int i = 14; i < 28; i++) *(u32x4*)(smem + K2_KV + i * 1024 + l16) = kt[i];
    }
    asm volatile("s_waitcnt lgkmcnt(0)" ::: "memory");
    BAR_ONLY();
    dma5(19);
    VMCNT(0);
    BAR_ONLY();
    {
      gu32* c = (gu32*)(size_t)(ws + WS_CNT + (8 + cb) * 128);
      for (int n = 0; n < (1 << 22); n++) {
        if (__hip_atomic_load(c, __ATOMIC_RELAXED, __HIP_MEMORY_SCOPE_AGENT) >= 32u) break;
        __builtin_amdgcn_s_sleep(1);
      }
      asm volatile("" ::: "memory");
      const int o = (lane >> 3) * (L * 2) + (((lane & 7) ^ ((lane >> 2) & 4)) << 4);
      const char* src = ws + WS_AT + (size_t)(c0 * L + l0) * 2;
#pragma unroll
      for (int i = 0; i < 10; i++)
        asm volatile("s_mov_b32 m0, %2\n\ts_nop 0\n\tglobal_load_lds_dwordx4 %0, %1 sc1" :: "v"(o), "s"(src + i * 8 * L * 2), "s"(lds0 + (i < 8 ? (i >> 1) * 3200 : 25600) + (i & 1) * 1024) : "memory", "m0");
      VMCNT(0);
    }
    BAR_ONLY();
    return;
  }
  const int t = wave & 1, kh = wave >> 1;
  u32x4 in0[2];
  int dsto[2];
#pragma unroll
  for (int rr = 0; rr < 2; rr++) {
    const int f = (wave + 8 * rr) * 49 + min(lane, 49), i = min(f, 776);
    const int ch = i / 259, xx = i - 259 * ch;
    dsto[rr] = (lane < 49 && f < 777) ? K2_CONV + ch * K2_CH + xx * 16 : -1;
    GLD4(in0[rr], (int)WS_UT + (ch == 2 ? chH : chA + ch) * (UT_STRIDE * 2) + (xx + 1) * 16, ws, "");
  }
  const int jg = wave & 1, jc = (wave >> 1) & 1, hiw = wave >> 2;
  const int fo = (lane & 15) * 16 + (lane >> 4) * 4;
  int ft[3];
  ft[0] = hiw ? 3 - jg : jg;
  ft[1] = hiw ? 4 + jg : (wave & 2) * 2 + ((wave & 1) ? 3 - odd : odd);
  ft[2] = 7 - jg;
  u32x2 us[3];
  float g_r;
  GLD1(g_r, (int)WS_GATE + (l0 + 32 * t + 16 * (kh & 1) + (lane & 15)) * 4, ws, "");
  unsigned dsA, dsH;
  asm volatile("global_load_ushort %0, %1, %2" : "=v"(dsA) : "v"((chA + jc) * 2), "s"(Dskip));
  asm volatile("global_load_ushort %0, %1, %2" : "=v"(dsH) : "v"((hiw ? chA + jc : chH) * 2), "s"(Dskip));
  u32x4 pf[8][2];
  const char* pqp = ws + WS_PQ + (size_t)((rowblk * 32 + kh * 8) * 4 + 2 * t) * 640;
  const int pqo = lane < 32 ? 128 + l16 : (lane & 15) * 8;
#define PQL(i0, i1) _Pragma("unroll") for (int i = i0; i < i1; i++) { GLD4(pf[i][0], pqo, pqp + i * 2560, ""); GLD4(pf[i][1], pqo, pqp + i * 2560, " offset:640"); }
  if (tid < 96) *(u32x4*)(smem + K2_CONV + (tid >> 5) * K2_CH + K2_KOFF + (tid & 31) * 16) = (u32x4){0u, 0u, 0u, 0u};
  asm volatile("s_waitcnt vmcnt(3)" : "+v"(in0[0]), "+v"(in0[1]), "+v"(cpf) :: "memory");
  STAMP(1, 2);
#pragma unroll
  for (int rr = 0; rr < 2; rr++) {
    const u32x4 a = in0[rr];
    unsigned w[6] = {a[0], a[1], a[2], a[3], (unsigned)__builtin_amdgcn_update_dpp(0, (int)a[0], 0x130, 0xf, 0xf, false),
                     (unsigned)__builtin_amdgcn_update_dpp(0, (int)a[1], 0x130, 0xf, 0xf, false)};
    unsigned o[5];
#pragma unroll
    for (int k = 0; k < 5; k++) o[k] = __builtin_amdgcn_alignbyte(w[k + 1], w[k], 2);
    if (dsto[rr] >= 0) {
      char* p = smem + dsto[rr];
#pragma unroll
      for (int q = 0; q < 2; q++) {
        *(u32x4*)(p + (2 * q) * K2_CS) = (u32x4){o[q], o[q + 1], o[q + 2], o[q + 3]};
        *(u32x4*)(p + (2 * q + 1) * K2_CS) = (u32x4){w[q + 1], w[q + 2], w[q + 3], w[q + 4]};
      }
    }
  }
  RAW_BARRIER();
  PQL(0, PQA);
  STAMP(1, 3);
  f32x4 ca[4], sv[4];
#pragma unroll
  for (int i = 0; i < 4; i++) { ca[i] = (f32x4){0.f, 0.f, 0.f, 0.f}; sv[i] = (f32x4){0.f, 0.f, 0.f, 0.f}; }
  {
    const int i = lane & 15, kg = lane >> 4, e = kg >> 1, bb = kg & 1;
    const int ap = lds0 + K2_CONV + (i & 3) * K2_CS + (2 * e - bb + 2) * 16 + (i >> 2) * 8;
    const int bp = lds0 + K2_CONV + K2_KOFF + (16 + i - e) * 32 + bb * 16;
    int apj = ap + jc * K2_CH + hiw * 192;
    int bpj = bp + jc * K2_CH - hiw * 192;
    if (jg == 0) {
#pragma unroll 1
      for (int it = 0; it < 3; it++) conv_unit<0>(ca, apj + it * 64, bpj - it * 64);
    } else {
#pragma unroll 1
      for (int it = 0; it < 3; it++) conv_unit<1>(ca, apj + it * 64, bpj - it * 64);
    }
    PQL(PQA, PQB);
    apj += 192; bpj -= 192;
    int g2 = jg;
    if (!hiw) {
#pragma unroll
      for (int q = 0; q < 4; q++) { sv[q] = ca[q]; ca[q] = (f32x4){0.f, 0.f, 0.f, 0.f}; }
      apj = ap + 2 * K2_CH + wave * 128; bpj = bp + 2 * K2_CH - wave * 128; g2 = odd;
    }
    if (g2 == 0) {
#pragma unroll 1
      for (int it = 0; it < 2; it++) conv_unit<0>(ca, apj + it * 64, bpj - it * 64);
    } else {
#pragma unroll 1
      for (int it = 0; it < 2; it++) conv_unit<1>(ca, apj + it * 64, bpj - it * 64);
    }
  }
  STAMP(1, 4);
  {
    const char* ua = smem + K2_CONV + 3 * K2_CS + (20 + fo) * 2;
    us[0] = *(const u32x2*)(ua + jc * K2_CH + ft[0] * 512); us[1] = *(const u32x2*)(ua + (hiw ? jc : 2) * K2_CH + ft[1] * 512);
    us[2] = *(const u32x2*)(ua + jc * K2_CH + ft[2] * 512);
  }
  PQL(PQB, 8);
  RAW_BARRIER();
  {
    if (tid == 0) *(unsigned*)(smem + K2_LDS - 16) = 0u;
    char* sp = smem + K2_STG + l16;
    if (hiw) {
      *(f32x4*)(sp + (wave & 3) * 4096) = ca[0];
    } else {
#pragma unroll
      for (int i = 1; i < 4; i++) *(f32x4*)(sp + wave * 4096 + i * 1024) = sv[i];
#pragma unroll
      for (int i = 0; i < 4; i++) if (i != wave) *(f32x4*)(sp + 16384 + (i * 3 + ((wave - i - 1) & 3)) * 1024) = ca[i];
    }
  }
  RAW_BARRIER();
  {
    const char* sp = smem + K2_STG + l16;
    asm volatile("s_waitcnt vmcnt(16)" : "+v"(g_r), "+v"(dsA), "+v"(dsH) :: "memory");
    const float dskA = lo_f(dsA), dskH = lo_f(dsH);
    auto fin = [&](f32x4 s, u32x2 uu, float dsk, int ch, int tile) {
      u32x2 ov;
#pragma unroll
      for (int e = 0; e < 2; e++) {
        unsigned yy = pack2(s[2 * e], s[2 * e + 1]);
        unsigned k = pack2(lo_f(uu[e]) * dsk, hi_f(uu[e]) * dsk);
        ov[e] = pack2(lo_f(yy) + lo_f(k), hi_f(yy) + hi_f(k));
      }
      const int o = (int)WS_AT + (ch * L + tile * 256 + fo) * 2;
      asm volatile("global_store_dwordx2 %0, %1, %2 sc1\n\ts_nop 1" :: "v"(o), "v"(ov), "s"(ws) : "memory");
    };
    if (hiw) {
      const char* pp = sp + (wave & 3) * 4096;
#pragma unroll
      for (int i = 1; i < 4; i++) fin(ca[i] + *(const f32x4*)(pp + i * 1024), us[i - 1], dskA, chA + jc, ft[i - 1]);
      VMCNT(3);
    } else {
      const char* hp = sp + 16384 + wave * 3072;
      const f32x4 own = wave == 0 ? ca[0] : wave == 1 ? ca[1] : wave == 2 ? ca[2] : ca[3];
      fin(sv[0] + *(const f32x4*)(sp + wave * 4096), us[0], dskA, chA + jc, ft[0]);
      fin((own + *(const f32x4*)(hp + 1024)) + (*(const f32x4*)hp + *(const f32x4*)(hp + 2048)), us[1], dskH, chH, ft[1]);
      VMCNT(2);
    }
  }
#pragma unroll
  for (int i = 0; i < 8; i++) asm volatile("" : "+v"(pf[i][0]), "+v"(pf[i][1]));
  RAW_BARRIER();
  STAMP(1, 5);
  const int n16 = lane & 15, g = lane >> 4;
  f32x4 acc[5][2];
  {
    const char* kp = smem + K2_KV + kh * 8 * 896;
    const int row1 = (n16 & 3) ? 20 : 16 + (n16 >> 2);
    const char* k00 = kp + (g < 3 ? n16 * 40 + 16 * g : 840);
    const char* k01 = kp + (g < 2 ? n16 * 40 + 16 * g + 8 : 840);
    const char* k10 = kp + (g < 3 ? row1 * 40 + 16 * g : 840);
    const char* k11 = kp + (g < 2 ? row1 * 40 + 16 * g + 8 : 840);
    const char* wm = smem + (160 * kh + 4 * g + (n16 >> 2)) * 160 + (lane & 3) * 8;
    const char* wt = smem + (160 * kh + 20 * (n16 >> 2) + 16 + g) * 160 + (lane & 3) * 8;
    u32x4 ka[8][2];
    auto rdK = [&](int i) {
      const u32x2 a0 = *(const u32x2*)(k00 + i * 896), a1 = *(const u32x2*)(k01 + i * 896), b0 = *(const u32x2*)(k10 + i * 896), b1 = *(const u32x2*)(k11 + i * 896);
      ka[i][0] = (u32x4){a0[0], a0[1], a1[0], a1[1]}; ka[i][1] = (u32x4){b0[0], b0[1], b1[0], b1[1]};
    };
    f32x4 c[2][2][2];
    const f32x4 z4 = {0.f, 0.f, 0.f, 0.f};
    auto stageA = [&](int i) {
#pragma unroll
      for (int tl = 1; tl >= 0; tl--)
#pragma unroll
        for (int nt = 0; nt < 2; nt++)
          c[i & 1][tl][nt] = __builtin_amdgcn_mfma_f32_16x16x32_bf16(__builtin_bit_cast(s16x8, ka[i][tl]), __builtin_bit_cast(s16x8, pf[i][nt]), z4, 0, 0, 0);
    };
    auto wfrag = [&](const char* p, int d) {
      const s16x4 lo = __builtin_amdgcn_ds_read_tr16_b64_v4i16((lds_s16x4_p)(p)), hi = __builtin_amdgcn_ds_read_tr16_b64_v4i16((lds_s16x4_p)(p + d));
      return __builtin_shufflevector(lo, hi, 0, 1, 2, 3, 4, 5, 6, 7);
    };
    u32x4 ob[2][2], o5[2];
    float p5[2];
    s16x8 w[3];
    auto proj = [&](int s, int q) {
      if (s < 0) return;
      const u32x4 bb = s < 4 ? ob[s & 1][q & 1] : o5[q & 1];
      acc[q >> 1][q & 1] = __builtin_amdgcn_mfma_f32_16x16x32_bf16(w[(q >> 1) % 3], __builtin_bit_cast(s16x8, bb), s == 0 ? z4 : acc[q >> 1][q & 1], 0, 0, 0);
    };
    auto rdW = [&](int s, int mt) { w[mt % 3] = s < 4 ? wfrag(wm + s * 6400 + mt * 32, 3200) : wfrag(wt + mt * 32, 12800); };
    auto sA = [&](int i, int q) {
      if (i < 8) c[i & 1][1 - (q >> 1)][q & 1] = __builtin_amdgcn_mfma_f32_16x16x32_bf16(__builtin_bit_cast(s16x8, ka[i][1 - (q >> 1)]), __builtin_bit_cast(s16x8, pf[i][q & 1]), z4, 0, 0, 0);
    };
    rdK(0);
    stageA(0);
    SB();
#pragma unroll
    for (int i = 0; i < 8; i++) {
      const int s = (i >> 1) - 1, q0 = (i & 1) * 5;
      if (i + 1 < 8 && i <= B6I) rdK(i + 1);
      if (i == B6I) { _Pragma("unroll") for (int j = i + 2; j < 8; j++) rdK(j); }
      SB();
      const f32x4 (&cc)[2][2] = c[i & 1];
      proj(s, q0);
      unsigned dz = pack2(cc[1][0][1], cc[1][1][1]);
      float d0 = lo_f(dz), d1 = hi_f(dz);
      asm volatile("" : "+v"(d0), "+v"(d1));
      SB();
      proj(s, q0 + 1);
      if (s >= 0 && !(i & 1)) rdW(s, 3);
      if (i & 1) rdW(s + 1, 2);
      dz = pack2(d0 + 1e-6f, d1 + 1e-6f);
      d0 = lo_f(dz);
      asm volatile("" : "+v"(d0), "+v"(dz));
      SB();
      proj(s, q0 + 2);
      float inv[2] = {__builtin_amdgcn_rcpf(d0), __builtin_amdgcn_rcpf(hi_f(dz))};
      asm volatile("" : "+v"(inv[0]), "+v"(inv[1]));
      SB();
      proj(s, q0 + 3);
      if (s >= 0 && !(i & 1)) rdW(s, 4);
      if (i & 1) rdW(s + 1, 0);
      f32x4 v0 = cc[0][0] * inv[0];
      float q50 = cc[1][0][0] * inv[0];
      asm volatile("" : "+v"(v0), "+v"(q50));
      SB();
      proj(s, q0 + 4);
      f32x4 v1 = cc[0][1] * inv[1];
      float q51 = cc[1][1][0] * inv[1];
      asm volatile("" : "+v"(v1), "+v"(q51));
      SB();
      sA(i + 1, 0);
      if (i & 1) rdW(s + 1, 1);
      ob[(i >> 1) & 1][0][2 * (i & 1)] = pack2(v0[0], v0[1]); ob[(i >> 1) & 1][0][2 * (i & 1) + 1] = pack2(v0[2], v0[3]);
      SB();
      sA(i + 1, 1);
      ob[(i >> 1) & 1][1][2 * (i & 1)] = pack2(v1[0], v1[1]); ob[(i >> 1) & 1][1][2 * (i & 1) + 1] = pack2(v1[2], v1[3]);
      SB();
      sA(i + 1, 2);
      if (i & 1) { o5[0][i >> 1] = pack2(p5[0], q50); o5[1][i >> 1] = pack2(p5[1], q51); } else { p5[0] = q50; p5[1] = q51; }
      SB();
      sA(i + 1, 3);
      SB();
      if (i == SIGI) {
        VMCNT(0);
        unsigned old = 0;
        if (lane == 0) old = __hip_atomic_fetch_add((unsigned*)(smem + K2_LDS - 16), 1u, __ATOMIC_RELAXED, __HIP_MEMORY_SCOPE_WORKGROUP);
        if (lane == 0 && old == 7u) __hip_atomic_fetch_add((gu32*)(size_t)(ws + WS_CNT + (8 + xcd) * 128), 1u, __ATOMIC_RELAXED, __HIP_MEMORY_SCOPE_AGENT);
      }
      if (i == B6I) { RAW_BARRIER(); }
    }
    s16x8 w2[7];
#pragma unroll
    for (int j = 0; j < 7; j++) w2[j] = j < 2 ? wfrag(wm + 3 * 6400 + (3 + j) * 32, 3200) : wfrag(wt + (j - 2) * 32, 12800);
    SB();
#pragma unroll
    for (int q = 0; q < 6; q++) proj(3, q);
#pragma unroll
    for (int q = 6; q < 20; q++) {
      const int mt = q < 10 ? q >> 1 : (q - 10) >> 1;
      acc[mt][q & 1] = __builtin_amdgcn_mfma_f32_16x16x32_bf16(w2[(q - 6) >> 1], __builtin_bit_cast(s16x8, q < 10 ? ob[1][q & 1] : o5[q & 1]), acc[mt][q & 1], 0, 0, 0);
    }
  }
  STAMP(1, 6);
  {
    char* sp = smem + 102400 + t * 30720 + l16;
#pragma unroll
    for (int q = 0; q < 10; q++) {
      const int own = q & 3;
      if (kh != own) *(f32x4*)(sp + (q * 3 + ((kh - own - 1) & 3)) * 1024) = acc[q >> 1][q & 1];
    }
  }
  RAW_BARRIER();
  STAMP(1, 7);
  {
    const char* sp = smem + 102400 + t * 30720 + l16;
    const float g1 = rb(1.f - g_r);
    const char* atp = smem + (kh >> 1) * 3200 + (4 * g + (n16 >> 2)) * 128 + (((32 * t + 16 * (kh & 1) + 4 * (lane & 3)) * 2) ^ ((lane & 8) << 3));
    u16* op = out + (size_t)(l0 + 32 * t + 16 * (kh & 1) + n16) * D + c0 + 16 * (kh >> 1) + 4 * g;
#pragma unroll
    for (int tl = 0; tl < 3; tl++) {
      if (tl == 2 && kh >= 2) break;
      f32x4 m;
      if (kh == 0)      m = acc[2 * tl][0];
      else if (kh == 1) m = acc[2 * tl][1];
      else if (kh == 2) m = acc[tl < 2 ? 2 * tl + 1 : 0][0];
      else              m = acc[tl < 2 ? 2 * tl + 1 : 0][1];
      const char* pp = sp + (tl * 4 + kh) * 3072;
      f32x4 p0 = *(const f32x4*)pp, p1 = *(const f32x4*)(pp + 1024), p2 = *(const f32x4*)(pp + 2048);
      f32x4 v = (m + p1) + (p0 + p2);
      s16x4 at = __builtin_amdgcn_ds_read_tr16_b64_v4i16((lds_s16x4_p)(atp + (tl < 2 ? tl * 6400 : 25600)));
      u32x2 au = __builtin_bit_cast(u32x2, at);
      u32x2 ov;
#pragma unroll
      for (int e = 0; e < 2; e++) {
        unsigned bq = pack2(v[2 * e], v[2 * e + 1]);
        unsigned gb = pack2(g1 * lo_f(bq), g1 * hi_f(bq));
        unsigned ga = pack2(g_r * lo_f(au[e]), g_r * hi_f(au[e]));
        ov[e] = pack2(lo_f(ga) + lo_f(gb), hi_f(ga) + hi_f(gb));
      }
      *(u32x2*)(op + 32 * tl) = ov;
    }
  }
  asm volatile("code_tail_mark:" ::: "memory");
  STAMP(1, 1);
}

extern "C" void kernel_launch(void* const* d_in, const int* in_sizes, int n_in, void* d_out, int out_size, void* d_ws, size_t ws_size,
                              hipStream_t stream) {
  const u16* u = (const u16*)d_in[0]; const u16* logA = (const u16*)d_in[1]; const u16* Aim = (const u16*)d_in[2];
  const u16* Cr = (const u16*)d_in[3]; const u16* Ci = (const u16*)d_in[4]; const u16* logdt = (const u16*)d_in[5];
  const u16* Dsk = (const u16*)d_in[6]; const u16* wqkv = (const u16*)d_in[8]; const u16* wo = (const u16*)d_in[9];
  const u16* gw = (const u16*)d_in[10];
  char* ws = (char*)d_ws;
  k1<<<NB_GEMM + NB_GATE + NB_KG, 512, 0, stream>>>(u, logA, Aim, Cr, Ci, logdt, wqkv, gw, wo, ws);
  k2<<<256, 576, 0, stream>>>(Dsk, wo, ws, (u16*)d_out);
}
```

```cpp
#include <hip/hip_runtime.h>
#include <type_traits>
#include <hip/hip_bf16.h>
#include <stdint.h>

typedef unsigned short u16;
typedef __attribute__((ext_vector_type(8))) short s16x8;
typedef __attribute__((ext_vector_type(4))) short s16x4;
typedef __attribute__((ext_vector_type(4))) float f32x4;
typedef __attribute__((ext_vector_type(16))) float f32x16;
typedef __attribute__((ext_vector_type(4))) unsigned u32x4;
typedef __attribute__((ext_vector_type(2))) unsigned u32x2;
typedef __attribute__((address_space(3))) s16x4* lds_s16x4_p;
typedef const __attribute__((address_space(1))) void* gptr_t;
typedef __attribute__((address_space(3))) void* lptr_t;

constexpr int L = 2048, D = 640, NS = 192, NH = 32;
constexpr int UT_PAD = 32, UT_STRIDE = L + UT_PAD;
constexpr size_t WS_UT   = 0;
constexpr size_t WS_KIMG = WS_UT + (size_t)D * UT_STRIDE * 2;
constexpr size_t WS_PQ   = WS_KIMG + (size_t)D * 4096;
constexpr size_t WS_PART = WS_PQ + (size_t)L * D * 2;
constexpr size_t WS_GATE = WS_PART + (size_t)32 * 16 * 1680;
constexpr size_t WS_KVT  = WS_GATE + (size_t)L * 4;
constexpr size_t WS_AT   = WS_KVT + (size_t)32 * 1024;
constexpr size_t WS_CNT  = WS_AT + (size_t)D * L * 2;
constexpr size_t WS_END  = WS_CNT + 4096;
#define TGET(v) do {} while (0)
#define STEPT(i) do {} while (0)
#define TPUT(kid, slot, thr, v) do {} while (0)
#define STAMP(kid, slot) do {} while (0)

__device__ __forceinline__ float b2f(u16 v) { return __uint_as_float(((unsigned)v) << 16); }
typedef __bf16 bf16x2_t __attribute__((ext_vector_type(2)));
typedef float f32x2 __attribute__((ext_vector_type(2)));
__device__ __forceinline__ unsigned pack2(float a, float b) { f32x2 v = {a, b}; bf16x2_t r = __builtin_convertvector(v, bf16x2_t); return __builtin_bit_cast(unsigned, r); }
__device__ __forceinline__ float lo_f(unsigned u) { return __uint_as_float(u << 16); }
__device__ __forceinline__ float hi_f(unsigned u) { return __uint_as_float(u & 0xffff0000u); }
__device__ __forceinline__ float rb(float f) { return lo_f(pack2(f, f)); }
__device__ __forceinline__ float rb1(float f) { return __uint_as_float(pack2(0.f, f)); }
typedef unsigned short u16x2 __attribute__((ext_vector_type(2)));
__device__ __forceinline__ unsigned phi2(float a, float b, f32x2 kl) {
  const f32x2 t = {rb1(a), rb1(b)};
  const f32x2 x = t * kl, p = t + (f32x2){1.f, 1.f};
  const float e0 = __builtin_amdgcn_fmed3f(__builtin_amdgcn_exp2f(x[0]), 0.f, 1.f);
  const float e1 = __builtin_amdgcn_fmed3f(__builtin_amdgcn_exp2f(x[1]), 0.f, 1.f);
  return pack2(__builtin_fmaxf(p[0], e0), __builtin_fmaxf(p[1], e1));
}
__device__ __forceinline__ unsigned phi_eps(unsigned r) { return pack2(lo_f(r) + 1e-6f, hi_f(r) + 1e-6f); }
#define VMCNT(n) asm volatile("s_waitcnt vmcnt(" #n ")" ::: "memory")
#define RAW_BARRIER() do { asm volatile("s_waitcnt lgkmcnt(0)" ::: "memory"); __builtin_amdgcn_s_barrier(); asm volatile("" ::: "memory"); } while (0)
__device__ __forceinline__ void glds16(const void* g, void* l) { __builtin_amdgcn_global_load_lds((gptr_t)g, (lptr_t)l, 16, 0, 0); }

#define OPQ_S(v) asm volatile("" : "+s"(v))
typedef f32x2 cf;
__device__ __forceinline__ float smul(float a, float b) { float o; asm("v_mul_f32 %0, %1, %2" : "=v"(o) : "v"(a), "v"(b)); return o; }
__device__ __forceinline__ float sfma(float a, float b, float c) { float o; asm("v_fma_f32 %0, %1, %2, %3" : "=v"(o) : "v"(a), "v"(b), "v"(c)); return o; }
__device__ __forceinline__ float sfnma(float a, float b, float c) { float o; asm("v_fma_f32 %0, -%1, %2, %3" : "=v"(o) : "v"(a), "v"(b), "v"(c)); return o; }
__device__ __forceinline__ float ssub(float a, float b) { float o; asm("v_sub_f32 %0, %1, %2" : "=v"(o) : "v"(a), "v"(b)); return o; }
__device__ __forceinline__ cf cmul(cf a, cf b) {
  cf o;
  asm("v_pk_mul_f32 %0, %1, %2 op_sel:[0,0] op_sel_hi:[0,1]\n\t"
      "v_pk_fma_f32 %0, %1, %2, %0 op_sel:[1,1,0] op_sel_hi:[1,0,1] neg_lo:[1,0,0]" : "=&v"(o) : "v"(a), "v"(b));
  return o;
}

#define CPF 3
#define LPF 1
#define PFD 0
#define PFM_ 1
#define NP0 5
#define SPRIO 3
#define NI0 208
#define NB2 3
#define TB0 1
#define TBD 2
#define MIDX ((rowblk & 3) + 4 * (cb & 7))
#define MPS 3
#define PT0 30
#define PT1 110
#define PTD 20
constexpr int K1_LDS = 81920;
#define GLDS 1
constexpr int NB_GEMM = 256, NB_GATE = GLDS ? 0 : 16, NB_KG = 240, NB_KC = 214;
#define KVAT 0
#define KV1 ""
#define KVB 0
#define ROT 0
#define TPQ 0
#define KVST
#define STG 0
#define SA0 8
#define SB0 8
#define SA1 0
#define SB1 8
#define WOT 1
#define KRR 1
#define B6I 5
#define WE 3
#define SGB 1
#define XB 1
#define UTE 0
#define NE 48
#define MPRIO 1
#define EPRIO 1
#define W7P 2
#define GSL 100
#define GPRIO 0
#define TPOL ""
#define JGX 1
#define LATEPQ 1
#define SIGI 2
#define NW0 3
#define K2PF 1
#define K2PFS "1"
#define KVP 1
#define PQA 2
#define PQB 4
#define STR2(x) #x
#define STR(x) STR2(x)
#define PQBS "2*(" STR(PQB) "-" STR(PQA) ")"
#define WTQ 0
#define WTP 0
#define WTO 0
#define WT 1
__device__ __forceinline__ void st16(void* p, u32x4 v) {
  asm volatile("global_store_dwordx4 %0, %1, off sc1\n\ts_nop 2" :: "v"(p), "v"(v) : "memory");
}

#define BAR_ONLY() do { asm volatile("" ::: "memory"); __builtin_amdgcn_s_barrier(); asm volatile("" ::: "memory"); } while (0)
#define SB() __builtin_amdgcn_sched_barrier(0)
template <bool ISQ>
__device__ __forceinline__ void mfma_part(char* smem, char* __restrict__ ws, int wave, f32x4 (&ep)[2][4], int zc) {
  const int lane = threadIdx.x & 63;
  char* sB = smem; char* sA = smem + 32768;
  const int wr = wave & 1, wc = (wave >> 1) & 1;
  const int g = lane >> 4, fr = lane & 15;
  f32x4 acc[4][4];
#pragma unroll
  for (int m = 0; m < 4; m++)
#pragma unroll
    for (int c = 0; c < 4; c++) acc[m][c] = (f32x4){0.f, 0.f, 0.f, 0.f};
  const int q4 = fr >> 2, p4 = lane & 3;
  int aoff[2];
#pragma unroll
  for (int ks = 0; ks < 2; ks++) aoff[ks] = (wr * 64 + fr) * 128 + (((4 * ks + g) ^ (fr & 7)) << 4);
  int boff[4];
  {
    const int f = q4 | ((g & 1) << 2);
#pragma unroll
    for (int c = 0; c < 4; c++) boff[c] = (8 * g + q4) * 256 + (((4 * wc + c) ^ f) << 5) + p4 * 8;
  }
  s16x8 af[2][4]; s16x4 bl[4], bh[4];
  auto rdA = [&](int buf, int m, const char* pa) { af[buf][m] = *(const s16x8*)(pa + m * 2048); };
  auto rdBl = [&](int c, const char* pb) { bl[c] = __builtin_amdgcn_ds_read_tr16_b64_v4i16((lds_s16x4_p)(pb + boff[c])); };
  auto rdBh = [&](int c, const char* pb) { bh[c] = __builtin_amdgcn_ds_read_tr16_b64_v4i16((lds_s16x4_p)(pb + boff[c] + 1024)); };
  __builtin_amdgcn_s_setprio(MPRIO);
  BAR_ONLY();
  STAMP(0, 2);
#pragma unroll
  for (int c = 0; c < 4; c++) rdA(0, c, sA + aoff[0]);
#pragma unroll
  for (int c = 0; c < 3; c++) { rdBl(c, sB); rdBh(c, sB); }
  int ao0 = 0, ao1 = 16384;
  auto grp = [&](int buf, int c, int m) {
    const s16x8 bf = __builtin_shufflevector(bl[c], bh[c], 0, 1, 2, 3, 4, 5, 6, 7);
    if (ISQ) acc[m][c] = __builtin_amdgcn_mfma_f32_16x16x32_bf16(bf, af[buf][m], acc[m][c], 0, 0, 0);
    else     acc[m][c] = __builtin_amdgcn_mfma_f32_16x16x32_bf16(af[buf][m], bf, acc[m][c], 0, 0, 0);
  };
#pragma unroll 1
  for (int j2 = 0; j2 < 5; j2++) {
#pragma unroll
    for (int st = 0; st < 4; st++) {
      const int hs = st & 1;
      SB();
      asm volatile("s_waitcnt lgkmcnt(2)" ::: "memory"); __builtin_amdgcn_s_barrier(); asm volatile("" ::: "memory");
      SB();
      const char* pa = sA + (hs == 0 ? ao0 + aoff[1] : ao1 + aoff[0]);
      const char* pc = sB + st * 8192;
      const char* pb = sB + ((st + 1) & 3) * 8192;
      grp(hs, 0, 0); SB(); rdBl(3, pc); rdBh(3, pc); SB();
      grp(hs, 0, 1); SB(); rdA(hs ^ 1, 0, pa); rdA(hs ^ 1, 1, pa); SB();
      grp(hs, 0, 2); SB(); rdA(hs ^ 1, 2, pa); rdA(hs ^ 1, 3, pa); SB();
      grp(hs, 0, 3); SB();
#pragma unroll
      for (int c = 1; c < 4; c++) {
        grp(hs, c, 0); SB(); rdBl(c - 1, pb); rdBh(c - 1, pb); SB();
        grp(hs, c, 1); grp(hs, c, 2); grp(hs, c, 3); SB();
      }
      if (hs == 1) { ao0 = ao1; ao1 = ao1 == 32768 ? 0 : ao1 + 16384; }
    }
  }
  STAMP(0, 3);
  int l3 = threadIdx.x; asm volatile("" : "+v"(l3));
  char* xch = smem + 32768 + (l3 >> 6) * 8192 + (l3 & 63) * 16;
  if (ISQ || wc == 0) {
#pragma unroll
    for (int i = 0; i < 8; i++) *(f32x4*)(xch + i * 1024) = acc[2 + (i >> 2)][i & 3];
  }
#pragma unroll
  for (int i = 0; i < 8; i++) ep[i >> 2][i & 3] = acc[i >> 2][i & 3];
  RAW_BARRIER();
  if (!ISQ && wc == 1) {
    char* fv = smem + 16384 + wr * 8192 + (l3 & 63) * 16;
#pragma unroll
    for (int c = 0; c < 4; c++)
#pragma unroll
      for (int h = 0; h < 2; h++) {
        u32x4 a = {pack2(acc[2 * h][c][0], acc[2 * h][c][1]), pack2(acc[2 * h][c][2], acc[2 * h][c][3]),
                   pack2(acc[2 * h + 1][c][0], acc[2 * h + 1][c][1]), pack2(acc[2 * h + 1][c][2], acc[2 * h + 1][c][3])};
        if ((c == 0 || c == 3) && 16 * c + (l3 & 15) == zc) a = (u32x4){0x3F803F80u, 0x3F803F80u, 0x3F803F80u, 0x3F803F80u};
        *(u32x4*)(fv + h * 4096 + c * 1024) = a;
      }
  }
}
__device__ __forceinline__ void dma4x(const int (&vo)[4], const char* sbase, int ldsa) {
  asm volatile("s_mov_b32 m0, %5\n\ts_nop 0\n\tglobal_load_lds_dwordx4 %0, %4\n\tglobal_load_lds_dwordx4 %1, %4 offset:1024\n\t"
               "global_load_lds_dwordx4 %2, %4 offset:2048\n\tglobal_load_lds_dwordx4 %3, %4 offset:3072"
               :: "v"(vo[0]), "v"(vo[1]), "v"(vo[2]), "v"(vo[3]), "s"(sbase), "s"(ldsa) : "memory", "m0");
}
__device__ __forceinline__ unsigned touch_off(int gb, int q, int& r) {
  const int par = gb & 1, pidx = gb >> 3;
  const int stg = (q * 1366) >> 16, q15 = q & 15;
  r = q - 48 * stg;
  const int ln = q15 + (par ? (q15 < 4 ? 6 : q15 < 10 ? 10 : 14) : (q15 < 6 ? 0 : q15 < 11 ? 4 : 9));
  const unsigned ox = (unsigned)((((gb >> 1) & 3) * 512 + pidx * 16 + q15) * 1280 + stg * 128);
  const unsigned ow = (unsigned)((64 * stg + pidx * 2 + (r >> 4) - 1) * 3840 + ln * 128);
  return r < 16 ? ox : ow;
}
#define TOUCH_LDS(p, la) asm volatile("s_mov_b32 m0, %1\n\ts_nop 0\n\tglobal_load_lds_dword %0, off" TPOL :: "v"(p), "s"(la) : "memory", "m0")
__device__ __forceinline__ void gemm_role(char* smem, const u16* __restrict__ x, const u16* __restrict__ W, const u16* __restrict__ wo, char* __restrict__ ws,
                                          int rowblk, int cb, int slot, int par) {
  const int tid = threadIdx.x, lane = tid & 63;
  const int wave = __builtin_amdgcn_readfirstlane(tid >> 6);
  const bool ISQ = cb < 5;
  const int row0 = rowblk * 128, col0 = cb * 128;
  const int kvj = cb - 5;
  const int wr = wave & 1, wc = (wave >> 1) & 1, hf = wave >> 2;
  f32x4 ep[2][4];
  unsigned pf3 = 0;
  const int wce = wc ^ hf;
  const int o = (60 * kvj) & 7, zc = o ? 0 : 60;
  unsigned eo[2];
  int ta[2], tb[2];
  auto eoffs = [&](bool isq) {
    int l4 = threadIdx.x; asm volatile("" : "+v"(l4));
    const unsigned g = (l4 >> 4) & 3, fr = l4 & 15;
    if (isq) {
#pragma unroll
      for (int c = 0; c < 4; c++) {
        const unsigned q4 = (unsigned)(col0 / 4 + 16 * wce + 4 * c) + g;
        const unsigned hd = (q4 * 205u) >> 10, d4 = q4 - 5u * hd;
        const unsigned off = hd * 320u + (d4 < 4u ? 16u + ((d4 * 17u) & 0x21u) + fr * 2u : fr);
        if (c & 1) eo[c >> 1] |= off << 16; else eo[c >> 1] = off;
      }
      ta[0] = ta[1] = tb[0] = tb[1] = 0;
    } else {
      const unsigned long long TA = o ? 0x323322211100ull : 0x103322211100ull, TB = o ? 0x003232121010ull : 0x333232121010ull;
#pragma unroll
      for (int i = 0; i < 2; i++) {
        const int p = wave < 4 ? wave : wave * 2 - 4 + i;
        ta[i] = (int)((TA >> (4 * p)) & 15); tb[i] = (int)((TB >> (4 * p)) & 15);
        const int kc0 = 16 * ta[i] + 4 * (int)g - o;
        const int hk = (kc0 >= 20) + (kc0 >= 40), dk0 = kc0 - 20 * hk;
        const int vcol = 16 * tb[i] + (int)fr;
        const int dv = vcol == zc ? 20 : vcol - o - 20 * hk;
        const bool ok = (kc0 >= 0) & (kc0 < (kvj == 10 ? 40 : 60)) & (dv >= 0) & ((dv < 20) | (vcol == zc)) & (i == 0 || wave >= 4);
        eo[i] = ok ? (unsigned)((hk * 16 * 21 + dv) * 80 + dk0 * 4) : ~0u;
      }
    }
    asm volatile("" : "+v"(eo[0]), "+v"(eo[1]));
    SB();
  };
  if (wave >= 4) {
    const int lw = wave & 1;
    const int l0 = (int)(size_t)(lptr_t)smem;
    const int pidx = (rowblk & 3) + 4 * slot;
    __builtin_amdgcn_s_setprio(SPRIO);
    if (wave < 6) {
      int voA[4], voB[4];
#pragma unroll
      for (int i = 0; i < 4; i++) {
        voA[i] = (64 * lw + 8 * i + (lane >> 3)) * 1280 + (((lane & 7) ^ (lane >> 3)) << 4) - 1024 * i;
        const int fB = (lane >> 4) | ((i >> 1) << 2);
        const int c16 = (((lane & 15) >> 1) ^ fB) * 2 + (lane & 1);
        int bcol = col0 + 8 * c16;
        if (!ISQ) bcol = 640 + 640 * (c16 >> 3) + min(((60 * kvj) & ~7) + 8 * (c16 & 7), 632);
        voB[i] = (16 * lw + 4 * i + (lane >> 4)) * 3840 + bcol * 2 - 1024 * i;
      }
      const char* pA = (const char*)x + row0 * 1280;
      const char* pB = (const char*)W;
      const int lA = l0 + 32768 + lw * 8192, lB = l0 + lw * 4096;
      auto issA = [&](int slot_off, int h) { dma4x(voA, pA + h * 32 * 1280, lA + slot_off + h * 4096); if (h) pA += 128; };
      auto issB = [&](int slot_off) { dma4x(voB, pB, lB + slot_off); pB += 32 * 3840; };
      TGET(tl0);
      VMCNT(0);
      TGET(tl1);
      issA(0, 0); issB(0); issA(0, 1);
      issB(8192); issA(16384, 0);
      VMCNT(8);
      TGET(tl2);
      BAR_ONLY();
      issB(16384); issA(16384, 1);
      VMCNT(12); BAR_ONLY();
      issB(24576); issA(32768, 0); issA(32768, 1);
      VMCNT(12); BAR_ONLY();
      issB(0); issA(0, 0);
      int ao = 0, bo = 8192;
#pragma unroll 1
      for (int j = 1; j < 7; j++) {
        VMCNT(12); BAR_ONLY();
        issB(bo); bo = (bo + 8192) & 32767; issA(ao, 1);
        ao = ao == 32768 ? 0 : ao + 16384;
        VMCNT(12); BAR_ONLY();
        issB(bo); bo = (bo + 8192) & 32767; issA(ao, 0);
      }
      VMCNT(12); BAR_ONLY();
      issB(8192); issA(0, 1);
      VMCNT(12); BAR_ONLY();
      issB(16384);
      VMCNT(8); BAR_ONLY();
      issB(24576);
      {
        const int o = (pidx * 200 + lw * 100 + min(lane, 49)) * 128;
        asm volatile("global_load_dword %0, %1, %2" TPOL "\n\tglobal_load_dword %0, %1, %3" TPOL : "+v"(pf3) : "v"(o), "s"(wo), "s"((const char*)wo + 6400));
      }
      eoffs(ISQ);
      VMCNT(6); BAR_ONLY();
      VMCNT(2); BAR_ONLY();
      BAR_ONLY();
      TPUT(0, 4, 256, tl0);
      TPUT(0, 5, 256, tl1); TPUT(0, 6, 256, tl2);
      BAR_ONLY();
    } else {
      {
        constexpr int NH = (NI0 - 2 * NE) / 2;
        int r;
        const unsigned to = touch_off(pidx * 8 + (rowblk >> 2) * 2 + par, 2 * NE + lw * NH + min(lane, NH - 1), r);
        TOUCH_LDS((r < 16 ? (const char*)x : (const char*)W) + to, l0 + 65536 + 512 + lw * 256);
        SB();
      }
      u32x4 trv[2][4];
      constexpr int CNT2 = 480 - NI0, NL2 = (CNT2 + 2 * NB2 - 1) / (2 * NB2);
      const char* pp[NB2];
#pragma unroll
      for (int i = 0; i < NB2; i++) {
        const int it = NI0 * 32 + ((pidx & 3) * 8 + (pidx >> 2)) * CNT2 + min((2 * i + lw) * NL2 + min(lane, NL2 - 1), CNT2 - 1);
        const int stg = it / 1536, r = it - 1536 * stg, q = r & 15;
      const int ln = par ? (q < 4 ? 6 + q : q < 10 ? 10 + q : 14 + q) : (q < 6 ? q : q < 11 ? 4 + q : 9 + q);
        pp[i] = r < 512 ? (const char*)x + ((rowblk >> 2) * 512 + r) * 1280 + stg * 128 : (const char*)W + (64 * stg + (r >> 4) - 32) * 3840 + ln * 128;
      }
      const int tb2 = TB0 + TBD * (pidx & 3);
      int tro;
      {
        const int fr = lane & 15, g = lane >> 4, p4 = lane & 3, r = 8 * g + (fr >> 2);
        tro = l0 + 32768 + r * 128 + (((4 * lw + (p4 >> 1)) ^ (r & 7)) << 4) + (p4 & 1) * 8;
      }
      auto transp = [&](int ao) {
#pragma unroll
        for (int hh = 0; hh < 2; hh++) {
          u32x2 tr[8];
          const int a = (tro ^ (32 * hh)) + ao, a2 = (a ^ 64) + 512;
          asm volatile("ds_read_b64_tr_b16 %0, %8\n\tds_read_b64_tr_b16 %1, %9\n\tds_read_b64_tr_b16 %2, %8 offset:4096\n\tds_read_b64_tr_b16 %3, %9 offset:4096\n\t"
                       "ds_read_b64_tr_b16 %4, %8 offset:8192\n\tds_read_b64_tr_b16 %5, %9 offset:8192\n\tds_read_b64_tr_b16 %6, %8 offset:12288\n\tds_read_b64_tr_b16 %7, %9 offset:12288\n\t"
                       "s_waitcnt lgkmcnt(0)"
                       : "=&v"(tr[0]), "=&v"(tr[1]), "=&v"(tr[2]), "=&v"(tr[3]), "=&v"(tr[4]), "=&v"(tr[5]), "=&v"(tr[6]), "=&v"(tr[7])
                       : "v"(a), "v"(a2) : "memory");
#pragma unroll
          for (int i = 0; i < 4; i++) trv[hh][i] = (u32x4){tr[2 * i][0], tr[2 * i][1], tr[2 * i + 1][0], tr[2 * i + 1][1]};
        }
      };
      unsigned pf2 = 0;
      auto burst = [&](int hs) {
        static_assert(NB2 == 3, "");
        asm volatile("s_cmp_lg_u32 %4, %5\n\ts_cbranch_scc1 1f\n\tglobal_load_dword %0, %1, off" TPOL "\n\tglobal_load_dword %0, %2, off" TPOL "\n\tglobal_load_dword %0, %3, off" TPOL "\n1:"
                     : "+v"(pf2) : "v"(pp[0]), "v"(pp[1]), "v"(pp[2]), "s"(hs), "s"(tb2) : "scc");
      };
      auto utst = [&]() {
      asm volatile("s_waitcnt vmcnt(0)" :: "v"(pf2) : "memory");
      if (cb < 10) {
        u16* dst = (u16*)(ws + WS_UT) + (size_t)(64 * cb + 32 * lw + (lane & 15)) * UT_STRIDE + 8 * (lane >> 4);
#pragma unroll
        for (int hh = 0; hh < 2; hh++) {
#pragma unroll
          for (int i = 0; i < 4; i++) st16(dst + hh * 16 * UT_STRIDE + UT_PAD + row0 + 32 * i, trv[hh][i]);
          if (rowblk == 0) st16(dst + hh * 16 * UT_STRIDE, (u32x4){0u, 0u, 0u, 0u});
        }
      }
      };
      eoffs(ISQ);
      VMCNT(0);
      BAR_ONLY();
      int ao = 0;
#pragma unroll 1
      for (int j = 0; j < 10; j++) {
        BAR_ONLY();
        if (!lw) STEPT(2 * j);
        burst(2 * j);
        if (j == cb) transp(ao);
        BAR_ONLY();
        if (!lw) STEPT(2 * j + 1);
        burst(2 * j + 1);
        ao = ao == 32768 ? 0 : ao + 16384;
      }
      utst();
      BAR_ONLY();
    }
    __builtin_amdgcn_s_setprio(EPRIO);
    char* xch = smem + 32768 + ((wave & 3) ^ 2) * 8192 + lane * 16;
    if (ISQ || wce == 0) {
#pragma unroll
      for (int i = 0; i < 8; i++) ep[i >> 2][i & 3] = *(const f32x4*)(xch + i * 1024);
    }
  } else if (ISQ) {
    eoffs(true);
    mfma_part<true>(smem, ws, wave, ep, 0);
  } else {
    eoffs(false);
    mfma_part<false>(smem, ws, wave, ep, zc);
  }
  int l2 = threadIdx.x; asm volatile("" : "+v"(l2));
  const int lane2 = l2 & 63;
  f32x2 kl = {1.44269504f, 1.44269504f};
  asm volatile("" : "+s"(kl));
  char* fb = smem;
  if (ISQ || wce == 0) {
    float mn[2] = {ep[0][0][0], ep[1][0][0]};
#pragma unroll
    for (int i = 0; i < 16; i++) mn[i & 1] = __builtin_fminf(__builtin_fminf(mn[i & 1], ep[i & 1][i >> 2][i & 2]), ep[i & 1][i >> 2][(i & 2) + 1]);
    const bool small = __builtin_amdgcn_ballot_w64(!(__builtin_fminf(mn[0], mn[1]) > -7.f)) != 0;
    char* base = ws + WS_PQ + (size_t)((rowblk * 2 + wr) * 128 + 2 * hf) * 640;
    char* fk = fb + (wr * 2 + hf) * 4096 + lane2 * 16;
    auto body = [&](bool isq, bool eps) {
#pragma unroll
      for (int hh = 0; hh < 2; hh++) {
        f32x2 t[8], x[8];
#pragma unroll
        for (int i = 0; i < 8; i++) { const f32x4 v = ep[(i >> 1) & 1][2 * hh + (i >> 2)]; t[i] = (f32x2){rb1(v[2 * (i & 1)]), rb1(v[2 * (i & 1) + 1])}; }
        SB();
#pragma unroll
        for (int i = 0; i < 8; i++) x[i] = t[i] * kl;
        SB();
#pragma unroll
        for (int i = 0; i < 8; i++) {
          x[i][0] = __builtin_amdgcn_fmed3f(__builtin_amdgcn_exp2f(x[i][0]), 0.f, 1.f); x[i][1] = __builtin_amdgcn_fmed3f(__builtin_amdgcn_exp2f(x[i][1]), 0.f, 1.f);
          t[i] = t[i] + (f32x2){1.f, 1.f};
        }
        SB();
#pragma unroll
        for (int i = 0; i < 8; i++) { t[i][0] = __builtin_fmaxf(t[i][0], x[i][0]); t[i][1] = __builtin_fmaxf(t[i][1], x[i][1]); }
        SB();
        u32x4 a[2];
#pragma unroll
        for (int i = 0; i < 8; i++) { a[i >> 2][i & 3] = pack2(t[i][0], t[i][1]); if (eps) a[i >> 2][i & 3] = phi_eps(a[i >> 2][i & 3]); }
        SB();
#pragma unroll
        for (int k = 0; k < 2; k++) {
          if (isq) {
            const unsigned off = (k ? eo[hh] >> 16 : eo[hh] & 0xffffu) * 8u;
            *(u32x2*)(base + off) = (u32x2){a[k][0], a[k][1]};
            *(u32x2*)(base + off + 640) = (u32x2){a[k][2], a[k][3]};
          } else {
            *(u32x4*)(fk + (2 * hh + k) * 1024) = a[k];
          }
        }
        SB();
      }
    };
    if (__builtin_expect(small, 0)) body(ISQ, true); else if (__builtin_expect(ISQ, 0)) body(true, false); else body(false, false);
  }
  if (!ISQ) {
    RAW_BARRIER();
    s16x8 fk[2][4], fv[2][4];
#pragma unroll
    for (int rg = 0; rg < 4; rg++)
#pragma unroll
      for (int i = 0; i < 2; i++) {
        fk[i][rg] = *(const s16x8*)(fb + ((ta[i] * 64 + lane2) << 4) + rg * 4096);
        fv[i][rg] = *(const s16x8*)(fb + 16384 + ((tb[i] * 64 + lane2) << 4) + rg * 4096);
      }
    f32x4 dd[2] = {{0.f, 0.f, 0.f, 0.f}, {0.f, 0.f, 0.f, 0.f}};
#pragma unroll
    for (int rg = 0; rg < 4; rg++)
#pragma unroll
      for (int i = 0; i < 2; i++) dd[i] = __builtin_amdgcn_mfma_f32_16x16x32_bf16(fk[i][rg], fv[i][rg], dd[i], 0, 0, 0);
    char* pb = ws + WS_PART + (size_t)((3 * kvj * 16 + rowblk) * 21) * 80;
#pragma unroll
    for (int i = 0; i < 2; i++)
      if (eo[i] != ~0u) *(f32x4*)(pb + eo[i]) = dd[i];
  }
  asm volatile("s_waitcnt vmcnt(0)" :: "v"(pf3) : "memory");
}

__device__ __forceinline__ cf zpow(float th, float tl, float rh, float rl, float p) {
  float e = __builtin_amdgcn_exp2f(th * p); e = fmaf(e, (tl * p) * 0.69314718f, e);
  float t = rh * p; float f = (t - __builtin_rintf(t)) + rl * p;
  cf o; o[0] = e * __builtin_amdgcn_cosf(f); o[1] = e * __builtin_amdgcn_sinf(f); return o;
}
#define PKM(o, a, b) "v_pk_mul_f32 " o ", " a ", " b " op_sel:[0,0] op_sel_hi:[0,1]\n\t"
#define PKF(o, a, b) "v_pk_fma_f32 " o ", " a ", " b ", " o " op_sel:[1,1,0] op_sel_hi:[1,0,1] neg_lo:[1,0,0]\n\t"
__device__ __forceinline__ void cmul4(cf& o0, cf& o1, cf& o2, cf& o3, cf a0, cf b0, cf a1, cf b1, cf a2, cf b2, cf a3, cf b3) {
  asm(PKM("%0", "%4", "%5") PKM("%1", "%6", "%7") PKM("%2", "%8", "%9") PKM("%3", "%10", "%11")
      PKF("%0", "%4", "%5") PKF("%1", "%6", "%7") PKF("%2", "%8", "%9") PKF("%3", "%10", "%11")
      : "=&v"(o0), "=&v"(o1), "=&v"(o2), "=&v"(o3)
      : "v"(a0), "v"(b0), "v"(a1), "v"(b1), "v"(a2), "v"(b2), "v"(a3), "v"(b3));
}
__device__ __forceinline__ void batch4(unsigned* h, cf b, cf t0, cf t1, cf t2, cf t3) {
  cf w0, w1, w2, w3;
  asm(PKM("%0", "%4", "%5") PKM("%1", "%4", "%6") PKM("%2", "%4", "%7") PKM("%3", "%4", "%8")
      PKF("%0", "%4", "%5") PKF("%1", "%4", "%6") PKF("%2", "%4", "%7") PKF("%3", "%4", "%8")
      : "=&v"(w0), "=&v"(w1), "=&v"(w2), "=&v"(w3) : "v"(b), "v"(t0), "v"(t1), "v"(t2), "v"(t3));
  h[0] = pack2(w0[0], w0[1]); h[1] = pack2(w1[0], w1[1]); h[2] = pack2(w2[0], w2[1]); h[3] = pack2(w3[0], w3[1]);
}
__device__ __forceinline__ void batch3(unsigned* h, cf b, cf t1, cf t2, cf t3) {
  cf w1, w2, w3;
  asm(PKM("%0", "%3", "%4") PKM("%1", "%3", "%5") PKM("%2", "%3", "%6")
      PKF("%0", "%3", "%4") PKF("%1", "%3", "%5") PKF("%2", "%3", "%6")
      : "=&v"(w1), "=&v"(w2), "=&v"(w3) : "v"(b), "v"(t1), "v"(t2), "v"(t3));
  h[0] = pack2(b[0], b[1]); h[1] = pack2(w1[0], w1[1]); h[2] = pack2(w2[0], w2[1]); h[3] = pack2(w3[0], w3[1]);
}
constexpr int KG_RS = 288, KG_PL = 16 * KG_RS;
__device__ __forceinline__ void kgen_role(char* smem, const u16* __restrict__ logA, const u16* __restrict__ Aim,
                                          const u16* __restrict__ Cr, const u16* __restrict__ Ci, const u16* __restrict__ logdt,
                                          const u16* __restrict__ x, const u16* __restrict__ W, const u16* __restrict__ wo,
                                          const u16* __restrict__ gw, char* __restrict__ ws, int blk, unsigned pfv) {
  const int tid = threadIdx.x, lane = tid & 63;
  const int wave = __builtin_amdgcn_readfirstlane(tid >> 6);
  constexpr int RS = KG_RS, PL = KG_PL;
  char* tb = smem + wave * (2 * PL);
  char* wp = tb + lane * 4;
  const char* rp = tb + (lane & 15) * RS + (lane >> 4) * 16;
  const int kq = blk >> 3, tbase = (blk & 7) * 240;
  const int nrep = kq >= 27 ? 0 : (wave == 7 ? 2 : 1);
  unsigned pa[5], pb[5] = {0u, 0u, 0u, 0u, 0u};
  {
    const int task = tbase + min(kq * 9 + wave, 239), pi = task * 64 + lane;
    pa[0] = logdt[task / 3]; pa[1] = logA[pi]; pa[2] = Aim[pi]; pa[3] = Cr[pi]; pa[4] = Ci[pi];
    if (wave == 7) {
      const int t2 = tbase + min(kq * 9 + 8, 239), p2 = t2 * 64 + lane;
      pb[0] = logdt[t2 / 3]; pb[1] = logA[p2]; pb[2] = Aim[p2]; pb[3] = Cr[p2]; pb[4] = Ci[p2];
    }
  }
  int tix[8];
#pragma unroll
  for (int e = 0; e < 8; e++) {
    const int j = (tid & 255) * 8 + 7 - e;
    const int p = (int)rb((float)j);
    const int hh = p >> 7, c = (p >> 3) & 15;
    int sh = 31 - __builtin_clz(hh | 1); sh = sh > 3 ? 3 : sh;
    const int row = 8 * sh + (((hh << 3) | (p & 7)) >> sh);
    tix[e] = 4096 + (tid >> 8) * (2 * PL) + (((row >> 2) * 16 + c) * 4 + (row & 3)) * 4;
    asm volatile("" : "+v"(tix[e]));
  }
  SB();
  asm volatile("" : "+v"(pb[0]), "+v"(pb[1]), "+v"(pb[2]), "+v"(pb[3]), "+v"(pb[4]));
  if (wave == 7) __builtin_amdgcn_s_setprio(W7P);
  const int gt = wave * 30 + (blk >> 3), grow = (blk & 7) * 256 + gt * 4 + (lane >> 4);
  u32x4 xv[5], gv[5];
  asm volatile("" : "+v"(pa[0]), "+v"(pa[1]), "+v"(pa[2]), "+v"(pa[3]), "+v"(pa[4]));
  if (gt < 64) {
    const u32x4* px = (const u32x4*)(x + (size_t)grow * D) + (lane & 15); const u32x4* pg = (const u32x4*)gw + (lane & 15);
#pragma unroll
    for (int i = 0; i < 5; i++) { xv[i] = px[16 * i]; gv[i] = pg[16 * i]; }
  }
  SB();
#pragma unroll 1
  for (int rep = 0; rep < nrep; rep++) {
    const float dt = rb(expf(lo_f(pa[0])));
    float ar;
    {
      const float la = lo_f(pa[1]);
      float h = la * 1.44269502f; float l = fmaf(la, 1.44269502f, -h) + la * 1.92596299e-8f;
      float e = __builtin_amdgcn_exp2f(h); e = fmaf(e, l * 0.69314718f, e);
      ar = -rb(e) * dt;
    }
    const float ai = lo_f(pa[2]) * dt;
    cf C; C[0] = 2.f * (lo_f(pa[3]) * dt); C[1] = 2.f * (lo_f(pa[4]) * dt);
#pragma unroll
    for (int i = 0; i < 5; i++) pa[i] = pb[i];
    const float th = ar * 1.44269502f, tl = fmaf(ar, 1.44269502f, -th) + ar * 1.92596299e-8f;
    const float rh = ai * 0.159154937f, rl = fmaf(ai, 0.159154937f, -rh) + ai * 6.42063833e-9f;
    const cf one = {1.f, 0.f};
    cf S1 = zpow(th, tl, rh, rl, 1.f), Q1 = zpow(th, tl, rh, rl, 8.f), R1 = zpow(th, tl, rh, rl, 128.f);
    Q1[1] = -Q1[1];
    cf S2, Q2, R2, G1, S3, S4, Q3, P1, R3, T1, G2, G3, S5, S6, S7, P2, CT1, T2, P3, CS4, CT2, CT3, CT4, dm0;
    cmul4(S2, Q2, R2, G1, S1, S1, Q1, Q1, R1, R1, C, R1);
    cmul4(S3, S4, Q3, P1, S2, S1, S2, S2, Q2, Q1, Q2, Q2);
    cmul4(R3, T1, G2, P2, R2, R1, R2, R2, C, R2, P1, P1);
    cmul4(S5, S6, S7, P3, S4, S1, S4, S2, S4, S3, P2, P1);
    cmul4(G3, CT1, T2, dm0, C, R3, C, T1, T1, T1, C, R1);
    cmul4(CS4, CT2, CT3, dm0, CT1, S4, C, T2, CT1, T2, C, R1);
    CT4 = cmul(CT2, T2);
    auto rows4 = [&](int pl, int row, int rs, cf b, cf t0, cf t1, cf t2, cf t3) {
      unsigned h[4];
      batch4(h, b, t0, t1, t2, t3);
#pragma unroll
      for (int k = 0; k < 4; k++) *(unsigned*)(wp + pl * PL + (row + k * rs) * RS) = h[k];
    };
    auto rows3 = [&](int pl, int row, int rs, cf b, cf t1, cf t2, cf t3) {
      unsigned h[4];
      batch3(h, b, t1, t2, t3);
#pragma unroll
      for (int k = 0; k < 4; k++) *(unsigned*)(wp + pl * PL + (row + k * rs) * RS) = h[k];
    };
    *(unsigned*)(wp) = 0x00003F80u;
    *(unsigned*)(wp + RS) = pack2(Q1[0], Q1[1]); *(unsigned*)(wp + 2 * RS) = pack2(Q2[0], Q2[1]); *(unsigned*)(wp + 3 * RS) = pack2(Q3[0], Q3[1]);
    rows3(0, 4, 1, P1, Q1, Q2, Q3); rows3(0, 8, 1, P2, Q1, Q2, Q3); rows3(0, 12, 1, P3, Q1, Q2, Q3);
    rows3(1, 0, 1, C, S1, S2, S3); rows4(1, 4, 1, C, S4, S5, S6, S7); rows3(1, 8, 1, G1, S1, S2, S3); rows4(1, 12, 1, G1, S4, S5, S6, S7);
    __builtin_amdgcn_wave_barrier();
    s16x8 bh[4];
    f32x4 res[3];
#pragma unroll
    for (int k = 0; k < 4; k++) bh[k] = *(const s16x8*)(rp + k * 64);
    auto mm = [&](int t, int pl) {
      f32x4 a = {0.f, 0.f, 0.f, 0.f};
#pragma unroll
      for (int k = 0; k < 4; k++) a = __builtin_amdgcn_mfma_f32_16x16x32_bf16(*(const s16x8*)(rp + pl * PL + k * 64), bh[k], a, 0, 0, 0);
      res[t] = a;
    };
    mm(0, 1);
    __builtin_amdgcn_wave_barrier();
    rows3(0, 0, 1, G2, S2, S4, S6); rows3(0, 4, 1, G3, S2, S4, S6); rows3(0, 8, 2, CT1, R1, R2, R3); rows3(0, 9, 2, CS4, R1, R2, R3);
    rows3(1, 0, 1, CT2, R1, R2, R3); rows3(1, 4, 1, CT3, R1, R2, R3);
    *(unsigned*)(wp + PL + 8 * RS) = pack2(CT4[0], CT4[1]);
    __builtin_amdgcn_wave_barrier();
    mm(1, 0); mm(2, 1);
    __builtin_amdgcn_wave_barrier();
    char* rs = (wave == 7 && rep == 0) ? smem + 8 * (2 * PL) : tb;
#pragma unroll
    for (int t = 0; t < 3; t++) *(f32x4*)(rs + t * 1024 + lane * 16) = res[t];
    __builtin_amdgcn_wave_barrier();
  }
  STAMP(0, 4);
  if (gt < 64) {
    float gs = 0.f;
#pragma unroll
    for (int i = 0; i < 5; i++)
#pragma unroll
      for (int e = 0; e < 4; e++) {
        unsigned p = pack2(lo_f(xv[i][e]) * lo_f(gv[i][e]), hi_f(xv[i][e]) * hi_f(gv[i][e]));
        gs = __builtin_amdgcn_fdot2_f32_bf16(__builtin_bit_cast(bf16x2_t, p), __builtin_bit_cast(bf16x2_t, 0x3F803F80u), gs, false);
      }
    gs += __shfl_xor(gs, 1); gs += __shfl_xor(gs, 2); gs += __shfl_xor(gs, 4); gs += __shfl_xor(gs, 8);
    if ((lane & 15) == 0) { float a = rb(gs); ((float*)(ws + WS_GATE))[grow] = rb(1.f / (1.f + __expf(-a))); }
  }
  if (blk == 239) ((unsigned*)(ws + WS_CNT))[tid] = 0u, ((unsigned*)(ws + WS_CNT))[tid + 512] = 0u;
  if (blk >= 216 && blk < 220) st16(ws + WS_KVT + (size_t)((blk - 216) * 512 + tid) * 16, (u32x4){~0u, ~0u, ~0u, ~0u});
  if (kq < 27) {
    __syncthreads();
#pragma unroll
    for (int it = 0; it < 2; it++) {
      const int e = tid + 512 * it;
      const int seg = e / 192, ent = e - 192 * seg;
      if (seg < 3) {
        const char* p0 = smem + 3 * seg * (2 * PL) + ent * 16;
        const f32x4 a = *(const f32x4*)p0 + *(const f32x4*)(p0 + (seg == 2 ? 2 : 1) * (2 * PL)) + *(const f32x4*)(p0 + (seg == 2 ? 1 : 2) * (2 * PL));
        *(f32x4*)(smem + 4096 + seg * (2 * PL) + ent * 16) = a;
      }
    }
    __syncthreads();
    const int d0 = 80 * (blk & 7) + 3 * kq + (tid >> 8);
    u32x4 hi;
#pragma unroll
    for (int e = 0; e < 4; e++) hi[e] = pack2(*(const float*)(smem + tix[2 * e]), *(const float*)(smem + tix[2 * e + 1]));
    st16(ws + WS_KIMG + (size_t)d0 * 4096 + (tid & 255) * 16, hi);
    if (wave < 4 && kq < 26) {
#pragma unroll
      for (int e = 0; e < 4; e++) hi[e] = pack2(*(const float*)(smem + tix[2 * e] + 4 * PL), *(const float*)(smem + tix[2 * e + 1] + 4 * PL));
      st16(ws + WS_KIMG + (size_t)(d0 + 2) * 4096 + tid * 16, hi);
    }
    VMCNT(0);
    if (lane < 16) {
      const int o = (int)WS_KIMG + (d0 + ((lane & 8) && wave < 4 && kq < 26 ? 2 : 0)) * 4096 + (wave & 3) * 1024 + (lane & 7) * 128;
      asm volatile("global_load_dword %0, %1, %2" : "+v"(pfv) : "v"(o), "s"(ws));
    }
  }
  asm volatile("s_waitcnt vmcnt(0)" :: "v"(pfv) : "memory");
}

__global__ __launch_bounds__(512, 4) void k1(const u16* __restrict__ x, const u16* __restrict__ logA, const u16* __restrict__ Aim,
                                          const u16* __restrict__ Cr, const u16* __restrict__ Ci, const u16* __restrict__ logdt,
                                          const u16* __restrict__ wqkv, const u16* __restrict__ gw, const u16* __restrict__ wo,
                                          char* __restrict__ ws) {
  __shared__ __attribute__((aligned(16))) char smem[K1_LDS];
  int b = blockIdx.x;
  unsigned cpf = 0;
  const int w4 = __builtin_amdgcn_readfirstlane(threadIdx.x >> 6) & 3;
  int r = 0; unsigned to = 0;
  if (b >= NB_KG) {
    __builtin_amdgcn_s_setprio(GPRIO);
    to = touch_off(b - NB_KG, w4 * NE + min((int)(threadIdx.x & 63), NE - 1), r);
    asm volatile("" : "+v"(to), "+v"(r));
  }
  asm volatile("" :: "s"(x), "s"(logA), "s"(Aim), "s"(Cr), "s"(Ci), "s"(logdt), "s"(wqkv), "s"(gw), "s"(wo), "s"(ws));
  if (b >= NB_KG && threadIdx.x >= 256 && threadIdx.x < 384) TOUCH_LDS((r < 16 ? (const char*)x : (const char*)wqkv) + to, (int)(size_t)(lptr_t)smem + 65536 + w4 * 256);
  if (b < NB_KG && threadIdx.x >= 501) {
    const char* pc; int len;
    asm volatile("s_getpc_b64 %0\n\ts_mov_b32 %1, code_tail_mark-_Z2k1PKtS0_S0_S0_S0_S0_S0_S0_S0_Pc-128" : "=s"(pc), "=s"(len));
    const int off = min((int)((threadIdx.x - 501) * 30 + (b >> 3)) * 128, len);
    asm volatile("global_load_dword %0, %1, %2" : "+v"(cpf) : "v"(off), "s"(pc));
  }
  if (b < 8 && threadIdx.x >= 490 && threadIdx.x < 500) asm volatile("global_load_dword %0, %1, %2" : "+v"(cpf) : "v"((threadIdx.x - 490) * 128), "s"(gw));
  STAMP(0, 0);
  b = b < NB_KG ? b + NB_GEMM : (b < NB_KG + NB_GEMM ? b - NB_KG : b);
  if (b < NB_GEMM) {
    const int xcd = b & 7, idx = b >> 3;
    const int rowblk = (xcd >> 1) * 4 + (idx & 3), slot = idx >> 2, par = xcd & 1;
    const int cb = par ? (slot < 2 ? slot + 3 : slot + 8) : (slot < 3 ? slot : slot + 2);
    gemm_role(smem, x, wqkv, wo, ws, rowblk, cb, slot, par);
    STAMP(0, 1);
    return;
  }
  b -= NB_GEMM;
  if (b < NB_KG) { kgen_role(smem, logA, Aim, Cr, Ci, logdt, x, wqkv, wo, gw, ws, b, cpf); STAMP(0, 1); return; }
}

constexpr int K2_LDS = 163840;
constexpr int K2_CS = 4160;
constexpr int K2_KOFF = 4 * K2_CS;
constexpr int K2_CH = K2_KOFF + 4608;
constexpr int K2_CONV = K2_LDS - 3 * K2_CH;
constexpr int K2_TAB = 81920;
constexpr int K2_FIN = K2_TAB + 9216;
constexpr int K2_KV = 102400;
constexpr int K2_STG = 131072;
constexpr int NB_KV = 32;
typedef __attribute__((address_space(1))) unsigned gu32;
__device__ __forceinline__ const char* uni(const char* p) {
  size_t v = (size_t)p;
  unsigned lo = __builtin_amdgcn_readfirstlane((unsigned)v), hi = __builtin_amdgcn_readfirstlane((unsigned)(v >> 32));
  size_t o = ((size_t)hi << 32) | lo;
  asm volatile("s_nop 4" : "+s"(o));
  return (const char*)o;
}
__device__ __forceinline__ void wait_count(char* p, unsigned target) {
  gu32* c = (gu32*)(size_t)p;
  for (int n = 0; n < (1 << 22); n++) {
    if (__hip_atomic_load(c, __ATOMIC_RELAXED, __HIP_MEMORY_SCOPE_AGENT) >= target) break;
    __builtin_amdgcn_s_sleep(8);
  }
}
#define GLD4(dst, voff, sbase, pol) asm volatile("global_load_dwordx4 %0, %1, %2" pol : "=v"(dst) : "v"(voff), "s"(sbase))
#define GLD2(dst, voff, sbase, pol) asm volatile("global_load_dwordx2 %0, %1, %2" pol : "=v"(dst) : "v"(voff), "s"(sbase))
#define GLD1(dst, voff, sbase, pol) asm volatile("global_load_dword %0, %1, %2" pol : "=v"(dst) : "v"(voff), "s"(sbase))
template <int G>
__device__ __forceinline__ void conv_unit(f32x4* acc, int ap, int bp) {
  constexpr int NT = 8 - G;
  u32x2 a0[NT], a1[NT]; s16x8 bh[NT];
#pragma unroll
  for (int j = 0; j < NT; j++)
    asm volatile("ds_read_b64 %0, %3 offset:%5\n\tds_read_b64 %1, %3 offset:%6\n\tds_read_b128 %2, %4 offset:%5"
                 : "=&v"(a0[j]), "=&v"(a1[j]), "=&v"(bh[j]) : "v"(ap), "v"(bp), "n"(512 * j), "n"(512 * j + 8) : "memory");
#pragma unroll
  for (int x = 0; x < NT; x++) {
    asm volatile("s_waitcnt lgkmcnt(%3)" : "+v"(a0[x]), "+v"(a1[x]), "+v"(bh[x]) : "n"(3 * (NT - 1 - x) > 15 ? 15 : 3 * (NT - 1 - x)) : "memory");
#pragma unroll
    for (int y = 0; y <= x; y++)
#pragma unroll
      for (int sw = 0; sw < 2; sw++) {
        if (sw && y == x) continue;
        const int j = sw ? y : x, m = sw ? x : y, T = j + m;
        const int ti = G == 0 ? (T == 0 ? 0 : T == 3 ? 1 : T == 4 ? 2 : T == 7 ? 3 : -1) : (T == 1 ? 0 : T == 2 ? 1 : T == 5 ? 2 : T == 6 ? 3 : -1);
        if (ti >= 0) acc[ti] = __builtin_amdgcn_mfma_f32_16x16x32_bf16(__builtin_bit_cast(s16x8, (u32x4){a0[j][0], a0[j][1], a1[j][0], a1[j][1]}), bh[m], acc[ti], 0, 0, 0);
      }
    SB();
  }
}

__global__ __launch_bounds__(576) void k2(const u16* __restrict__ Dskip, const u16* __restrict__ wo, char* __restrict__ ws, u16* __restrict__ out) {
  __shared__ __attribute__((aligned(16))) char smem[K2_LDS];
  const int tid = threadIdx.x, lane = tid & 63;
  const int wave = __builtin_amdgcn_readfirstlane(tid >> 6);
  const int b = blockIdx.x;
  unsigned cpf = 0;
  if (b < 32 && tid < 192) {
    const char* pc; int len;
    asm volatile("s_getpc_b64 %0\n\ts_mov_b32 %1, code_tail_mark-_Z2k2PKtS0_PcPt-128" : "=s"(pc), "=s"(len));
    const int off = min(tid * 128, len);
    asm volatile("global_load_dword %0, %1, %2" : "+v"(cpf) : "v"(off), "s"(pc));
  }
  STAMP(1, 0);
  const int lds0 = (int)(size_t)(lptr_t)smem;
  const int l16 = lane * 16;
  const int xcd = b & 7, idx = b >> 3;
  const int odd = idx & 1;
  const int chA = 80 * xcd + 5 * (idx >> 1) + 3 * odd, chH = 80 * xcd + 5 * (idx >> 1) + 2;
  const int rowblk = xcd * 4 + (idx >> 3), cb = idx & 7;
  const int l0 = rowblk * 64, c0 = cb * 80;
  const int r = lane & 31, h = lane >> 5;
  if (wave == 8) {
    __builtin_amdgcn_s_setprio(3);
    const char* wsrc = (const char*)wo + cb * 160;
    int wv[5];
#pragma unroll
    for (int j = 0; j < 5; j++) { const int f = 64 * j + lane, row = f / 10; wv[j] = row * 1280 + (f - 10 * row) * 16 - (j & 3) * 1024; }
    auto dma5 = [&](int grp) {
      asm volatile("s_mov_b32 m0, %6\n\ts_nop 0\n\tglobal_load_lds_dwordx4 %0, %5\n\tglobal_load_lds_dwordx4 %1, %5 offset:1024\n\t"
                   "global_load_lds_dwordx4 %2, %5 offset:2048\n\tglobal_load_lds_dwordx4 %3, %5 offset:3072\n\t"
                   "s_add_u32 m0, m0, 0x1000\n\ts_nop 0\n\tglobal_load_lds_dwordx4 %4, %5"
                   :: "v"(wv[0]), "v"(wv[1]), "v"(wv[2]), "v"(wv[3]), "v"(wv[4]), "s"(wsrc + grp * 40960), "s"(lds0 + grp * 5120) : "memory", "m0", "scc");
    };
    auto dma4 = [&](const char* src, int dst) {
      asm volatile("s_mov_b32 m0, %2\n\ts_nop 0\n\tglobal_load_lds_dwordx4 %0, %1\n\tglobal_load_lds_dwordx4 %0, %1 offset:1024\n\t"
                   "global_load_lds_dwordx4 %0, %1 offset:2048\n\tglobal_load_lds_dwordx4 %0, %1 offset:3072"
                   :: "v"(l16), "s"(src), "s"(lds0 + dst) : "memory", "m0");
    };
    auto kimg = [&]() {
      dma4(ws + WS_KIMG + chA * 4096, K2_CONV + K2_KOFF + 512);
      dma4(ws + WS_KIMG + chA * 4096 + 4096, K2_CONV + K2_CH + K2_KOFF + 512);
      dma4(ws + WS_KIMG + chH * 4096, K2_CONV + 2 * K2_CH + K2_KOFF + 512);
    };
    if (b < NB_KV) {
      f32x4 kvp[32];
      const char* src = ws + WS_PART + (size_t)b * 16 * 1680;
      const int o2 = min(lane + 64, 104) * 16;
#pragma unroll
      for (int i = 0; i < 16; i++) { GLD4(kvp[i], l16, src + i * 1680, ""); GLD4(kvp[16 + i], o2, src + i * 1680, ""); }
      kimg();
#pragma unroll
      for (int i = 0; i < NW0; i++) dma5(i);
      asm volatile("s_waitcnt vmcnt(%0)" :: "n"(5 * NW0) : "memory");
      BAR_ONLY();
#pragma unroll
      for (int i = 0; i < 32; i += 8)
        asm volatile("" : "+v"(kvp[i]), "+v"(kvp[i + 1]), "+v"(kvp[i + 2]), "+v"(kvp[i + 3]), "+v"(kvp[i + 4]), "+v"(kvp[i + 5]), "+v"(kvp[i + 6]), "+v"(kvp[i + 7]));
#pragma unroll
      for (int st = 1; st < 16; st <<= 1)
#pragma unroll
        for (int i = 0; i < 32; i += 2 * st) kvp[i] += kvp[i + st];
      const u32x2 o0 = {pack2(kvp[0][0], kvp[0][1]), pack2(kvp[0][2], kvp[0][3])}, o1 = {lane < 41 ? pack2(kvp[16][0], kvp[16][1]) : 0u, lane < 41 ? pack2(kvp[16][2], kvp[16][3]) : 0u};
      const int o = (int)WS_KVT + b * 896 + lane * 8;
      asm volatile("global_store_dwordx2 %0, %1, %2 sc1" :: "v"(o), "v"(o0), "s"(ws) : "memory");
      if (lane < 48) asm volatile("global_store_dwordx2 %0, %1, %2 offset:512 sc1\n\ts_nop 1" :: "v"(o), "v"(o1), "s"(ws) : "memory");
      KVST;
    } else {
      kimg();
#pragma unroll
      for (int i = 0; i < NW0; i++) dma5(i);
      asm volatile("s_waitcnt vmcnt(%0)" :: "n"(5 * NW0) : "memory");
      BAR_ONLY();
    }
#pragma unroll 1
    for (int p = NW0; p < 19; p++) {
      dma5(p);
    }
    {
      u32x4 kt[28];
      const char* kp = ws + WS_KVT;
#define KVL(pol) _Pragma("unroll") for (int i = 0; i < 28; i++) GLD4(kt[i], l16, kp + i * 1024, pol);
#define KVW() _Pragma("unroll") for (int i = 0; i < 28; i += 7) asm volatile("" : "+v"(kt[i]), "+v"(kt[i + 1]), "+v"(kt[i + 2]), "+v"(kt[i + 3]), "+v"(kt[i + 4]), "+v"(kt[i + 5]), "+v"(kt[i + 6]));
      KVL(KV1);
      VMCNT(0);
      KVW();
      for (int n = 0; n < (1 << 14); n++) {
        unsigned m = 0;
#pragma unroll
        for (int i = 0; i < 28; i++) m = max(max(m, kt[i][0]), kt[i][2]);
        if (__builtin_amdgcn_ballot_w64(m == ~0u) == 0ull) break;
        KVL(" sc1");
        VMCNT(0);
        KVW();
      }
      BAR_ONLY();
#pragma unroll
      for (int i = 0; i < 14; i++) *(u32x4*)(smem + K2_KV + i * 1024 + l16) = kt[i];
      BAR_ONLY();
#pragma unroll
      for (int i = 14; i < 28; i++) *(u32x4*)(smem + K2_KV + i * 1024 + l16) = kt[i];
    }
    asm volatile("s_waitcnt lgkmcnt(0)" ::: "memory");
    BAR_ONLY();
    dma5(19);
    VMCNT(0);
    BAR_ONLY();
    {
      gu32* c = (gu32*)(size_t)(ws + WS_CNT + (8 + cb) * 128);
      for (int n = 0; n < (1 << 22); n++) {
        if (__hip_atomic_load(c, __ATOMIC_RELAXED, __HIP_MEMORY_SCOPE_AGENT) >= 32u) break;
        __builtin_amdgcn_s_sleep(1);
      }
      asm volatile("" ::: "memory");
      const int o = (lane >> 3) * (L * 2) + (((lane & 7) ^ ((lane >> 2) & 4)) << 4);
      const char* src = ws + WS_AT + (size_t)(c0 * L + l0) * 2;
#pragma unroll
      for (int i = 0; i < 10; i++)
        asm volatile("s_mov_b32 m0, %2\n\ts_nop 0\n\tglobal_load_lds_dwordx4 %0, %1 sc1" :: "v"(o), "s"(src + i * 8 * L * 2), "s"(lds0 + (i < 8 ? (i >> 1) * 3200 : 25600) + (i & 1) * 1024) : "memory", "m0");
      VMCNT(0);
    }
    BAR_ONLY();
    return;
  }
  const int t = wave & 1, kh = wave >> 1;
  u32x4 in0[2];
  int dsto[2];
#pragma unroll
  for (int rr = 0; rr < 2; rr++) {
    const int f = (wave + 8 * rr) * 49 + min(lane, 49), i = min(f, 776);
    const int ch = i / 259, xx = i - 259 * ch;
    dsto[rr] = (lane < 49 && f < 777) ? K2_CONV + ch * K2_CH + xx * 16 : -1;
    GLD4(in0[rr], (int)WS_UT + (ch == 2 ? chH : chA + ch) * (UT_STRIDE * 2) + (xx + 1) * 16, ws, "");
  }
  const int jg = (wave & 1) ^ JGX, jc = (wave >> 1) & 1, hiw = wave >> 2;
  const int fo = (lane & 15) * 16 + (lane >> 4) * 4;
  int ft[3];
  ft[0] = hiw ? 3 - jg : jg;
  ft[1] = hiw ? 4 + jg : (wave & 2) * 2 + ((wave & 1) ? 3 - odd : odd);
  ft[2] = 7 - jg;
  u32x2 us[3];
  float g_r;
  GLD1(g_r, (int)WS_GATE + (l0 + 32 * t + 16 * (kh & 1) + (lane & 15)) * 4, ws, "");
  unsigned dsA, dsH;
  asm volatile("global_load_ushort %0, %1, %2" : "=v"(dsA) : "v"((chA + jc) * 2), "s"(Dskip));
  asm volatile("global_load_ushort %0, %1, %2" : "=v"(dsH) : "v"((hiw ? chA + jc : chH) * 2), "s"(Dskip));
  u32x4 pf[8][2];
  const char* pqp = ws + WS_PQ + (size_t)((rowblk * 32 + kh * 8) * 4 + 2 * t) * 640;
  const int pqo = lane < 32 ? 128 + l16 : (lane & 15) * 8;
#define PQL(i0, i1) _Pragma("unroll") for (int i = i0; i < i1; i++) { GLD4(pf[i][0], pqo, pqp + i * 2560, ""); GLD4(pf[i][1], pqo, pqp + i * 2560, " offset:640"); }
  if (tid < 96) *(u32x4*)(smem + K2_CONV + (tid >> 5) * K2_CH + K2_KOFF + (tid & 31) * 16) = (u32x4){0u, 0u, 0u, 0u};
  asm volatile("s_waitcnt vmcnt(3)" : "+v"(in0[0]), "+v"(in0[1]), "+v"(cpf) :: "memory");
  STAMP(1, 2);
#pragma unroll
  for (int rr = 0; rr < 2; rr++) {
    const u32x4 a = in0[rr];
    unsigned w[6] = {a[0], a[1], a[2], a[3], (unsigned)__builtin_amdgcn_update_dpp(0, (int)a[0], 0x130, 0xf, 0xf, false),
                     (unsigned)__builtin_amdgcn_update_dpp(0, (int)a[1], 0x130, 0xf, 0xf, false)};
    unsigned o[5];
#pragma unroll
    for (int k = 0; k < 5; k++) o[k] = __builtin_amdgcn_alignbyte(w[k + 1], w[k], 2);
    if (dsto[rr] >= 0) {
      char* p = smem + dsto[rr];
#pragma unroll
      for (int q = 0; q < 2; q++) {
        *(u32x4*)(p + (2 * q) * K2_CS) = (u32x4){o[q], o[q + 1], o[q + 2], o[q + 3]};
        *(u32x4*)(p + (2 * q + 1) * K2_CS) = (u32x4){w[q + 1], w[q + 2], w[q + 3], w[q + 4]};
      }
    }
  }
  RAW_BARRIER();
  PQL(0, PQA);
  STAMP(1, 3);
  f32x4 ca[4], sv[4];
#pragma unroll
  for (int i = 0; i < 4; i++) { ca[i] = (f32x4){0.f, 0.f, 0.f, 0.f}; sv[i] = (f32x4){0.f, 0.f, 0.f, 0.f}; }
  {
    const int i = lane & 15, kg = lane >> 4, e = kg >> 1, bb = kg & 1;
    const int ap = lds0 + K2_CONV + (i & 3) * K2_CS + (2 * e - bb + 2) * 16 + (i >> 2) * 8;
    const int bp = lds0 + K2_CONV + K2_KOFF + (16 + i - e) * 32 + bb * 16;
    int apj = ap + jc * K2_CH + hiw * 192;
    int bpj = bp + jc * K2_CH - hiw * 192;
    if (jg == 0) {
#pragma unroll 1
      for (int it = 0; it < 3; it++) conv_unit<0>(ca, apj + it * 64, bpj - it * 64);
    } else {
#pragma unroll 1
      for (int it = 0; it < 3; it++) conv_unit<1>(ca, apj + it * 64, bpj - it * 64);
    }
    PQL(PQA, PQB);
    apj += 192; bpj -= 192;
    int g2 = jg;
    if (!hiw) {
#pragma unroll
      for (int q = 0; q < 4; q++) { sv[q] = ca[q]; ca[q] = (f32x4){0.f, 0.f, 0.f, 0.f}; }
      apj = ap + 2 * K2_CH + wave * 128; bpj = bp + 2 * K2_CH - wave * 128; g2 = odd;
    }
    if (g2 == 0) {
#pragma unroll 1
      for (int it = 0; it < 2; it++) conv_unit<0>(ca, apj + it * 64, bpj - it * 64);
    } else {
#pragma unroll 1
      for (int it = 0; it < 2; it++) conv_unit<1>(ca, apj + it * 64, bpj - it * 64);
    }
  }
  STAMP(1, 4);
  {
    const char* ua = smem + K2_CONV + 3 * K2_CS + (20 + fo) * 2;
    us[0] = *(const u32x2*)(ua + jc * K2_CH + ft[0] * 512); us[1] = *(const u32x2*)(ua + (hiw ? jc : 2) * K2_CH + ft[1] * 512);
    us[2] = *(const u32x2*)(ua + jc * K2_CH + ft[2] * 512);
  }
  PQL(PQB, 8);
  RAW_BARRIER();
  {
    if (tid == 0) *(unsigned*)(smem + K2_LDS - 16) = 0u;
    char* sp = smem + K2_STG + l16;
    if (hiw) {
      *(f32x4*)(sp + (wave & 3) * 4096) = ca[0];
    } else {
#pragma unroll
      for (int i = 1; i < 4; i++) *(f32x4*)(sp + wave * 4096 + i * 1024) = sv[i];
#pragma unroll
      for (int i = 0; i < 4; i++) if (i != wave) *(f32x4*)(sp + 16384 + (i * 3 + ((wave - i - 1) & 3)) * 1024) = ca[i];
    }
  }
  RAW_BARRIER();
  {
    const char* sp = smem + K2_STG + l16;
    asm volatile("s_waitcnt vmcnt(%3)" : "+v"(g_r), "+v"(dsA), "+v"(dsH) : "n"(2 * (8 - PQB)) : "memory");
    const float dskA = lo_f(dsA), dskH = lo_f(dsH);
    auto fin = [&](f32x4 s, u32x2 uu, float dsk, int ch, int tile) {
      u32x2 ov;
#pragma unroll
      for (int e = 0; e < 2; e++) {
        const f32x2 kk = (f32x2){lo_f(uu[e]), hi_f(uu[e])} * dsk;
        const f32x2 rr = (f32x2){rb1(s[2 * e]), rb1(s[2 * e + 1])} + (f32x2){rb1(kk[0]), rb1(kk[1])};
        ov[e] = pack2(rr[0], rr[1]);
      }
      const int o = (int)WS_AT + (ch * L + tile * 256 + fo) * 2;
      asm volatile("global_store_dwordx2 %0, %1, %2 sc1\n\ts_nop 1" :: "v"(o), "v"(ov), "s"(ws) : "memory");
    };
    if (hiw) {
      const char* pp = sp + (wave & 3) * 4096;
#pragma unroll
      for (int i = 1; i < 4; i++) fin(ca[i] + *(const f32x4*)(pp + i * 1024), us[i - 1], dskA, chA + jc, ft[i - 1]);
    } else {
      const char* hp = sp + 16384 + wave * 3072;
      const f32x4 own = wave == 0 ? ca[0] : wave == 1 ? ca[1] : wave == 2 ? ca[2] : ca[3];
      fin(sv[0] + *(const f32x4*)(sp + wave * 4096), us[0], dskA, chA + jc, ft[0]);
      fin((own + *(const f32x4*)(hp + 1024)) + (*(const f32x4*)hp + *(const f32x4*)(hp + 2048)), us[1], dskH, chH, ft[1]);
    }
  }
#pragma unroll
  for (int i = 0; i < (LATEPQ ? PQB : 8); i++) asm volatile("" : "+v"(pf[i][0]), "+v"(pf[i][1]));
  RAW_BARRIER();
  STAMP(1, 5);
  const int n16 = lane & 15, g = lane >> 4;
  f32x4 acc[5][2];
  {
    const char* kp = smem + K2_KV + kh * 8 * 896;
    const int row1 = (n16 & 3) ? 20 : 16 + (n16 >> 2);
    const char* k00 = kp + (g < 3 ? n16 * 40 + 16 * g : 840);
    const char* k01 = kp + (g < 2 ? n16 * 40 + 16 * g + 8 : 840);
    const char* k10 = kp + (g < 3 ? row1 * 40 + 16 * g : 840);
    const char* k11 = kp + (g < 2 ? row1 * 40 + 16 * g + 8 : 840);
    const char* wm = smem + (160 * kh + 4 * g + (n16 >> 2)) * 160 + (lane & 3) * 8;
    const char* wt = smem + (160 * kh + 20 * (n16 >> 2) + 16 + g) * 160 + (lane & 3) * 8;
    u32x4 ka[8][2];
    auto rdK = [&](int i) {
      const u32x2 a0 = *(const u32x2*)(k00 + i * 896), a1 = *(const u32x2*)(k01 + i * 896), b0 = *(const u32x2*)(k10 + i * 896), b1 = *(const u32x2*)(k11 + i * 896);
      ka[i][0] = (u32x4){a0[0], a0[1], a1[0], a1[1]}; ka[i][1] = (u32x4){b0[0], b0[1], b1[0], b1[1]};
    };
    f32x4 c[2][2][2];
    const f32x4 z4 = {0.f, 0.f, 0.f, 0.f};
    auto stageA = [&](int i) {
#pragma unroll
      for (int tl = 1; tl >= 0; tl--)
#pragma unroll
        for (int nt = 0; nt < 2; nt++)
          c[i & 1][tl][nt] = __builtin_amdgcn_mfma_f32_16x16x32_bf16(__builtin_bit_cast(s16x8, ka[i][tl]), __builtin_bit_cast(s16x8, pf[i][nt]), z4, 0, 0, 0);
    };
    auto wfrag = [&](const char* p, int d) {
      const s16x4 lo = __builtin_amdgcn_ds_read_tr16_b64_v4i16((lds_s16x4_p)(p)), hi = __builtin_amdgcn_ds_read_tr16_b64_v4i16((lds_s16x4_p)(p + d));
      return __builtin_shufflevector(lo, hi, 0, 1, 2, 3, 4, 5, 6, 7);
    };
    u32x4 ob[2][2], o5[2];
    float p5[2];
    s16x8 w[3];
    auto proj = [&](int s, int q) {
      if (s < 0) return;
      const u32x4 bb = s < 4 ? ob[s & 1][q & 1] : o5[q & 1];
      acc[q >> 1][q & 1] = __builtin_amdgcn_mfma_f32_16x16x32_bf16(w[(q >> 1) % 3], __builtin_bit_cast(s16x8, bb), s == 0 ? z4 : acc[q >> 1][q & 1], 0, 0, 0);
    };
    auto rdW = [&](int s, int mt) { w[mt % 3] = s < 4 ? wfrag(wm + s * 6400 + mt * 32, 3200) : wfrag(wt + mt * 32, 12800); };
    auto sA = [&](int i, int q) {
      if (i < 8) c[i & 1][1 - (q >> 1)][q & 1] = __builtin_amdgcn_mfma_f32_16x16x32_bf16(__builtin_bit_cast(s16x8, ka[i][1 - (q >> 1)]), __builtin_bit_cast(s16x8, pf[i][q & 1]), z4, 0, 0, 0);
    };
    rdK(0);
    stageA(0);
    SB();
#pragma unroll
    for (int i = 0; i < 8; i++) {
      const int s = (i >> 1) - 1, q0 = (i & 1) * 5;
      if (i + 1 < 8 && i <= B6I) rdK(i + 1);
      if (i == B6I) { _Pragma("unroll") for (int j = i + 2; j < 8; j++) rdK(j); }
      SB();
      const f32x4 (&cc)[2][2] = c[i & 1];
      proj(s, q0);
      f32x2 dd = {rb1(cc[1][0][1]), rb1(cc[1][1][1])};
      dd += (f32x2){1e-6f, 1e-6f};
      asm volatile("" : "+v"(dd));
      SB();
      proj(s, q0 + 1);
      if (s >= 0 && !(i & 1)) rdW(s, 3);
      if (i & 1) rdW(s + 1, 2);
      float d0 = rb1(dd[0]), d1 = rb1(dd[1]);
      asm volatile("" : "+v"(d0), "+v"(d1));
      SB();
      proj(s, q0 + 2);
      float inv[2] = {__builtin_amdgcn_rcpf(d0), __builtin_amdgcn_rcpf(d1)};
      asm volatile("" : "+v"(inv[0]), "+v"(inv[1]));
      SB();
      proj(s, q0 + 3);
      if (s >= 0 && !(i & 1)) rdW(s, 4);
      if (i & 1) rdW(s + 1, 0);
      f32x4 v0 = cc[0][0] * inv[0];
      float q50 = cc[1][0][0] * inv[0];
      asm volatile("" : "+v"(v0), "+v"(q50));
      SB();
      proj(s, q0 + 4);
      f32x4 v1 = cc[0][1] * inv[1];
      float q51 = cc[1][1][0] * inv[1];
      asm volatile("" : "+v"(v1), "+v"(q51));
      SB();
      sA(i + 1, 0);
      if (i & 1) rdW(s + 1, 1);
      ob[(i >> 1) & 1][0][2 * (i & 1)] = pack2(v0[0], v0[1]); ob[(i >> 1) & 1][0][2 * (i & 1) + 1] = pack2(v0[2], v0[3]);
      SB();
      sA(i + 1, 1);
      ob[(i >> 1) & 1][1][2 * (i & 1)] = pack2(v1[0], v1[1]); ob[(i >> 1) & 1][1][2 * (i & 1) + 1] = pack2(v1[2], v1[3]);
      SB();
      sA(i + 1, 2);
      if (i & 1) { o5[0][i >> 1] = pack2(p5[0], q50); o5[1][i >> 1] = pack2(p5[1], q51); } else { p5[0] = q50; p5[1] = q51; }
      SB();
      sA(i + 1, 3);
      SB();
      if (i == SIGI) {
        VMCNT(0);
        static_assert(SIGI <= PQB - 2, "");
#pragma unroll
        for (int j = PQB; j < 8; j++) asm volatile("" : "+v"(pf[j][0]), "+v"(pf[j][1]));
        unsigned old = 0;
        if (lane == 0) old = __hip_atomic_fetch_add((unsigned*)(smem + K2_LDS - 16), 1u, __ATOMIC_RELAXED, __HIP_MEMORY_SCOPE_WORKGROUP);
        if (lane == 0 && old == 7u) __hip_atomic_fetch_add((gu32*)(size_t)(ws + WS_CNT + (8 + xcd) * 128), 1u, __ATOMIC_RELAXED, __HIP_MEMORY_SCOPE_AGENT);
      }
      if (i == B6I) { RAW_BARRIER(); }
    }
    s16x8 w2[7];
#pragma unroll
    for (int j = 0; j < 7; j++) w2[j] = j < 2 ? wfrag(wm + 3 * 6400 + (3 + j) * 32, 3200) : wfrag(wt + (j - 2) * 32, 12800);
    SB();
#pragma unroll
    for (int q = 0; q < 6; q++) proj(3, q);
#pragma unroll
    for (int q = 6; q < 20; q++) {
      const int mt = q < 10 ? q >> 1 : (q - 10) >> 1;
      acc[mt][q & 1] = __builtin_amdgcn_mfma_f32_16x16x32_bf16(w2[(q - 6) >> 1], __builtin_bit_cast(s16x8, q < 10 ? ob[1][q & 1] : o5[q & 1]), acc[mt][q & 1], 0, 0, 0);
    }
  }
  STAMP(1, 6);
  {
    char* sp = smem + 102400 + t * 30720 + l16;
#pragma unroll
    for (int q = 0; q < 10; q++) {
      const int own = q & 3;
      if (kh != own) *(f32x4*)(sp + (q * 3 + ((kh - own - 1) & 3)) * 1024) = acc[q >> 1][q & 1];
    }
  }
  RAW_BARRIER();
  STAMP(1, 7);
  {
    const char* sp = smem + 102400 + t * 30720 + l16;
    const float g1 = rb(1.f - g_r);
    const char* atp = smem + (kh >> 1) * 3200 + (4 * g + (n16 >> 2)) * 128 + (((32 * t + 16 * (kh & 1) + 4 * (lane & 3)) * 2) ^ ((lane & 8) << 3));
    u16* op = out + (size_t)(l0 + 32 * t + 16 * (kh & 1) + n16) * D + c0 + 16 * (kh >> 1) + 4 * g;
#pragma unroll
    for (int tl = 0; tl < 3; tl++) {
      if (tl == 2 && kh >= 2) break;
      f32x4 m;
      if (kh == 0)      m = acc[2 * tl][0];
      else if (kh == 1) m = acc[2 * tl][1];
      else if (kh == 2) m = acc[tl < 2 ? 2 * tl + 1 : 0][0];
      else              m = acc[tl < 2 ? 2 * tl + 1 : 0][1];
      const char* pp = sp + (tl * 4 + kh) * 3072;
      f32x4 p0 = *(const f32x4*)pp, p1 = *(const f32x4*)(pp + 1024), p2 = *(const f32x4*)(pp + 2048);
      f32x4 v = (m + p1) + (p0 + p2);
      s16x4 at = __builtin_amdgcn_ds_read_tr16_b64_v4i16((lds_s16x4_p)(atp + (tl < 2 ? tl * 6400 : 25600)));
      u32x2 au = __builtin_bit_cast(u32x2, at);
      u32x2 ov;
#pragma unroll
      for (int e = 0; e < 2; e++) {
        const f32x2 gb = (f32x2){rb1(v[2 * e]), rb1(v[2 * e + 1])} * g1, ga = (f32x2){lo_f(au[e]), hi_f(au[e])} * g_r;
        const f32x2 rr = (f32x2){rb1(ga[0]), rb1(ga[1])} + (f32x2){rb1(gb[0]), rb1(gb[1])};
        ov[e] = pack2(rr[0], rr[1]);
      }
      *(u32x2*)(op + 32 * tl) = ov;
    }
  }
  asm volatile("code_tail_mark:" ::: "memory");
  STAMP(1, 1);
}

extern "C" void kernel_launch(void* const* d_in, const int* in_sizes, int n_in, void* d_out, int out_size, void* d_ws, size_t ws_size,
                              hipStream_t stream) {
  const u16* u = (const u16*)d_in[0]; const u16* logA = (const u16*)d_in[1]; const u16* Aim = (const u16*)d_in[2];
  const u16* Cr = (const u16*)d_in[3]; const u16* Ci = (const u16*)d_in[4]; const u16* logdt = (const u16*)d_in[5];
  const u16* Dsk = (const u16*)d_in[6]; const u16* wqkv = (const u16*)d_in[8]; const u16* wo = (const u16*)d_in[9];
  const u16* gw = (const u16*)d_in[10];
  char* ws = (char*)d_ws;
  k1<<<NB_GEMM + NB_GATE + NB_KG, 512, 0, stream>>>(u, logA, Aim, Cr, Ci, logdt, wqkv, gw, wo, ws);
  k2<<<256, 576, 0, stream>>>(Dsk, wo, ws, (u16*)d_out);
}
```

```cpp
#include <hip/hip_runtime.h>
#include <type_traits>
#include <hip/hip_bf16.h>
#include <stdint.h>

typedef unsigned short u16;
typedef __attribute__((ext_vector_type(8))) short s16x8;
typedef __attribute__((ext_vector_type(4))) short s16x4;
typedef __attribute__((ext_vector_type(4))) float f32x4;
typedef __attribute__((ext_vector_type(16))) float f32x16;
typedef __attribute__((ext_vector_type(4))) unsigned u32x4;
typedef __attribute__((ext_vector_type(2))) unsigned u32x2;
typedef __attribute__((address_space(3))) s16x4* lds_s16x4_p;
typedef const __attribute__((address_space(1))) void* gptr_t;
typedef __attribute__((address_space(3))) void* lptr_t;

constexpr int L = 2048, D = 640, NS = 192, NH = 32;
constexpr int UT_PAD = 32, UT_STRIDE = L + UT_PAD;
constexpr size_t WS_UT   = 0;
constexpr size_t WS_KIMG = WS_UT + (size_t)D * UT_STRIDE * 2;
constexpr size_t WS_PQ   = WS_KIMG + (size_t)D * 4096;
constexpr size_t WS_PART = WS_PQ + (size_t)L * D * 2;
constexpr size_t WS_GATE = WS_PART + (size_t)32 * 16 * 1680;
constexpr size_t WS_KVT  = WS_GATE + (size_t)L * 4;
constexpr size_t WS_AT   = WS_KVT + (size_t)32 * 1024;
constexpr size_t WS_CNT  = WS_AT + (size_t)D * L * 2;
constexpr size_t WS_END  = WS_CNT + 4096;
#define TGET(v) do {} while (0)
#define STEPT(i) do {} while (0)
#define TPUT(kid, slot, thr, v) do {} while (0)
#define STAMP(kid, slot) do {} while (0)

__device__ __forceinline__ float b2f(u16 v) { return __uint_as_float(((unsigned)v) << 16); }
typedef __bf16 bf16x2_t __attribute__((ext_vector_type(2)));
typedef float f32x2 __attribute__((ext_vector_type(2)));
__device__ __forceinline__ unsigned pack2(float a, float b) { f32x2 v = {a, b}; bf16x2_t r = __builtin_convertvector(v, bf16x2_t); return __builtin_bit_cast(unsigned, r); }
__device__ __forceinline__ float lo_f(unsigned u) { return __uint_as_float(u << 16); }
__device__ __forceinline__ float hi_f(unsigned u) { return __uint_as_float(u & 0xffff0000u); }
__device__ __forceinline__ float rb(float f) { return lo_f(pack2(f, f)); }
__device__ __forceinline__ float rb1(float f) { return __uint_as_float(pack2(0.f, f)); }
typedef unsigned short u16x2 __attribute__((ext_vector_type(2)));
__device__ __forceinline__ unsigned phi2(float a, float b, f32x2 kl) {
  const f32x2 t = {rb1(a), rb1(b)};
  const f32x2 x = t * kl, p = t + (f32x2){1.f, 1.f};
  const float e0 = __builtin_amdgcn_fmed3f(__builtin_amdgcn_exp2f(x[0]), 0.f, 1.f);
  const float e1 = __builtin_amdgcn_fmed3f(__builtin_amdgcn_exp2f(x[1]), 0.f, 1.f);
  return pack2(__builtin_fmaxf(p[0], e0), __builtin_fmaxf(p[1], e1));
}
__device__ __forceinline__ unsigned phi_eps(unsigned r) { return pack2(lo_f(r) + 1e-6f, hi_f(r) + 1e-6f); }
#define VMCNT(n) asm volatile("s_waitcnt vmcnt(" #n ")" ::: "memory")
#define RAW_BARRIER() do { asm volatile("s_waitcnt lgkmcnt(0)" ::: "memory"); __builtin_amdgcn_s_barrier(); asm volatile("" ::: "memory"); } while (0)
__device__ __forceinline__ void glds16(const void* g, void* l) { __builtin_amdgcn_global_load_lds((gptr_t)g, (lptr_t)l, 16, 0, 0); }

#define OPQ_S(v) asm volatile("" : "+s"(v))
typedef f32x2 cf;
__device__ __forceinline__ float smul(float a, float b) { float o; asm("v_mul_f32 %0, %1, %2" : "=v"(o) : "v"(a), "v"(b)); return o; }
__device__ __forceinline__ float sfma(float a, float b, float c) { float o; asm("v_fma_f32 %0, %1, %2, %3" : "=v"(o) : "v"(a), "v"(b), "v"(c)); return o; }
__device__ __forceinline__ float sfnma(float a, float b, float c) { float o; asm("v_fma_f32 %0, -%1, %2, %3" : "=v"(o) : "v"(a), "v"(b), "v"(c)); return o; }
__device__ __forceinline__ float ssub(float a, float b) { float o; asm("v_sub_f32 %0, %1, %2" : "=v"(o) : "v"(a), "v"(b)); return o; }
__device__ __forceinline__ cf cmul(cf a, cf b) {
  cf o;
  asm("v_pk_mul_f32 %0, %1, %2 op_sel:[0,0] op_sel_hi:[0,1]\n\t"
      "v_pk_fma_f32 %0, %1, %2, %0 op_sel:[1,1,0] op_sel_hi:[1,0,1] neg_lo:[1,0,0]" : "=&v"(o) : "v"(a), "v"(b));
  return o;
}

#define CPF 3
#define LPF 1
#define PFD 0
#define PFM_ 1
#define NP0 5
#define SPRIO 3
#define NI0 208
#define NB2 3
#define TB0 1
#define TBD 2
#define MIDX ((rowblk & 3) + 4 * (cb & 7))
#define MPS 3
#define PT0 30
#define PT1 110
#define PTD 20
constexpr int K1_LDS = 81920;
#define GLDS 1
constexpr int NB_GEMM = 256, NB_GATE = GLDS ? 0 : 16, NB_KG = 240, NB_KC = 214;
#define KVAT 0
#define KV1 ""
#define KVB 0
#define ROT 0
#define TPQ 0
#define KVST
#define STG 0
#define SA0 8
#define SB0 8
#define SA1 0
#define SB1 8
#define WOT 1
#define KRR 1
#define B6I 4
#define WE 3
#define SGB 1
#define XB 1
#define UTE 0
#define NE 48
#define MPRIO 1
#define EPRIO 1
#define W7P 2
#define GSL 100
#define GPRIO 0
#define TPOL ""
#define FLAGS 1
#define PADN 0
#define PADM 544
#define JGX 1
#define LATEPQ 1
#define SIGI 2
#define NW0 3
#define K2PF 1
#define K2PFS "1"
#define KVP 1
#define PQA 2
#define PQB 4
#define STR2(x) #x
#define STR(x) STR2(x)
#define PQBS "2*(" STR(PQB) "-" STR(PQA) ")"
#define WTQ 0
#define WTP 0
#define WTO 0
#define WT 1
__device__ __forceinline__ void st16(void* p, u32x4 v) {
  asm volatile("global_store_dwordx4 %0, %1, off sc1\n\ts_nop 2" :: "v"(p), "v"(v) : "memory");
}

#define BAR_ONLY() do { asm volatile("" ::: "memory"); __builtin_amdgcn_s_barrier(); asm volatile("" ::: "memory"); } while (0)
#define SB() __builtin_amdgcn_sched_barrier(0)
template <bool ISQ>
__device__ __forceinline__ void mfma_part(char* smem, char* __restrict__ ws, int wave, f32x4 (&ep)[2][4], int zc) {
  const int lane = threadIdx.x & 63;
  char* sB = smem; char* sA = smem + 32768;
  const int wr = wave & 1, wc = (wave >> 1) & 1;
  const int g = lane >> 4, fr = lane & 15;
  f32x4 acc[4][4];
#pragma unroll
  for (int m = 0; m < 4; m++)
#pragma unroll
    for (int c = 0; c < 4; c++) acc[m][c] = (f32x4){0.f, 0.f, 0.f, 0.f};
  const int q4 = fr >> 2, p4 = lane & 3;
  int aoff[2];
#pragma unroll
  for (int ks = 0; ks < 2; ks++) aoff[ks] = (wr * 64 + fr) * 128 + (((4 * ks + g) ^ (fr & 7)) << 4);
  int boff[4];
  {
    const int f = q4 | ((g & 1) << 2);
#pragma unroll
    for (int c = 0; c < 4; c++) boff[c] = (8 * g + q4) * 256 + (((4 * wc + c) ^ f) << 5) + p4 * 8;
  }
  s16x8 af[2][4]; s16x4 bl[4], bh[4];
  auto rdA = [&](int buf, int m, const char* pa) { af[buf][m] = *(const s16x8*)(pa + m * 2048); };
  auto rdBl = [&](int c, const char* pb) { bl[c] = __builtin_amdgcn_ds_read_tr16_b64_v4i16((lds_s16x4_p)(pb + boff[c])); };
  auto rdBh = [&](int c, const char* pb) { bh[c] = __builtin_amdgcn_ds_read_tr16_b64_v4i16((lds_s16x4_p)(pb + boff[c] + 1024)); };
  __builtin_amdgcn_s_setprio(MPRIO);
  BAR_ONLY();
  STAMP(0, 2);
#pragma unroll
  for (int c = 0; c < 4; c++) rdA(0, c, sA + aoff[0]);
#pragma unroll
  for (int c = 0; c < 3; c++) { rdBl(c, sB); rdBh(c, sB); }
  int ao0 = 0, ao1 = 16384;
  auto grp = [&](int buf, int c, int m) {
    const s16x8 bf = __builtin_shufflevector(bl[c], bh[c], 0, 1, 2, 3, 4, 5, 6, 7);
    if (ISQ) acc[m][c] = __builtin_amdgcn_mfma_f32_16x16x32_bf16(bf, af[buf][m], acc[m][c], 0, 0, 0);
    else     acc[m][c] = __builtin_amdgcn_mfma_f32_16x16x32_bf16(af[buf][m], bf, acc[m][c], 0, 0, 0);
  };
#pragma unroll 1
  for (int j2 = 0; j2 < 5; j2++) {
#pragma unroll
    for (int st = 0; st < 4; st++) {
      const int hs = st & 1;
      SB();
      asm volatile("s_waitcnt lgkmcnt(2)" ::: "memory"); __builtin_amdgcn_s_barrier(); asm volatile("" ::: "memory");
      SB();
      const char* pa = sA + (hs == 0 ? ao0 + aoff[1] : ao1 + aoff[0]);
      const char* pc = sB + st * 8192;
      const char* pb = sB + ((st + 1) & 3) * 8192;
      grp(hs, 0, 0); SB(); rdBl(3, pc); rdBh(3, pc); SB();
      grp(hs, 0, 1); SB(); rdA(hs ^ 1, 0, pa); rdA(hs ^ 1, 1, pa); SB();
      grp(hs, 0, 2); SB(); rdA(hs ^ 1, 2, pa); rdA(hs ^ 1, 3, pa); SB();
      grp(hs, 0, 3); SB();
#pragma unroll
      for (int c = 1; c < 4; c++) {
        grp(hs, c, 0); SB(); rdBl(c - 1, pb); rdBh(c - 1, pb); SB();
        grp(hs, c, 1); grp(hs, c, 2); grp(hs, c, 3); SB();
      }
      if (hs == 1) { ao0 = ao1; ao1 = ao1 == 32768 ? 0 : ao1 + 16384; }
    }
  }
  STAMP(0, 3);
  int l3 = threadIdx.x; asm volatile("" : "+v"(l3));
  char* xch = smem + 32768 + (l3 >> 6) * 8192 + (l3 & 63) * 16;
  if (ISQ || wc == 0) {
#pragma unroll
    for (int i = 0; i < 8; i++) *(f32x4*)(xch + i * 1024) = acc[2 + (i >> 2)][i & 3];
  }
#pragma unroll
  for (int i = 0; i < 8; i++) ep[i >> 2][i & 3] = acc[i >> 2][i & 3];
  RAW_BARRIER();
  if (!ISQ && wc == 1) {
    char* fv = smem + 16384 + wr * 8192 + (l3 & 63) * 16;
#pragma unroll
    for (int c = 0; c < 4; c++)
#pragma unroll
      for (int h = 0; h < 2; h++) {
        u32x4 a = {pack2(acc[2 * h][c][0], acc[2 * h][c][1]), pack2(acc[2 * h][c][2], acc[2 * h][c][3]),
                   pack2(acc[2 * h + 1][c][0], acc[2 * h + 1][c][1]), pack2(acc[2 * h + 1][c][2], acc[2 * h + 1][c][3])};
        if ((c == 0 || c == 3) && 16 * c + (l3 & 15) == zc) a = (u32x4){0x3F803F80u, 0x3F803F80u, 0x3F803F80u, 0x3F803F80u};
        *(u32x4*)(fv + h * 4096 + c * 1024) = a;
      }
  }
}
__device__ __forceinline__ void dma4x(const int (&vo)[4], const char* sbase, int ldsa) {
  asm volatile("s_mov_b32 m0, %5\n\ts_nop 0\n\tglobal_load_lds_dwordx4 %0, %4\n\tglobal_load_lds_dwordx4 %1, %4 offset:1024\n\t"
               "global_load_lds_dwordx4 %2, %4 offset:2048\n\tglobal_load_lds_dwordx4 %3, %4 offset:3072"
               :: "v"(vo[0]), "v"(vo[1]), "v"(vo[2]), "v"(vo[3]), "s"(sbase), "s"(ldsa) : "memory", "m0");
}
__device__ __forceinline__ unsigned touch_off(int gb, int q, int& r) {
  const int par = gb & 1, pidx = gb >> 3;
  const int stg = (q * 1366) >> 16, q15 = q & 15;
  r = q - 48 * stg;
  const int ln = q15 + (par ? (q15 < 4 ? 6 : q15 < 10 ? 10 : 14) : (q15 < 6 ? 0 : q15 < 11 ? 4 : 9));
  const unsigned ox = (unsigned)((((gb >> 1) & 3) * 512 + pidx * 16 + q15) * 1280 + stg * 128);
  const unsigned ow = (unsigned)((64 * stg + pidx * 2 + (r >> 4) - 1) * 3840 + ln * 128);
  return r < 16 ? ox : ow;
}
#define TOUCH_LDS(p, la) asm volatile("s_mov_b32 m0, %1\n\ts_nop 0\n\tglobal_load_lds_dword %0, off" TPOL :: "v"(p), "s"(la) : "memory", "m0")
__device__ __forceinline__ void gemm_role(char* smem, const u16* __restrict__ x, const u16* __restrict__ W, const u16* __restrict__ wo, char* __restrict__ ws,
                                          int rowblk, int cb, int slot, int par) {
  const int tid = threadIdx.x, lane = tid & 63;
  const int wave = __builtin_amdgcn_readfirstlane(tid >> 6);
  const bool ISQ = cb < 5;
  const int row0 = rowblk * 128, col0 = cb * 128;
  const int kvj = cb - 5;
  const int wr = wave & 1, wc = (wave >> 1) & 1, hf = wave >> 2;
  f32x4 ep[2][4];
  unsigned pf3 = 0;
  const int wce = wc ^ hf;
  const int o = (60 * kvj) & 7, zc = o ? 0 : 60;
  unsigned eo[2];
  int ta[2], tb[2];
  auto eoffs = [&](bool isq) {
    int l4 = threadIdx.x; asm volatile("" : "+v"(l4));
    const unsigned g = (l4 >> 4) & 3, fr = l4 & 15;
    if (isq) {
#pragma unroll
      for (int c = 0; c < 4; c++) {
        const unsigned q4 = (unsigned)(col0 / 4 + 16 * wce + 4 * c) + g;
        const unsigned hd = (q4 * 205u) >> 10, d4 = q4 - 5u * hd;
        const unsigned off = hd * 320u + (d4 < 4u ? 16u + ((d4 * 17u) & 0x21u) + fr * 2u : fr);
        if (c & 1) eo[c >> 1] |= off << 16; else eo[c >> 1] = off;
      }
      ta[0] = ta[1] = tb[0] = tb[1] = 0;
    } else {
      const unsigned long long TA = o ? 0x323322211100ull : 0x103322211100ull, TB = o ? 0x003232121010ull : 0x333232121010ull;
#pragma unroll
      for (int i = 0; i < 2; i++) {
        const int p = wave < 4 ? wave : wave * 2 - 4 + i;
        ta[i] = (int)((TA >> (4 * p)) & 15); tb[i] = (int)((TB >> (4 * p)) & 15);
        const int kc0 = 16 * ta[i] + 4 * (int)g - o;
        const int hk = (kc0 >= 20) + (kc0 >= 40), dk0 = kc0 - 20 * hk;
        const int vcol = 16 * tb[i] + (int)fr;
        const int dv = vcol == zc ? 20 : vcol - o - 20 * hk;
        const bool ok = (kc0 >= 0) & (kc0 < (kvj == 10 ? 40 : 60)) & (dv >= 0) & ((dv < 20) | (vcol == zc)) & (i == 0 || wave >= 4);
        eo[i] = ok ? (unsigned)((hk * 16 * 21 + dv) * 80 + dk0 * 4) : ~0u;
      }
    }
    asm volatile("" : "+v"(eo[0]), "+v"(eo[1]));
    SB();
  };
  if (wave >= 4) {
    const int lw = wave & 1;
    const int l0 = (int)(size_t)(lptr_t)smem;
    const int pidx = (rowblk & 3) + 4 * slot;
    __builtin_amdgcn_s_setprio(SPRIO);
    if (wave < 6) {
      int voA[4], voB[4];
#pragma unroll
      for (int i = 0; i < 4; i++) {
        voA[i] = (64 * lw + 8 * i + (lane >> 3)) * 1280 + (((lane & 7) ^ (lane >> 3)) << 4) - 1024 * i;
        const int fB = (lane >> 4) | ((i >> 1) << 2);
        const int c16 = (((lane & 15) >> 1) ^ fB) * 2 + (lane & 1);
        int bcol = col0 + 8 * c16;
        if (!ISQ) bcol = 640 + 640 * (c16 >> 3) + min(((60 * kvj) & ~7) + 8 * (c16 & 7), 632);
        voB[i] = (16 * lw + 4 * i + (lane >> 4)) * 3840 + bcol * 2 - 1024 * i;
      }
      const char* pA = (const char*)x + row0 * 1280;
      const char* pB = (const char*)W;
      const int lA = l0 + 32768 + lw * 8192, lB = l0 + lw * 4096;
      auto issA = [&](int slot_off, int h) { dma4x(voA, pA + h * 32 * 1280, lA + slot_off + h * 4096); if (h) pA += 128; };
      auto issB = [&](int slot_off) { dma4x(voB, pB, lB + slot_off); pB += 32 * 3840; };
      TGET(tl0);
      VMCNT(0);
      TGET(tl1);
      issA(0, 0); issB(0); issA(0, 1);
      issB(8192); issA(16384, 0);
      VMCNT(8);
      TGET(tl2);
      BAR_ONLY();
      issB(16384); issA(16384, 1);
      VMCNT(12); BAR_ONLY();
      issB(24576); issA(32768, 0); issA(32768, 1);
      VMCNT(12); BAR_ONLY();
      issB(0); issA(0, 0);
      int ao = 0, bo = 8192;
#pragma unroll 1
      for (int j = 1; j < 7; j++) {
        VMCNT(12); BAR_ONLY();
        issB(bo); bo = (bo + 8192) & 32767; issA(ao, 1);
        ao = ao == 32768 ? 0 : ao + 16384;
        VMCNT(12); BAR_ONLY();
        issB(bo); bo = (bo + 8192) & 32767; issA(ao, 0);
      }
      VMCNT(12); BAR_ONLY();
      issB(8192); issA(0, 1);
      VMCNT(12); BAR_ONLY();
      issB(16384);
      VMCNT(8); BAR_ONLY();
      issB(24576);
      {
        const int o = (pidx * 200 + lw * 100 + min(lane, 49)) * 128;
        asm volatile("global_load_dword %0, %1, %2" TPOL "\n\tglobal_load_dword %0, %1, %3" TPOL : "+v"(pf3) : "v"(o), "s"(wo), "s"((const char*)wo + 6400));
      }
      eoffs(ISQ);
      VMCNT(6); BAR_ONLY();
      VMCNT(2); BAR_ONLY();
      BAR_ONLY();
      TPUT(0, 4, 256, tl0);
      TPUT(0, 5, 256, tl1); TPUT(0, 6, 256, tl2);
      BAR_ONLY();
    } else {
      {
        constexpr int NH = (NI0 - 2 * NE) / 2;
        int r;
        const unsigned to = touch_off(pidx * 8 + (rowblk >> 2) * 2 + par, 2 * NE + lw * NH + min(lane, NH - 1), r);
        TOUCH_LDS((r < 16 ? (const char*)x : (const char*)W) + to, l0 + 65536 + 512 + lw * 256);
        SB();
      }
      u32x4 trv[2][4];
      constexpr int CNT2 = 480 - NI0, NL2 = (CNT2 + 2 * NB2 - 1) / (2 * NB2);
      const char* pp[NB2];
#pragma unroll
      for (int i = 0; i < NB2; i++) {
        const int it = NI0 * 32 + ((pidx & 3) * 8 + (pidx >> 2)) * CNT2 + min((2 * i + lw) * NL2 + min(lane, NL2 - 1), CNT2 - 1);
        const int stg = it / 1536, r = it - 1536 * stg, q = r & 15;
      const int ln = par ? (q < 4 ? 6 + q : q < 10 ? 10 + q : 14 + q) : (q < 6 ? q : q < 11 ? 4 + q : 9 + q);
        pp[i] = r < 512 ? (const char*)x + ((rowblk >> 2) * 512 + r) * 1280 + stg * 128 : (const char*)W + (64 * stg + (r >> 4) - 32) * 3840 + ln * 128;
      }
      const int tb2 = TB0 + TBD * (pidx & 3);
      int tro;
      {
        const int fr = lane & 15, g = lane >> 4, p4 = lane & 3, r = 8 * g + (fr >> 2);
        tro = l0 + 32768 + r * 128 + (((4 * lw + (p4 >> 1)) ^ (r & 7)) << 4) + (p4 & 1) * 8;
      }
      auto transp = [&](int ao) {
#pragma unroll
        for (int hh = 0; hh < 2; hh++) {
          u32x2 tr[8];
          const int a = (tro ^ (32 * hh)) + ao, a2 = (a ^ 64) + 512;
          asm volatile("ds_read_b64_tr_b16 %0, %8\n\tds_read_b64_tr_b16 %1, %9\n\tds_read_b64_tr_b16 %2, %8 offset:4096\n\tds_read_b64_tr_b16 %3, %9 offset:4096\n\t"
                       "ds_read_b64_tr_b16 %4, %8 offset:8192\n\tds_read_b64_tr_b16 %5, %9 offset:8192\n\tds_read_b64_tr_b16 %6, %8 offset:12288\n\tds_read_b64_tr_b16 %7, %9 offset:12288\n\t"
                       "s_waitcnt lgkmcnt(0)"
                       : "=&v"(tr[0]), "=&v"(tr[1]), "=&v"(tr[2]), "=&v"(tr[3]), "=&v"(tr[4]), "=&v"(tr[5]), "=&v"(tr[6]), "=&v"(tr[7])
                       : "v"(a), "v"(a2) : "memory");
#pragma unroll
          for (int i = 0; i < 4; i++) trv[hh][i] = (u32x4){tr[2 * i][0], tr[2 * i][1], tr[2 * i + 1][0], tr[2 * i + 1][1]};
        }
      };
      unsigned pf2 = 0;
      auto burst = [&](int hs) {
        static_assert(NB2 == 3, "");
        asm volatile("s_cmp_lg_u32 %4, %5\n\ts_cbranch_scc1 1f\n\tglobal_load_dword %0, %1, off" TPOL "\n\tglobal_load_dword %0, %2, off" TPOL "\n\tglobal_load_dword %0, %3, off" TPOL "\n1:"
                     : "+v"(pf2) : "v"(pp[0]), "v"(pp[1]), "v"(pp[2]), "s"(hs), "s"(tb2) : "scc");
      };
      auto utst = [&]() {
      asm volatile("s_waitcnt vmcnt(0)" :: "v"(pf2) : "memory");
      if (cb < 10) {
        u16* dst = (u16*)(ws + WS_UT) + (size_t)(64 * cb + 32 * lw + (lane & 15)) * UT_STRIDE + 8 * (lane >> 4);
#pragma unroll
        for (int hh = 0; hh < 2; hh++) {
#pragma unroll
          for (int i = 0; i < 4; i++) st16(dst + hh * 16 * UT_STRIDE + UT_PAD + row0 + 32 * i, trv[hh][i]);
          if (rowblk == 0) st16(dst + hh * 16 * UT_STRIDE, (u32x4){0u, 0u, 0u, 0u});
        }
      }
      };
      eoffs(ISQ);
      VMCNT(0);
      BAR_ONLY();
      int ao = 0;
#pragma unroll 1
      for (int j = 0; j < 10; j++) {
        BAR_ONLY();
        if (!lw) STEPT(2 * j);
        burst(2 * j);
        if (j == cb) transp(ao);
        BAR_ONLY();
        if (!lw) STEPT(2 * j + 1);
        burst(2 * j + 1);
        ao = ao == 32768 ? 0 : ao + 16384;
      }
      utst();
      BAR_ONLY();
    }
    __builtin_amdgcn_s_setprio(EPRIO);
    char* xch = smem + 32768 + ((wave & 3) ^ 2) * 8192 + lane * 16;
    if (ISQ || wce == 0) {
#pragma unroll
      for (int i = 0; i < 8; i++) ep[i >> 2][i & 3] = *(const f32x4*)(xch + i * 1024);
    }
  } else if (ISQ) {
    eoffs(true);
    mfma_part<true>(smem, ws, wave, ep, 0);
  } else {
    eoffs(false);
    mfma_part<false>(smem, ws, wave, ep, zc);
  }
  int l2 = threadIdx.x; asm volatile("" : "+v"(l2));
  const int lane2 = l2 & 63;
  f32x2 kl = {1.44269504f, 1.44269504f};
  asm volatile("" : "+s"(kl));
  char* fb = smem;
  if (ISQ || wce == 0) {
    float mn[2] = {ep[0][0][0], ep[1][0][0]};
#pragma unroll
    for (int i = 0; i < 16; i++) mn[i & 1] = __builtin_fminf(__builtin_fminf(mn[i & 1], ep[i & 1][i >> 2][i & 2]), ep[i & 1][i >> 2][(i & 2) + 1]);
    const bool small = __builtin_amdgcn_ballot_w64(!(__builtin_fminf(mn[0], mn[1]) > -7.f)) != 0;
    char* base = ws + WS_PQ + (size_t)((rowblk * 2 + wr) * 128 + 2 * hf) * 640;
    char* fk = fb + (wr * 2 + hf) * 4096 + lane2 * 16;
    auto body = [&](bool isq, bool eps) {
#pragma unroll
      for (int hh = 0; hh < 2; hh++) {
        f32x2 t[8], x[8];
#pragma unroll
        for (int i = 0; i < 8; i++) { const f32x4 v = ep[(i >> 1) & 1][2 * hh + (i >> 2)]; t[i] = (f32x2){rb1(v[2 * (i & 1)]), rb1(v[2 * (i & 1) + 1])}; }
        SB();
#pragma unroll
        for (int i = 0; i < 8; i++) x[i] = t[i] * kl;
        SB();
#pragma unroll
        for (int i = 0; i < 8; i++) {
          x[i][0] = __builtin_amdgcn_fmed3f(__builtin_amdgcn_exp2f(x[i][0]), 0.f, 1.f); x[i][1] = __builtin_amdgcn_fmed3f(__builtin_amdgcn_exp2f(x[i][1]), 0.f, 1.f);
          t[i] = t[i] + (f32x2){1.f, 1.f};
        }
        SB();
#pragma unroll
        for (int i = 0; i < 8; i++) { t[i][0] = __builtin_fmaxf(t[i][0], x[i][0]); t[i][1] = __builtin_fmaxf(t[i][1], x[i][1]); }
        SB();
        u32x4 a[2];
#pragma unroll
        for (int i = 0; i < 8; i++) { a[i >> 2][i & 3] = pack2(t[i][0], t[i][1]); if (eps) a[i >> 2][i & 3] = phi_eps(a[i >> 2][i & 3]); }
        SB();
#pragma unroll
        for (int k = 0; k < 2; k++) {
          if (isq) {
            const unsigned off = (k ? eo[hh] >> 16 : eo[hh] & 0xffffu) * 8u;
            *(u32x2*)(base + off) = (u32x2){a[k][0], a[k][1]};
            *(u32x2*)(base + off + 640) = (u32x2){a[k][2], a[k][3]};
          } else {
            *(u32x4*)(fk + (2 * hh + k) * 1024) = a[k];
          }
        }
        SB();
      }
    };
    if (__builtin_expect(small, 0)) body(ISQ, true); else if (__builtin_expect(ISQ, 0)) body(true, false); else body(false, false);
  }
  if (!ISQ) {
    RAW_BARRIER();
    s16x8 fk[2][4], fv[2][4];
#pragma unroll
    for (int rg = 0; rg < 4; rg++)
#pragma unroll
      for (int i = 0; i < 2; i++) {
        fk[i][rg] = *(const s16x8*)(fb + ((ta[i] * 64 + lane2) << 4) + rg * 4096);
        fv[i][rg] = *(const s16x8*)(fb + 16384 + ((tb[i] * 64 + lane2) << 4) + rg * 4096);
      }
    f32x4 dd[2] = {{0.f, 0.f, 0.f, 0.f}, {0.f, 0.f, 0.f, 0.f}};
#pragma unroll
    for (int rg = 0; rg < 4; rg++)
#pragma unroll
      for (int i = 0; i < 2; i++) dd[i] = __builtin_amdgcn_mfma_f32_16x16x32_bf16(fk[i][rg], fv[i][rg], dd[i], 0, 0, 0);
    char* pb = ws + WS_PART + (size_t)((3 * kvj * 16 + rowblk) * 21) * 80;
#pragma unroll
    for (int i = 0; i < 2; i++)
      if (eo[i] != ~0u) *(f32x4*)(pb + eo[i]) = dd[i];
  }
  asm volatile("s_waitcnt vmcnt(0)" :: "v"(pf3) : "memory");
}

__device__ __forceinline__ cf zpow(float th, float tl, float rh, float rl, float p) {
  float e = __builtin_amdgcn_exp2f(th * p); e = fmaf(e, (tl * p) * 0.69314718f, e);
  float t = rh * p; float f = (t - __builtin_rintf(t)) + rl * p;
  cf o; o[0] = e * __builtin_amdgcn_cosf(f); o[1] = e * __builtin_amdgcn_sinf(f); return o;
}
#define PKM(o, a, b) "v_pk_mul_f32 " o ", " a ", " b " op_sel:[0,0] op_sel_hi:[0,1]\n\t"
#define PKF(o, a, b) "v_pk_fma_f32 " o ", " a ", " b ", " o " op_sel:[1,1,0] op_sel_hi:[1,0,1] neg_lo:[1,0,0]\n\t"
__device__ __forceinline__ void cmul4(cf& o0, cf& o1, cf& o2, cf& o3, cf a0, cf b0, cf a1, cf b1, cf a2, cf b2, cf a3, cf b3) {
  asm(PKM("%0", "%4", "%5") PKM("%1", "%6", "%7") PKM("%2", "%8", "%9") PKM("%3", "%10", "%11")
      PKF("%0", "%4", "%5") PKF("%1", "%6", "%7") PKF("%2", "%8", "%9") PKF("%3", "%10", "%11")
      : "=&v"(o0), "=&v"(o1), "=&v"(o2), "=&v"(o3)
      : "v"(a0), "v"(b0), "v"(a1), "v"(b1), "v"(a2), "v"(b2), "v"(a3), "v"(b3));
}
__device__ __forceinline__ void batch4(unsigned* h, cf b, cf t0, cf t1, cf t2, cf t3) {
  cf w0, w1, w2, w3;
  asm(PKM("%0", "%4", "%5") PKM("%1", "%4", "%6") PKM("%2", "%4", "%7") PKM("%3", "%4", "%8")
      PKF("%0", "%4", "%5") PKF("%1", "%4", "%6") PKF("%2", "%4", "%7") PKF("%3", "%4", "%8")
      : "=&v"(w0), "=&v"(w1), "=&v"(w2), "=&v"(w3) : "v"(b), "v"(t0), "v"(t1), "v"(t2), "v"(t3));
  h[0] = pack2(w0[0], w0[1]); h[1] = pack2(w1[0], w1[1]); h[2] = pack2(w2[0], w2[1]); h[3] = pack2(w3[0], w3[1]);
}
__device__ __forceinline__ void batch3(unsigned* h, cf b, cf t1, cf t2, cf t3) {
  cf w1, w2, w3;
  asm(PKM("%0", "%3", "%4") PKM("%1", "%3", "%5") PKM("%2", "%3", "%6")
      PKF("%0", "%3", "%4") PKF("%1", "%3", "%5") PKF("%2", "%3", "%6")
      : "=&v"(w1), "=&v"(w2), "=&v"(w3) : "v"(b), "v"(t1), "v"(t2), "v"(t3));
  h[0] = pack2(b[0], b[1]); h[1] = pack2(w1[0], w1[1]); h[2] = pack2(w2[0], w2[1]); h[3] = pack2(w3[0], w3[1]);
}
constexpr int KG_RS = 288, KG_PL = 16 * KG_RS;
__device__ __forceinline__ void kgen_role(char* smem, const u16* __restrict__ logA, const u16* __restrict__ Aim,
                                          const u16* __restrict__ Cr, const u16* __restrict__ Ci, const u16* __restrict__ logdt,
                                          const u16* __restrict__ x, const u16* __restrict__ W, const u16* __restrict__ wo,
                                          const u16* __restrict__ gw, char* __restrict__ ws, int blk, unsigned pfv) {
  const int tid = threadIdx.x, lane = tid & 63;
  const int wave = __builtin_amdgcn_readfirstlane(tid >> 6);
  constexpr int RS = KG_RS, PL = KG_PL;
  char* tb = smem + wave * (2 * PL);
  char* wp = tb + lane * 4;
  const char* rp = tb + (lane & 15) * RS + (lane >> 4) * 16;
  const int kq = blk >> 3, tbase = (blk & 7) * 240;
  const int nrep = kq >= 27 ? 0 : (wave == 7 ? 2 : 1);
  unsigned pa[5], pb[5] = {0u, 0u, 0u, 0u, 0u};
  {
    const int task = tbase + min(kq * 9 + wave, 239), pi = task * 64 + lane;
    pa[0] = logdt[task / 3]; pa[1] = logA[pi]; pa[2] = Aim[pi]; pa[3] = Cr[pi]; pa[4] = Ci[pi];
    if (wave == 7) {
      const int t2 = tbase + min(kq * 9 + 8, 239), p2 = t2 * 64 + lane;
      pb[0] = logdt[t2 / 3]; pb[1] = logA[p2]; pb[2] = Aim[p2]; pb[3] = Cr[p2]; pb[4] = Ci[p2];
    }
  }
  int tix[8];
  {
    auto tpos = [&](int g, int& sh) {
      const int hh = g >> 4;
      sh = min(31 - __builtin_clz(hh | 1), 3);
      const int row = 8 * sh + ((hh << 3) >> sh);
      return 4096 + (tid >> 8) * (2 * PL) + ((row >> 2) * 16 + (g & 15)) * 16 + (row & 3) * 4;
    };
    int sh, s1;
    const int g = tid & 255, a0 = tpos(g, sh), a1 = tpos(g + 1, s1);
    const unsigned tab[8] = {0u, 0x1u, 0x102u, 0x10203u, 0x10240u | ((g & 1) ? 0xff000000u : 0u), 0xff010241u, 0xffff0342u, 0xffffff43u};
#pragma unroll
    for (int e = 0; e < 8; e++) {
      const unsigned v = __builtin_amdgcn_ubfe(tab[e], 8 * sh, 8);
      tix[7 - e] = (e >= 4 && v == 255u) ? a1 : a0 + 4 * (int)v;
      asm volatile("" : "+v"(tix[7 - e]));
    }
  }
  SB();
  asm volatile("" : "+v"(pb[0]), "+v"(pb[1]), "+v"(pb[2]), "+v"(pb[3]), "+v"(pb[4]));
  if (wave == 7) __builtin_amdgcn_s_setprio(W7P);
  const int gt = wave * 30 + (blk >> 3), grow = (blk & 7) * 256 + gt * 4 + (lane >> 4);
  u32x4 xv[5], gv[5];
  asm volatile("" : "+v"(pa[0]), "+v"(pa[1]), "+v"(pa[2]), "+v"(pa[3]), "+v"(pa[4]));
  if (gt < 64) {
    const u32x4* px = (const u32x4*)(x + (size_t)grow * D) + (lane & 15); const u32x4* pg = (const u32x4*)gw + (lane & 15);
#pragma unroll
    for (int i = 0; i < 5; i++) { xv[i] = px[16 * i]; gv[i] = pg[16 * i]; }
  }
  SB();
#pragma unroll 1
  for (int rep = 0; rep < nrep; rep++) {
    const float dt = rb(expf(lo_f(pa[0])));
    float ar;
    {
      const float la = lo_f(pa[1]);
      float h = la * 1.44269502f; float l = fmaf(la, 1.44269502f, -h) + la * 1.92596299e-8f;
      float e = __builtin_amdgcn_exp2f(h); e = fmaf(e, l * 0.69314718f, e);
      ar = -rb(e) * dt;
    }
    const float ai = lo_f(pa[2]) * dt;
    cf C; C[0] = 2.f * (lo_f(pa[3]) * dt); C[1] = 2.f * (lo_f(pa[4]) * dt);
#pragma unroll
    for (int i = 0; i < 5; i++) pa[i] = pb[i];
    const float th = ar * 1.44269502f, tl = fmaf(ar, 1.44269502f, -th) + ar * 1.92596299e-8f;
    const float rh = ai * 0.159154937f, rl = fmaf(ai, 0.159154937f, -rh) + ai * 6.42063833e-9f;
    const cf one = {1.f, 0.f};
    cf S1 = zpow(th, tl, rh, rl, 1.f), Q1 = zpow(th, tl, rh, rl, 8.f), R1 = zpow(th, tl, rh, rl, 128.f);
    Q1[1] = -Q1[1];
    cf S2, Q2, R2, G1, S3, S4, Q3, P1, R3, T1, G2, G3, S5, S6, S7, P2, CT1, T2, P3, CS4, CT2, CT3, CT4, dm0;
    cmul4(S2, Q2, R2, G1, S1, S1, Q1, Q1, R1, R1, C, R1);
    cmul4(S3, S4, Q3, P1, S2, S1, S2, S2, Q2, Q1, Q2, Q2);
    cmul4(R3, T1, G2, P2, R2, R1, R2, R2, C, R2, P1, P1);
    cmul4(S5, S6, S7, P3, S4, S1, S4, S2, S4, S3, P2, P1);
    cmul4(G3, CT1, T2, dm0, C, R3, C, T1, T1, T1, C, R1);
    cmul4(CS4, CT2, CT3, dm0, CT1, S4, C, T2, CT1, T2, C, R1);
    CT4 = cmul(CT2, T2);
    auto rows4 = [&](int pl, int row, int rs, cf b, cf t0, cf t1, cf t2, cf t3) {
      unsigned h[4];
      batch4(h, b, t0, t1, t2, t3);
#pragma unroll
      for (int k = 0; k < 4; k++) *(unsigned*)(wp + pl * PL + (row + k * rs) * RS) = h[k];
    };
    auto rows3 = [&](int pl, int row, int rs, cf b, cf t1, cf t2, cf t3) {
      unsigned h[4];
      batch3(h, b, t1, t2, t3);
#pragma unroll
      for (int k = 0; k < 4; k++) *(unsigned*)(wp + pl * PL + (row + k * rs) * RS) = h[k];
    };
    *(unsigned*)(wp) = 0x00003F80u;
    *(unsigned*)(wp + RS) = pack2(Q1[0], Q1[1]); *(unsigned*)(wp + 2 * RS) = pack2(Q2[0], Q2[1]); *(unsigned*)(wp + 3 * RS) = pack2(Q3[0], Q3[1]);
    rows3(0, 4, 1, P1, Q1, Q2, Q3); rows3(0, 8, 1, P2, Q1, Q2, Q3); rows3(0, 12, 1, P3, Q1, Q2, Q3);
    rows3(1, 0, 1, C, S1, S2, S3); rows4(1, 4, 1, C, S4, S5, S6, S7); rows3(1, 8, 1, G1, S1, S2, S3); rows4(1, 12, 1, G1, S4, S5, S6, S7);
    __builtin_amdgcn_wave_barrier();
    s16x8 bh[4];
    f32x4 res[3];
#pragma unroll
    for (int k = 0; k < 4; k++) bh[k] = *(const s16x8*)(rp + k * 64);
    auto mm = [&](int t, int pl) {
      f32x4 a = {0.f, 0.f, 0.f, 0.f};
#pragma unroll
      for (int k = 0; k < 4; k++) a = __builtin_amdgcn_mfma_f32_16x16x32_bf16(*(const s16x8*)(rp + pl * PL + k * 64), bh[k], a, 0, 0, 0);
      res[t] = a;
    };
    mm(0, 1);
    __builtin_amdgcn_wave_barrier();
    rows3(0, 0, 1, G2, S2, S4, S6); rows3(0, 4, 1, G3, S2, S4, S6); rows3(0, 8, 2, CT1, R1, R2, R3); rows3(0, 9, 2, CS4, R1, R2, R3);
    rows3(1, 0, 1, CT2, R1, R2, R3); rows3(1, 4, 1, CT3, R1, R2, R3);
    *(unsigned*)(wp + PL + 8 * RS) = pack2(CT4[0], CT4[1]);
    __builtin_amdgcn_wave_barrier();
    mm(1, 0); mm(2, 1);
    __builtin_amdgcn_wave_barrier();
    char* rs = (wave == 7 && rep == 0) ? smem + 8 * (2 * PL) : tb;
#pragma unroll
    for (int t = 0; t < 3; t++) *(f32x4*)(rs + t * 1024 + lane * 16) = res[t];
    __builtin_amdgcn_wave_barrier();
  }
  STAMP(0, 4);
  if (gt < 64) {
    float gs = 0.f;
#pragma unroll
    for (int i = 0; i < 5; i++)
#pragma unroll
      for (int e = 0; e < 4; e++) {
        unsigned p = pack2(lo_f(xv[i][e]) * lo_f(gv[i][e]), hi_f(xv[i][e]) * hi_f(gv[i][e]));
        gs = __builtin_amdgcn_fdot2_f32_bf16(__builtin_bit_cast(bf16x2_t, p), __builtin_bit_cast(bf16x2_t, 0x3F803F80u), gs, false);
      }
    gs += __shfl_xor(gs, 1); gs += __shfl_xor(gs, 2); gs += __shfl_xor(gs, 4); gs += __shfl_xor(gs, 8);
    if ((lane & 15) == 0) { float a = rb(gs); ((float*)(ws + WS_GATE))[grow] = rb(1.f / (1.f + __expf(-a))); }
  }
  if (blk == 239) ((unsigned*)(ws + WS_CNT))[tid] = 0u, ((unsigned*)(ws + WS_CNT))[tid + 512] = 0u;
  if (blk >= 216 && blk < 220) st16(ws + WS_KVT + (size_t)((blk - 216) * 512 + tid) * 16, (u32x4){~0u, ~0u, ~0u, ~0u});
  if (kq < 27) {
    __syncthreads();
#pragma unroll
    for (int it = 0; it < 2; it++) {
      const int e = tid + 512 * it;
      const int seg = e / 192, ent = e - 192 * seg;
      if (seg < 3) {
        const char* p0 = smem + 3 * seg * (2 * PL) + ent * 16;
        const f32x4 a = *(const f32x4*)p0 + *(const f32x4*)(p0 + (seg == 2 ? 2 : 1) * (2 * PL)) + *(const f32x4*)(p0 + (seg == 2 ? 1 : 2) * (2 * PL));
        *(f32x4*)(smem + 4096 + seg * (2 * PL) + ent * 16) = a;
      }
    }
    __syncthreads();
    const int d0 = 80 * (blk & 7) + 3 * kq + (tid >> 8);
    u32x4 hi;
#pragma unroll
    for (int e = 0; e < 4; e++) hi[e] = pack2(*(const float*)(smem + tix[2 * e]), *(const float*)(smem + tix[2 * e + 1]));
    st16(ws + WS_KIMG + (size_t)d0 * 4096 + (tid & 255) * 16, hi);
    if (wave < 4 && kq < 26) {
#pragma unroll
      for (int e = 0; e < 4; e++) hi[e] = pack2(*(const float*)(smem + tix[2 * e] + 4 * PL), *(const float*)(smem + tix[2 * e + 1] + 4 * PL));
      st16(ws + WS_KIMG + (size_t)(d0 + 2) * 4096 + tid * 16, hi);
    }
    VMCNT(0);
    if (lane < 16) {
      const int o = (int)WS_KIMG + (d0 + ((lane & 8) && wave < 4 && kq < 26 ? 2 : 0)) * 4096 + (wave & 3) * 1024 + (lane & 7) * 128;
      asm volatile("global_load_dword %0, %1, %2" : "+v"(pfv) : "v"(o), "s"(ws));
    }
  }
  asm volatile("s_waitcnt vmcnt(0)" :: "v"(pfv) : "memory");
}

__attribute__((used, noinline)) __device__ void code_gap0() { asm volatile(".fill %0, 4, 0xBF800000" :: "n"(PADM)); }
__global__ __launch_bounds__(512, 4) void k1(const u16* __restrict__ x, const u16* __restrict__ logA, const u16* __restrict__ Aim,
                                          const u16* __restrict__ Cr, const u16* __restrict__ Ci, const u16* __restrict__ logdt,
                                          const u16* __restrict__ wqkv, const u16* __restrict__ gw, const u16* __restrict__ wo,
                                          char* __restrict__ ws) {
  __shared__ __attribute__((aligned(16))) char smem[K1_LDS];
  int b = blockIdx.x;
  unsigned cpf = 0;
  const int w4 = __builtin_amdgcn_readfirstlane(threadIdx.x >> 6) & 3;
  int r = 0; unsigned to = 0;
  if (b >= NB_KG) {
    __builtin_amdgcn_s_setprio(GPRIO);
    to = touch_off(b - NB_KG, w4 * NE + min((int)(threadIdx.x & 63), NE - 1), r);
    asm volatile("" : "+v"(to), "+v"(r));
  }
  asm volatile("" :: "s"(x), "s"(logA), "s"(Aim), "s"(Cr), "s"(Ci), "s"(logdt), "s"(wqkv), "s"(gw), "s"(wo), "s"(ws));
  if (b >= NB_KG && threadIdx.x >= 256 && threadIdx.x < 384) TOUCH_LDS((r < 16 ? (const char*)x : (const char*)wqkv) + to, (int)(size_t)(lptr_t)smem + 65536 + w4 * 256);
  if (b < NB_KG && threadIdx.x >= 501) {
    const char* pc; int len;
    asm volatile("s_getpc_b64 %0\n\ts_mov_b32 %1, code_tail_mark-_Z2k1PKtS0_S0_S0_S0_S0_S0_S0_S0_Pc-128" : "=s"(pc), "=s"(len));
    const int off = min((int)((threadIdx.x - 501) * 30 + (b >> 3)) * 128, len);
    asm volatile("global_load_dword %0, %1, %2" : "+v"(cpf) : "v"(off), "s"(pc));
  }
  if (b < 8 && threadIdx.x >= 490 && threadIdx.x < 500) asm volatile("global_load_dword %0, %1, %2" : "+v"(cpf) : "v"((threadIdx.x - 490) * 128), "s"(gw));
  STAMP(0, 0);
  b = b < NB_KG ? b + NB_GEMM : (b < NB_KG + NB_GEMM ? b - NB_KG : b);
  if (b < NB_GEMM) {
    const int xcd = b & 7, idx = b >> 3;
    const int rowblk = (xcd >> 1) * 4 + (idx & 3), slot = idx >> 2, par = xcd & 1;
    const int cb = par ? (slot < 2 ? slot + 3 : slot + 8) : (slot < 3 ? slot : slot + 2);
    gemm_role(smem, x, wqkv, wo, ws, rowblk, cb, slot, par);
    STAMP(0, 1);
    return;
  }
  b -= NB_GEMM;
  if (b < NB_KG) { kgen_role(smem, logA, Aim, Cr, Ci, logdt, x, wqkv, wo, gw, ws, b, cpf); STAMP(0, 1); return; }
}

constexpr int K2_LDS = 163840;
constexpr int K2_CS = 4160;
constexpr int K2_KOFF = 4 * K2_CS;
constexpr int K2_CH = K2_KOFF + 4608;
constexpr int K2_CONV = K2_LDS - 3 * K2_CH;
constexpr int K2_TAB = 81920;
constexpr int K2_FIN = K2_TAB + 9216;
constexpr int K2_KV = 102400;
constexpr int K2_STG = 131072;
constexpr int NB_KV = 32;
typedef __attribute__((address_space(1))) unsigned gu32;
__device__ __forceinline__ const char* uni(const char* p) {
  size_t v = (size_t)p;
  unsigned lo = __builtin_amdgcn_readfirstlane((unsigned)v), hi = __builtin_amdgcn_readfirstlane((unsigned)(v >> 32));
  size_t o = ((size_t)hi << 32) | lo;
  asm volatile("s_nop 4" : "+s"(o));
  return (const char*)o;
}
__device__ __forceinline__ void wait_count(char* p, unsigned target) {
  gu32* c = (gu32*)(size_t)p;
  for (int n = 0; n < (1 << 22); n++) {
    if (__hip_atomic_load(c, __ATOMIC_RELAXED, __HIP_MEMORY_SCOPE_AGENT) >= target) break;
    __builtin_amdgcn_s_sleep(8);
  }
}
#define GLD4(dst, voff, sbase, pol) asm volatile("global_load_dwordx4 %0, %1, %2" pol : "=v"(dst) : "v"(voff), "s"(sbase))
#define GLD2(dst, voff, sbase, pol) asm volatile("global_load_dwordx2 %0, %1, %2" pol : "=v"(dst) : "v"(voff), "s"(sbase))
#define GLD1(dst, voff, sbase, pol) asm volatile("global_load_dword %0, %1, %2" pol : "=v"(dst) : "v"(voff), "s"(sbase))
template <int G>
__device__ __forceinline__ void conv_unit(f32x4* acc, int ap, int bp) {
  constexpr int NT = 8 - G;
  u32x2 a0[NT], a1[NT]; s16x8 bh[NT];
#pragma unroll
  for (int j = 0; j < NT; j++)
    asm volatile("ds_read_b64 %0, %3 offset:%5\n\tds_read_b64 %1, %3 offset:%6\n\tds_read_b128 %2, %4 offset:%5"
                 : "=&v"(a0[j]), "=&v"(a1[j]), "=&v"(bh[j]) : "v"(ap), "v"(bp), "n"(512 * j), "n"(512 * j + 8) : "memory");
#pragma unroll
  for (int x = 0; x < NT; x++) {
    asm volatile("s_waitcnt lgkmcnt(%3)" : "+v"(a0[x]), "+v"(a1[x]), "+v"(bh[x]) : "n"(3 * (NT - 1 - x) > 15 ? 15 : 3 * (NT - 1 - x)) : "memory");
#pragma unroll
    for (int y = 0; y <= x; y++)
#pragma unroll
      for (int sw = 0; sw < 2; sw++) {
        if (sw && y == x) continue;
        const int j = sw ? y : x, m = sw ? x : y, T = j + m;
        const int ti = G == 0 ? (T == 0 ? 0 : T == 3 ? 1 : T == 4 ? 2 : T == 7 ? 3 : -1) : (T == 1 ? 0 : T == 2 ? 1 : T == 5 ? 2 : T == 6 ? 3 : -1);
        if (ti >= 0) acc[ti] = __builtin_amdgcn_mfma_f32_16x16x32_bf16(__builtin_bit_cast(s16x8, (u32x4){a0[j][0], a0[j][1], a1[j][0], a1[j][1]}), bh[m], acc[ti], 0, 0, 0);
      }
    SB();
  }
}

__global__ __launch_bounds__(576) void k2(const u16* __restrict__ Dskip, const u16* __restrict__ wo, char* __restrict__ ws, u16* __restrict__ out) {
  __shared__ __attribute__((aligned(16))) char smem[K2_LDS];
  const int tid = threadIdx.x, lane = tid & 63;
  const int wave = __builtin_amdgcn_readfirstlane(tid >> 6);
  const int b = blockIdx.x;
  unsigned cpf = 0;
  if (b < 32 && tid < 192) {
    const char* pc; int len;
    asm volatile("s_getpc_b64 %0\n\ts_mov_b32 %1, code_tail_mark-_Z2k2PKtS0_PcPt-128" : "=s"(pc), "=s"(len));
    const int off = min(tid * 128, len);
    asm volatile("global_load_dword %0, %1, %2" : "+v"(cpf) : "v"(off), "s"(pc));
  }
  STAMP(1, 0);
  const int lds0 = (int)(size_t)(lptr_t)smem;
  const int l16 = lane * 16;
  const int xcd = b & 7, idx = b >> 3;
  const int odd = idx & 1;
  const int chA = 80 * xcd + 5 * (idx >> 1) + 3 * odd, chH = 80 * xcd + 5 * (idx >> 1) + 2;
  const int rowblk = xcd * 4 + (idx >> 3), cb = idx & 7;
  const int l0 = rowblk * 64, c0 = cb * 80;
  const int r = lane & 31, h = lane >> 5;
  if (wave == 8) {
    __builtin_amdgcn_s_setprio(3);
    const char* wsrc = (const char*)wo + cb * 160;
    int wv[5];
#pragma unroll
    for (int j = 0; j < 5; j++) { const int f = 64 * j + lane, row = f / 10; wv[j] = row * 1280 + (f - 10 * row) * 16 - (j & 3) * 1024; }
    auto dma5 = [&](int grp) {
      asm volatile("s_mov_b32 m0, %6\n\ts_nop 0\n\tglobal_load_lds_dwordx4 %0, %5\n\tglobal_load_lds_dwordx4 %1, %5 offset:1024\n\t"
                   "global_load_lds_dwordx4 %2, %5 offset:2048\n\tglobal_load_lds_dwordx4 %3, %5 offset:3072\n\t"
                   "s_add_u32 m0, m0, 0x1000\n\ts_nop 0\n\tglobal_load_lds_dwordx4 %4, %5"
                   :: "v"(wv[0]), "v"(wv[1]), "v"(wv[2]), "v"(wv[3]), "v"(wv[4]), "s"(wsrc + grp * 40960), "s"(lds0 + grp * 5120) : "memory", "m0", "scc");
    };
    auto dma4 = [&](const char* src, int dst) {
      asm volatile("s_mov_b32 m0, %2\n\ts_nop 0\n\tglobal_load_lds_dwordx4 %0, %1\n\tglobal_load_lds_dwordx4 %0, %1 offset:1024\n\t"
                   "global_load_lds_dwordx4 %0, %1 offset:2048\n\tglobal_load_lds_dwordx4 %0, %1 offset:3072"
                   :: "v"(l16), "s"(src), "s"(lds0 + dst) : "memory", "m0");
    };
    auto kimg = [&]() {
      dma4(ws + WS_KIMG + chA * 4096, K2_CONV + K2_KOFF + 512);
      dma4(ws + WS_KIMG + chA * 4096 + 4096, K2_CONV + K2_CH + K2_KOFF + 512);
      dma4(ws + WS_KIMG + chH * 4096, K2_CONV + 2 * K2_CH + K2_KOFF + 512);
    };
    if (b < NB_KV) {
      f32x4 kvp[32];
      const char* src = ws + WS_PART + (size_t)b * 16 * 1680;
      const int o2 = min(lane + 64, 104) * 16;
#pragma unroll
      for (int i = 0; i < 16; i++) { GLD4(kvp[i], l16, src + i * 1680, ""); GLD4(kvp[16 + i], o2, src + i * 1680, ""); }
      kimg();
#pragma unroll
      for (int i = 0; i < NW0; i++) dma5(i);
      asm volatile("s_waitcnt vmcnt(%0)" :: "n"(5 * NW0) : "memory");
      BAR_ONLY();
#pragma unroll
      for (int i = 0; i < 32; i += 8)
        asm volatile("" : "+v"(kvp[i]), "+v"(kvp[i + 1]), "+v"(kvp[i + 2]), "+v"(kvp[i + 3]), "+v"(kvp[i + 4]), "+v"(kvp[i + 5]), "+v"(kvp[i + 6]), "+v"(kvp[i + 7]));
#pragma unroll
      for (int st = 1; st < 16; st <<= 1)
#pragma unroll
        for (int i = 0; i < 32; i += 2 * st) kvp[i] += kvp[i + st];
      const u32x2 o0 = {pack2(kvp[0][0], kvp[0][1]), pack2(kvp[0][2], kvp[0][3])}, o1 = {lane < 41 ? pack2(kvp[16][0], kvp[16][1]) : 0u, lane < 41 ? pack2(kvp[16][2], kvp[16][3]) : 0u};
      const int o = (int)WS_KVT + b * 896 + lane * 8;
      asm volatile("global_store_dwordx2 %0, %1, %2 sc1" :: "v"(o), "v"(o0), "s"(ws) : "memory");
      if (lane < 48) asm volatile("global_store_dwordx2 %0, %1, %2 offset:512 sc1\n\ts_nop 1" :: "v"(o), "v"(o1), "s"(ws) : "memory");
      KVST;
    } else {
      kimg();
#pragma unroll
      for (int i = 0; i < NW0; i++) dma5(i);
      asm volatile("s_waitcnt vmcnt(%0)" :: "n"(5 * NW0) : "memory");
      BAR_ONLY();
    }
    static_assert(NW0 <= 3 && B6I <= 4, "");
#pragma unroll 1
    for (int p = NW0; p < 13; p++) {
      dma5(p < 9 ? p + 2 * (p / 3) : p + 6);
    }
    {
      u32x4 kt[28];
      const char* kp = ws + WS_KVT;
#define KVL(pol) _Pragma("unroll") for (int i = 0; i < 28; i++) GLD4(kt[i], l16, kp + i * 1024, pol);
#define KVW() _Pragma("unroll") for (int i = 0; i < 28; i += 7) asm volatile("" : "+v"(kt[i]), "+v"(kt[i + 1]), "+v"(kt[i + 2]), "+v"(kt[i + 3]), "+v"(kt[i + 4]), "+v"(kt[i + 5]), "+v"(kt[i + 6]));
      KVL(KV1);
      VMCNT(0);
      KVW();
      for (int n = 0; n < (1 << 14); n++) {
        unsigned m = 0;
#pragma unroll
        for (int i = 0; i < 28; i++) m = max(max(m, kt[i][0]), kt[i][2]);
        if (__builtin_amdgcn_ballot_w64(m == ~0u) == 0ull) break;
        KVL(" sc1");
        VMCNT(0);
        KVW();
      }
      BAR_ONLY();
#pragma unroll
      for (int i = 0; i < 28; i++) *(u32x4*)(smem + K2_KV + i * 1024 + l16) = kt[i];
    }
    asm volatile("s_waitcnt lgkmcnt(0)" ::: "memory");
    BAR_ONLY();
    dma5(3); dma5(8); dma5(13); dma5(4); dma5(9); dma5(14); dma5(19);
    VMCNT(0);
    BAR_ONLY();
    {
      gu32* c = (gu32*)(size_t)(ws + WS_CNT + (8 + cb) * 128 + (lane & 31) * 4);
      for (int n = 0; n < (1 << 22); n++) {
        if (__builtin_amdgcn_ballot_w64(__hip_atomic_load(c, __ATOMIC_RELAXED, __HIP_MEMORY_SCOPE_AGENT) == 0u) == 0ull) break;
        __builtin_amdgcn_s_sleep(1);
      }
      asm volatile("" ::: "memory");
      const int o = (lane >> 3) * (L * 2) + (((lane & 7) ^ ((lane >> 2) & 4)) << 4);
      const char* src = ws + WS_AT + (size_t)(c0 * L + l0) * 2;
#pragma unroll
      for (int i = 0; i < 10; i++)
        asm volatile("s_mov_b32 m0, %2\n\ts_nop 0\n\tglobal_load_lds_dwordx4 %0, %1 sc1" :: "v"(o), "s"(src + i * 8 * L * 2), "s"(lds0 + (i < 8 ? (i >> 1) * 3200 : 25600) + (i & 1) * 1024) : "memory", "m0");
      VMCNT(0);
    }
    BAR_ONLY();
    return;
  }
  const int t = wave & 1, kh = wave >> 1;
  u32x4 in0[2];
  int dsto[2];
#pragma unroll
  for (int rr = 0; rr < 2; rr++) {
    const int f = (wave + 8 * rr) * 49 + min(lane, 49), i = min(f, 776);
    const int ch = i / 259, xx = i - 259 * ch;
    dsto[rr] = (lane < 49 && f < 777) ? K2_CONV + ch * K2_CH + xx * 16 : -1;
    GLD4(in0[rr], (int)WS_UT + (ch == 2 ? chH : chA + ch) * (UT_STRIDE * 2) + (xx + 1) * 16, ws, "");
  }
  const int jg = (wave & 1) ^ JGX, jc = (wave >> 1) & 1, hiw = wave >> 2;
  const int fo = (lane & 15) * 16 + (lane >> 4) * 4;
  int ft[3];
  ft[0] = hiw ? 3 - jg : jg;
  ft[1] = hiw ? 4 + jg : (wave & 2) * 2 + ((wave & 1) ? 3 - odd : odd);
  ft[2] = 7 - jg;
  u32x2 us[3];
  float g_r;
  GLD1(g_r, (int)WS_GATE + (l0 + 32 * t + 16 * (kh & 1) + (lane & 15)) * 4, ws, "");
  unsigned dsA, dsH;
  asm volatile("global_load_ushort %0, %1, %2" : "=v"(dsA) : "v"((chA + jc) * 2), "s"(Dskip));
  asm volatile("global_load_ushort %0, %1, %2" : "=v"(dsH) : "v"((hiw ? chA + jc : chH) * 2), "s"(Dskip));
  u32x4 pf[8][2];
  const char* pqp = ws + WS_PQ + (size_t)((rowblk * 32 + kh * 8) * 4 + 2 * t) * 640;
  const int pqo = lane < 32 ? 128 + l16 : (lane & 15) * 8;
#define PQL(i0, i1) _Pragma("unroll") for (int i = i0; i < i1; i++) { GLD4(pf[i][0], pqo, pqp + i * 2560, ""); GLD4(pf[i][1], pqo, pqp + i * 2560, " offset:640"); }
  if (tid < 96) *(u32x4*)(smem + K2_CONV + (tid >> 5) * K2_CH + K2_KOFF + (tid & 31) * 16) = (u32x4){0u, 0u, 0u, 0u};
  asm volatile("s_waitcnt vmcnt(3)" : "+v"(in0[0]), "+v"(in0[1]), "+v"(cpf) :: "memory");
  STAMP(1, 2);
#pragma unroll
  for (int rr = 0; rr < 2; rr++) {
    const u32x4 a = in0[rr];
    unsigned w[6] = {a[0], a[1], a[2], a[3], (unsigned)__builtin_amdgcn_update_dpp(0, (int)a[0], 0x130, 0xf, 0xf, false),
                     (unsigned)__builtin_amdgcn_update_dpp(0, (int)a[1], 0x130, 0xf, 0xf, false)};
    unsigned o[5];
#pragma unroll
    for (int k = 0; k < 5; k++) o[k] = __builtin_amdgcn_alignbyte(w[k + 1], w[k], 2);
    if (dsto[rr] >= 0) {
      char* p = smem + dsto[rr];
#pragma unroll
      for (int q = 0; q < 2; q++) {
        *(u32x4*)(p + (2 * q) * K2_CS) = (u32x4){o[q], o[q + 1], o[q + 2], o[q + 3]};
        *(u32x4*)(p + (2 * q + 1) * K2_CS) = (u32x4){w[q + 1], w[q + 2], w[q + 3], w[q + 4]};
      }
    }
  }
  RAW_BARRIER();
  PQL(0, PQA);
  STAMP(1, 3);
  f32x4 ca[4], sv[4];
#pragma unroll
  for (int i = 0; i < 4; i++) { ca[i] = (f32x4){0.f, 0.f, 0.f, 0.f}; sv[i] = (f32x4){0.f, 0.f, 0.f, 0.f}; }
  {
    const int i = lane & 15, kg = lane >> 4, e = kg >> 1, bb = kg & 1;
    const int ap = lds0 + K2_CONV + (i & 3) * K2_CS + (2 * e - bb + 2) * 16 + (i >> 2) * 8;
    const int bp = lds0 + K2_CONV + K2_KOFF + (16 + i - e) * 32 + bb * 16;
    int apj = ap + jc * K2_CH + hiw * 192;
    int bpj = bp + jc * K2_CH - hiw * 192;
    if (jg == 0) {
#pragma unroll 1
      for (int it = 0; it < 3; it++) conv_unit<0>(ca, apj + it * 64, bpj - it * 64);
    } else {
#pragma unroll 1
      for (int it = 0; it < 3; it++) conv_unit<1>(ca, apj + it * 64, bpj - it * 64);
    }
    PQL(PQA, PQB);
    apj += 192; bpj -= 192;
    int g2 = jg;
    if (!hiw) {
#pragma unroll
      for (int q = 0; q < 4; q++) { sv[q] = ca[q]; ca[q] = (f32x4){0.f, 0.f, 0.f, 0.f}; }
      apj = ap + 2 * K2_CH + wave * 128; bpj = bp + 2 * K2_CH - wave * 128; g2 = odd;
    }
    if (g2 == 0) {
#pragma unroll 1
      for (int it = 0; it < 2; it++) conv_unit<0>(ca, apj + it * 64, bpj - it * 64);
    } else {
#pragma unroll 1
      for (int it = 0; it < 2; it++) conv_unit<1>(ca, apj + it * 64, bpj - it * 64);
    }
  }
  STAMP(1, 4);
  {
    const char* ua = smem + K2_CONV + 3 * K2_CS + (20 + fo) * 2;
    us[0] = *(const u32x2*)(ua + jc * K2_CH + ft[0] * 512); us[1] = *(const u32x2*)(ua + (hiw ? jc : 2) * K2_CH + ft[1] * 512);
    us[2] = *(const u32x2*)(ua + jc * K2_CH + ft[2] * 512);
  }
  PQL(PQB, 8);
  auto stj = [&](int job, int tile) { return 15360 + (job >> 1) * 25600 + ((job & 1) * 4 + tile) * 1024; };
  auto sth = [&](int i, int o) { return i < 3 ? 66560 + (i * 3 + o) * 1024 : 15360 + 9216 + o * 25600; };
  {
    char* sp = smem + l16;
    if (hiw) {
      *(f32x4*)(sp + stj(wave & 3, 0)) = ca[0];
    } else {
#pragma unroll
      for (int i = 1; i < 4; i++) *(f32x4*)(sp + stj(wave, i)) = sv[i];
#pragma unroll
      for (int i = 0; i < 4; i++) if (i != wave) *(f32x4*)(sp + sth(i, (wave - i - 1) & 3)) = ca[i];
    }
  }
  RAW_BARRIER();
  {
    if (tid == 0) *(unsigned*)(smem + K2_LDS - 16) = 0u;
    const char* sp = smem + l16;
    asm volatile("s_waitcnt vmcnt(%3)" : "+v"(g_r), "+v"(dsA), "+v"(dsH) : "n"(2 * (8 - PQB)) : "memory");
    const float dskA = lo_f(dsA), dskH = lo_f(dsH);
    auto fin = [&](f32x4 s, u32x2 uu, float dsk, int ch, int tile) {
      u32x2 ov;
#pragma unroll
      for (int e = 0; e < 2; e++) {
        const f32x2 kk = (f32x2){lo_f(uu[e]), hi_f(uu[e])} * dsk;
        const f32x2 rr = (f32x2){rb1(s[2 * e]), rb1(s[2 * e + 1])} + (f32x2){rb1(kk[0]), rb1(kk[1])};
        ov[e] = pack2(rr[0], rr[1]);
      }
      const int o = (int)WS_AT + (ch * L + tile * 256 + fo) * 2;
      asm volatile("global_store_dwordx2 %0, %1, %2 sc1\n\ts_nop 1" :: "v"(o), "v"(ov), "s"(ws) : "memory");
    };
    if (hiw) {
      const char* pp = sp + stj(wave & 3, 0);
#pragma unroll
      for (int i = 1; i < 4; i++) fin(ca[i] + *(const f32x4*)(pp + i * 1024), us[i - 1], dskA, chA + jc, ft[i - 1]);
    } else {
      const char* hp = sp + sth(wave, 0);
      const int hs = wave < 3 ? 1024 : 25600;
      const f32x4 own = wave == 0 ? ca[0] : wave == 1 ? ca[1] : wave == 2 ? ca[2] : ca[3];
      fin(sv[0] + *(const f32x4*)(sp + stj(wave, 0)), us[0], dskA, chA + jc, ft[0]);
      fin((own + *(const f32x4*)(hp + hs)) + (*(const f32x4*)hp + *(const f32x4*)(hp + 2 * hs)), us[1], dskH, chH, ft[1]);
    }
  }
#pragma unroll
  for (int i = 0; i < (LATEPQ ? PQB : 8); i++) asm volatile("" : "+v"(pf[i][0]), "+v"(pf[i][1]));
  RAW_BARRIER();
  STAMP(1, 5);
  const int n16 = lane & 15, g = lane >> 4;
  f32x4 acc[5][2];
  {
    const char* kp = smem + K2_KV + kh * 8 * 896;
    const int row1 = (n16 & 3) ? 20 : 16 + (n16 >> 2);
    const char* k00 = kp + (g < 3 ? n16 * 40 + 16 * g : 840);
    const char* k01 = kp + (g < 2 ? n16 * 40 + 16 * g + 8 : 840);
    const char* k10 = kp + (g < 3 ? row1 * 40 + 16 * g : 840);
    const char* k11 = kp + (g < 2 ? row1 * 40 + 16 * g + 8 : 840);
    const char* wm = smem + (160 * kh + 4 * g + (n16 >> 2)) * 160 + (lane & 3) * 8;
    const char* wt = smem + (160 * kh + 20 * (n16 >> 2) + 16 + g) * 160 + (lane & 3) * 8;
    u32x4 ka[8][2];
    auto rdK = [&](int i) {
      const u32x2 a0 = *(const u32x2*)(k00 + i * 896), a1 = *(const u32x2*)(k01 + i * 896), b0 = *(const u32x2*)(k10 + i * 896), b1 = *(const u32x2*)(k11 + i * 896);
      ka[i][0] = (u32x4){a0[0], a0[1], a1[0], a1[1]}; ka[i][1] = (u32x4){b0[0], b0[1], b1[0], b1[1]};
    };
    f32x4 c[2][2][2];
    const f32x4 z4 = {0.f, 0.f, 0.f, 0.f};
    auto stageA = [&](int i) {
#pragma unroll
      for (int tl = 1; tl >= 0; tl--)
#pragma unroll
        for (int nt = 0; nt < 2; nt++)
          c[i & 1][tl][nt] = __builtin_amdgcn_mfma_f32_16x16x32_bf16(__builtin_bit_cast(s16x8, ka[i][tl]), __builtin_bit_cast(s16x8, pf[i][nt]), z4, 0, 0, 0);
    };
    auto wfrag = [&](const char* p, int d) {
      const s16x4 lo = __builtin_amdgcn_ds_read_tr16_b64_v4i16((lds_s16x4_p)(p)), hi = __builtin_amdgcn_ds_read_tr16_b64_v4i16((lds_s16x4_p)(p + d));
      return __builtin_shufflevector(lo, hi, 0, 1, 2, 3, 4, 5, 6, 7);
    };
    u32x4 ob[2][2], o5[2];
    float p5[2];
    s16x8 w[3];
    auto proj = [&](int s, int q) {
      if (s < 0) return;
      const u32x4 bb = s < 4 ? ob[s & 1][q & 1] : o5[q & 1];
      acc[q >> 1][q & 1] = __builtin_amdgcn_mfma_f32_16x16x32_bf16(w[(q >> 1) % 3], __builtin_bit_cast(s16x8, bb), s == 0 ? z4 : acc[q >> 1][q & 1], 0, 0, 0);
    };
    auto rdW = [&](int s, int mt) { w[mt % 3] = s < 4 ? wfrag(wm + s * 6400 + mt * 32, 3200) : wfrag(wt + mt * 32, 12800); };
    auto sA = [&](int i, int q) {
      if (i < 8) c[i & 1][1 - (q >> 1)][q & 1] = __builtin_amdgcn_mfma_f32_16x16x32_bf16(__builtin_bit_cast(s16x8, ka[i][1 - (q >> 1)]), __builtin_bit_cast(s16x8, pf[i][q & 1]), z4, 0, 0, 0);
    };
    rdK(0);
    stageA(0);
    SB();
#pragma unroll
    for (int i = 0; i < 8; i++) {
      const int s = (i >> 1) - 1, q0 = (i & 1) * 5;
      if (i + 1 < 8 && i <= B6I) rdK(i + 1);
      if (i == B6I) { _Pragma("unroll") for (int j = i + 2; j < 8; j++) rdK(j); }
      SB();
      const f32x4 (&cc)[2][2] = c[i & 1];
      proj(s, q0);
      f32x2 dd = {rb1(cc[1][0][1]), rb1(cc[1][1][1])};
      dd += (f32x2){1e-6f, 1e-6f};
      asm volatile("" : "+v"(dd));
      SB();
      proj(s, q0 + 1);
      if (s >= 0 && !(i & 1)) rdW(s, 3);
      if (i & 1) rdW(s + 1, 2);
      float d0 = rb1(dd[0]), d1 = rb1(dd[1]);
      asm volatile("" : "+v"(d0), "+v"(d1));
      SB();
      proj(s, q0 + 2);
      float inv[2] = {__builtin_amdgcn_rcpf(d0), __builtin_amdgcn_rcpf(d1)};
      asm volatile("" : "+v"(inv[0]), "+v"(inv[1]));
      SB();
      proj(s, q0 + 3);
      if (s >= 0 && !(i & 1)) rdW(s, 4);
      if (i & 1) rdW(s + 1, 0);
      f32x4 v0 = cc[0][0] * inv[0];
      float q50 = cc[1][0][0] * inv[0];
      asm volatile("" : "+v"(v0), "+v"(q50));
      SB();
      proj(s, q0 + 4);
      f32x4 v1 = cc[0][1] * inv[1];
      float q51 = cc[1][1][0] * inv[1];
      asm volatile("" : "+v"(v1), "+v"(q51));
      SB();
      sA(i + 1, 0);
      if (i & 1) rdW(s + 1, 1);
      ob[(i >> 1) & 1][0][2 * (i & 1)] = pack2(v0[0], v0[1]); ob[(i >> 1) & 1][0][2 * (i & 1) + 1] = pack2(v0[2], v0[3]);
      SB();
      sA(i + 1, 1);
      ob[(i >> 1) & 1][1][2 * (i & 1)] = pack2(v1[0], v1[1]); ob[(i >> 1) & 1][1][2 * (i & 1) + 1] = pack2(v1[2], v1[3]);
      SB();
      sA(i + 1, 2);
      if (i & 1) { o5[0][i >> 1] = pack2(p5[0], q50); o5[1][i >> 1] = pack2(p5[1], q51); } else { p5[0] = q50; p5[1] = q51; }
      SB();
      sA(i + 1, 3);
      SB();
      if (i == SIGI) {
        VMCNT(0);
        static_assert(SIGI <= PQB - 2, "");
#pragma unroll
        for (int j = PQB; j < 8; j++) asm volatile("" : "+v"(pf[j][0]), "+v"(pf[j][1]));
        unsigned old = 0;
        if (lane == 0) old = __hip_atomic_fetch_add((unsigned*)(smem + K2_LDS - 16), 1u, __ATOMIC_RELAXED, __HIP_MEMORY_SCOPE_WORKGROUP);
        if (lane == 0 && old == 7u) __hip_atomic_store((gu32*)(size_t)(ws + WS_CNT + (8 + xcd) * 128 + idx * 4), 1u, __ATOMIC_RELAXED, __HIP_MEMORY_SCOPE_AGENT);
      }
      if (i == B6I) { RAW_BARRIER(); }
    }
    s16x8 w2[7];
#pragma unroll
    for (int j = 0; j < 7; j++) w2[j] = j < 2 ? wfrag(wm + 3 * 6400 + (3 + j) * 32, 3200) : wfrag(wt + (j - 2) * 32, 12800);
    SB();
#pragma unroll
    for (int q = 0; q < 6; q++) proj(3, q);
#pragma unroll
    for (int q = 6; q < 20; q++) {
      const int mt = q < 10 ? q >> 1 : (q - 10) >> 1;
      acc[mt][q & 1] = __builtin_amdgcn_mfma_f32_16x16x32_bf16(w2[(q - 6) >> 1], __builtin_bit_cast(s16x8, q < 10 ? ob[1][q & 1] : o5[q & 1]), acc[mt][q & 1], 0, 0, 0);
    }
  }
  STAMP(1, 6);
  {
    char* sp = smem + 102400 + t * 30720 + l16;
#pragma unroll
    for (int q = 0; q < 10; q++) {
      const int own = q & 3;
      if (kh != own) *(f32x4*)(sp + (q * 3 + ((kh - own - 1) & 3)) * 1024) = acc[q >> 1][q & 1];
    }
  }
  RAW_BARRIER();
  STAMP(1, 7);
  {
    const char* sp = smem + 102400 + t * 30720 + l16;
    const float g1 = rb(1.f - g_r);
    const char* atp = smem + (kh >> 1) * 3200 + (4 * g + (n16 >> 2)) * 128 + (((32 * t + 16 * (kh & 1) + 4 * (lane & 3)) * 2) ^ ((lane & 8) << 3));
    u16* op = out + (size_t)(l0 + 32 * t + 16 * (kh & 1) + n16) * D + c0 + 16 * (kh >> 1) + 4 * g;
#pragma unroll
    for (int tl = 0; tl < 3; tl++) {
      if (tl == 2 && kh >= 2) break;
      f32x4 m;
      if (kh == 0)      m = acc[2 * tl][0];
      else if (kh == 1) m = acc[2 * tl][1];
      else if (kh == 2) m = acc[tl < 2 ? 2 * tl + 1 : 0][0];
      else              m = acc[tl < 2 ? 2 * tl + 1 : 0][1];
      const char* pp = sp + (tl * 4 + kh) * 3072;
      f32x4 p0 = *(const f32x4*)pp, p1 = *(const f32x4*)(pp + 1024), p2 = *(const f32x4*)(pp + 2048);
      f32x4 v = (m + p1) + (p0 + p2);
      s16x4 at = __builtin_amdgcn_ds_read_tr16_b64_v4i16((lds_s16x4_p)(atp + (tl < 2 ? tl * 6400 : 25600)));
      u32x2 au = __builtin_bit_cast(u32x2, at);
      u32x2 ov;
#pragma unroll
      for (int e = 0; e < 2; e++) {
        const f32x2 gb = (f32x2){rb1(v[2 * e]), rb1(v[2 * e + 1])} * g1, ga = (f32x2){lo_f(au[e]), hi_f(au[e])} * g_r;
        const f32x2 rr = (f32x2){rb1(ga[0]), rb1(ga[1])} + (f32x2){rb1(gb[0]), rb1(gb[1])};
        ov[e] = pack2(rr[0], rr[1]);
      }
      *(u32x2*)(op + 32 * tl) = ov;
    }
  }
  asm volatile("code_tail_mark:" ::: "memory");
  STAMP(1, 1);
}

extern "C" void kernel_launch(void* const* d_in, const int* in_sizes, int n_in, void* d_out, int out_size, void* d_ws, size_t ws_size,
                              hipStream_t stream) {
  const u16* u = (const u16*)d_in[0]; const u16* logA = (const u16*)d_in[1]; const u16* Aim = (const u16*)d_in[2];
  const u16* Cr = (const u16*)d_in[3]; const u16* Ci = (const u16*)d_in[4]; const u16* logdt = (const u16*)d_in[5];
  const u16* Dsk = (const u16*)d_in[6]; const u16* wqkv = (const u16*)d_in[8]; const u16* wo = (const u16*)d_in[9];
  const u16* gw = (const u16*)d_in[10];
  char* ws = (char*)d_ws;
  k1<<<NB_GEMM + NB_GATE + NB_KG, 512, 0, stream>>>(u, logA, Aim, Cr, Ci, logdt, wqkv, gw, wo, ws);
  k2<<<256, 576, 0, stream>>>(Dsk, wo, ws, (u16*)d_out);
}
```
